```python
import torch

def _softmax(x, axis=-1):
    return torch.softmax(x, dim=axis)
def _gelu(x):
    return torch.nn.functional.gelu(x, approximate="tanh")
def _silu(x):
    return torch.nn.functional.silu(x)
def _sigmoid(x):
    return torch.sigmoid(x)
def _layernorm(x, axis=-1, eps=1e-5):
    m = x.mean(dim=axis, keepdim=True)
    v = x.var(dim=axis, keepdim=True, unbiased=False)
    return (x - m) / torch.sqrt(v + eps)
def _scatter_add(tgt, idx, src, axis=0):
    return tgt.clone().index_add_(axis, idx, src)
def _segment_sum(src, seg, n):
    out = torch.zeros((n,) + src.shape[1:], dtype=src.dtype)
    return out.index_add_(0, seg, src)
def _pad(x, pw):
    flat = tuple(v for pair in reversed(pw) for v in pair)
    return torch.nn.functional.pad(x, flat)

kernel_name = 'parallel_sum__grouped_kv_mqa__glu_gated_swiglu__prompt'

def setup_inputs():

    _w = (lambda a: (lambda t: t.to(torch.bfloat16) if t.is_floating_point() else t)(torch.as_tensor(a)))
    def _A_setup():
        B, S, D, H = 1, 2048, 640, 32
        Dh = D // H
        return {
            "x": _w((lambda *s: torch.randn(*s, dtype=torch.bfloat16) * 0.05)(B, S, D)),
            "wq": _w((lambda *s: torch.randn(*s, dtype=torch.bfloat16) * 0.05)(D, H * Dh)),
            "wk": _w((lambda *s: torch.randn(*s, dtype=torch.bfloat16) * 0.05)(D, Dh)),
            "wv": _w((lambda *s: torch.randn(*s, dtype=torch.bfloat16) * 0.05)(D, Dh)),
            "wo": _w((lambda *s: torch.randn(*s, dtype=torch.bfloat16) * 0.05)(H * Dh, D)),
        }
    _A_in = _A_setup()

    def _B_setup():
        B, S, D, F = 1, 2048, 640, 1536
        return {
            "x": _w((lambda *s: torch.randn(*s, dtype=torch.bfloat16) * 0.05)(B, S, D)),
            "w1": _w((lambda *s: torch.randn(*s, dtype=torch.bfloat16) * 0.4)(D, F) * (2.0 / D) ** 0.5),
            "w3": _w((lambda *s: torch.randn(*s, dtype=torch.bfloat16) * 0.4)(D, F) * (2.0 / D) ** 0.5),
            "w2": _w((lambda *s: torch.randn(*s, dtype=torch.bfloat16) * 0.4)(F, D) * (2.0 / F) ** 0.5),
        }
    _B_in = _B_setup()

    return {
        "A_x": _A_in["x"],
        "A_wq": _A_in["wq"],
        "A_wk": _A_in["wk"],
        "A_wv": _A_in["wv"],
        "A_wo": _A_in["wo"],
        "B_x": _B_in["x"],
        "B_w1": _B_in["w1"],
        "B_w3": _B_in["w3"],
        "B_w2": _B_in["w2"],
    }

def reference(A_x, A_wq, A_wk, A_wv, A_wo, B_x, B_w1, B_w3, B_w2):
    def _A_ref(x, wq, wk, wv, wo):
        B, S, D = x.shape
        H = 32
        Dh = D // H
        q = (lambda x, *d: x.permute(*d))((x @ wq).reshape(B, S, H, Dh), 0, 2, 1, 3)
        k = x @ wk
        v = x @ wv
        scores = torch.einsum("bhqd,bkd->bhqk", q, k) * (Dh ** -0.5)
        p = _softmax(scores, axis=-1)
        o = torch.einsum("bhqk,bkd->bhqd", p, v)
        return (lambda x, *d: x.permute(*d))(o, 0, 2, 1, 3).reshape(B, S, H * Dh) @ wo

    def _B_ref(x, w1, w3, w2):
        gate = torch.einsum("bsd,df->bsf", x, w1)
        up = torch.einsum("bsd,df->bsf", x, w3)
        h = _silu(gate) * up
        out = torch.einsum("bsf,fd->bsd", h, w2)
        return out

    x = A_x
    xn = _layernorm(x, axis=-1)
    return x + _A_ref(xn, A_wq, A_wk, A_wv, A_wo) + _B_ref(xn, B_w1, B_w3, B_w2)
```

```cpp
#include <hip/hip_runtime.h>
#include <hip/hip_bf16.h>
#include <stdint.h>

#define AS1 __attribute__((address_space(1)))
#define AS3 __attribute__((address_space(3)))
typedef __bf16 bf16x2 __attribute__((ext_vector_type(2)));
typedef __bf16 bf16x8 __attribute__((ext_vector_type(8)));
typedef short s16x4 __attribute__((ext_vector_type(4)));
typedef short s16x8 __attribute__((ext_vector_type(8)));
typedef float f32x2 __attribute__((ext_vector_type(2)));
typedef float f32x4 __attribute__((ext_vector_type(4)));
typedef float f32x16 __attribute__((ext_vector_type(16)));
typedef unsigned u32x2 __attribute__((ext_vector_type(2)));
typedef unsigned u32x4 __attribute__((ext_vector_type(4)));
typedef AS1 unsigned gu32;
typedef AS1 unsigned long long gu64;

#define ATT_PRIO 2
#define GEM_PRIO 0
#define EPI_PRIO 3
#define CODE_PF 0
#define GU_DELAY 8
#define SPLIT_MODE 0

constexpr int SEQ = 2048, DM = 640, FF = 1536;
constexpr unsigned WS_KF = 0;
constexpr unsigned WS_HF = 16384;
constexpr unsigned WS_OF = 32768;
constexpr unsigned WS_FLAG_BYTES = 49152;
constexpr unsigned KVD = 5376;
constexpr unsigned KV_K1 = 2048, KV_V = 2560, KV_VT = 1408;
constexpr unsigned WS_KV = 65536;
constexpr unsigned WS_HT = WS_KV + 176128;
constexpr unsigned WS_OT = WS_HT + 64 * 96 * 1024;
constexpr unsigned WS_QT = WS_OT + 64 * 40 * 1024;
constexpr unsigned WS_PT = WS_QT + 64 * 32 * 2048;
constexpr unsigned WS_ST = WS_PT + 256 * 3 * 32 * 256;
constexpr unsigned L_PANEL = 0;
constexpr unsigned L_KV = 81920;
constexpr unsigned L_WQ = 81920;
constexpr unsigned L_GU = 124928;
constexpr unsigned L_WKV = 124928;
constexpr unsigned L_MISC = 161792;
constexpr unsigned L_TOTAL = 163840;
constexpr unsigned CHUNK = 2 * KVD;
constexpr int NCHUNK = 16;
enum { C_AP = 0  , C_GP = 4  , C_DP = 8  , C_PP = 12  , C_WP = 16  , C_KVP = 20, C_KVFIN, C_KVRDY, C_KN, C_VM, C_N };

struct Params { const char* x; const char* wq; const char* wk; const char* wv; const char* wo; const char* w1; const char* w3; const char* w2; char* out; char* ws; };

#define SBAR() __builtin_amdgcn_sched_barrier(0)
#define PIN(x) asm volatile("" : "+v"(x))
#define CFENCE() asm volatile("" ::: "memory")
#define WAITV(n) asm volatile("s_waitcnt vmcnt(" #n ")" ::: "memory")
#define MFMA(a, b, c) __builtin_amdgcn_mfma_f32_32x32x16_bf16((a), (b), (c), 0, 0, 0)
#define RLX_AGENT __ATOMIC_RELAXED, __HIP_MEMORY_SCOPE_AGENT
#define RLX_WG __ATOMIC_RELAXED, __HIP_MEMORY_SCOPE_WORKGROUP

#define STAMP(id) do {} while (0)
#define STAMPC(id) do {} while (0)

#define LP(T, off) ((AS3 T*)(uintptr_t)(unsigned)(off))
__device__ __forceinline__ bf16x8 ldsf(unsigned off) { return *LP(const bf16x8, off); }
__device__ __forceinline__ s16x4 ldstr(unsigned off) { return __builtin_amdgcn_ds_read_tr16_b64_v4i16(LP(s16x4, off)); }
__device__ __forceinline__ bf16x8 ldstr2(unsigned off0, unsigned off1) {
    s16x4 a = ldstr(off0), b = ldstr(off1);
    return __builtin_bit_cast(bf16x8, __builtin_shufflevector(a, b, 0, 1, 2, 3, 4, 5, 6, 7));
}
__device__ __forceinline__ unsigned pk2(float lo, float hi) { bf16x2 v = {(__bf16)lo, (__bf16)hi}; return __builtin_bit_cast(unsigned, v); }
__device__ __forceinline__ float bflo(unsigned u) { return __uint_as_float(u << 16); }
__device__ __forceinline__ float bfhi(unsigned u) { return __uint_as_float(u & 0xffff0000u); }
__device__ __forceinline__ float rbf(float f) { return (float)(__bf16)f; }

__device__ __forceinline__ void glds16(unsigned voff, const char* sbase, unsigned ldsdst) {
    unsigned keep;
    asm volatile("s_mov_b32 %0, m0\n\ts_mov_b32 m0, %3\n\ts_nop 2\n\tglobal_load_lds_dwordx4 %1, %2\n\ts_mov_b32 m0, %0"
                 : "=&s"(keep) : "v"(voff), "s"(sbase), "s"(ldsdst) : "memory");
}
__device__ __forceinline__ void glds16_sc1(unsigned voff, const char* sbase, unsigned ldsdst) {
    unsigned keep;
    asm volatile("s_mov_b32 %0, m0\n\ts_mov_b32 m0, %3\n\ts_nop 2\n\tglobal_load_lds_dwordx4 %1, %2 sc1\n\ts_mov_b32 m0, %0"
                 : "=&s"(keep) : "v"(voff), "s"(sbase), "s"(ldsdst) : "memory");
}
__device__ __forceinline__ void glds16x3_sc1(unsigned voff, const char* sbase, unsigned ldsdst) {
    unsigned keep;
    asm volatile("s_mov_b32 %0, m0\n\ts_mov_b32 m0, %3\n\ts_nop 2\n\tglobal_load_lds_dwordx4 %1, %2 sc1\n\tglobal_load_lds_dwordx4 %1, %2 offset:1024 sc1\n\tglobal_load_lds_dwordx4 %1, %2 offset:2048 sc1\n\ts_mov_b32 m0, %0"
                 : "=&s"(keep) : "v"(voff), "s"(sbase), "s"(ldsdst) : "memory");
}
#define GLOAD16(dst, voff, sbase, imm) asm volatile("s_nop 4\n\tglobal_load_dwordx4 %0, %1, %2 offset:%3" : "=v"(dst) : "v"(voff), "s"(sbase), "i"(imm) : "memory")

__device__ __forceinline__ AS3 unsigned* lcnt(int c) { return LP(unsigned, L_MISC) + c; }
__device__ __forceinline__ void larrive(int c, int lane) { CFENCE(); if (lane == 0) __hip_atomic_fetch_add(lcnt(c), 1u, RLX_WG); CFENCE(); }
__device__ __forceinline__ void lset(int c, unsigned v, int lane) { CFENCE(); if (lane == 0) __hip_atomic_store(lcnt(c), v, RLX_WG); CFENCE(); }
__device__ __forceinline__ void lwait(int c, unsigned target) {
    CFENCE();
    _Pragma("clang loop unroll(disable)") for (unsigned spins = 0; spins < (1u << 22); ++spins) {
        if ((unsigned)__builtin_amdgcn_readfirstlane(__hip_atomic_load(lcnt(c), RLX_WG)) >= target) break;
        __builtin_amdgcn_s_sleep(1);
    }
    CFENCE();
}
__device__ __forceinline__ void lwait_all(int c, int n, unsigned target, int lane) {
    CFENCE();
    AS3 unsigned* q = lcnt(c) + (lane < n ? lane : 0);
    _Pragma("clang loop unroll(disable)") for (unsigned spins = 0; spins < (1u << 16); ++spins) {
        if (__all(__hip_atomic_load(q, RLX_WG) >= target)) break;
        __builtin_amdgcn_s_sleep(1);
    }
    CFENCE();
}
__device__ __forceinline__ void lpost(int c, unsigned v) { CFENCE(); *(volatile AS3 unsigned*)lcnt(c) = v; CFENCE(); }
__device__ __forceinline__ void linc(unsigned vaddr, unsigned vone) {
    asm volatile("s_mov_b64 exec, 1\n\tds_add_u32 %0, %1\n\ts_mov_b64 exec, -1" :: "v"(vaddr), "v"(vone) : "memory");
}
__device__ __forceinline__ unsigned lpeek(int c, int lane) { return *((volatile AS3 unsigned*)lcnt(c) + (lane & 3)); }
__device__ __forceinline__ void lcheck(unsigned pk, int c, unsigned target, int lane) {
    if (__builtin_expect(__any(pk < target), 0)) lwait_all(c, 4, target, lane);
    CFENCE();
}
__device__ __forceinline__ void gwait(const char* ws, unsigned off, int n, unsigned tag, int lane) {
    gu32* f = (gu32*)(ws + off) + (lane < n ? lane : 0);
    _Pragma("clang loop unroll(disable)") for (unsigned spins = 0; spins < (1u << 14); ++spins) {
        unsigned v = __hip_atomic_load(f, RLX_AGENT);
        if (__all(v == tag)) break;
        __builtin_amdgcn_s_sleep(6);
    }
    CFENCE();
}
__device__ __forceinline__ float gwait64(const char* ws, unsigned off, unsigned tag, int lane) {
    gu64* f = (gu64*)(ws + off) + lane;
    unsigned long long v = 0;
    _Pragma("clang loop unroll(disable)") for (unsigned spins = 0; spins < (1u << 14); ++spins) {
        v = __hip_atomic_load(f, RLX_AGENT);
        if (__all((unsigned)(v >> 32) == tag)) break;
        __builtin_amdgcn_s_sleep(6);
    }
    CFENCE();
    return __uint_as_float((unsigned)v);
}
__device__ __forceinline__ __amdgpu_buffer_rsrc_t mkrsrc(const char* base, unsigned bytes) {
    return __builtin_amdgcn_make_buffer_rsrc((void*)base, 0, bytes, 0x00020000);
}
__device__ __forceinline__ float dpp_sum16(float v) {
    v += __int_as_float(__builtin_amdgcn_update_dpp(0, __float_as_int(v), 0x128, 0xf, 0xf, false));
    v += __int_as_float(__builtin_amdgcn_update_dpp(0, __float_as_int(v), 0x124, 0xf, 0xf, false));
    v += __int_as_float(__builtin_amdgcn_update_dpp(0, __float_as_int(v), 0x122, 0xf, 0xf, false));
    v += __int_as_float(__builtin_amdgcn_update_dpp(0, __float_as_int(v), 0x121, 0xf, 0xf, false));
    return v;
}
__device__ __forceinline__ float wave_max(float v) {
#pragma unroll
    for (int o = 32; o >= 1; o >>= 1) v = fmaxf(v, __shfl_xor(v, o));
    return v;
}
__device__ __forceinline__ bf16x8 pack8(const f32x16& a, int b, float sc) {
    u32x4 r = {pk2(a[b] * sc, a[b + 1] * sc), pk2(a[b + 2] * sc, a[b + 3] * sc), pk2(a[b + 4] * sc, a[b + 5] * sc), pk2(a[b + 6] * sc, a[b + 7] * sc)};
    return __builtin_bit_cast(bf16x8, r);
}

template <int I> struct IC { static constexpr int value = I; };
template <int B, int E, class F> __device__ __forceinline__ void static_for(F&& f) { if constexpr (B < E) { f(IC<B>{}); static_for<B + 1, E>(f); } }
#define WAITVN(n) asm volatile("s_waitcnt vmcnt(%0)" :: "i"(n) : "memory")
__device__ __forceinline__ void GLOAD16S(u32x4& dst, unsigned voff, const char* sbase) { asm volatile("global_load_dwordx4 %0, %1, %2 sc1" : "=v"(dst) : "v"(voff), "s"(sbase) : "memory"); }
__device__ __forceinline__ void GLOAD16N(u32x4& dst, unsigned voff, const char* sbase) { asm volatile("global_load_dwordx4 %0, %1, %2" : "=v"(dst) : "v"(voff), "s"(sbase) : "memory"); }
__device__ __forceinline__ void GLOAD16N_IF(u32x4& dst, unsigned voff, const char* sbase, int cond) {
    asm volatile("s_cmp_eq_u32 %3, 0\n\ts_cbranch_scc1 1f\n\tglobal_load_dwordx4 %0, %1, %2\n1:" : "=v"(dst) : "v"(voff), "s"(sbase), "s"(cond) : "memory", "scc"); }
template <int N> __device__ __forceinline__ void waitv5(u32x4& a, u32x4& b, u32x4& c, u32x4& d, u32x4& e) { asm volatile("s_waitcnt vmcnt(%5)" : "+v"(a), "+v"(b), "+v"(c), "+v"(d), "+v"(e) : "i"(N) : "memory"); }
__device__ __forceinline__ void gload8x2_sc1(unsigned long long& a, unsigned long long& b, unsigned voff, const char* sbase) {
    asm volatile("global_load_dwordx2 %0, %2, %3 sc1\n\tglobal_load_dwordx2 %1, %2, %3 offset:512 sc1" : "=&v"(a), "=&v"(b) : "v"(voff), "s"(sbase) : "memory"); }
__device__ __forceinline__ void waitv8x2(unsigned long long& a, unsigned long long& b) { asm volatile("s_waitcnt vmcnt(0)" : "+v"(a), "+v"(b) :: "memory"); }
__device__ __forceinline__ void PIN2(u32x4& a, u32x4& b) { asm volatile("" : "+v"(a), "+v"(b) :: "memory"); }
__device__ __forceinline__ u32x4 scale8(unsigned x0, unsigned x1, unsigned x2, unsigned x3, unsigned clo, unsigned chi, float c) {
    unsigned o0, o1, o2, o3; float t0, t1, t2, t3, t4, t5, t6, t7;
    asm("v_dot2_f32_bf16 %4, %12, %16, %18\n\tv_dot2_f32_bf16 %5, %12, %17, %18\n\tv_dot2_f32_bf16 %6, %13, %16, %18\n\tv_dot2_f32_bf16 %7, %13, %17, %18\n\t"
        "v_dot2_f32_bf16 %8, %14, %16, %18\n\tv_dot2_f32_bf16 %9, %14, %17, %18\n\tv_dot2_f32_bf16 %10, %15, %16, %18\n\tv_dot2_f32_bf16 %11, %15, %17, %18\n\t"
        "v_cvt_pk_bf16_f32 %0, %4, %5\n\tv_cvt_pk_bf16_f32 %1, %6, %7\n\tv_cvt_pk_bf16_f32 %2, %8, %9\n\tv_cvt_pk_bf16_f32 %3, %10, %11"
        : "=&v"(o0), "=&v"(o1), "=&v"(o2), "=&v"(o3), "=&v"(t0), "=&v"(t1), "=&v"(t2), "=&v"(t3), "=&v"(t4), "=&v"(t5), "=&v"(t6), "=&v"(t7)
        : "v"(x0), "v"(x1), "v"(x2), "v"(x3), "v"(clo), "v"(chi), "v"(c));
    return u32x4{o0, o1, o2, o3};
}
template <int N> __device__ __forceinline__ void waitv3(u32x4& a, u32x4& b, u32x4& c) { asm volatile("s_waitcnt vmcnt(%3)" : "+v"(a), "+v"(b), "+v"(c) : "i"(N) : "memory"); }

constexpr float QSCALE = 0.22360679774997896f * 1.4426950408889634f;
constexpr float BOUND_MAX = 50.f;

struct AttSync { int lane; const char* kv; unsigned dvoff; unsigned wo; int w; };
__device__ __forceinline__ unsigned kvslot(int c) { return L_KV + ((c + 2) & 3) * CHUNK; }
__device__ __forceinline__ void att_dma(const AttSync& a, int c) { glds16x3_sc1(a.dvoff, a.kv + (size_t)c * CHUNK, kvslot(c) + a.wo); }
constexpr unsigned AP0 = 16;
#define RING_X(c) do { WAITV(3); lpost(C_AP + as.w, AP0 + (c) + 1); } while (0)
#define RING_Y(c, pk) do { lcheck(pk, C_AP, AP0 + (c) + 1, lane); const int cn_ = (c) + 3 < NCHUNK ? (c) + 3 : NCHUNK - 1;   \
                           glds16x3_sc1(as.dvoff, as.kv + (size_t)cn_ * CHUNK, kvslot((c) + 3) + as.wo); } while (0)

__device__ __forceinline__ void att_fast(const AttSync& as, bf16x8 Q0a, bf16x8 Q1a0, bf16x8 Q1a1, bf16x8 Q0b, bf16x8 Q1b0, bf16x8 Q1b1, f32x16& Oa, f32x16& Ob) {
    const int lane = as.lane, r = lane & 31, h = lane >> 5;
    const unsigned ak0 = lane * 16, ak1 = h ? KV_V + 20 * 16 : KV_K1 + r * 16;
    const unsigned av = KV_V + h * 352 + (r <= 20 ? r : 21) * 16;
    const f32x16 zero = {};
    f32x16 Sa, Sb = {};
    bf16x8 K0[2], K1[2], V0[2], V1[2];
    u32x4 Pa0, Pa1, Pb0 = {}, Pb1 = {};
    unsigned pk = 0;
    V0[1] = bf16x8{}; V1[1] = bf16x8{};
    unsigned vk0 = L_KV + ak0, vk1 = L_KV + ak1, vv = L_KV + av;
    PIN(vk0); PIN(vk1); PIN(vv);
    K0[0] = ldsf(vk0 + 2 * CHUNK); K1[0] = ldsf(vk1 + 2 * CHUNK); K0[1] = ldsf(vk0 + 2 * CHUNK + 1024); V1[0] = ldsf(vv + 2 * CHUNK + 704); V0[0] = ldsf(vv + 2 * CHUNK);
    Sa = MFMA(K0[0], Q0a, zero); Sa = MFMA(K1[0], Q1a0, Sa);
#define EX4(S, i) do { S[i] = __builtin_amdgcn_exp2f(S[i]); S[i + 1] = __builtin_amdgcn_exp2f(S[i + 1]); S[i + 2] = __builtin_amdgcn_exp2f(S[i + 2]); S[i + 3] = __builtin_amdgcn_exp2f(S[i + 3]); } while (0)
#define GAP(MF, LD, T, k, PW, j, S, i) do { MF; SBAR(); LD; PW[j] = pk2(T[k], T[k + 1]); PW[j + 1] = pk2(T[k + 2], T[k + 3]); PIN(PW); SBAR(); EX4(S, i); PIN(S); SBAR(); } while (0)
#define BC(x) __builtin_bit_cast(bf16x8, x)
#define STEP(e, d, QB1, QA1N, KN, L3, LK0, LK1, LV) do { \
        GAP(Ob = MFMA(V0[e ^ 1], BC(Pb0), Ob), (void)0,                  Sb, 12, Pb1, 2, Sa, 0); \
        GAP(Sb = MFMA(K0[e], Q0b, zero),       (void)0,                  Sa, 0, Pa0, 0, Sa, 4); \
        GAP(Sb = MFMA(K1[d], QB1, Sb),         L3,                       Sa, 4, Pa0, 2, Sa, 8); \
        GAP(Ob = MFMA(V1[e ^ 1], BC(Pb1), Ob), K0[e] = ldsf(LK0),        Sa, 8, Pa1, 0, Sa, 12); \
        GAP(Oa = MFMA(V0[e], BC(Pa0), Oa),     LK1,                      Sa, 12, Pa1, 2, Sb, 0); \
        GAP(Sa = MFMA(K0[e ^ 1], Q0a, zero),   V1[e ^ 1] = ldsf((LV) + 704), Sb, 0, Pb0, 0, Sb, 4); \
        GAP(Sa = MFMA(K1[KN], QA1N, Sa),       V0[e ^ 1] = ldsf(LV),     Sb, 4, Pb0, 2, Sb, 8); \
        GAP(Oa = MFMA(V1[e], BC(Pa1), Oa),     (void)0,                  Sb, 8, Pb1, 0, Sb, 12); \
    } while (0)
#define CHUNK4(c, B, N) do { \
        STEP(0, 0, Q1b0, Q1a1, 0, (void)0, vk0 + (B) + KVD, K1[1] = ldsf(vk1 + (B) + KVD), vv + (B) + KV_VT); \
        RING_X(c); \
        STEP(1, 0, Q1b1, Q1a0, 1, pk = lpeek(C_AP, lane), vk0 + (B) + KVD + 1024, (void)0, vv + (B) + KVD); \
        RING_Y(c, pk); \
        STEP(0, 1, Q1b0, Q1a1, 1, (void)0, vk0 + (N), K1[0] = ldsf(vk1 + (N)), vv + (B) + KVD + KV_VT); \
        STEP(1, 1, Q1b1, Q1a0, 0, (void)0, vk0 + (N) + 1024, (void)0, vv + (N)); \
    } while (0)
    for (int c = 0; c < NCHUNK; c += 4) {
        CHUNK4(c, 2 * CHUNK, 3 * CHUNK); CHUNK4(c + 1, 3 * CHUNK, 0); CHUNK4(c + 2, 0, CHUNK); CHUNK4(c + 3, CHUNK, 2 * CHUNK);
    }
#undef CHUNK4
    Pb1[2] = pk2(Sb[12], Sb[13]); Pb1[3] = pk2(Sb[14], Sb[15]);
    Ob = MFMA(V0[1], BC(Pb0), Ob); Ob = MFMA(V1[1], BC(Pb1), Ob);
#undef STEP
#undef GAP
#undef EX4
}

__device__ __forceinline__ void att_safe(const AttSync& as, bf16x8 Q0a, bf16x8 Q1a0, bf16x8 Q1a1, bf16x8 Q0b, bf16x8 Q1b0, bf16x8 Q1b1, f32x16& Oa, f32x16& Ob) {
    const int lane = as.lane, r = lane & 31, h = lane >> 5;
    const unsigned ak0 = lane * 16, ak1 = h ? KV_V + 20 * 16 : KV_K1 + r * 16;
    const unsigned av = KV_V + h * 352 + (r <= 20 ? r : 21) * 16;
    const f32x16 zero = {};
    float ma = -INFINITY, mb = -INFINITY;
    for (int c = 0; c < NCHUNK; ++c) {
        const unsigned b0 = kvslot(c);
        for (int t = 0; t < 4; ++t) {
            if (t == 1) RING_X(c);
            if (t == 2) RING_Y(c, 0u);
            const unsigned base = b0 + (t >> 1) * KVD; const int e = t & 1;
            bf16x8 K0 = ldsf(base + ak0 + e * 1024), K1 = ldsf(base + ak1), V0 = ldsf(base + av + e * KV_VT), V1 = ldsf(base + av + e * KV_VT + 704);
#pragma unroll
            for (int q = 0; q < 2; ++q) {
                f32x16 S = MFMA(K0, q ? Q0b : Q0a, zero); S = MFMA(K1, q ? (e ? Q1b1 : Q1b0) : (e ? Q1a1 : Q1a0), S);
                float mx = S[0];
#pragma unroll
                for (int i = 1; i < 16; ++i) mx = fmaxf(mx, S[i]);
                mx = fmaxf(mx, __shfl_xor(mx, 32));
                float& m = q ? mb : ma; f32x16& O = q ? Ob : Oa;
                const float mn = fmaxf(m, mx);
                const float al = __builtin_amdgcn_exp2f(m - mn);
                m = mn;
#pragma unroll
                for (int i = 0; i < 16; ++i) { O[i] *= al; S[i] = __builtin_amdgcn_exp2f(S[i] - mn); }
                O = MFMA(V0, pack8(S, 0, 1.f), O); O = MFMA(V1, pack8(S, 8, 1.f), O);
            }
        }
    }
}

__device__ __forceinline__ void xload4(u32x4 (&x)[5], const char* xb, unsigned vo) {
    asm volatile("global_load_dwordx4 %0, %5, %6\n\tglobal_load_dwordx4 %1, %5, %6 offset:256\n\tglobal_load_dwordx4 %2, %5, %6 offset:512\n\t"
                 "global_load_dwordx4 %3, %5, %6 offset:768\n\tglobal_load_dwordx4 %4, %5, %6 offset:1024"
                 : "=&v"(x[0]), "=&v"(x[1]), "=&v"(x[2]), "=&v"(x[3]), "=&v"(x[4]) : "v"(vo), "s"(xb) : "memory");
}
__device__ __forceinline__ void ln_rows(const u32x4 (&xr)[5], int row0, int lane) {
    const bf16x2 ones = {(__bf16)1.f, (__bf16)1.f};
    float s0 = 0.f, s1 = 0.f, q0 = 0.f, q1 = 0.f;
#pragma unroll
    for (int u = 0; u < 5; ++u)
#pragma unroll
        for (int e = 0; e < 4; e += 2) {
            const unsigned xe = xr[u][e], xf = xr[u][e + 1];
            const bf16x2 v = __builtin_bit_cast(bf16x2, xe), v2 = __builtin_bit_cast(bf16x2, xf);
            s0 = __builtin_amdgcn_fdot2_f32_bf16(v, ones, s0, false); q0 = __builtin_amdgcn_fdot2_f32_bf16(v, v, q0, false);
            s1 = __builtin_amdgcn_fdot2_f32_bf16(v2, ones, s1, false); q1 = __builtin_amdgcn_fdot2_f32_bf16(v2, v2, q1, false);
        }
    const float s = dpp_sum16(s0 + s1), ss = dpp_sum16(q0 + q1);
    const float mean = s * (1.f / 640.f);
    const float var = fmaxf(ss * (1.f / 640.f) - mean * mean, 0.f);
    const __bf16 rsb = (__bf16)__builtin_amdgcn_rsqf(rbf(rbf(var) + 1e-5f));
    const float mo = -rbf(mean) * (float)rsb;
    const unsigned clo = (unsigned)__builtin_bit_cast(unsigned short, rsb), chi = clo << 16;
    const int row = row0 + (lane >> 4), c = lane & 15;
    const unsigned wa = L_PANEL + (row >> 5) * 40960 + (c >> 1) * 1024 + (c & 1) * 512 + (((row & 31) ^ (c & 7)) << 4);
    static_for<0, 5>([&](auto U) {
        constexpr int u = U.value;
        *LP(u32x4, wa + u * 8192) = scale8(xr[u][0], xr[u][1], xr[u][2], xr[u][3], clo, chi, mo);
    });
}
#define BLOCK_BARRIER() asm volatile("s_waitcnt lgkmcnt(0)\n\ts_barrier" ::: "memory")

template <int PH>
__global__ void __launch_bounds__(512, 2) mega(Params p) {
    constexpr bool PRO = PH & 1, ATT = PH & 2, DN = PH & 8, WO = PH & 16;
    const int w = __builtin_amdgcn_readfirstlane(threadIdx.x >> 6);
    const int b = blockIdx.x, xc = b & 7, j = b >> 3;
    const int ti = (xc >> 1) * 8 + (j >> 2), g = (xc & 1) * 4 + (j & 3);
    const unsigned tag = 1u;
    char* ws = p.ws;
    STAMP(0);
    if (threadIdx.x < C_N) *lcnt(threadIdx.x) = threadIdx.x == C_WP ? 100u : 0u;
    const bool haskv = (g >> 2) == (ti & 1);
    const char* wqb = p.wq + g * 160;
    const int kmt = g & 1;
    const char* xb = p.x + (size_t)ti * 64 * 1280;
#define LANE_CONSTS() int lane = __builtin_amdgcn_mbcnt_hi(~0u, __builtin_amdgcn_mbcnt_lo(~0u, 0u)); PIN(lane); \
    const int r = lane & 31, h = lane >> 5, tq = (lane & 15) >> 2, tp = lane & 3, tb = (lane >> 4) & 1; (void)r; (void)h; (void)tq; (void)tp; (void)tb

    if (w < 4) {
        LANE_CONSTS();
        const unsigned xvo = (lane >> 4) * 1280 + (lane & 15) * 16;
        bf16x8 Q0a, Q0b; u32x2 q1a, q1b;
        const int head = 4 * g + w;
        const AttSync as{lane, ws + WS_KV, (unsigned)((w < 3 ? w * 3072 : CHUNK - 3072) + lane * 16), (unsigned)(w < 3 ? w * 3072 : CHUNK - 3072), w};
        __builtin_amdgcn_s_setprio(3);
        if constexpr (PRO) {
            u32x4 x0[5], x1[5], x2[5], x3[5];
            const int ra = kmt * 32 + w * 8, rb = (kmt ^ 1) * 32 + w * 8;
            xload4(x0, xb + (size_t)ra * 1280, xvo); xload4(x1, xb + (size_t)(ra + 4) * 1280, xvo);
            xload4(x2, xb + (size_t)rb * 1280, xvo); xload4(x3, xb + (size_t)(rb + 4) * 1280, xvo);
            BLOCK_BARRIER();
            waitv5<15>(x0[0], x0[1], x0[2], x0[3], x0[4]);
            STAMP(1);
            ln_rows(x0, ra, lane);
            waitv5<10>(x1[0], x1[1], x1[2], x1[3], x1[4]);
            ln_rows(x1, ra + 4, lane);
            lpost(C_AP + w, 1);
            waitv5<5>(x2[0], x2[1], x2[2], x2[3], x2[4]);
            ln_rows(x2, rb, lane);
            waitv5<0>(x3[0], x3[1], x3[2], x3[3], x3[4]);
            ln_rows(x3, rb + 4, lane);
            lpost(C_AP + w, 2);
            unsigned pa[4];
#pragma unroll
            for (int s = 0; s < 4; ++s) pa[s] = L_PANEL + h * 512 + ((r ^ ((2 * s + h) & 7)) << 4);
            f32x16 qa = {}, qb = {};
            const unsigned wa = L_WQ + (8 * h + tq) * 160 + w * 40 + tb * 32 + (tb ? 0 : tp * 8);
            lwait_all(C_AP, 4, 2, lane); lwait_all(C_PP, 4, 1, lane);
            STAMP(2);
            bf16x8 fw[2][4], fa[2][4], fb[2][4];
#pragma unroll
            for (int s4 = 0; s4 < 4; ++s4) { fw[0][s4] = ldstr2(wa + s4 * 2560, wa + s4 * 2560 + 640); fa[0][s4] = ldsf(pa[s4] + s4 * 1024); fb[0][s4] = ldsf(pa[s4] + 40960 + s4 * 1024); }
            unsigned pk = 0;
            static_for<0, 10>([&](auto S) {
                constexpr int st = S.value, nx = st + 1;
                if constexpr (st == 3 || st == 5 || st == 7) lcheck(pk, C_WP, (st - 1) / 2, lane);
#pragma unroll
                for (int s4 = 0; s4 < 4; ++s4) {
                    qa = MFMA(fw[st & 1][s4], fa[st & 1][s4], qa); SBAR();
                    if constexpr (nx < 10) { const unsigned o = wa + (nx & 3) * 10240 + s4 * 2560; fw[nx & 1][s4] = ldstr2(o, o + 640); SBAR(); }
                    qb = MFMA(fw[st & 1][s4], fb[st & 1][s4], qb); SBAR();
                    if constexpr (nx < 10) { fa[nx & 1][s4] = ldsf(pa[s4] + (nx * 4 + s4) * 1024); fb[nx & 1][s4] = ldsf(pa[s4] + 40960 + (nx * 4 + s4) * 1024); SBAR(); }
                    if (s4 == 2 && (st == 2 || st == 4 || st == 6)) { pk = lpeek(C_WP, lane); SBAR(); }
                }
                if constexpr (!(st & 1) && st <= 6) lpost(C_AP + w, 3 + st / 2);
            });
            lpost(C_AP + w, 8);
            Q0a = pack8(qa, 0, QSCALE); Q0b = pack8(qb, 0, QSCALE);
            q1a = u32x2{pk2(qa[8] * QSCALE, qa[9] * QSCALE), pk2(qa[10] * QSCALE, qa[11] * QSCALE)};
            q1b = u32x2{pk2(qb[8] * QSCALE, qb[9] * QSCALE), pk2(qb[10] * QSCALE, qb[11] * QSCALE)};
            if (h) { q1a = u32x2{0u, 0u}; q1b = u32x2{0u, 0u}; }
            if constexpr (!ATT) {
                char* qt = ws + WS_QT + (size_t)((2 * ti) * 32 + head) * 2048 + lane * 16;
                *(bf16x8*)qt = Q0a; *(u32x2*)(qt + 1024) = q1a; *(bf16x8*)(qt + 65536) = Q0b; *(u32x2*)(qt + 65536 + 1024) = q1b;
            }
            STAMP(3);
        } else {
            BLOCK_BARRIER();
            if constexpr (ATT) {
                const char* qt = ws + WS_QT + (size_t)((2 * ti) * 32 + head) * 2048 + lane * 16;
                Q0a = *(const bf16x8*)qt; q1a = *(const u32x2*)(qt + 1024); Q0b = *(const bf16x8*)(qt + 65536); q1b = *(const u32x2*)(qt + 65536 + 1024);
            }
        }
        if constexpr (ATT) {
            float na = 0.f, nb = 0.f;
            {
                const u32x4 a0 = __builtin_bit_cast(u32x4, Q0a), b0 = __builtin_bit_cast(u32x4, Q0b);
#pragma unroll
                for (int e = 0; e < 4; ++e) { na += bflo(a0[e]) * bflo(a0[e]) + bfhi(a0[e]) * bfhi(a0[e]); nb += bflo(b0[e]) * bflo(b0[e]) + bfhi(b0[e]) * bfhi(b0[e]); }
#pragma unroll
                for (int e = 0; e < 2; ++e) { na += bflo(q1a[e]) * bflo(q1a[e]) + bfhi(q1a[e]) * bfhi(q1a[e]); nb += bflo(q1b[e]) * bflo(q1b[e]) + bfhi(q1b[e]) * bfhi(q1b[e]); }
                na += __shfl_xor(na, 32); nb += __shfl_xor(nb, 32);
            }
            if constexpr (PRO) lwait_all(C_AP, 4, 8, lane);
            lwait(C_KVRDY, 1);
            STAMP(4);
            att_dma(as, 2);
            const float kn2 = __uint_as_float(*lcnt(C_KN)), vmx = __uint_as_float(*lcnt(C_VM));
            const float Ma = __builtin_sqrtf(na * kn2) * 1.01f, Mb = __builtin_sqrtf(nb * kn2) * 1.01f;
            const bool fast = __all(Ma <= BOUND_MAX && Mb <= BOUND_MAX && vmx <= 1e30f);
            const unsigned sa = (fast && h) ? pk2(-Ma, 0.f) : q1a[0], sb = (fast && h) ? pk2(-Mb, 0.f) : q1b[0];
            const u32x4 a0 = {sa, q1a[1], 0u, 0u}, a1 = {h ? sa : 0u, 0u, q1a[0], q1a[1]}, b0 = {sb, q1b[1], 0u, 0u}, b1 = {h ? sb : 0u, 0u, q1b[0], q1b[1]};
            f32x16 Oa = {}, Ob = {};
            lwait(C_KVRDY, 2);
            __builtin_amdgcn_s_setprio(ATT_PRIO);
            STAMP(5); STAMPC(12);
            if (fast) att_fast(as, Q0a, BC(a0), BC(a1), Q0b, BC(b0), BC(b1), Oa, Ob);
            else att_safe(as, Q0a, BC(a0), BC(a1), Q0b, BC(b0), BC(b1), Oa, Ob);
            STAMP(6); STAMPC(13);
            const __amdgpu_buffer_rsrc_t ro = mkrsrc(ws + WS_OT, 64 * 40 * 1024);
#pragma unroll
            for (int q = 0; q < 2; ++q) {
                const f32x16& O = q ? Ob : Oa;
                const float inv = 1.f / __shfl(O[8], r + 32);
                const unsigned base = ((2 * ti + q) * 40 + 5 * g) * 1024;
                __builtin_amdgcn_raw_buffer_store_b128(__builtin_bit_cast(u32x4, pack8(O, 0, inv)), ro, base + w * 1024 + lane * 16, 0, 16);
                if (!h) {
                    u32x2 t = {pk2(O[8] * inv, O[9] * inv), pk2(O[10] * inv, O[11] * inv)};
                    __builtin_amdgcn_raw_buffer_store_b64(t, ro, base + 4096 + ((w >> 1) * 32 + r) * 16 + (w & 1) * 8, 0, 16);
                }
            }
            if constexpr (WO) {
                WAITV(0);
                if (lane < 2) __hip_atomic_store((gu32*)(ws + WS_OF) + (2 * ti + lane) * 32 + head, tag, RLX_AGENT);
            }
            STAMP(7);
        }
    } else {
        LANE_CONSTS();
        const unsigned xvo = (lane >> 4) * 1280 + (lane & 15) * 16;
        const int v = w - 4;
        __builtin_amdgcn_s_setprio(3);
        const unsigned bq = ((tb * 32 + (tp >> 1) * 16) ^ ((unsigned)(tq >> 1) << 6)) + (tp & 1) * 8;
        auto kv_ready = [&]() {
            float kn2 = gwait64(ws, WS_KF + xc * 1024, tag, lane), vmx = gwait64(ws, WS_KF + xc * 1024 + 512, tag, lane);
            if constexpr (PRO) lwait_all(C_AP, 4, 6, lane);
#pragma unroll
            for (int n = 0; n < 7; ++n) glds16x3_sc1(lane * 16 + n * 3072, ws + WS_KV, L_KV + 2 * CHUNK + n * 3072);
            kn2 = wave_max(kn2); vmx = wave_max(vmx);
            if (lane == 0) { *lcnt(C_KN) = __float_as_uint(kn2); *lcnt(C_VM) = __float_as_uint(vmx); }
            lpost(C_KVRDY, 1);
            WAITV(0);
            lpost(C_KVRDY, 2);
        };
        if constexpr (PRO) {
            const int kvm = 2 * ti + (g & 1); const bool isV = (g >> 1) & 1;
            const char* wkvb = isV ? p.wv : p.wk;
            BLOCK_BARRIER();
            if (haskv) {
#pragma unroll
                for (int n = 0; n < 7; ++n) {
                    int piece = v + 4 * n; piece = piece > 24 ? 24 : piece;
                    glds16(piece * 1024 + lane * 16, wkvb, L_WKV + piece * 1024);
                }
            }
#pragma unroll
            for (int n = 0; n < 10; ++n) {
                const unsigned c = (v + 4 * n) * 64 + lane, row = c / 10u, col = c - row * 10u;
                glds16(row * 1280 + col * 16, wqb, L_WQ + (v + 4 * n) * 1024);
            }
            u32x4 wst[3][7];
            const int v3 = v ? v - 1 : 0;
            auto wst_issue = [&](auto I) {
                constexpr int rr = I.value / 7, i = I.value % 7;
                int piece = v3 + 3 * i; piece = piece > 19 ? 19 : piece;
                const unsigned c = (40 + 20 * rr + piece) * 64 + lane, row = c / 10u, col = c - row * 10u;
                GLOAD16N_IF(wst[rr][i], row * 1280 + col * 16, wqb, v);
            };
            static_for<0, 7>(wst_issue);
            const int rot = (ti & 7) * 4;
            const char* wsrc = ((v >> 1) ? p.w3 : p.w1) + g * 384 + (size_t)(v & 1) * 8 * 3072;
            unsigned gvo[3], gwa[3];
#pragma unroll
            for (int n = 0; n < 3; ++n) {
                const unsigned c = n * 64 + lane, row = c / 24u, col = c - row * 24u;
                gvo[n] = row * 3072 + col * 16;
                gwa[n] = L_GU + (v >> 1) * 6144 + ((v & 1) * 8 + row) * 384 + ((col ^ ((row & 2) << 1)) << 4);
            }
            constexpr int GQ = 5;
            u32x4 gq[GQ][3];
            auto gissue = [&](auto S) {
                constexpr int s = S.value, q = s % GQ;
                int ks = s + rot; ks = ks >= 40 ? ks - 40 : ks;
                const char* sb = wsrc + (size_t)ks * 16 * 3072;
                GLOAD16N(gq[q][0], gvo[0], sb); GLOAD16N(gq[q][1], gvo[1], sb); GLOAD16N(gq[q][2], gvo[2], sb);
            };
            auto gwrite = [&](auto S) {
                constexpr int s = S.value, q = s % GQ, last = s + GQ - 1 < 39 ? s + GQ - 1 : 39;
                waitv3<3 * (last - s)>(gq[q][0], gq[q][1], gq[q][2]);
#pragma unroll
                for (int n = 0; n < 3; ++n) *LP(u32x4, gwa[n] + (s % 3) * 12288) = gq[q][n];
                if constexpr (s + GQ < 40) gissue(IC<s + GQ>{});
            };
            STAMP(1);
            if (v) WAITV(17); else WAITV(10);
            larrive(C_KVP, lane);
            STAMP(2);
            if (haskv) {
                f32x16 acc = {};
                const unsigned wa = L_WKV + (v * 160 + 8 * h + tq) * 40 + tb * 32 + (tb ? 0 : tp * 8);
                const unsigned xo = L_PANEL + kmt * 40960 + v * 10240 + h * 512;
                lwait(C_KVP, 4);
                bf16x8 wf[10], xf[10];
#pragma unroll
                for (int s = 0; s < 10; ++s) wf[s] = ldstr2(wa + s * 640, wa + s * 640 + 160);
                lwait_all(C_AP, 4, 1, lane);
#pragma unroll
                for (int s = 0; s < 10; ++s) xf[s] = ldsf(xo + s * 1024 + ((r ^ ((2 * s + 4 * v + h) & 7)) << 4));
                SBAR();
                if (isV) {
#pragma unroll
                    for (int s = 0; s < 10; ++s) acc = MFMA(xf[s], wf[s], acc);
                } else {
#pragma unroll
                    for (int s = 0; s < 10; ++s) acc = MFMA(wf[s], xf[s], acc);
                }
                if (v) WAITV(7); else WAITV(0);
                lpost(C_PP + v, 1);
                if (v) {
#pragma unroll
                    for (int e = 0; e < 4; ++e) *LP(f32x4, L_WKV + v * 6400 + e * 1024 + lane * 16) = f32x4{acc[4 * e], acc[4 * e + 1], acc[4 * e + 2], acc[4 * e + 3]};
                    larrive(C_KVP, lane);
                } else {
                    STAMP(12);
                    lwait(C_KVP, 7);
                    STAMP(13);
#pragma unroll
                    for (int o = 1; o < 4; ++o)
#pragma unroll
                        for (int e = 0; e < 4; ++e) {
                            const f32x4 t = *LP(const f32x4, L_WKV + o * 6400 + e * 1024 + lane * 16);
                            acc[4 * e] += t[0]; acc[4 * e + 1] += t[1]; acc[4 * e + 2] += t[2]; acc[4 * e + 3] += t[3];
                        }
                    const __amdgpu_buffer_rsrc_t rk = mkrsrc(ws + WS_KV, 32 * KVD);
                    const unsigned base = ti * KVD, ke = g & 1;
                    const u32x4 f0 = __builtin_bit_cast(u32x4, pack8(acc, 0, 1.f)), f1 = __builtin_bit_cast(u32x4, pack8(acc, 8, 1.f));
                    float mx;
                    if (!isV) {
                        __builtin_amdgcn_raw_buffer_store_b128(f0, rk, base + ke * 1024 + lane * 16, 0, 16);
                        const u32x2 k1 = {f1[0], f1[1]};
                        if (!h) __builtin_amdgcn_raw_buffer_store_b64(k1, rk, base + KV_K1 + r * 16 + ke * 8, 0, 16);
                        float n2 = 0.f;
#pragma unroll
                        for (int e = 0; e < 4; ++e) n2 += bflo(f0[e]) * bflo(f0[e]) + bfhi(f0[e]) * bfhi(f0[e]);
                        if (!h) n2 += bflo(f1[0]) * bflo(f1[0]) + bfhi(f1[0]) * bfhi(f1[0]) + bflo(f1[1]) * bflo(f1[1]) + bfhi(f1[1]) * bfhi(f1[1]);
                        n2 += __shfl_xor(n2, 32);
                        mx = (n2 == n2) ? n2 : INFINITY;
                    } else {
                        const unsigned cst = r == 20 ? 0x3f803f80u : 0u;
                        const u32x4 c4 = {cst, cst, cst, cst};
                        if (r <= 21) {
                            __builtin_amdgcn_raw_buffer_store_b128(r >= 20 ? c4 : f0, rk, base + KV_V + ke * KV_VT + h * 352 + r * 16, 0, 16);
                            __builtin_amdgcn_raw_buffer_store_b128(r >= 20 ? c4 : f1, rk, base + KV_V + ke * KV_VT + 704 + h * 352 + r * 16, 0, 16);
                        }
                        float vm = 0.f;
#pragma unroll
                        for (int e = 0; e < 16; ++e) { const float a = fabsf(acc[e]); vm = (a <= vm) ? vm : ((a == a) ? a : INFINITY); }
                        mx = r < 20 ? vm : 0.f;
                    }
                    lset(C_KVFIN, 1u, lane);
                    STAMP(14);
                    mx = wave_max(mx);
                    WAITV(0);
                    STAMP(15);
                    if (lane < 8) __hip_atomic_store((gu64*)(ws + WS_KF + lane * 1024 + (isV ? 512 : 0)) + kvm, ((unsigned long long)tag << 32) | __float_as_uint(mx), RLX_AGENT);
                }
            }
            else {
                if (v) WAITV(7); else WAITV(0);
                lpost(C_PP + v, 1);
            }
            STAMP(3);
            if (haskv) lwait(C_KVFIN, 1);
            static_for<7, 21>(wst_issue);
            if (v) {
                static_for<0, 3>([&](auto R) {
                    constexpr int rr = R.value;
                    waitv5<14 - 7 * rr>(wst[rr][0], wst[rr][1], wst[rr][2], wst[rr][3], wst[rr][4]); PIN2(wst[rr][5], wst[rr][6]);
                    lwait_all(C_AP, 4, 3 + rr, lane);
#pragma unroll
                    for (int i = 0; i < 7; ++i) { int piece = v3 + 3 * i; piece = piece > 19 ? 19 : piece; *LP(u32x4, L_WQ + (rr & 1) * 20480 + piece * 1024 + lane * 16) = wst[rr][i]; }
                    lpost(C_WP + v, rr + 1);
                });
            } else if constexpr (ATT) kv_ready();
            lwait_all(C_AP, 4, GU_DELAY, lane);
            static_for<0, GQ>(gissue);
            STAMP(4);
            {
                const int mt = v & 1, fh = v >> 1;
                f32x16 acc[6] = {};
                const unsigned ba = L_GU + (8 * h + tq) * 384;
                unsigned bn[3];
#pragma unroll
                for (int n = 0; n < 3; ++n) bn[n] = ba + (((fh * 3 + n) * 64) ^ bq);
                unsigned pa[4];
#pragma unroll
                for (int s = 0; s < 4; ++s) pa[s] = L_PANEL + mt * 40960 + rot * 1024 + h * 512 + ((r ^ ((2 * s + h) & 7)) << 4);
                bf16x8 wf[2][6], xf[2];
                unsigned vaddr = L_MISC + (C_GP + v) * 4, vone = 1u; PIN(vaddr); PIN(vone);
                auto rdx = [&](auto S) {
                    constexpr int s = S.value;
                    if constexpr ((s & 3) == 0 && s > 0) { if (s + rot == 40) { pa[0] -= 40960; pa[1] -= 40960; pa[2] -= 40960; pa[3] -= 40960; } }
                    xf[s & 1] = ldsf(pa[s & 3] + s * 1024);
                };
                auto rdw = [&](auto S, auto N) {
                    constexpr int s = S.value, n = N.value;
                    const unsigned o = bn[n % 3] + (n / 3) * 6144 + (s % 3) * 12288; wf[s & 1][n] = ldstr2(o, o + 1536);
                };
                static_for<0, 3>(gwrite);
                linc(vaddr, vone); lwait_all(C_GP, 4, 1u, lane);
                rdx(IC<0>{}); static_for<0, 6>([&](auto N) { rdw(IC<0>{}, N); });
                linc(vaddr, vone);
                __builtin_amdgcn_s_setprio(GEM_PRIO);
                static_for<0, 40>([&](auto S) {
                    constexpr int s = S.value; constexpr bool nx = s + 1 < 40;
                    unsigned pk = 0;
                    acc[0] = MFMA(wf[s & 1][0], xf[s & 1], acc[0]); SBAR();
                    if constexpr (nx) { rdx(IC<s + 1>{}); rdw(IC<s + 1>{}, IC<0>{}); rdw(IC<s + 1>{}, IC<1>{}); } SBAR();
                    acc[1] = MFMA(wf[s & 1][1], xf[s & 1], acc[1]); SBAR();
                    if constexpr (nx) { rdw(IC<s + 1>{}, IC<2>{}); rdw(IC<s + 1>{}, IC<3>{}); } SBAR();
                    acc[2] = MFMA(wf[s & 1][2], xf[s & 1], acc[2]); SBAR();
                    if constexpr (nx) { rdw(IC<s + 1>{}, IC<4>{}); rdw(IC<s + 1>{}, IC<5>{}); }
                    if constexpr (s + 2 < 40) pk = lpeek(C_GP, lane);
                    SBAR();
                    acc[3] = MFMA(wf[s & 1][3], xf[s & 1], acc[3]); SBAR();
                    acc[4] = MFMA(wf[s & 1][4], xf[s & 1], acc[4]); SBAR();
                    if constexpr (s + 2 < 40) lcheck(pk, C_GP, s + 2, lane);
                    if constexpr (s + 3 < 40) gwrite(IC<s + 3>{});
                    linc(vaddr, vone);
                    SBAR();
                    acc[5] = MFMA(wf[s & 1][5], xf[s & 1], acc[5]); SBAR();
                });
                __builtin_amdgcn_s_setprio(3);
                STAMP(6);
                const __amdgpu_buffer_rsrc_t rh = mkrsrc(ws + WS_HT, 64 * 96 * 1024);
#pragma unroll
                for (int n = 0; n < 3; ++n) {
                    f32x16 hv;
#pragma unroll
                    for (int e = 0; e < 16; ++e) {
                        const float gg = acc[n][e];
                        hv[e] = gg * __builtin_amdgcn_rcpf(1.f + __builtin_amdgcn_exp2f(-1.4426950408889634f * gg)) * acc[n + 3][e];
                    }
                    const unsigned base = ((2 * ti + mt) * 96 + g * 12 + fh * 6 + n * 2) * 1024 + lane * 16;
                    __builtin_amdgcn_raw_buffer_store_b128(__builtin_bit_cast(u32x4, pack8(hv, 0, 1.f)), rh, base, 0, 16);
                    __builtin_amdgcn_raw_buffer_store_b128(__builtin_bit_cast(u32x4, pack8(hv, 8, 1.f)), rh, base + 1024, 0, 16);
                }
                if constexpr (DN) {
                    WAITV(0);
                    if (lane == 0) __hip_atomic_store((gu32*)(ws + WS_HF) + (2 * ti + mt) * 16 + g * 2 + fh, tag, RLX_AGENT);
                }
                STAMP(7);
            }
        } else {
            BLOCK_BARRIER();
            if constexpr (ATT) { if (v == 0) kv_ready(); }
        }
        if constexpr (DN || WO) {
            LANE_CONSTS();
            const unsigned bq = ((tb * 32 + (tp >> 1) * 16) ^ ((unsigned)(tq >> 1) << 6)) + (tp & 1) * 8;
            const unsigned drow = lane >> 3, dcol = ((lane & 7) ^ (((lane >> 4) & 1) << 2)) * 16;
            const int G = xc >> 1, r0 = G < 2 ? 6 * G : 12 + 5 * (G - 2), nr = G < 2 ? 6 : 5;
            if (j < 5 * nr) {
                const int dr = r0 + j / 5, dc = (xc & 1) * 5 + j % 5;
                const int mfirst = dr < 20 ? 3 * dr : 60 + 2 * (dr - 20), nm = dr < 20 ? 3 : 2;
                const unsigned dvo = drow * 1280 + dcol;
                const unsigned myring = L_PANEL + v * 20480;
                f32x16 acc0 = {}, acc1 = {};
                if constexpr (PRO) { lwait_all(C_GP, 4, 42, lane); lwait_all(C_AP, 4, 8, lane); }
                if constexpr (DN) {
                    f32x16 da[3][2] = {};
                    const int drot = (j % 5) * 4;
                    const char* bsrc = p.w2 + (size_t)v * 384 * 1280 + dc * 128;
                    const char* asrc = ws + WS_HT + ((size_t)mfirst * 96 + v * 24) * 1024;
                    const unsigned am1 = lane * 16 + 98304, am2 = lane * 16 + (nm == 3 ? 196608 : 98304);
                    u32x4 aq[8][3];
                    auto bissue = [&](auto K) {
                        constexpr int k = K.value;
                        int kr = k + drot; kr = kr >= 24 ? kr - 24 : kr;
                        const char* sb = bsrc + (size_t)kr * 16 * 1280;
                        glds16(dvo, sb, myring + (k % 10) * 2048); glds16(dvo + 8 * 1280, sb, myring + (k % 10) * 2048 + 1024);
                    };
                    auto aissue = [&](auto K) {
                        constexpr int k = K.value;
                        int kr = k + drot; kr = kr >= 24 ? kr - 24 : kr;
                        const char* sb = asrc + (size_t)kr * 1024;
                        GLOAD16S(aq[k & 7][0], lane * 16, sb); GLOAD16S(aq[k & 7][1], am1, sb); GLOAD16S(aq[k & 7][2], am2, sb);
                    };
                    static_for<0, 8>(bissue);
                    if constexpr (PRO) {
                        gu32* f = (gu32*)(ws + WS_HF) + (mfirst + ((lane >> 2) < nm ? (lane >> 2) : 0)) * 16 + 4 * v + (lane & 3);
                        _Pragma("clang loop unroll(disable)") for (unsigned spins = 0; spins < (1u << 14); ++spins) {
                            if (__all(__hip_atomic_load(f, RLX_AGENT) == tag)) break;
                            __builtin_amdgcn_s_sleep(6);
                        }
                        CFENCE();
                    }
                    STAMP(8);
                    static_for<0, 8>(aissue);
                    const unsigned ba0 = myring + (4 * h + tq) * 128 + bq, ba1 = ba0 ^ 64;
                    bf16x8 w0[2], w1[2];
                    WAITVN(24);
                    __builtin_amdgcn_s_setprio(GEM_PRIO);
                    w0[0] = ldstr2(ba0, ba0 + 1024); w1[0] = ldstr2(ba1, ba1 + 1024);
                    static_for<0, 24>([&](auto K) {
                        constexpr int k = K.value;
                        constexpr int yk = k <= 7 ? 21 + 2 * k : (k < 16 ? 35 : 5 * (23 - k));
                        constexpr int n = k + 1 <= 7 ? yk : yk - 2;
                        waitv3<(n < 0 ? 0 : n)>(aq[k & 7][0], aq[k & 7][1], aq[k & 7][2]);
                        constexpr unsigned o = ((k + 1) % 10) * 2048;
#define DXF(m) __builtin_bit_cast(bf16x8, aq[k & 7][m])
                        SBAR();
                        da[0][0] = MFMA(w0[k & 1], DXF(0), da[0][0]); SBAR();
                        if constexpr (k + 1 < 24) w0[(k + 1) & 1] = ldstr2(ba0 + o, ba0 + o + 1024);
                        SBAR();
                        da[0][1] = MFMA(w1[k & 1], DXF(0), da[0][1]); SBAR();
                        if constexpr (k + 1 < 24) w1[(k + 1) & 1] = ldstr2(ba1 + o, ba1 + o + 1024);
                        SBAR();
                        da[1][0] = MFMA(w0[k & 1], DXF(1), da[1][0]); SBAR();
                        if constexpr (k + 8 < 24) bissue(IC<k + 8>{});
                        SBAR();
                        da[1][1] = MFMA(w1[k & 1], DXF(1), da[1][1]); SBAR();
                        da[2][0] = MFMA(w0[k & 1], DXF(2), da[2][0]); SBAR();
                        da[2][1] = MFMA(w1[k & 1], DXF(2), da[2][1]); SBAR();
                        if constexpr (k + 8 < 24) aissue(IC<k + 8>{});
                    });
                    __builtin_amdgcn_s_setprio(3);
                    STAMP(9);
                    if constexpr (WO) {
#pragma unroll
                        for (int u = 0; u < 20; ++u) glds16(dvo + u * 8 * 1280, p.wo + (size_t)v * 160 * 1280 + dc * 128, myring + u * 1024);
                    }
#pragma unroll
                    for (int n = 0; n < 2; ++n) {
#pragma unroll
                        for (int m = 0; m < 3; ++m) {
                            if (m != v) {
                                const unsigned dst = L_GU + (m * 3 + (v > m ? v - 1 : v)) * 4096 + lane * 16;
#pragma unroll
                                for (int e = 0; e < 4; ++e) *LP(f32x4, dst + e * 1024) = f32x4{da[m][n][4 * e], da[m][n][4 * e + 1], da[m][n][4 * e + 2], da[m][n][4 * e + 3]};
                            }
                        }
                        lpost(C_DP + v, 2 * n + 1); lwait_all(C_DP, 4, 2 * n + 1, lane);
                        if (v < 3) {
                            f32x16 t = v == 0 ? da[0][n] : (v == 1 ? da[1][n] : da[2][n]);
#pragma unroll
                            for (int o = 0; o < 3; ++o)
#pragma unroll
                                for (int e = 0; e < 4; ++e) {
                                    const f32x4 u = *LP(const f32x4, L_GU + (v * 3 + o) * 4096 + e * 1024 + lane * 16);
                                    t[4 * e] += u[0]; t[4 * e + 1] += u[1]; t[4 * e + 2] += u[2]; t[4 * e + 3] += u[3];
                                }
                            if (n) acc1 = t; else acc0 = t;
                        }
                        if (n == 0) { lpost(C_DP + v, 2); lwait_all(C_DP, 4, 2, lane); }
                    }
                    if constexpr (!WO) {
                        if (v < 3) {
                            float* pt = (float*)(ws + WS_PT) + (size_t)(b * 3 + v) * 2048 + lane;
#pragma unroll
                            for (int e = 0; e < 16; ++e) { pt[e * 64] = acc0[e]; pt[1024 + e * 64] = acc1[e]; }
                        }
                    }
                } else {
#pragma unroll
                    for (int u = 0; u < 20; ++u) glds16(dvo + u * 8 * 1280, p.wo + (size_t)v * 160 * 1280 + dc * 128, myring + u * 1024);
                    if (v < 3) {
                        const float* pt = (const float*)(ws + WS_PT) + (size_t)(b * 3 + v) * 2048 + lane;
#pragma unroll
                        for (int e = 0; e < 16; ++e) { acc0[e] = pt[e * 64]; acc1[e] = pt[1024 + e * 64]; }
                    }
                }
                if constexpr (WO) { WAITV(0); lpost(C_DP + v, 4); }
                if (v < nm) {
                    const int m = mfirst + v;
                    const unsigned ba0 = L_PANEL + (4 * h + tq) * 128 + bq, ba1 = ba0 ^ 64;
                    if constexpr (WO) {
                        if constexpr (ATT) gwait(ws, WS_OF + m * 128, 32, tag, lane);
                        STAMP(10);
                        const __amdgpu_buffer_rsrc_t ro = mkrsrc(ws + WS_OT + (size_t)m * 40960, 40960);
                        u32x4 of[40];
#pragma unroll
                        for (int s = 0; s < 40; ++s) of[s] = __builtin_amdgcn_raw_buffer_load_b128(ro, lane * 16, s * 1024, 16);
                        const char* xrow = p.x + (size_t)(m * 32 + r) * 1280 + dc * 128 + h * 16;
                        u32x4 xres[4];
#pragma unroll
                        for (int e = 0; e < 4; ++e) xres[e] = *(const u32x4*)(xrow + e * 32);
                        lwait_all(C_DP, 4, 4, lane);
                        const unsigned bl0 = L_PANEL + (40 * h + 16 + tq) * 128 + bq, bl1 = bl0 ^ 64;
#pragma unroll
                        for (int s = 0; s < 40; ++s) {
                            const int gg = s / 5, jj = s % 5;
                            const bf16x8 xf = __builtin_bit_cast(bf16x8, of[s]);
                            if (jj < 4) {
                                const unsigned o = (80 * gg + 20 * jj) * 128;
                                acc0 = MFMA(ldstr2(ba0 + o, ba0 + o + 1024), xf, acc0);
                                acc1 = MFMA(ldstr2(ba1 + o, ba1 + o + 1024), xf, acc1);
                            } else {
                                const unsigned o = 80 * gg * 128;
                                acc0 = MFMA(ldstr2(bl0 + o, bl0 + o + 2560), xf, acc0);
                                acc1 = MFMA(ldstr2(bl1 + o, bl1 + o + 2560), xf, acc1);
                            }
                        }
                        char* orow = p.out + (size_t)(m * 32 + r) * 1280 + dc * 128 + h * 16;
#pragma unroll
                        for (int n = 0; n < 2; ++n) {
                            const f32x16& a = n ? acc1 : acc0;
#pragma unroll
                            for (int qq = 0; qq < 2; ++qq) {
                                float lo[4], hi[4];
#pragma unroll
                                for (int e = 0; e < 4; ++e) {
                                    auto sw = __builtin_amdgcn_permlane32_swap(__float_as_uint(a[8 * qq + e]), __float_as_uint(a[8 * qq + 4 + e]), false, false);
                                    lo[e] = __uint_as_float(sw[0]); hi[e] = __uint_as_float(sw[1]);
                                }
                                const u32x4 xv = xres[2 * n + qq];
                                const u32x4 ov = {pk2(lo[0] + bflo(xv[0]), lo[1] + bfhi(xv[0])), pk2(lo[2] + bflo(xv[1]), lo[3] + bfhi(xv[1])),
                                                  pk2(hi[0] + bflo(xv[2]), hi[1] + bfhi(xv[2])), pk2(hi[2] + bflo(xv[3]), hi[3] + bfhi(xv[3]))};
                                *(u32x4*)(orow + (2 * n + qq) * 32) = ov;
                            }
                        }
                        STAMP(11);
                    }
                }
            }
        }
    }
}

template <int PH>
static void launch(const Params& p, hipStream_t stream) {
    (void)hipFuncSetAttribute((const void*)mega<PH>, hipFuncAttributeMaxDynamicSharedMemorySize, L_TOTAL);
    mega<PH><<<256, 512, L_TOTAL, stream>>>(p);
}

extern "C" void kernel_launch(void* const* d_in, const int* in_sizes, int n_in, void* d_out, int out_size, void* d_ws, size_t ws_size, hipStream_t stream) {
    Params p;
    p.x = (const char*)d_in[0]; p.wq = (const char*)d_in[1]; p.wk = (const char*)d_in[2]; p.wv = (const char*)d_in[3]; p.wo = (const char*)d_in[4];
    p.w1 = (const char*)d_in[6]; p.w3 = (const char*)d_in[7]; p.w2 = (const char*)d_in[8];
    p.out = (char*)d_out; p.ws = (char*)d_ws;
    (void)hipMemsetAsync(d_ws, 0, WS_FLAG_BYTES, stream);
    launch<27>(p, stream);
}
```

```cpp
#include <hip/hip_runtime.h>
#include <hip/hip_bf16.h>
#include <stdint.h>

#define AS1 __attribute__((address_space(1)))
#define AS3 __attribute__((address_space(3)))
typedef __bf16 bf16x2 __attribute__((ext_vector_type(2)));
typedef __bf16 bf16x8 __attribute__((ext_vector_type(8)));
typedef short s16x4 __attribute__((ext_vector_type(4)));
typedef short s16x8 __attribute__((ext_vector_type(8)));
typedef float f32x2 __attribute__((ext_vector_type(2)));
typedef float f32x4 __attribute__((ext_vector_type(4)));
typedef float f32x16 __attribute__((ext_vector_type(16)));
typedef unsigned u32x2 __attribute__((ext_vector_type(2)));
typedef unsigned u32x4 __attribute__((ext_vector_type(4)));
typedef AS1 unsigned gu32;
typedef AS1 unsigned long long gu64;

#define ATT_PRIO 2
#define GEM_PRIO 0
#define EPI_PRIO 3
#define CODE_PF 0
#define GU_DELAY 8
#define SPLIT_MODE 0

constexpr int SEQ = 2048, DM = 640, FF = 1536;
constexpr unsigned WS_KF = 0;
constexpr unsigned WS_HF = 16384;
constexpr unsigned WS_OF = 32768;
constexpr unsigned WS_EP = 49152;
constexpr unsigned KVD = 5376;
constexpr unsigned KV_K1 = 2048, KV_V = 2560, KV_VT = 1408;
constexpr unsigned WS_KV = 65536;
constexpr unsigned WS_HT = WS_KV + 176128;
constexpr unsigned WS_OT = WS_HT + 64 * 96 * 1024;
constexpr unsigned WS_QT = WS_OT + 64 * 40 * 1024;
constexpr unsigned WS_PT = WS_QT + 64 * 32 * 2048;
constexpr unsigned WS_ST = WS_PT + 256 * 3 * 32 * 256;
constexpr unsigned L_PANEL = 0;
constexpr unsigned L_KV = 81920;
constexpr unsigned L_WQ = 81920;
constexpr unsigned L_GU = 124928;
constexpr unsigned L_WKV = 124928;
constexpr unsigned L_MISC = 161792;
constexpr unsigned L_TOTAL = 163840;
constexpr unsigned CHUNK = 2 * KVD;
constexpr int NCHUNK = 16;
enum { C_AP = 0  , C_GP = 4  , C_DP = 8  , C_PP = 12  , C_WP = 16  , C_KVP = 20, C_KVFIN, C_KVRDY, C_KN, C_VM, C_N };

struct Params { const char* x; const char* wq; const char* wk; const char* wv; const char* wo; const char* w1; const char* w3; const char* w2; char* out; char* ws; };

#define SBAR() __builtin_amdgcn_sched_barrier(0)
#define PIN(x) asm volatile("" : "+v"(x))
#define CFENCE() asm volatile("" ::: "memory")
#define WAITV(n) asm volatile("s_waitcnt vmcnt(" #n ")" ::: "memory")
#define MFMA(a, b, c) __builtin_amdgcn_mfma_f32_32x32x16_bf16((a), (b), (c), 0, 0, 0)
#define RLX_AGENT __ATOMIC_RELAXED, __HIP_MEMORY_SCOPE_AGENT
#define RLX_WG __ATOMIC_RELAXED, __HIP_MEMORY_SCOPE_WORKGROUP

#define TMO(spins, lim, code, tgt) do {} while (0)
#define STAMP(id) do {} while (0)
#define STAMPC(id) do {} while (0)

#define LP(T, off) ((AS3 T*)(uintptr_t)(unsigned)(off))
__device__ __forceinline__ bf16x8 ldsf(unsigned off) { return *LP(const bf16x8, off); }
__device__ __forceinline__ s16x4 ldstr(unsigned off) { return __builtin_amdgcn_ds_read_tr16_b64_v4i16(LP(s16x4, off)); }
__device__ __forceinline__ bf16x8 ldstr2(unsigned off0, unsigned off1) {
    s16x4 a = ldstr(off0), b = ldstr(off1);
    return __builtin_bit_cast(bf16x8, __builtin_shufflevector(a, b, 0, 1, 2, 3, 4, 5, 6, 7));
}
__device__ __forceinline__ unsigned pk2(float lo, float hi) { bf16x2 v = {(__bf16)lo, (__bf16)hi}; return __builtin_bit_cast(unsigned, v); }
__device__ __forceinline__ float bflo(unsigned u) { return __uint_as_float(u << 16); }
__device__ __forceinline__ float bfhi(unsigned u) { return __uint_as_float(u & 0xffff0000u); }
__device__ __forceinline__ float rbf(float f) { return (float)(__bf16)f; }

__device__ __forceinline__ void glds16(unsigned voff, const char* sbase, unsigned ldsdst) {
    unsigned keep;
    asm volatile("s_mov_b32 %0, m0\n\ts_mov_b32 m0, %3\n\ts_nop 2\n\tglobal_load_lds_dwordx4 %1, %2\n\ts_mov_b32 m0, %0"
                 : "=&s"(keep) : "v"(voff), "s"(sbase), "s"(ldsdst) : "memory");
}
__device__ __forceinline__ void glds16_sc1(unsigned voff, const char* sbase, unsigned ldsdst) {
    unsigned keep;
    asm volatile("s_mov_b32 %0, m0\n\ts_mov_b32 m0, %3\n\ts_nop 2\n\tglobal_load_lds_dwordx4 %1, %2 sc1\n\ts_mov_b32 m0, %0"
                 : "=&s"(keep) : "v"(voff), "s"(sbase), "s"(ldsdst) : "memory");
}
__device__ __forceinline__ void glds16x3_sc1(unsigned voff, const char* sbase, unsigned ldsdst) {
    unsigned keep;
    asm volatile("s_mov_b32 %0, m0\n\ts_mov_b32 m0, %3\n\ts_nop 2\n\tglobal_load_lds_dwordx4 %1, %2 sc1\n\tglobal_load_lds_dwordx4 %1, %2 offset:1024 sc1\n\tglobal_load_lds_dwordx4 %1, %2 offset:2048 sc1\n\ts_mov_b32 m0, %0"
                 : "=&s"(keep) : "v"(voff), "s"(sbase), "s"(ldsdst) : "memory");
}
#define GLOAD16(dst, voff, sbase, imm) asm volatile("s_nop 4\n\tglobal_load_dwordx4 %0, %1, %2 offset:%3" : "=v"(dst) : "v"(voff), "s"(sbase), "i"(imm) : "memory")

__device__ __forceinline__ AS3 unsigned* lcnt(int c) { return LP(unsigned, L_MISC) + c; }
__device__ __forceinline__ void larrive(int c, int lane) { CFENCE(); if (lane == 0) __hip_atomic_fetch_add(lcnt(c), 1u, RLX_WG); CFENCE(); }
__device__ __forceinline__ void lset(int c, unsigned v, int lane) { CFENCE(); if (lane == 0) __hip_atomic_store(lcnt(c), v, RLX_WG); CFENCE(); }
__device__ __forceinline__ void lwait(int c, unsigned target) {
    CFENCE();
    _Pragma("clang loop unroll(disable)") for (unsigned spins = 0; spins < (1u << 22); ++spins) {
        if ((unsigned)__builtin_amdgcn_readfirstlane(__hip_atomic_load(lcnt(c), RLX_WG)) >= target) break;
        TMO(spins, 1u << 16, 100 + c, target);
        __builtin_amdgcn_s_sleep(1);
    }
    CFENCE();
}
__device__ __forceinline__ void lwait_all(int c, int n, unsigned target, int lane) {
    CFENCE();
    AS3 unsigned* q = lcnt(c) + (lane < n ? lane : 0);
    _Pragma("clang loop unroll(disable)") for (unsigned spins = 0; spins < (1u << 16); ++spins) {
        if (__all(__hip_atomic_load(q, RLX_WG) >= target)) break;
        TMO(spins, 1u << 16, 200 + c, target);
        __builtin_amdgcn_s_sleep(1);
    }
    CFENCE();
}
__device__ __forceinline__ void lpost(int c, unsigned v) { CFENCE(); *(volatile AS3 unsigned*)lcnt(c) = v; CFENCE(); }
__device__ __forceinline__ void linc(unsigned vaddr, unsigned vone) {
    asm volatile("s_mov_b64 exec, 1\n\tds_add_u32 %0, %1\n\ts_mov_b64 exec, -1" :: "v"(vaddr), "v"(vone) : "memory");
}
__device__ __forceinline__ unsigned lpeek(int c, int lane) { return *((volatile AS3 unsigned*)lcnt(c) + (lane & 3)); }
__device__ __forceinline__ void lcheck(unsigned pk, int c, unsigned target, int lane) {
    if (__builtin_expect(__any(pk < target), 0)) lwait_all(c, 4, target, lane);
    CFENCE();
}
__device__ __forceinline__ void gwait(const char* ws, unsigned off, int n, unsigned tag, int lane) {
    gu32* f = (gu32*)(ws + off) + (lane < n ? lane : 0);
    _Pragma("clang loop unroll(disable)") for (unsigned spins = 0; spins < (1u << 14); ++spins) {
        unsigned v = __hip_atomic_load(f, RLX_AGENT);
        if (__all(v == tag)) break;
        TMO(spins, 1u << 14, 300, off);
        __builtin_amdgcn_s_sleep(6);
    }
    CFENCE();
}
__device__ __forceinline__ float gwait64(const char* ws, unsigned off, unsigned tag, int lane) {
    gu64* f = (gu64*)(ws + off) + lane;
    unsigned long long v = 0;
    _Pragma("clang loop unroll(disable)") for (unsigned spins = 0; spins < (1u << 14); ++spins) {
        v = __hip_atomic_load(f, RLX_AGENT);
        if (__all((unsigned)(v >> 32) == tag)) break;
        __builtin_amdgcn_s_sleep(6);
    }
    CFENCE();
    return __uint_as_float((unsigned)v);
}
__device__ __forceinline__ __amdgpu_buffer_rsrc_t mkrsrc(const char* base, unsigned bytes) {
    return __builtin_amdgcn_make_buffer_rsrc((void*)base, 0, bytes, 0x00020000);
}
__device__ __forceinline__ float dpp_sum16(float v) {
    v += __int_as_float(__builtin_amdgcn_update_dpp(0, __float_as_int(v), 0x128, 0xf, 0xf, false));
    v += __int_as_float(__builtin_amdgcn_update_dpp(0, __float_as_int(v), 0x124, 0xf, 0xf, false));
    v += __int_as_float(__builtin_amdgcn_update_dpp(0, __float_as_int(v), 0x122, 0xf, 0xf, false));
    v += __int_as_float(__builtin_amdgcn_update_dpp(0, __float_as_int(v), 0x121, 0xf, 0xf, false));
    return v;
}
__device__ __forceinline__ float wave_max(float v) {
    int x = __float_as_int(v);
    x = max(x, __builtin_amdgcn_update_dpp(0, x, 0x128, 0xf, 0xf, false)); x = max(x, __builtin_amdgcn_update_dpp(0, x, 0x124, 0xf, 0xf, false));
    x = max(x, __builtin_amdgcn_update_dpp(0, x, 0x122, 0xf, 0xf, false)); x = max(x, __builtin_amdgcn_update_dpp(0, x, 0x121, 0xf, 0xf, false));
    const int a = max(__builtin_amdgcn_readlane(x, 0), __builtin_amdgcn_readlane(x, 16)), b = max(__builtin_amdgcn_readlane(x, 32), __builtin_amdgcn_readlane(x, 48));
    return __int_as_float(max(a, b));
}
__device__ __forceinline__ bf16x8 pack8(const f32x16& a, int b, float sc) {
    u32x4 r = {pk2(a[b] * sc, a[b + 1] * sc), pk2(a[b + 2] * sc, a[b + 3] * sc), pk2(a[b + 4] * sc, a[b + 5] * sc), pk2(a[b + 6] * sc, a[b + 7] * sc)};
    return __builtin_bit_cast(bf16x8, r);
}

template <int I> struct IC { static constexpr int value = I; };
template <int B, int E, class F> __device__ __forceinline__ void static_for(F&& f) { if constexpr (B < E) { f(IC<B>{}); static_for<B + 1, E>(f); } }
#define WAITVN(n) asm volatile("s_waitcnt vmcnt(%0)" :: "i"(n) : "memory")
__device__ __forceinline__ void GLOAD16S(u32x4& dst, unsigned voff, const char* sbase) { asm volatile("global_load_dwordx4 %0, %1, %2 sc1" : "=v"(dst) : "v"(voff), "s"(sbase) : "memory"); }
__device__ __forceinline__ void GLOAD16N(u32x4& dst, unsigned voff, const char* sbase) { asm volatile("global_load_dwordx4 %0, %1, %2" : "=v"(dst) : "v"(voff), "s"(sbase) : "memory"); }
__device__ __forceinline__ void GLOAD16N_IF(u32x4& dst, unsigned voff, const char* sbase, int cond) {
    asm volatile("s_cmp_eq_u32 %3, 0\n\ts_cbranch_scc1 1f\n\tglobal_load_dwordx4 %0, %1, %2\n1:" : "=v"(dst) : "v"(voff), "s"(sbase), "s"(cond) : "memory", "scc"); }
template <int N> __device__ __forceinline__ void waitv5(u32x4& a, u32x4& b, u32x4& c, u32x4& d, u32x4& e) { asm volatile("s_waitcnt vmcnt(%5)" : "+v"(a), "+v"(b), "+v"(c), "+v"(d), "+v"(e) : "i"(N) : "memory"); }
__device__ __forceinline__ void gload8x2_sc1(unsigned long long& a, unsigned long long& b, unsigned voff, const char* sbase) {
    asm volatile("global_load_dwordx2 %0, %2, %3 sc1\n\tglobal_load_dwordx2 %1, %2, %3 offset:512 sc1" : "=&v"(a), "=&v"(b) : "v"(voff), "s"(sbase) : "memory"); }
__device__ __forceinline__ void waitv8x2(unsigned long long& a, unsigned long long& b) { asm volatile("s_waitcnt vmcnt(0)" : "+v"(a), "+v"(b) :: "memory"); }
__device__ __forceinline__ void PIN2(u32x4& a, u32x4& b) { asm volatile("" : "+v"(a), "+v"(b) :: "memory"); }
__device__ __forceinline__ u32x4 scale8(unsigned x0, unsigned x1, unsigned x2, unsigned x3, unsigned clo, unsigned chi, float c) {
    unsigned o0, o1, o2, o3; float t0, t1, t2, t3, t4, t5, t6, t7;
    asm("v_dot2_f32_bf16 %4, %12, %16, %18\n\tv_dot2_f32_bf16 %5, %12, %17, %18\n\tv_dot2_f32_bf16 %6, %13, %16, %18\n\tv_dot2_f32_bf16 %7, %13, %17, %18\n\t"
        "v_dot2_f32_bf16 %8, %14, %16, %18\n\tv_dot2_f32_bf16 %9, %14, %17, %18\n\tv_dot2_f32_bf16 %10, %15, %16, %18\n\tv_dot2_f32_bf16 %11, %15, %17, %18\n\t"
        "v_cvt_pk_bf16_f32 %0, %4, %5\n\tv_cvt_pk_bf16_f32 %1, %6, %7\n\tv_cvt_pk_bf16_f32 %2, %8, %9\n\tv_cvt_pk_bf16_f32 %3, %10, %11"
        : "=&v"(o0), "=&v"(o1), "=&v"(o2), "=&v"(o3), "=&v"(t0), "=&v"(t1), "=&v"(t2), "=&v"(t3), "=&v"(t4), "=&v"(t5), "=&v"(t6), "=&v"(t7)
        : "v"(x0), "v"(x1), "v"(x2), "v"(x3), "v"(clo), "v"(chi), "v"(c));
    return u32x4{o0, o1, o2, o3};
}
template <int N> __device__ __forceinline__ void waitv3(u32x4& a, u32x4& b, u32x4& c) { asm volatile("s_waitcnt vmcnt(%3)" : "+v"(a), "+v"(b), "+v"(c) : "i"(N) : "memory"); }

constexpr float QSCALE = 0.22360679774997896f * 1.4426950408889634f;
constexpr float BOUND_MAX = 50.f;

struct AttSync { int lane; const char* kv; unsigned dvoff; unsigned wo; int w; };
__device__ __forceinline__ unsigned kvslot(int c) { return L_KV + ((c + 2) & 3) * CHUNK; }
__device__ __forceinline__ void att_dma(const AttSync& a, int c) { glds16x3_sc1(a.dvoff, a.kv + (size_t)c * CHUNK, kvslot(c) + a.wo); }
constexpr unsigned AP0 = 16;
#define RING_X(c) do { WAITV(3); lpost(C_AP + as.w, AP0 + (c) + 1); } while (0)
#define RING_Y(c, pk) do { lcheck(pk, C_AP, AP0 + (c) + 1, lane); const int cn_ = (c) + 3 < NCHUNK ? (c) + 3 : NCHUNK - 1;   \
                           glds16x3_sc1(as.dvoff, as.kv + (size_t)cn_ * CHUNK, kvslot((c) + 3) + as.wo); } while (0)

__device__ __forceinline__ void att_fast(const AttSync& as, bf16x8 Q0a, bf16x8 Q1a0, bf16x8 Q1a1, bf16x8 Q0b, bf16x8 Q1b0, bf16x8 Q1b1, f32x16& Oa, f32x16& Ob) {
    const int lane = as.lane, r = lane & 31, h = lane >> 5;
    const unsigned ak0 = lane * 16, ak1 = h ? KV_V + 20 * 16 : KV_K1 + r * 16;
    const unsigned av = KV_V + h * 352 + (r <= 20 ? r : 21) * 16;
    const f32x16 zero = {};
    f32x16 Sa, Sb = {};
    bf16x8 K0[2], K1[2], V0[2], V1[2];
    u32x4 Pa0, Pa1, Pb0 = {}, Pb1 = {};
    unsigned pk = 0;
    V0[1] = bf16x8{}; V1[1] = bf16x8{};
    unsigned vk0 = L_KV + ak0, vk1 = L_KV + ak1, vv = L_KV + av;
    PIN(vk0); PIN(vk1); PIN(vv);
    K0[0] = ldsf(vk0 + 2 * CHUNK); K1[0] = ldsf(vk1 + 2 * CHUNK); K0[1] = ldsf(vk0 + 2 * CHUNK + 1024); V1[0] = ldsf(vv + 2 * CHUNK + 704); V0[0] = ldsf(vv + 2 * CHUNK);
    Sa = MFMA(K0[0], Q0a, zero); Sa = MFMA(K1[0], Q1a0, Sa);
#define EX4(S, i) do { S[i] = __builtin_amdgcn_exp2f(S[i]); S[i + 1] = __builtin_amdgcn_exp2f(S[i + 1]); S[i + 2] = __builtin_amdgcn_exp2f(S[i + 2]); S[i + 3] = __builtin_amdgcn_exp2f(S[i + 3]); } while (0)
#define GAP(MF, LD, T, k, PW, j, S, i) do { MF; SBAR(); LD; PW[j] = pk2(T[k], T[k + 1]); PW[j + 1] = pk2(T[k + 2], T[k + 3]); PIN(PW); SBAR(); EX4(S, i); PIN(S); SBAR(); } while (0)
#define BC(x) __builtin_bit_cast(bf16x8, x)
#define STEP(e, d, QB1, QA1N, KN, L3, LK0, LK1, LV) do { \
        GAP(Ob = MFMA(V0[e ^ 1], BC(Pb0), Ob), (void)0,                  Sb, 12, Pb1, 2, Sa, 0); \
        GAP(Sb = MFMA(K0[e], Q0b, zero),       (void)0,                  Sa, 0, Pa0, 0, Sa, 4); \
        GAP(Sb = MFMA(K1[d], QB1, Sb),         L3,                       Sa, 4, Pa0, 2, Sa, 8); \
        GAP(Ob = MFMA(V1[e ^ 1], BC(Pb1), Ob), K0[e] = ldsf(LK0),        Sa, 8, Pa1, 0, Sa, 12); \
        GAP(Oa = MFMA(V0[e], BC(Pa0), Oa),     LK1,                      Sa, 12, Pa1, 2, Sb, 0); \
        GAP(Sa = MFMA(K0[e ^ 1], Q0a, zero),   V1[e ^ 1] = ldsf((LV) + 704), Sb, 0, Pb0, 0, Sb, 4); \
        GAP(Sa = MFMA(K1[KN], QA1N, Sa),       V0[e ^ 1] = ldsf(LV),     Sb, 4, Pb0, 2, Sb, 8); \
        GAP(Oa = MFMA(V1[e], BC(Pa1), Oa),     (void)0,                  Sb, 8, Pb1, 0, Sb, 12); \
    } while (0)
#define CHUNK4(c, B, N) do { \
        STEP(0, 0, Q1b0, Q1a1, 0, (void)0, vk0 + (B) + KVD, K1[1] = ldsf(vk1 + (B) + KVD), vv + (B) + KV_VT); \
        RING_X(c); \
        STEP(1, 0, Q1b1, Q1a0, 1, pk = lpeek(C_AP, lane), vk0 + (B) + KVD + 1024, (void)0, vv + (B) + KVD); \
        RING_Y(c, pk); \
        STEP(0, 1, Q1b0, Q1a1, 1, (void)0, vk0 + (N), K1[0] = ldsf(vk1 + (N)), vv + (B) + KVD + KV_VT); \
        STEP(1, 1, Q1b1, Q1a0, 0, (void)0, vk0 + (N) + 1024, (void)0, vv + (N)); \
    } while (0)
    for (int c = 0; c < NCHUNK; c += 4) {
        CHUNK4(c, 2 * CHUNK, 3 * CHUNK); CHUNK4(c + 1, 3 * CHUNK, 0); CHUNK4(c + 2, 0, CHUNK); CHUNK4(c + 3, CHUNK, 2 * CHUNK);
    }
#undef CHUNK4
    Pb1[2] = pk2(Sb[12], Sb[13]); Pb1[3] = pk2(Sb[14], Sb[15]);
    Ob = MFMA(V0[1], BC(Pb0), Ob); Ob = MFMA(V1[1], BC(Pb1), Ob);
#undef STEP
#undef GAP
#undef EX4
}

__device__ __forceinline__ void att_safe(const AttSync& as, bf16x8 Q0a, bf16x8 Q1a0, bf16x8 Q1a1, bf16x8 Q0b, bf16x8 Q1b0, bf16x8 Q1b1, f32x16& Oa, f32x16& Ob) {
    const int lane = as.lane, r = lane & 31, h = lane >> 5;
    const unsigned ak0 = lane * 16, ak1 = h ? KV_V + 20 * 16 : KV_K1 + r * 16;
    const unsigned av = KV_V + h * 352 + (r <= 20 ? r : 21) * 16;
    const f32x16 zero = {};
    float ma = -INFINITY, mb = -INFINITY;
    for (int c = 0; c < NCHUNK; ++c) {
        const unsigned b0 = kvslot(c);
        for (int t = 0; t < 4; ++t) {
            if (t == 1) RING_X(c);
            if (t == 2) RING_Y(c, 0u);
            const unsigned base = b0 + (t >> 1) * KVD; const int e = t & 1;
            bf16x8 K0 = ldsf(base + ak0 + e * 1024), K1 = ldsf(base + ak1), V0 = ldsf(base + av + e * KV_VT), V1 = ldsf(base + av + e * KV_VT + 704);
#pragma unroll
            for (int q = 0; q < 2; ++q) {
                f32x16 S = MFMA(K0, q ? Q0b : Q0a, zero); S = MFMA(K1, q ? (e ? Q1b1 : Q1b0) : (e ? Q1a1 : Q1a0), S);
                float mx = S[0];
#pragma unroll
                for (int i = 1; i < 16; ++i) mx = fmaxf(mx, S[i]);
                mx = fmaxf(mx, __shfl_xor(mx, 32));
                float& m = q ? mb : ma; f32x16& O = q ? Ob : Oa;
                const float mn = fmaxf(m, mx);
                const float al = __builtin_amdgcn_exp2f(m - mn);
                m = mn;
#pragma unroll
                for (int i = 0; i < 16; ++i) { O[i] *= al; S[i] = __builtin_amdgcn_exp2f(S[i] - mn); }
                O = MFMA(V0, pack8(S, 0, 1.f), O); O = MFMA(V1, pack8(S, 8, 1.f), O);
            }
        }
    }
}

__device__ __forceinline__ void xload4(u32x4 (&x)[5], const char* xb, unsigned vo) {
    asm volatile("global_load_dwordx4 %0, %5, %6\n\tglobal_load_dwordx4 %1, %5, %6 offset:256\n\tglobal_load_dwordx4 %2, %5, %6 offset:512\n\t"
                 "global_load_dwordx4 %3, %5, %6 offset:768\n\tglobal_load_dwordx4 %4, %5, %6 offset:1024"
                 : "=&v"(x[0]), "=&v"(x[1]), "=&v"(x[2]), "=&v"(x[3]), "=&v"(x[4]) : "v"(vo), "s"(xb) : "memory");
}
__device__ __forceinline__ void ln_rows(const u32x4 (&xr)[5], int row0, int lane) {
    const bf16x2 ones = {(__bf16)1.f, (__bf16)1.f};
    float s0 = 0.f, s1 = 0.f, q0 = 0.f, q1 = 0.f;
#pragma unroll
    for (int u = 0; u < 5; ++u)
#pragma unroll
        for (int e = 0; e < 4; e += 2) {
            const unsigned xe = xr[u][e], xf = xr[u][e + 1];
            const bf16x2 v = __builtin_bit_cast(bf16x2, xe), v2 = __builtin_bit_cast(bf16x2, xf);
            s0 = __builtin_amdgcn_fdot2_f32_bf16(v, ones, s0, false); q0 = __builtin_amdgcn_fdot2_f32_bf16(v, v, q0, false);
            s1 = __builtin_amdgcn_fdot2_f32_bf16(v2, ones, s1, false); q1 = __builtin_amdgcn_fdot2_f32_bf16(v2, v2, q1, false);
        }
    const float s = dpp_sum16(s0 + s1), ss = dpp_sum16(q0 + q1);
    const float mean = s * (1.f / 640.f);
    const float var = fmaxf(ss * (1.f / 640.f) - mean * mean, 0.f);
    const __bf16 rsb = (__bf16)__builtin_amdgcn_rsqf(rbf(rbf(var) + 1e-5f));
    const float mo = -rbf(mean) * (float)rsb;
    const unsigned clo = (unsigned)__builtin_bit_cast(unsigned short, rsb), chi = clo << 16;
    const int row = row0 + (lane >> 4), c = lane & 15;
    const unsigned wa = L_PANEL + (row >> 5) * 40960 + (c >> 1) * 1024 + (c & 1) * 512 + (((row & 31) ^ (c & 7)) << 4);
    static_for<0, 5>([&](auto U) {
        constexpr int u = U.value;
        *LP(u32x4, wa + u * 8192) = scale8(xr[u][0], xr[u][1], xr[u][2], xr[u][3], clo, chi, mo);
    });
}
#define BLOCK_BARRIER() asm volatile("s_waitcnt lgkmcnt(0)\n\ts_barrier" ::: "memory")

template <int PH>
__global__ void __launch_bounds__(512, 2) mega(Params p) {
    constexpr bool PRO = PH & 1, ATT = PH & 2, DN = PH & 8, WO = PH & 16;
    const int w = __builtin_amdgcn_readfirstlane(threadIdx.x >> 6);
    const int b = blockIdx.x, xc = b & 7, j = b >> 3;
    const int ti = (xc >> 1) * 8 + (j >> 2), g = (xc & 1) * 4 + (j & 3);
    unsigned tag = 1u, epv = 0u;
    constexpr bool FUSED = PH == 27;
    if constexpr (FUSED) asm volatile("global_load_dword %0, %1, %2 sc1" : "=v"(epv) : "v"(0u), "s"(p.ws + WS_EP) : "memory");
#define GET_TAG() do { if constexpr (FUSED) { asm volatile("s_nop 0\n\tv_readfirstlane_b32 %0, %1\n\ts_nop 3" : "=s"(tag) : "v"(epv) : "memory"); tag += 0x9E3779B1u; } } while (0)
    char* ws = p.ws;
    STAMP(0);
    if (threadIdx.x < C_N) *lcnt(threadIdx.x) = (threadIdx.x == C_WP || threadIdx.x == C_WP + 1 || threadIdx.x == C_PP || threadIdx.x == C_PP + 1) ? 100u : 0u;
    const bool haskv = (g >> 2) == (ti & 1);
    const char* wqb = p.wq + g * 160;
    const int kmt = g & 1;
    const char* xb = p.x + (size_t)ti * 64 * 1280;
#define LANE_CONSTS() int lane = __builtin_amdgcn_mbcnt_hi(~0u, __builtin_amdgcn_mbcnt_lo(~0u, 0u)); PIN(lane); \
    const int r = lane & 31, h = lane >> 5, tq = (lane & 15) >> 2, tp = lane & 3, tb = (lane >> 4) & 1; (void)r; (void)h; (void)tq; (void)tp; (void)tb

    if (w < 4) {
        LANE_CONSTS();
        const unsigned xvo = (lane >> 4) * 1280 + (lane & 15) * 16;
        bf16x8 Q0a, Q0b; u32x2 q1a, q1b;
        const int head = 4 * g + w;
        const AttSync as{lane, ws + WS_KV, (unsigned)((w < 3 ? w * 3072 : CHUNK - 3072) + lane * 16), (unsigned)(w < 3 ? w * 3072 : CHUNK - 3072), w};
        __builtin_amdgcn_s_setprio(3);
        if constexpr (PRO) {
            u32x4 x0[5], x1[5], x2[5], x3[5];
            const int ra = kmt * 32 + w * 8, rb = (kmt ^ 1) * 32 + w * 8;
            xload4(x0, xb + (size_t)ra * 1280, xvo); xload4(x1, xb + (size_t)(ra + 4) * 1280, xvo);
            xload4(x2, xb + (size_t)rb * 1280, xvo); xload4(x3, xb + (size_t)(rb + 4) * 1280, xvo);
            BLOCK_BARRIER();
            waitv5<15>(x0[0], x0[1], x0[2], x0[3], x0[4]);
            GET_TAG();
            STAMP(1);
            ln_rows(x0, ra, lane);
            waitv5<10>(x1[0], x1[1], x1[2], x1[3], x1[4]);
            ln_rows(x1, ra + 4, lane);
            lpost(C_AP + w, 1);
            waitv5<5>(x2[0], x2[1], x2[2], x2[3], x2[4]);
            ln_rows(x2, rb, lane);
            waitv5<0>(x3[0], x3[1], x3[2], x3[3], x3[4]);
            ln_rows(x3, rb + 4, lane);
            lpost(C_AP + w, 2);
            unsigned pa[4];
#pragma unroll
            for (int s = 0; s < 4; ++s) pa[s] = L_PANEL + h * 512 + ((r ^ ((2 * s + h) & 7)) << 4);
            f32x16 qa = {}, qb = {};
            const unsigned wa = L_WQ + (8 * h + tq) * 160 + w * 40 + tb * 32 + (tb ? 0 : tp * 8);
            lwait_all(C_AP, 4, 2, lane); lwait_all(C_PP, 4, 1, lane);
            STAMP(2);
            bf16x8 fw[2][4], fa[2][4], fb[2][4];
#pragma unroll
            for (int s4 = 0; s4 < 4; ++s4) { fw[0][s4] = ldstr2(wa + s4 * 2560, wa + s4 * 2560 + 640); fa[0][s4] = ldsf(pa[s4] + s4 * 1024); fb[0][s4] = ldsf(pa[s4] + 40960 + s4 * 1024); }
            unsigned pk = 0;
            static_for<0, 10>([&](auto S) {
                constexpr int st = S.value, nx = st + 1;
                if constexpr (st == 3 || st == 5 || st == 7) lcheck(pk, C_WP, (st - 1) / 2, lane);
#pragma unroll
                for (int s4 = 0; s4 < 4; ++s4) {
                    qa = MFMA(fw[st & 1][s4], fa[st & 1][s4], qa); SBAR();
                    if constexpr (nx < 10) { const unsigned o = wa + (nx & 3) * 10240 + s4 * 2560; fw[nx & 1][s4] = ldstr2(o, o + 640); SBAR(); }
                    qb = MFMA(fw[st & 1][s4], fb[st & 1][s4], qb); SBAR();
                    if constexpr (nx < 10) { fa[nx & 1][s4] = ldsf(pa[s4] + (nx * 4 + s4) * 1024); fb[nx & 1][s4] = ldsf(pa[s4] + 40960 + (nx * 4 + s4) * 1024); SBAR(); }
                    if (s4 == 2 && (st == 2 || st == 4 || st == 6)) { pk = lpeek(C_WP, lane); SBAR(); }
                }
                if constexpr (!(st & 1) && st <= 6) lpost(C_AP + w, 3 + st / 2);
            });
            lpost(C_AP + w, 8);
            Q0a = pack8(qa, 0, QSCALE); Q0b = pack8(qb, 0, QSCALE);
            q1a = u32x2{pk2(qa[8] * QSCALE, qa[9] * QSCALE), pk2(qa[10] * QSCALE, qa[11] * QSCALE)};
            q1b = u32x2{pk2(qb[8] * QSCALE, qb[9] * QSCALE), pk2(qb[10] * QSCALE, qb[11] * QSCALE)};
            if (h) { q1a = u32x2{0u, 0u}; q1b = u32x2{0u, 0u}; }
            if constexpr (!ATT) {
                char* qt = ws + WS_QT + (size_t)((2 * ti) * 32 + head) * 2048 + lane * 16;
                *(bf16x8*)qt = Q0a; *(u32x2*)(qt + 1024) = q1a; *(bf16x8*)(qt + 65536) = Q0b; *(u32x2*)(qt + 65536 + 1024) = q1b;
            }
            STAMP(3);
        } else {
            BLOCK_BARRIER();
            if constexpr (ATT) {
                const char* qt = ws + WS_QT + (size_t)((2 * ti) * 32 + head) * 2048 + lane * 16;
                Q0a = *(const bf16x8*)qt; q1a = *(const u32x2*)(qt + 1024); Q0b = *(const bf16x8*)(qt + 65536); q1b = *(const u32x2*)(qt + 65536 + 1024);
            }
        }
        if constexpr (ATT) {
            float na = 0.f, nb = 0.f;
            {
                const u32x4 a0 = __builtin_bit_cast(u32x4, Q0a), b0 = __builtin_bit_cast(u32x4, Q0b);
#pragma unroll
                for (int e = 0; e < 4; ++e) { na += bflo(a0[e]) * bflo(a0[e]) + bfhi(a0[e]) * bfhi(a0[e]); nb += bflo(b0[e]) * bflo(b0[e]) + bfhi(b0[e]) * bfhi(b0[e]); }
#pragma unroll
                for (int e = 0; e < 2; ++e) { na += bflo(q1a[e]) * bflo(q1a[e]) + bfhi(q1a[e]) * bfhi(q1a[e]); nb += bflo(q1b[e]) * bflo(q1b[e]) + bfhi(q1b[e]) * bfhi(q1b[e]); }
                na += __shfl_xor(na, 32); nb += __shfl_xor(nb, 32);
            }
            if constexpr (PRO) lwait_all(C_AP, 4, 8, lane);
            lwait(C_KVRDY, 1);
            STAMP(4);
            att_dma(as, 2);
            lwait(C_KVRDY, 2);
            const float kn2 = __uint_as_float(*lcnt(C_KN)), vmx = __uint_as_float(*lcnt(C_VM));
            const float Ma = __builtin_sqrtf(na * kn2) * 1.01f, Mb = __builtin_sqrtf(nb * kn2) * 1.01f;
            const bool fast = __all(Ma <= BOUND_MAX && Mb <= BOUND_MAX && vmx <= 1e30f);
            const unsigned sa = (fast && h) ? pk2(-Ma, 0.f) : q1a[0], sb = (fast && h) ? pk2(-Mb, 0.f) : q1b[0];
            const u32x4 a0 = {sa, q1a[1], 0u, 0u}, a1 = {h ? sa : 0u, 0u, q1a[0], q1a[1]}, b0 = {sb, q1b[1], 0u, 0u}, b1 = {h ? sb : 0u, 0u, q1b[0], q1b[1]};
            f32x16 Oa = {}, Ob = {};
            __builtin_amdgcn_s_setprio(ATT_PRIO);
            STAMP(5); STAMPC(12);
            if (fast) att_fast(as, Q0a, BC(a0), BC(a1), Q0b, BC(b0), BC(b1), Oa, Ob);
            else att_safe(as, Q0a, BC(a0), BC(a1), Q0b, BC(b0), BC(b1), Oa, Ob);
            STAMP(6); STAMPC(13);
            const __amdgpu_buffer_rsrc_t ro = mkrsrc(ws + WS_OT, 64 * 40 * 1024);
#pragma unroll
            for (int q = 0; q < 2; ++q) {
                const f32x16& O = q ? Ob : Oa;
                const float inv = 1.f / __shfl(O[8], r + 32);
                const unsigned base = ((2 * ti + q) * 40 + 5 * g) * 1024;
                __builtin_amdgcn_raw_buffer_store_b128(__builtin_bit_cast(u32x4, pack8(O, 0, inv)), ro, base + w * 1024 + lane * 16, 0, 16);
                if (!h) {
                    u32x2 t = {pk2(O[8] * inv, O[9] * inv), pk2(O[10] * inv, O[11] * inv)};
                    __builtin_amdgcn_raw_buffer_store_b64(t, ro, base + 4096 + ((w >> 1) * 32 + r) * 16 + (w & 1) * 8, 0, 16);
                }
            }
            if constexpr (WO) {
                WAITV(0);
                if (lane < 2) __hip_atomic_store((gu32*)(ws + WS_OF) + (2 * ti + lane) * 32 + head, tag, RLX_AGENT);
            }
            STAMP(7);
        }
    } else {
        LANE_CONSTS();
        const unsigned xvo = (lane >> 4) * 1280 + (lane & 15) * 16;
        const int v = w - 4;
        __builtin_amdgcn_s_setprio(3);
        const unsigned bq = ((tb * 32 + (tp >> 1) * 16) ^ ((unsigned)(tq >> 1) << 6)) + (tp & 1) * 8;
        auto kv_ready = [&]() {
            gu64* f = (gu64*)(ws + WS_KF + xc * 1024) + lane;
            unsigned long long fk = 0, fv = 0;
            _Pragma("clang loop unroll(disable)") for (unsigned spins = 0; spins < (1u << 14); ++spins) {
                fk = __hip_atomic_load(f, RLX_AGENT); fv = __hip_atomic_load(f + 64, RLX_AGENT);
                if (__all((unsigned)(fk >> 32) == tag && (unsigned)(fv >> 32) == tag)) break;
                TMO(spins, 1u << 14, 301, tag);
                __builtin_amdgcn_s_sleep(2);
            }
            lpost(C_KVRDY, 1);
            if constexpr (PRO) lwait_all(C_AP, 4, 6, lane);
#pragma unroll
            for (int n = 0; n < 7; ++n) glds16x3_sc1(lane * 16 + n * 3072, ws + WS_KV, L_KV + 2 * CHUNK + n * 3072);
            lpost(C_KN, __float_as_uint(wave_max(__uint_as_float((unsigned)fk)))); lpost(C_VM, __float_as_uint(wave_max(__uint_as_float((unsigned)fv))));
            WAITV(0);
            lpost(C_KVRDY, 2);
        };
        if constexpr (PRO) {
            const int kvm = 2 * ti + (g & 1); const bool isV = (g >> 1) & 1;
            const char* wkvb = isV ? p.wv : p.wk;
            BLOCK_BARRIER();
            const int rot = (ti & 7) * 4;
            const char* wsrc = ((v >> 1) ? p.w3 : p.w1) + g * 384 + (size_t)(v & 1) * 8 * 3072;
            unsigned gvo[3], gwa[3];
#pragma unroll
            for (int n = 0; n < 3; ++n) {
                const unsigned c = n * 64 + lane, row = c / 24u, col = c - row * 24u;
                gvo[n] = row * 3072 + col * 16;
                gwa[n] = L_GU + (v >> 1) * 6144 + ((v & 1) * 8 + row) * 384 + ((col ^ ((row & 2) << 1)) << 4);
            }
            constexpr int GQ = 5;
            u32x4 gq[GQ][3];
            auto gissue = [&](auto S) {
                constexpr int s = S.value, q = s % GQ;
                int ks = s + rot; ks = ks >= 40 ? ks - 40 : ks;
                const char* sb = wsrc + (size_t)ks * 16 * 3072;
                GLOAD16N(gq[q][0], gvo[0], sb); GLOAD16N(gq[q][1], gvo[1], sb); GLOAD16N(gq[q][2], gvo[2], sb);
            };
            auto gwrite = [&](auto S) {
                constexpr int s = S.value, q = s % GQ, last = s + GQ - 1 < 39 ? s + GQ - 1 : 39;
                waitv3<3 * (last - s)>(gq[q][0], gq[q][1], gq[q][2]);
#pragma unroll
                for (int n = 0; n < 3; ++n) *LP(u32x4, gwa[n] + (s % 3) * 12288) = gq[q][n];
                if constexpr (s + GQ < 40) gissue(IC<s + GQ>{});
            };
            if (v >= 2) {
                const int v2 = v - 2;
#pragma unroll
                for (int n = 0; n < 20; ++n) {
                    const unsigned c = (v2 + 2 * n) * 64 + lane, row = c / 10u, col = c - row * 10u;
                    glds16(row * 1280 + col * 16, wqb, L_WQ + (v2 + 2 * n) * 1024);
                }
                u32x4 wst[3][10];
                auto wst_issue = [&](auto I) {
                    constexpr int rr = I.value / 10, i = I.value % 10;
                    const unsigned c = (40 + 20 * rr + v2 + 2 * i) * 64 + lane, row = c / 10u, col = c - row * 10u;
                    GLOAD16N(wst[rr][i], row * 1280 + col * 16, wqb);
                };
                static_for<0, 10>(wst_issue);
                WAITV(10); lpost(C_PP + v, 1);
                GET_TAG();
                STAMP(2);
                if (haskv) lwait(C_KVFIN, 1);
                static_for<10, 30>(wst_issue);
                static_for<0, 3>([&](auto R) {
                    constexpr int rr = R.value;
                    waitv5<20 - 10 * rr>(wst[rr][0], wst[rr][1], wst[rr][2], wst[rr][3], wst[rr][4]); waitv5<63>(wst[rr][5], wst[rr][6], wst[rr][7], wst[rr][8], wst[rr][9]);
                    lwait_all(C_AP, 4, 3 + rr, lane);
#pragma unroll
                    for (int i = 0; i < 10; ++i) *LP(u32x4, L_WQ + (rr & 1) * 20480 + (v2 + 2 * i) * 1024 + lane * 16) = wst[rr][i];
                    lpost(C_WP + v, rr + 1);
                });
            } else {
              if (haskv) {
#pragma unroll
                for (int n = 0; n < 13; ++n) {
                    const unsigned o = v * 12800 + (n < 12 ? n * 1024 : 11776);
                    glds16(o + lane * 16, wkvb, L_WKV + o);
                }
                f32x16 acc = {};
                const unsigned wa = L_WKV + (v * 320 + 8 * h + tq) * 40 + tb * 32 + (tb ? 0 : tp * 8);
                unsigned xa[4];
#pragma unroll
                for (int s = 0; s < 4; ++s) xa[s] = L_PANEL + kmt * 40960 + v * 20480 + h * 512 + ((r ^ ((2 * s + h) & 7)) << 4);
                WAITV(0);
                GET_TAG();
                STAMP(2);
                bf16x8 wf[20], xf[20];
#pragma unroll
                for (int s = 0; s < 20; ++s) wf[s] = ldstr2(wa + s * 640, wa + s * 640 + 160);
                lwait_all(C_AP, 4, 1, lane);
#pragma unroll
                for (int s = 0; s < 20; ++s) xf[s] = ldsf(xa[s & 3] + s * 1024);
                SBAR();
                if (isV) {
#pragma unroll
                    for (int s = 0; s < 20; ++s) acc = MFMA(xf[s], wf[s], acc);
                } else {
#pragma unroll
                    for (int s = 0; s < 20; ++s) acc = MFMA(wf[s], xf[s], acc);
                }
                if (v) {
#pragma unroll
                    for (int e = 0; e < 4; ++e) *LP(f32x4, L_WKV + 12800 + e * 1024 + lane * 16) = f32x4{acc[4 * e], acc[4 * e + 1], acc[4 * e + 2], acc[4 * e + 3]};
                    lpost(C_KVP, 1);
                } else {
                    STAMP(12);
                    lwait(C_KVP, 1);
                    STAMP(13);
#pragma unroll
                    for (int e = 0; e < 4; ++e) {
                        const f32x4 t = *LP(const f32x4, L_WKV + 12800 + e * 1024 + lane * 16);
                        acc[4 * e] += t[0]; acc[4 * e + 1] += t[1]; acc[4 * e + 2] += t[2]; acc[4 * e + 3] += t[3];
                    }
                    const __amdgpu_buffer_rsrc_t rk = mkrsrc(ws + WS_KV, 32 * KVD);
                    const unsigned base = ti * KVD, ke = g & 1;
                    const u32x4 f0 = __builtin_bit_cast(u32x4, pack8(acc, 0, 1.f)), f1 = __builtin_bit_cast(u32x4, pack8(acc, 8, 1.f));
                    float mx;
                    if (!isV) {
                        __builtin_amdgcn_raw_buffer_store_b128(f0, rk, base + ke * 1024 + lane * 16, 0, 16);
                        const u32x2 k1 = {f1[0], f1[1]};
                        if (!h) __builtin_amdgcn_raw_buffer_store_b64(k1, rk, base + KV_K1 + r * 16 + ke * 8, 0, 16);
                        lpost(C_KVFIN, 1);
                        float n2 = 0.f;
#pragma unroll
                        for (int e = 0; e < 4; ++e) n2 += bflo(f0[e]) * bflo(f0[e]) + bfhi(f0[e]) * bfhi(f0[e]);
                        if (!h) n2 += bflo(f1[0]) * bflo(f1[0]) + bfhi(f1[0]) * bfhi(f1[0]) + bflo(f1[1]) * bflo(f1[1]) + bfhi(f1[1]) * bfhi(f1[1]);
                        n2 += __shfl_xor(n2, 32);
                        mx = (n2 == n2) ? n2 : INFINITY;
                    } else {
                        const unsigned cst = r == 20 ? 0x3f803f80u : 0u;
                        const u32x4 c4 = {cst, cst, cst, cst};
                        if (r <= 21) {
                            __builtin_amdgcn_raw_buffer_store_b128(r >= 20 ? c4 : f0, rk, base + KV_V + ke * KV_VT + h * 352 + r * 16, 0, 16);
                            __builtin_amdgcn_raw_buffer_store_b128(r >= 20 ? c4 : f1, rk, base + KV_V + ke * KV_VT + 704 + h * 352 + r * 16, 0, 16);
                        }
                        lpost(C_KVFIN, 1);
                        float vm = 0.f;
#pragma unroll
                        for (int e = 0; e < 16; ++e) { const float a = fabsf(acc[e]); vm = (a <= vm) ? vm : ((a == a) ? a : INFINITY); }
                        mx = r < 20 ? vm : 0.f;
                    }
                    STAMP(14);
                    mx = wave_max(mx);
                    WAITV(0);
                    STAMP(15);
                    if (lane < 8) __hip_atomic_store((gu64*)(ws + WS_KF + lane * 1024 + (isV ? 512 : 0)) + kvm, ((unsigned long long)tag << 32) | __float_as_uint(mx), RLX_AGENT);
                }
            }
              else { WAITV(0); GET_TAG(); }
              STAMP(3);
              if constexpr (ATT) { if (v == 0) kv_ready(); }
            }
            if (haskv) lwait(C_KVFIN, 1);
            lwait_all(C_AP, 4, GU_DELAY, lane);
            static_for<0, GQ>(gissue);
            STAMP(4);
            {
                const int mt = v & 1, fh = v >> 1;
                f32x16 acc[6] = {};
                const unsigned ba = L_GU + (8 * h + tq) * 384;
                unsigned bn[3];
#pragma unroll
                for (int n = 0; n < 3; ++n) bn[n] = ba + (((fh * 3 + n) * 64) ^ bq);
                unsigned pa[4];
#pragma unroll
                for (int s = 0; s < 4; ++s) pa[s] = L_PANEL + mt * 40960 + rot * 1024 + h * 512 + ((r ^ ((2 * s + h) & 7)) << 4);
                bf16x8 wf[2][6], xf[2];
                unsigned vaddr = L_MISC + (C_GP + v) * 4, vone = 1u; PIN(vaddr); PIN(vone);
                auto rdx = [&](auto S) {
                    constexpr int s = S.value;
                    if constexpr ((s & 3) == 0 && s > 0) { if (s + rot == 40) { pa[0] -= 40960; pa[1] -= 40960; pa[2] -= 40960; pa[3] -= 40960; } }
                    xf[s & 1] = ldsf(pa[s & 3] + s * 1024);
                };
                auto rdw = [&](auto S, auto N) {
                    constexpr int s = S.value, n = N.value;
                    const unsigned o = bn[n % 3] + (n / 3) * 6144 + (s % 3) * 12288; wf[s & 1][n] = ldstr2(o, o + 1536);
                };
                static_for<0, 3>(gwrite);
                linc(vaddr, vone); lwait_all(C_GP, 4, 1u, lane);
                rdx(IC<0>{}); static_for<0, 6>([&](auto N) { rdw(IC<0>{}, N); });
                linc(vaddr, vone);
                __builtin_amdgcn_s_setprio(GEM_PRIO);
                static_for<0, 40>([&](auto S) {
                    constexpr int s = S.value; constexpr bool nx = s + 1 < 40;
                    unsigned pk = 0;
                    acc[0] = MFMA(wf[s & 1][0], xf[s & 1], acc[0]); SBAR();
                    if constexpr (nx) { rdx(IC<s + 1>{}); rdw(IC<s + 1>{}, IC<0>{}); rdw(IC<s + 1>{}, IC<1>{}); } SBAR();
                    acc[1] = MFMA(wf[s & 1][1], xf[s & 1], acc[1]); SBAR();
                    if constexpr (nx) { rdw(IC<s + 1>{}, IC<2>{}); rdw(IC<s + 1>{}, IC<3>{}); } SBAR();
                    acc[2] = MFMA(wf[s & 1][2], xf[s & 1], acc[2]); SBAR();
                    if constexpr (nx) { rdw(IC<s + 1>{}, IC<4>{}); rdw(IC<s + 1>{}, IC<5>{}); }
                    if constexpr (s + 2 < 40) pk = lpeek(C_GP, lane);
                    SBAR();
                    acc[3] = MFMA(wf[s & 1][3], xf[s & 1], acc[3]); SBAR();
                    acc[4] = MFMA(wf[s & 1][4], xf[s & 1], acc[4]); SBAR();
                    if constexpr (s + 2 < 40) lcheck(pk, C_GP, s + 2, lane);
                    if constexpr (s + 3 < 40) gwrite(IC<s + 3>{});
                    linc(vaddr, vone);
                    SBAR();
                    acc[5] = MFMA(wf[s & 1][5], xf[s & 1], acc[5]); SBAR();
                });
                __builtin_amdgcn_s_setprio(3);
                STAMP(6);
                const __amdgpu_buffer_rsrc_t rh = mkrsrc(ws + WS_HT, 64 * 96 * 1024);
#pragma unroll
                for (int n = 0; n < 3; ++n) {
                    f32x16 hv;
#pragma unroll
                    for (int e = 0; e < 16; ++e) {
                        const float gg = acc[n][e];
                        hv[e] = gg * __builtin_amdgcn_rcpf(1.f + __builtin_amdgcn_exp2f(-1.4426950408889634f * gg)) * acc[n + 3][e];
                    }
                    const unsigned base = ((2 * ti + mt) * 96 + g * 12 + fh * 6 + n * 2) * 1024 + lane * 16;
                    __builtin_amdgcn_raw_buffer_store_b128(__builtin_bit_cast(u32x4, pack8(hv, 0, 1.f)), rh, base, 0, 16);
                    __builtin_amdgcn_raw_buffer_store_b128(__builtin_bit_cast(u32x4, pack8(hv, 8, 1.f)), rh, base + 1024, 0, 16);
                }
                if constexpr (DN) {
                    WAITV(0);
                    if (lane == 0) __hip_atomic_store((gu32*)(ws + WS_HF) + (2 * ti + mt) * 16 + g * 2 + fh, tag, RLX_AGENT);
                }
                STAMP(7);
            }
        } else {
            BLOCK_BARRIER();
            if constexpr (ATT) { if (v == 0) kv_ready(); }
        }
        if constexpr (DN || WO) {
            LANE_CONSTS();
            const unsigned bq = ((tb * 32 + (tp >> 1) * 16) ^ ((unsigned)(tq >> 1) << 6)) + (tp & 1) * 8;
            const unsigned drow = lane >> 3, dcol = ((lane & 7) ^ (((lane >> 4) & 1) << 2)) * 16;
            const int G = xc >> 1, r0 = G < 2 ? 6 * G : 12 + 5 * (G - 2), nr = G < 2 ? 6 : 5;
            if constexpr (FUSED) {
                if (b == 255 && v == 0) {
                    gu32* f = (gu32*)(ws + WS_HF) + lane;
                    _Pragma("clang loop unroll(disable)") for (unsigned spins = 0; spins < (1u << 14); ++spins) {
                        bool ok = true;
#pragma unroll
                        for (int n = 0; n < 16; ++n) ok &= __hip_atomic_load(f + n * 64, RLX_AGENT) == tag;
                        if (__all(ok)) break;
                        TMO(spins, 1u << 14, 303, tag);
                        __builtin_amdgcn_s_sleep(32);
                    }
                    if (lane == 0) __hip_atomic_store((gu32*)(ws + WS_EP), tag, RLX_AGENT);
                }
            }
            if (j < 5 * nr) {
                const int dr = r0 + j / 5, dc = (xc & 1) * 5 + j % 5;
                const int mfirst = dr < 20 ? 3 * dr : 60 + 2 * (dr - 20), nm = dr < 20 ? 3 : 2;
                const unsigned dvo = drow * 1280 + dcol;
                const unsigned myring = L_PANEL + v * 20480;
                f32x16 acc0 = {}, acc1 = {};
                if constexpr (PRO) { lwait_all(C_GP, 4, 42, lane); lwait_all(C_AP, 4, 8, lane); }
                if constexpr (DN) {
                    f32x16 da[3][2] = {};
                    const int drot = (j % 5) * 4;
                    const char* bsrc = p.w2 + (size_t)v * 384 * 1280 + dc * 128;
                    const char* asrc = ws + WS_HT + ((size_t)mfirst * 96 + v * 24) * 1024;
                    const unsigned am1 = lane * 16 + 98304, am2 = lane * 16 + (nm == 3 ? 196608 : 98304);
                    u32x4 aq[8][3];
                    auto bissue = [&](auto K) {
                        constexpr int k = K.value;
                        int kr = k + drot; kr = kr >= 24 ? kr - 24 : kr;
                        const char* sb = bsrc + (size_t)kr * 16 * 1280;
                        glds16(dvo, sb, myring + (k % 10) * 2048); glds16(dvo + 8 * 1280, sb, myring + (k % 10) * 2048 + 1024);
                    };
                    auto aissue = [&](auto K) {
                        constexpr int k = K.value;
                        int kr = k + drot; kr = kr >= 24 ? kr - 24 : kr;
                        const char* sb = asrc + (size_t)kr * 1024;
                        GLOAD16S(aq[k & 7][0], lane * 16, sb); GLOAD16S(aq[k & 7][1], am1, sb); GLOAD16S(aq[k & 7][2], am2, sb);
                    };
                    static_for<0, 8>(bissue);
                    if constexpr (PRO) {
                        gu32* f = (gu32*)(ws + WS_HF) + (mfirst + ((lane >> 2) < nm ? (lane >> 2) : 0)) * 16 + 4 * v + (lane & 3);
                        _Pragma("clang loop unroll(disable)") for (unsigned spins = 0; spins < (1u << 14); ++spins) {
                            if (__all(__hip_atomic_load(f, RLX_AGENT) == tag)) break;
                            TMO(spins, 1u << 14, 302, tag);
                            __builtin_amdgcn_s_sleep(6);
                        }
                        CFENCE();
                    }
                    STAMP(8);
                    static_for<0, 8>(aissue);
                    const unsigned ba0 = myring + (4 * h + tq) * 128 + bq, ba1 = ba0 ^ 64;
                    bf16x8 w0[2], w1[2];
                    WAITVN(24);
                    __builtin_amdgcn_s_setprio(GEM_PRIO);
                    w0[0] = ldstr2(ba0, ba0 + 1024); w1[0] = ldstr2(ba1, ba1 + 1024);
                    static_for<0, 24>([&](auto K) {
                        constexpr int k = K.value;
                        constexpr int yk = k <= 7 ? 21 + 2 * k : (k < 16 ? 35 : 5 * (23 - k));
                        constexpr int n = k + 1 <= 7 ? yk : yk - 2;
                        waitv3<(n < 0 ? 0 : n)>(aq[k & 7][0], aq[k & 7][1], aq[k & 7][2]);
                        constexpr unsigned o = ((k + 1) % 10) * 2048;
#define DXF(m) __builtin_bit_cast(bf16x8, aq[k & 7][m])
                        SBAR();
                        da[0][0] = MFMA(w0[k & 1], DXF(0), da[0][0]); SBAR();
                        if constexpr (k + 1 < 24) w0[(k + 1) & 1] = ldstr2(ba0 + o, ba0 + o + 1024);
                        SBAR();
                        da[0][1] = MFMA(w1[k & 1], DXF(0), da[0][1]); SBAR();
                        if constexpr (k + 1 < 24) w1[(k + 1) & 1] = ldstr2(ba1 + o, ba1 + o + 1024);
                        SBAR();
                        da[1][0] = MFMA(w0[k & 1], DXF(1), da[1][0]); SBAR();
                        if constexpr (k + 8 < 24) bissue(IC<k + 8>{});
                        SBAR();
                        da[1][1] = MFMA(w1[k & 1], DXF(1), da[1][1]); SBAR();
                        da[2][0] = MFMA(w0[k & 1], DXF(2), da[2][0]); SBAR();
                        da[2][1] = MFMA(w1[k & 1], DXF(2), da[2][1]); SBAR();
                        if constexpr (k + 8 < 24) aissue(IC<k + 8>{});
                    });
                    __builtin_amdgcn_s_setprio(3);
                    STAMP(9);
                    if constexpr (WO) {
#pragma unroll
                        for (int u = 0; u < 20; ++u) glds16(dvo + u * 8 * 1280, p.wo + (size_t)v * 160 * 1280 + dc * 128, myring + u * 1024);
                    }
#pragma unroll
                    for (int n = 0; n < 2; ++n) {
#pragma unroll
                        for (int m = 0; m < 3; ++m) {
                            if (m != v) {
                                const unsigned dst = L_GU + (m * 3 + (v > m ? v - 1 : v)) * 4096 + lane * 16;
#pragma unroll
                                for (int e = 0; e < 4; ++e) *LP(f32x4, dst + e * 1024) = f32x4{da[m][n][4 * e], da[m][n][4 * e + 1], da[m][n][4 * e + 2], da[m][n][4 * e + 3]};
                            }
                        }
                        lpost(C_DP + v, 2 * n + 1); lwait_all(C_DP, 4, 2 * n + 1, lane);
                        if (v < 3) {
                            f32x16 t = v == 0 ? da[0][n] : (v == 1 ? da[1][n] : da[2][n]);
#pragma unroll
                            for (int o = 0; o < 3; ++o)
#pragma unroll
                                for (int e = 0; e < 4; ++e) {
                                    const f32x4 u = *LP(const f32x4, L_GU + (v * 3 + o) * 4096 + e * 1024 + lane * 16);
                                    t[4 * e] += u[0]; t[4 * e + 1] += u[1]; t[4 * e + 2] += u[2]; t[4 * e + 3] += u[3];
                                }
                            if (n) acc1 = t; else acc0 = t;
                        }
                        if (n == 0) { lpost(C_DP + v, 2); lwait_all(C_DP, 4, 2, lane); }
                    }
                    if constexpr (!WO) {
                        if (v < 3) {
                            float* pt = (float*)(ws + WS_PT) + (size_t)(b * 3 + v) * 2048 + lane;
#pragma unroll
                            for (int e = 0; e < 16; ++e) { pt[e * 64] = acc0[e]; pt[1024 + e * 64] = acc1[e]; }
                        }
                    }
                } else {
#pragma unroll
                    for (int u = 0; u < 20; ++u) glds16(dvo + u * 8 * 1280, p.wo + (size_t)v * 160 * 1280 + dc * 128, myring + u * 1024);
                    if (v < 3) {
                        const float* pt = (const float*)(ws + WS_PT) + (size_t)(b * 3 + v) * 2048 + lane;
#pragma unroll
                        for (int e = 0; e < 16; ++e) { acc0[e] = pt[e * 64]; acc1[e] = pt[1024 + e * 64]; }
                    }
                }
                if constexpr (WO) { WAITV(0); lpost(C_DP + v, 4); }
                if (v < nm) {
                    const int m = mfirst + v;
                    const unsigned ba0 = L_PANEL + (4 * h + tq) * 128 + bq, ba1 = ba0 ^ 64;
                    if constexpr (WO) {
                        if constexpr (ATT) gwait(ws, WS_OF + m * 128, 32, tag, lane);
                        STAMP(10);
                        const __amdgpu_buffer_rsrc_t ro = mkrsrc(ws + WS_OT + (size_t)m * 40960, 40960);
                        u32x4 of[40];
#pragma unroll
                        for (int s = 0; s < 40; ++s) of[s] = __builtin_amdgcn_raw_buffer_load_b128(ro, lane * 16, s * 1024, 16);
                        const char* xrow = p.x + (size_t)(m * 32 + r) * 1280 + dc * 128 + h * 16;
                        u32x4 xres[4];
#pragma unroll
                        for (int e = 0; e < 4; ++e) xres[e] = *(const u32x4*)(xrow + e * 32);
                        lwait_all(C_DP, 4, 4, lane);
                        const unsigned bl0 = L_PANEL + (40 * h + 16 + tq) * 128 + bq, bl1 = bl0 ^ 64;
#pragma unroll
                        for (int s = 0; s < 40; ++s) {
                            const int gg = s / 5, jj = s % 5;
                            const bf16x8 xf = __builtin_bit_cast(bf16x8, of[s]);
                            if (jj < 4) {
                                const unsigned o = (80 * gg + 20 * jj) * 128;
                                acc0 = MFMA(ldstr2(ba0 + o, ba0 + o + 1024), xf, acc0);
                                acc1 = MFMA(ldstr2(ba1 + o, ba1 + o + 1024), xf, acc1);
                            } else {
                                const unsigned o = 80 * gg * 128;
                                acc0 = MFMA(ldstr2(bl0 + o, bl0 + o + 2560), xf, acc0);
                                acc1 = MFMA(ldstr2(bl1 + o, bl1 + o + 2560), xf, acc1);
                            }
                        }
                        char* orow = p.out + (size_t)(m * 32 + r) * 1280 + dc * 128 + h * 16;
#pragma unroll
                        for (int n = 0; n < 2; ++n) {
                            const f32x16& a = n ? acc1 : acc0;
#pragma unroll
                            for (int qq = 0; qq < 2; ++qq) {
                                float lo[4], hi[4];
#pragma unroll
                                for (int e = 0; e < 4; ++e) {
                                    auto sw = __builtin_amdgcn_permlane32_swap(__float_as_uint(a[8 * qq + e]), __float_as_uint(a[8 * qq + 4 + e]), false, false);
                                    lo[e] = __uint_as_float(sw[0]); hi[e] = __uint_as_float(sw[1]);
                                }
                                const u32x4 xv = xres[2 * n + qq];
                                const u32x4 ov = {pk2(lo[0] + bflo(xv[0]), lo[1] + bfhi(xv[0])), pk2(lo[2] + bflo(xv[1]), lo[3] + bfhi(xv[1])),
                                                  pk2(hi[0] + bflo(xv[2]), hi[1] + bfhi(xv[2])), pk2(hi[2] + bflo(xv[3]), hi[3] + bfhi(xv[3]))};
                                *(u32x4*)(orow + (2 * n + qq) * 32) = ov;
                            }
                        }
                        STAMP(11);
                    }
                }
            }
        }
    }
}

template <int PH>
static void launch(const Params& p, hipStream_t stream) {
    (void)hipFuncSetAttribute((const void*)mega<PH>, hipFuncAttributeMaxDynamicSharedMemorySize, L_TOTAL);
    mega<PH><<<256, 512, L_TOTAL, stream>>>(p);
}

extern "C" void kernel_launch(void* const* d_in, const int* in_sizes, int n_in, void* d_out, int out_size, void* d_ws, size_t ws_size, hipStream_t stream) {
    Params p;
    p.x = (const char*)d_in[0]; p.wq = (const char*)d_in[1]; p.wk = (const char*)d_in[2]; p.wv = (const char*)d_in[3]; p.wo = (const char*)d_in[4];
    p.w1 = (const char*)d_in[6]; p.w3 = (const char*)d_in[7]; p.w2 = (const char*)d_in[8];
    p.out = (char*)d_out; p.ws = (char*)d_ws;
    launch<27>(p, stream);
}
```

```cpp
#include <hip/hip_runtime.h>
#include <hip/hip_bf16.h>
#include <stdint.h>

#define AS1 __attribute__((address_space(1)))
#define AS3 __attribute__((address_space(3)))
typedef __bf16 bf16x2 __attribute__((ext_vector_type(2)));
typedef __bf16 bf16x8 __attribute__((ext_vector_type(8)));
typedef short s16x4 __attribute__((ext_vector_type(4)));
typedef short s16x8 __attribute__((ext_vector_type(8)));
typedef float f32x2 __attribute__((ext_vector_type(2)));
typedef float f32x4 __attribute__((ext_vector_type(4)));
typedef float f32x16 __attribute__((ext_vector_type(16)));
typedef unsigned u32x2 __attribute__((ext_vector_type(2)));
typedef unsigned u32x4 __attribute__((ext_vector_type(4)));
typedef AS1 unsigned gu32;
typedef AS1 unsigned long long gu64;

#define ATT_PRIO 2
#define GEM_PRIO 0
#define GU_HI 0
#define DN_HI 0
#define MPRIO(mask, i) __builtin_amdgcn_s_setprio(((mask) >> (i)) & 1 ? 3 : 0)
#define CODE_PF 0
#define GU_DELAY 2
#define GU_ROT 4
#define GU_GQ 5
#define SPLIT_MODE 0

constexpr int SEQ = 2048, DM = 640, FF = 1536;
constexpr unsigned WS_KF = 0;
constexpr unsigned WS_HF = 16384;
constexpr unsigned WS_OF = 32768;
constexpr unsigned WS_EP = 49152;
constexpr unsigned KVD = 5376;
constexpr unsigned KV_K1 = 2048, KV_V = 2560, KV_VT = 1408;
constexpr unsigned WS_KV = 65536;
constexpr unsigned WS_HT = WS_KV + 176128;
constexpr unsigned WS_OT = WS_HT + 64 * 96 * 1024;
constexpr unsigned WS_QT = WS_OT + 64 * 40 * 1024;
constexpr unsigned WS_PT = WS_QT + 64 * 32 * 2048;
constexpr unsigned WS_ST = WS_PT + 256 * 3 * 32 * 256;
constexpr unsigned L_PANEL = 0;
constexpr unsigned L_KV = 81920;
constexpr unsigned L_WQ = 81920;
constexpr unsigned L_GU = 124928;
constexpr unsigned GSL = 12320;
constexpr unsigned L_WKV = 124928;
constexpr unsigned L_MISC = 161920;
constexpr unsigned L_TOTAL = 163840;
constexpr unsigned CHUNK = 2 * KVD;
constexpr int NCHUNK = 16;
enum { C_AP = 0  , C_GP = 4  , C_DP = 8  , C_PP = 12  , C_WP = 16  , C_KVP = 20, C_KVFIN, C_KVRDY, C_KN, C_VM, C_N };

struct Params { const char* x; const char* wq; const char* wk; const char* wv; const char* wo; const char* w1; const char* w3; const char* w2; char* out; char* ws; };

#define SBAR() __builtin_amdgcn_sched_barrier(0)
#define PIN(x) asm volatile("" : "+v"(x))
#define CFENCE() asm volatile("" ::: "memory")
#define WAITV(n) asm volatile("s_waitcnt vmcnt(" #n ")" ::: "memory")
#define MFMA(a, b, c) __builtin_amdgcn_mfma_f32_32x32x16_bf16((a), (b), (c), 0, 0, 0)
#define RLX_AGENT __ATOMIC_RELAXED, __HIP_MEMORY_SCOPE_AGENT
#define RLX_WG __ATOMIC_RELAXED, __HIP_MEMORY_SCOPE_WORKGROUP

#define TMO(spins, lim, code, tgt) do {} while (0)
#define STAMP(id) do {} while (0)
#define STAMPC(id) do {} while (0)

#define LP(T, off) ((AS3 T*)(uintptr_t)(unsigned)(off))
__device__ __forceinline__ bf16x8 ldsf(unsigned off) { return *LP(const bf16x8, off); }
__device__ __forceinline__ s16x4 ldstr(unsigned off) { return __builtin_amdgcn_ds_read_tr16_b64_v4i16(LP(s16x4, off)); }
__device__ __forceinline__ bf16x8 ldstr2(unsigned off0, unsigned off1) {
    s16x4 a = ldstr(off0), b = ldstr(off1);
    return __builtin_bit_cast(bf16x8, __builtin_shufflevector(a, b, 0, 1, 2, 3, 4, 5, 6, 7));
}
__device__ __forceinline__ unsigned pk2(float lo, float hi) { bf16x2 v = {(__bf16)lo, (__bf16)hi}; return __builtin_bit_cast(unsigned, v); }
__device__ __forceinline__ float bflo(unsigned u) { return __uint_as_float(u << 16); }
__device__ __forceinline__ float bfhi(unsigned u) { return __uint_as_float(u & 0xffff0000u); }
__device__ __forceinline__ float rbf(float f) { return (float)(__bf16)f; }

__device__ __forceinline__ void glds16(unsigned voff, const char* sbase, unsigned ldsdst) {
    unsigned keep;
    asm volatile("s_mov_b32 %0, m0\n\ts_mov_b32 m0, %3\n\ts_nop 2\n\tglobal_load_lds_dwordx4 %1, %2\n\ts_mov_b32 m0, %0"
                 : "=&s"(keep) : "v"(voff), "s"(sbase), "s"(ldsdst) : "memory");
}
__device__ __forceinline__ void glds16_sc1(unsigned voff, const char* sbase, unsigned ldsdst) {
    unsigned keep;
    asm volatile("s_mov_b32 %0, m0\n\ts_mov_b32 m0, %3\n\ts_nop 2\n\tglobal_load_lds_dwordx4 %1, %2 sc1\n\ts_mov_b32 m0, %0"
                 : "=&s"(keep) : "v"(voff), "s"(sbase), "s"(ldsdst) : "memory");
}
__device__ __forceinline__ void glds16x3_sc1(unsigned voff, const char* sbase, unsigned ldsdst) {
    unsigned keep;
    asm volatile("s_mov_b32 %0, m0\n\ts_mov_b32 m0, %3\n\ts_nop 2\n\tglobal_load_lds_dwordx4 %1, %2 sc1\n\tglobal_load_lds_dwordx4 %1, %2 offset:1024 sc1\n\tglobal_load_lds_dwordx4 %1, %2 offset:2048 sc1\n\ts_mov_b32 m0, %0"
                 : "=&s"(keep) : "v"(voff), "s"(sbase), "s"(ldsdst) : "memory");
}
__device__ __forceinline__ void glds16x3v(unsigned v0, unsigned v1, unsigned v2, const char* sbase, unsigned ldsdst) {
    unsigned keep;
    asm volatile("s_mov_b32 %0, m0\n\ts_mov_b32 m0, %5\n\ts_nop 0\n\tglobal_load_lds_dwordx4 %1, %4\n\tglobal_load_lds_dwordx4 %2, %4 offset:1024\n\tglobal_load_lds_dwordx4 %3, %4 offset:2048\n\ts_mov_b32 m0, %0"
                 : "=&s"(keep) : "v"(v0), "v"(v1), "v"(v2), "s"(sbase), "s"(ldsdst) : "memory");
}
__device__ __forceinline__ void l2touch(const char* ga) {
    unsigned keep;
    asm volatile("s_mov_b32 %0, m0\n\ts_mov_b32 m0, %2\n\ts_nop 0\n\tglobal_load_lds_dword %1, off\n\ts_mov_b32 m0, %0" : "=&s"(keep) : "v"(ga), "s"(L_MISC + 1024) : "memory");
}
#define GLOAD16(dst, voff, sbase, imm) asm volatile("s_nop 4\n\tglobal_load_dwordx4 %0, %1, %2 offset:%3" : "=v"(dst) : "v"(voff), "s"(sbase), "i"(imm) : "memory")

__device__ __forceinline__ AS3 unsigned* lcnt(int c) { return LP(unsigned, L_MISC) + c; }
__device__ __forceinline__ void larrive(int c, int lane) { CFENCE(); if (lane == 0) __hip_atomic_fetch_add(lcnt(c), 1u, RLX_WG); CFENCE(); }
__device__ __forceinline__ void lset(int c, unsigned v, int lane) { CFENCE(); if (lane == 0) __hip_atomic_store(lcnt(c), v, RLX_WG); CFENCE(); }
__device__ __forceinline__ void lwait(int c, unsigned target) {
    CFENCE();
    _Pragma("clang loop unroll(disable)") for (unsigned spins = 0; spins < (1u << 22); ++spins) {
        if ((unsigned)__builtin_amdgcn_readfirstlane(__hip_atomic_load(lcnt(c), RLX_WG)) >= target) break;
        TMO(spins, 1u << 16, 100 + c, target);
        __builtin_amdgcn_s_sleep(1);
    }
    CFENCE();
}
__device__ __forceinline__ void lwait_all(int c, int n, unsigned target, int lane) {
    CFENCE();
    AS3 unsigned* q = lcnt(c) + (lane < n ? lane : 0);
    _Pragma("clang loop unroll(disable)") for (unsigned spins = 0; spins < (1u << 16); ++spins) {
        if (__all(__hip_atomic_load(q, RLX_WG) >= target)) break;
        TMO(spins, 1u << 16, 200 + c, target);
        __builtin_amdgcn_s_sleep(1);
    }
    CFENCE();
}
__device__ __forceinline__ void lpost(int c, unsigned v) { CFENCE(); *(volatile AS3 unsigned*)lcnt(c) = v; CFENCE(); }
__device__ __forceinline__ void linc(unsigned vaddr, unsigned vone) {
    asm volatile("s_mov_b64 exec, 1\n\tds_add_u32 %0, %1\n\ts_mov_b64 exec, -1" :: "v"(vaddr), "v"(vone) : "memory");
}
__device__ __forceinline__ unsigned lpeek(int c, int lane) { return *((volatile AS3 unsigned*)lcnt(c) + (lane & 3)); }
__device__ __forceinline__ void lcheck(unsigned pk, int c, unsigned target, int lane) {
    if (__builtin_expect(__any(pk < target), 0)) lwait_all(c, 4, target, lane);
    CFENCE();
}
__device__ __forceinline__ void gwait(const char* ws, unsigned off, int n, unsigned tag, int lane) {
    gu32* f = (gu32*)(ws + off) + (lane < n ? lane : 0);
    _Pragma("clang loop unroll(disable)") for (unsigned spins = 0; spins < (1u << 14); ++spins) {
        unsigned v = __hip_atomic_load(f, RLX_AGENT);
        if (__all(v == tag)) break;
        TMO(spins, 1u << 14, 300, off);
        __builtin_amdgcn_s_sleep(6);
    }
    CFENCE();
}
__device__ __forceinline__ float gwait64(const char* ws, unsigned off, unsigned tag, int lane) {
    gu64* f = (gu64*)(ws + off) + lane;
    unsigned long long v = 0;
    _Pragma("clang loop unroll(disable)") for (unsigned spins = 0; spins < (1u << 14); ++spins) {
        v = __hip_atomic_load(f, RLX_AGENT);
        if (__all((unsigned)(v >> 32) == tag)) break;
        __builtin_amdgcn_s_sleep(6);
    }
    CFENCE();
    return __uint_as_float((unsigned)v);
}
__device__ __forceinline__ __amdgpu_buffer_rsrc_t mkrsrc(const char* base, unsigned bytes) {
    return __builtin_amdgcn_make_buffer_rsrc((void*)base, 0, bytes, 0x00020000);
}
__device__ __forceinline__ float dpp_sum16(float v) {
    v += __int_as_float(__builtin_amdgcn_update_dpp(0, __float_as_int(v), 0x128, 0xf, 0xf, false));
    v += __int_as_float(__builtin_amdgcn_update_dpp(0, __float_as_int(v), 0x124, 0xf, 0xf, false));
    v += __int_as_float(__builtin_amdgcn_update_dpp(0, __float_as_int(v), 0x122, 0xf, 0xf, false));
    v += __int_as_float(__builtin_amdgcn_update_dpp(0, __float_as_int(v), 0x121, 0xf, 0xf, false));
    return v;
}
__device__ __forceinline__ float wave_max(float v) {
    int x = __float_as_int(v);
    x = max(x, __builtin_amdgcn_update_dpp(0, x, 0x128, 0xf, 0xf, false)); x = max(x, __builtin_amdgcn_update_dpp(0, x, 0x124, 0xf, 0xf, false));
    x = max(x, __builtin_amdgcn_update_dpp(0, x, 0x122, 0xf, 0xf, false)); x = max(x, __builtin_amdgcn_update_dpp(0, x, 0x121, 0xf, 0xf, false));
    const int a = max(__builtin_amdgcn_readlane(x, 0), __builtin_amdgcn_readlane(x, 16)), b = max(__builtin_amdgcn_readlane(x, 32), __builtin_amdgcn_readlane(x, 48));
    return __int_as_float(max(a, b));
}
__device__ __forceinline__ bf16x8 pack8(const f32x16& a, int b, float sc) {
    u32x4 r = {pk2(a[b] * sc, a[b + 1] * sc), pk2(a[b + 2] * sc, a[b + 3] * sc), pk2(a[b + 4] * sc, a[b + 5] * sc), pk2(a[b + 6] * sc, a[b + 7] * sc)};
    return __builtin_bit_cast(bf16x8, r);
}

template <int I> struct IC { static constexpr int value = I; };
template <int B, int E, class F> __device__ __forceinline__ void static_for(F&& f) { if constexpr (B < E) { f(IC<B>{}); static_for<B + 1, E>(f); } }
#define WAITVN(n) asm volatile("s_waitcnt vmcnt(%0)" :: "i"(n) : "memory")
__device__ __forceinline__ void GLOAD16S(u32x4& dst, unsigned voff, const char* sbase) { asm volatile("global_load_dwordx4 %0, %1, %2 sc1" : "=v"(dst) : "v"(voff), "s"(sbase) : "memory"); }
__device__ __forceinline__ void GLOAD16N(u32x4& dst, unsigned voff, const char* sbase) { asm volatile("global_load_dwordx4 %0, %1, %2" : "=v"(dst) : "v"(voff), "s"(sbase) : "memory"); }
__device__ __forceinline__ void GLOAD16N_IF(u32x4& dst, unsigned voff, const char* sbase, int cond) {
    asm volatile("s_cmp_eq_u32 %3, 0\n\ts_cbranch_scc1 1f\n\tglobal_load_dwordx4 %0, %1, %2\n1:" : "=v"(dst) : "v"(voff), "s"(sbase), "s"(cond) : "memory", "scc"); }
template <int N> __device__ __forceinline__ void waitv5(u32x4& a, u32x4& b, u32x4& c, u32x4& d, u32x4& e) { asm volatile("s_waitcnt vmcnt(%5)" : "+v"(a), "+v"(b), "+v"(c), "+v"(d), "+v"(e) : "i"(N) : "memory"); }
__device__ __forceinline__ void gload8x2_sc1(unsigned long long& a, unsigned long long& b, unsigned voff, const char* sbase) {
    asm volatile("global_load_dwordx2 %0, %2, %3 sc1\n\tglobal_load_dwordx2 %1, %2, %3 offset:512 sc1" : "=&v"(a), "=&v"(b) : "v"(voff), "s"(sbase) : "memory"); }
__device__ __forceinline__ void waitv8x2(unsigned long long& a, unsigned long long& b) { asm volatile("s_waitcnt vmcnt(0)" : "+v"(a), "+v"(b) :: "memory"); }
__device__ __forceinline__ void PIN2(u32x4& a, u32x4& b) { asm volatile("" : "+v"(a), "+v"(b) :: "memory"); }
__device__ __forceinline__ u32x4 scale8(unsigned x0, unsigned x1, unsigned x2, unsigned x3, unsigned clo, unsigned chi, float c) {
    unsigned o0, o1, o2, o3; float t0, t1, t2, t3, t4, t5, t6, t7;
    asm("v_dot2_f32_bf16 %4, %12, %16, %18\n\tv_dot2_f32_bf16 %5, %12, %17, %18\n\tv_dot2_f32_bf16 %6, %13, %16, %18\n\tv_dot2_f32_bf16 %7, %13, %17, %18\n\t"
        "v_dot2_f32_bf16 %8, %14, %16, %18\n\tv_dot2_f32_bf16 %9, %14, %17, %18\n\tv_dot2_f32_bf16 %10, %15, %16, %18\n\tv_dot2_f32_bf16 %11, %15, %17, %18\n\t"
        "v_cvt_pk_bf16_f32 %0, %4, %5\n\tv_cvt_pk_bf16_f32 %1, %6, %7\n\tv_cvt_pk_bf16_f32 %2, %8, %9\n\tv_cvt_pk_bf16_f32 %3, %10, %11"
        : "=&v"(o0), "=&v"(o1), "=&v"(o2), "=&v"(o3), "=&v"(t0), "=&v"(t1), "=&v"(t2), "=&v"(t3), "=&v"(t4), "=&v"(t5), "=&v"(t6), "=&v"(t7)
        : "v"(x0), "v"(x1), "v"(x2), "v"(x3), "v"(clo), "v"(chi), "v"(c));
    return u32x4{o0, o1, o2, o3};
}
template <int N> __device__ __forceinline__ void waitv3(u32x4& a, u32x4& b, u32x4& c) { asm volatile("s_waitcnt vmcnt(%3)" : "+v"(a), "+v"(b), "+v"(c) : "i"(N) : "memory"); }

constexpr float QSCALE = 0.22360679774997896f * 1.4426950408889634f;
constexpr float BOUND_MAX = 50.f;

struct AttSync { int lane; const char* kv; unsigned dvoff; unsigned wo; int w; };
__device__ __forceinline__ unsigned kvslot(int c) { return L_KV + ((c + 2) & 3) * CHUNK; }
__device__ __forceinline__ void att_dma(const AttSync& a, int c) { glds16x3_sc1(a.dvoff, a.kv + (size_t)c * CHUNK, kvslot(c) + a.wo); }
constexpr unsigned AP0 = 16;
#define RING_X(c) do { WAITV(3); lpost(C_AP + as.w, AP0 + (c) + 1); } while (0)
#define RP_A
#define RP_B
#define RING_Y(c, pk) do { RP_A lcheck(pk, C_AP, AP0 + (c) + 1, lane); const int cn_ = (c) + 3 < NCHUNK ? (c) + 3 : NCHUNK - 1;   \
                           glds16x3_sc1(as.dvoff, as.kv + (size_t)cn_ * CHUNK, kvslot((c) + 3) + as.wo); RP_B } while (0)

__device__ __forceinline__ void att_fast(const AttSync& as, bf16x8 Q0a, bf16x8 Q1a0, bf16x8 Q1a1, bf16x8 Q0b, bf16x8 Q1b0, bf16x8 Q1b1, f32x16& Oa, f32x16& Ob) {
    const int lane = as.lane, r = lane & 31, h = lane >> 5;
    const unsigned ak0 = lane * 16, ak1 = h ? KV_V + 20 * 16 : KV_K1 + r * 16;
    const unsigned av = KV_V + h * 352 + (r <= 20 ? r : 21) * 16;
    const f32x16 zero = {};
    f32x16 Sa, Sb = {};
    bf16x8 K0[2], K1[2], V0[2], V1[2];
    u32x4 Pa0, Pa1, Pb0 = {}, Pb1 = {};
    unsigned pk = 0;
    unsigned long long rp0 = 0, rp1 = 0;
    V0[1] = bf16x8{}; V1[1] = bf16x8{};
    unsigned vk0 = L_KV + ak0, vk1 = L_KV + ak1, vv = L_KV + av;
    PIN(vk0); PIN(vk1); PIN(vv);
    K0[0] = ldsf(vk0 + 2 * CHUNK); K1[0] = ldsf(vk1 + 2 * CHUNK); K0[1] = ldsf(vk0 + 2 * CHUNK + 1024); V1[0] = ldsf(vv + 2 * CHUNK + 704); V0[0] = ldsf(vv + 2 * CHUNK);
    Sa = MFMA(K0[0], Q0a, zero); Sa = MFMA(K1[0], Q1a0, Sa);
#define EX4(S, i) do { S[i] = __builtin_amdgcn_exp2f(S[i]); S[i + 1] = __builtin_amdgcn_exp2f(S[i + 1]); S[i + 2] = __builtin_amdgcn_exp2f(S[i + 2]); S[i + 3] = __builtin_amdgcn_exp2f(S[i + 3]); } while (0)
#define GAP(MF, LD, T, k, PW, j, S, i) do { MF; SBAR(); LD; PW[j] = pk2(T[k], T[k + 1]); PW[j + 1] = pk2(T[k + 2], T[k + 3]); PIN(PW); SBAR(); EX4(S, i); PIN(S); SBAR(); } while (0)
#define BC(x) __builtin_bit_cast(bf16x8, x)
#define STEP(e, d, QB1, QA1N, KN, L3, LK0, LK1, LV) do { \
        GAP(Ob = MFMA(V0[e ^ 1], BC(Pb0), Ob), (void)0,                  Sb, 12, Pb1, 2, Sa, 0); \
        GAP(Sb = MFMA(K0[e], Q0b, zero),       (void)0,                  Sa, 0, Pa0, 0, Sa, 4); \
        GAP(Sb = MFMA(K1[d], QB1, Sb),         L3,                       Sa, 4, Pa0, 2, Sa, 8); \
        GAP(Ob = MFMA(V1[e ^ 1], BC(Pb1), Ob), K0[e] = ldsf(LK0),        Sa, 8, Pa1, 0, Sa, 12); \
        GAP(Oa = MFMA(V0[e], BC(Pa0), Oa),     LK1,                      Sa, 12, Pa1, 2, Sb, 0); \
        GAP(Sa = MFMA(K0[e ^ 1], Q0a, zero),   V1[e ^ 1] = ldsf((LV) + 704), Sb, 0, Pb0, 0, Sb, 4); \
        GAP(Sa = MFMA(K1[KN], QA1N, Sa),       V0[e ^ 1] = ldsf(LV),     Sb, 4, Pb0, 2, Sb, 8); \
        GAP(Oa = MFMA(V1[e], BC(Pa1), Oa),     (void)0,                  Sb, 8, Pb1, 0, Sb, 12); \
    } while (0)
#define CHUNK4(c, B, N) do { \
        STEP(0, 0, Q1b0, Q1a1, 0, (void)0, vk0 + (B) + KVD, K1[1] = ldsf(vk1 + (B) + KVD), vv + (B) + KV_VT); \
        RING_X(c); \
        STEP(1, 0, Q1b1, Q1a0, 1, pk = lpeek(C_AP, lane), vk0 + (B) + KVD + 1024, (void)0, vv + (B) + KVD); \
        RING_Y(c, pk); \
        STEP(0, 1, Q1b0, Q1a1, 1, (void)0, vk0 + (N), K1[0] = ldsf(vk1 + (N)), vv + (B) + KVD + KV_VT); \
        STEP(1, 1, Q1b1, Q1a0, 0, (void)0, vk0 + (N) + 1024, (void)0, vv + (N)); \
    } while (0)
    for (int c = 0; c < NCHUNK; c += 4) {
        CHUNK4(c, 2 * CHUNK, 3 * CHUNK); CHUNK4(c + 1, 3 * CHUNK, 0); CHUNK4(c + 2, 0, CHUNK); CHUNK4(c + 3, CHUNK, 2 * CHUNK);
    }
#undef CHUNK4
    Pb1[2] = pk2(Sb[12], Sb[13]); Pb1[3] = pk2(Sb[14], Sb[15]);
    Ob = MFMA(V0[1], BC(Pb0), Ob); Ob = MFMA(V1[1], BC(Pb1), Ob);
#undef STEP
#undef GAP
#undef EX4
}

__device__ __forceinline__ void att_safe(const AttSync& as, bf16x8 Q0a, bf16x8 Q1a0, bf16x8 Q1a1, bf16x8 Q0b, bf16x8 Q1b0, bf16x8 Q1b1, f32x16& Oa, f32x16& Ob) {
    const int lane = as.lane, r = lane & 31, h = lane >> 5;
    const unsigned ak0 = lane * 16, ak1 = h ? KV_V + 20 * 16 : KV_K1 + r * 16;
    const unsigned av = KV_V + h * 352 + (r <= 20 ? r : 21) * 16;
    const f32x16 zero = {};
    float ma = -INFINITY, mb = -INFINITY;
    unsigned long long rp0 = 0, rp1 = 0;
    for (int c = 0; c < NCHUNK; ++c) {
        const unsigned b0 = kvslot(c);
        for (int t = 0; t < 4; ++t) {
            if (t == 1) RING_X(c);
            if (t == 2) RING_Y(c, 0u);
            const unsigned base = b0 + (t >> 1) * KVD; const int e = t & 1;
            bf16x8 K0 = ldsf(base + ak0 + e * 1024), K1 = ldsf(base + ak1), V0 = ldsf(base + av + e * KV_VT), V1 = ldsf(base + av + e * KV_VT + 704);
#pragma unroll
            for (int q = 0; q < 2; ++q) {
                f32x16 S = MFMA(K0, q ? Q0b : Q0a, zero); S = MFMA(K1, q ? (e ? Q1b1 : Q1b0) : (e ? Q1a1 : Q1a0), S);
                float mx = S[0];
#pragma unroll
                for (int i = 1; i < 16; ++i) mx = fmaxf(mx, S[i]);
                mx = fmaxf(mx, __shfl_xor(mx, 32));
                float& m = q ? mb : ma; f32x16& O = q ? Ob : Oa;
                const float mn = fmaxf(m, mx);
                const float al = __builtin_amdgcn_exp2f(m - mn);
                m = mn;
#pragma unroll
                for (int i = 0; i < 16; ++i) { O[i] *= al; S[i] = __builtin_amdgcn_exp2f(S[i] - mn); }
                O = MFMA(V0, pack8(S, 0, 1.f), O); O = MFMA(V1, pack8(S, 8, 1.f), O);
            }
        }
    }
}

__device__ __forceinline__ void xload4(u32x4 (&x)[5], const char* xb, unsigned vo) {
    asm volatile("global_load_dwordx4 %0, %5, %6\n\tglobal_load_dwordx4 %1, %5, %6 offset:256\n\tglobal_load_dwordx4 %2, %5, %6 offset:512\n\t"
                 "global_load_dwordx4 %3, %5, %6 offset:768\n\tglobal_load_dwordx4 %4, %5, %6 offset:1024"
                 : "=&v"(x[0]), "=&v"(x[1]), "=&v"(x[2]), "=&v"(x[3]), "=&v"(x[4]) : "v"(vo), "s"(xb) : "memory");
}
__device__ __forceinline__ void ln_rows(const u32x4 (&xr)[5], int row0, int lane) {
    f32x2 xv[20];
    f32x2 s0 = {0.f, 0.f}, s1 = {0.f, 0.f}, q0 = {0.f, 0.f}, q1 = {0.f, 0.f};
#pragma unroll
    for (int i = 0; i < 20; i += 2) {
        const unsigned a = xr[i >> 2][i & 3], b = xr[i >> 2][(i & 3) + 1];
        xv[i] = f32x2{bflo(a), bfhi(a)}; xv[i + 1] = f32x2{bflo(b), bfhi(b)};
        s0 += xv[i]; q0 = __builtin_elementwise_fma(xv[i], xv[i], q0);
        s1 += xv[i + 1]; q1 = __builtin_elementwise_fma(xv[i + 1], xv[i + 1], q1);
    }
    s0 += s1; q0 += q1;
    const float s = dpp_sum16(s0[0] + s0[1]), ss = dpp_sum16(q0[0] + q0[1]);
    const float mean = s * (1.f / 640.f);
    const float var = fmaxf(ss * (1.f / 640.f) - mean * mean, 0.f);
    const float rs = __builtin_amdgcn_rcpf(rbf(__builtin_amdgcn_sqrtf(rbf(rbf(var) + 1e-5f)))), mo = -rbf(mean) * rs;
    const f32x2 rs2 = {rs, rs}, mo2 = {mo, mo};
    const int row = row0 + (lane >> 4), c = lane & 15;
    const unsigned wa = L_PANEL + (row >> 5) * 40960 + (c >> 1) * 1024 + (c & 1) * 512 + (((row & 31) ^ (c & 7)) << 4);
#pragma unroll
    for (int u = 0; u < 5; ++u) {
        u32x4 o;
#pragma unroll
        for (int e = 0; e < 4; ++e) { const f32x2 y = __builtin_elementwise_fma(xv[4 * u + e], rs2, mo2); o[e] = pk2(y[0], y[1]); }
        *LP(u32x4, wa + u * 8192) = o;
    }
}
#define BLOCK_BARRIER() asm volatile("s_waitcnt lgkmcnt(0)\n\ts_barrier" ::: "memory")

template <int PH>
__global__ void __launch_bounds__(512, 2) mega(Params p) {
    constexpr bool PRO = PH & 1, ATT = PH & 2, DN = PH & 8, WO = PH & 16;
    const int w = __builtin_amdgcn_readfirstlane(threadIdx.x >> 6);
    const int b = blockIdx.x, xc = b & 7, j = b >> 3;
    const int ti = (xc >> 1) * 8 + (j >> 2), g = (xc & 1) * 4 + (j & 3);
    unsigned tag = 1u, epv = 0u;
    constexpr bool FUSED = PH == 27;
    if constexpr (FUSED) asm volatile("global_load_dword %0, %1, %2 sc1" : "=v"(epv) : "v"(0u), "s"(p.ws + WS_EP) : "memory");
#define GET_TAG() do { if constexpr (FUSED) { asm volatile("s_nop 0\n\tv_readfirstlane_b32 %0, %1\n\ts_nop 3" : "=s"(tag) : "v"(epv) : "memory"); tag += 0x9E3779B1u; } } while (0)
    char* ws = p.ws;
    STAMP(0);
    if (threadIdx.x < C_N) *lcnt(threadIdx.x) = (threadIdx.x == C_WP || threadIdx.x == C_WP + 1 || threadIdx.x == C_PP || threadIdx.x == C_PP + 1) ? 100u : 0u;
    const bool haskv = (g >> 2) == (ti & 1);
    const char* wqb = p.wq + g * 160;
    const int kmt = g & 1;
    const char* xb = p.x + (size_t)ti * 64 * 1280;
#define LANE_CONSTS() int lane = __builtin_amdgcn_mbcnt_hi(~0u, __builtin_amdgcn_mbcnt_lo(~0u, 0u)); PIN(lane); \
    const int r = lane & 31, h = lane >> 5, tq = (lane & 15) >> 2, tp = lane & 3, tb = (lane >> 4) & 1; (void)r; (void)h; (void)tq; (void)tp; (void)tb

    if (w < 4) {
        LANE_CONSTS();
        const unsigned xvo = (lane >> 4) * 1280 + (lane & 15) * 16;
        bf16x8 Q0a, Q0b; u32x2 q1a, q1b;
        const int head = 4 * g + w;
        const AttSync as{lane, ws + WS_KV, (unsigned)((w < 3 ? w * 3072 : CHUNK - 3072) + lane * 16), (unsigned)(w < 3 ? w * 3072 : CHUNK - 3072), w};
        __builtin_amdgcn_s_setprio(3);
        unsigned long long fk = 0, fv = 0; bool early = false;
        if constexpr (PRO) {
            u32x4 x0[5], x1[5], x2[5], x3[5];
            const int ra = kmt * 32 + w * 8, rb = (kmt ^ 1) * 32 + w * 8;
            xload4(x0, xb + (size_t)ra * 1280, xvo); xload4(x1, xb + (size_t)(ra + 4) * 1280, xvo);
            xload4(x2, xb + (size_t)rb * 1280, xvo); xload4(x3, xb + (size_t)(rb + 4) * 1280, xvo);
            BLOCK_BARRIER();
            waitv5<15>(x0[0], x0[1], x0[2], x0[3], x0[4]);
            GET_TAG();
            STAMP(1);
            ln_rows(x0, ra, lane);
            waitv5<10>(x1[0], x1[1], x1[2], x1[3], x1[4]);
            ln_rows(x1, ra + 4, lane);
            lpost(C_AP + w, 1);
            waitv5<5>(x2[0], x2[1], x2[2], x2[3], x2[4]);
            ln_rows(x2, rb, lane);
            waitv5<0>(x3[0], x3[1], x3[2], x3[3], x3[4]);
            ln_rows(x3, rb + 4, lane);
            lpost(C_AP + w, 2);
            unsigned pa[4];
#pragma unroll
            for (int s = 0; s < 4; ++s) pa[s] = L_PANEL + h * 512 + ((r ^ ((2 * s + h) & 7)) << 4);
            f32x16 qa = {}, qb = {};
            const unsigned wa = L_WQ + (8 * h + tq) * 160 + w * 40 + tb * 32 + (tb ? 0 : tp * 8);
            lwait_all(C_AP, 4, 2, lane); lwait_all(C_PP, 4, 1, lane);
            STAMP(2);
            bf16x8 fw[2][4], fa[2][4], fb[2][4];
#pragma unroll
            for (int s4 = 0; s4 < 4; ++s4) { fw[0][s4] = ldstr2(wa + s4 * 2560, wa + s4 * 2560 + 640); fa[0][s4] = ldsf(pa[s4] + s4 * 1024); fb[0][s4] = ldsf(pa[s4] + 40960 + s4 * 1024); }
            unsigned pk = 0;
            static_for<0, 10>([&](auto S) {
                constexpr int st = S.value, nx = st + 1;
                if constexpr (st == 3 || st == 5 || st == 7) lcheck(pk, C_WP, (st - 1) / 2, lane);
                if constexpr (st == 6 && ATT) gload8x2_sc1(fk, fv, lane * 8, ws + WS_KF + xc * 1024);
                if constexpr (st == 8 && ATT) {
                    waitv8x2(fk, fv);
                    early = __all((unsigned)(fk >> 32) == tag && (unsigned)(fv >> 32) == tag);
                    if (early) { lwait_all(C_AP, 4, 6, lane); att_dma(as, 0); att_dma(as, 1); }
                    STAMP(8);
                }
#pragma unroll
                for (int s4 = 0; s4 < 4; ++s4) {
                    qa = MFMA(fw[st & 1][s4], fa[st & 1][s4], qa); SBAR();
                    if constexpr (nx < 10) { const unsigned o = wa + (nx & 3) * 10240 + s4 * 2560; fw[nx & 1][s4] = ldstr2(o, o + 640); SBAR(); }
                    qb = MFMA(fw[st & 1][s4], fb[st & 1][s4], qb); SBAR();
                    if constexpr (nx < 10) { fa[nx & 1][s4] = ldsf(pa[s4] + (nx * 4 + s4) * 1024); fb[nx & 1][s4] = ldsf(pa[s4] + 40960 + (nx * 4 + s4) * 1024); SBAR(); }
                    if (s4 == 2 && (st == 2 || st == 4 || st == 6)) { pk = lpeek(C_WP, lane); SBAR(); }
                }
                if constexpr (!(st & 1) && st <= 6) lpost(C_AP + w, 3 + st / 2);
            });
            lpost(C_AP + w, 8);
            Q0a = pack8(qa, 0, QSCALE); Q0b = pack8(qb, 0, QSCALE);
            q1a = u32x2{pk2(qa[8] * QSCALE, qa[9] * QSCALE), pk2(qa[10] * QSCALE, qa[11] * QSCALE)};
            q1b = u32x2{pk2(qb[8] * QSCALE, qb[9] * QSCALE), pk2(qb[10] * QSCALE, qb[11] * QSCALE)};
            if (h) { q1a = u32x2{0u, 0u}; q1b = u32x2{0u, 0u}; }
            if constexpr (!ATT) {
                char* qt = ws + WS_QT + (size_t)((2 * ti) * 32 + head) * 2048 + lane * 16;
                *(bf16x8*)qt = Q0a; *(u32x2*)(qt + 1024) = q1a; *(bf16x8*)(qt + 65536) = Q0b; *(u32x2*)(qt + 65536 + 1024) = q1b;
            }
            STAMP(3);
        } else {
            BLOCK_BARRIER();
            if constexpr (ATT) {
                const char* qt = ws + WS_QT + (size_t)((2 * ti) * 32 + head) * 2048 + lane * 16;
                Q0a = *(const bf16x8*)qt; q1a = *(const u32x2*)(qt + 1024); Q0b = *(const bf16x8*)(qt + 65536); q1b = *(const u32x2*)(qt + 65536 + 1024);
            }
        }
        if constexpr (ATT) {
            float na = 0.f, nb = 0.f;
            {
                const u32x4 a0 = __builtin_bit_cast(u32x4, Q0a), b0 = __builtin_bit_cast(u32x4, Q0b);
#pragma unroll
                for (int e = 0; e < 4; ++e) { na += bflo(a0[e]) * bflo(a0[e]) + bfhi(a0[e]) * bfhi(a0[e]); nb += bflo(b0[e]) * bflo(b0[e]) + bfhi(b0[e]) * bfhi(b0[e]); }
#pragma unroll
                for (int e = 0; e < 2; ++e) { na += bflo(q1a[e]) * bflo(q1a[e]) + bfhi(q1a[e]) * bfhi(q1a[e]); nb += bflo(q1b[e]) * bflo(q1b[e]) + bfhi(q1b[e]) * bfhi(q1b[e]); }
                na += __shfl_xor(na, 32); nb += __shfl_xor(nb, 32);
            }
            if (!early) {
                gu64* f = (gu64*)(ws + WS_KF + xc * 1024) + lane;
                _Pragma("clang loop unroll(disable)") for (unsigned spins = 0; spins < (1u << 14); ++spins) {
                    fk = __hip_atomic_load(f, RLX_AGENT); fv = __hip_atomic_load(f + 64, RLX_AGENT);
                    if (__all((unsigned)(fk >> 32) == tag && (unsigned)(fv >> 32) == tag)) break;
                    TMO(spins, 1u << 14, 301, tag);
                    __builtin_amdgcn_s_sleep(4);
                }
                if constexpr (PRO) lwait_all(C_AP, 4, 6, lane);
                att_dma(as, 0); att_dma(as, 1);
            }
            STAMP(4);
            if constexpr (PRO) lwait_all(C_AP, 4, 8, lane);
            att_dma(as, 2);
            const float kn2 = wave_max(__uint_as_float((unsigned)fk)), vmx = wave_max(__uint_as_float((unsigned)fv));
            const float Ma = __builtin_sqrtf(na * kn2) * 1.01f, Mb = __builtin_sqrtf(nb * kn2) * 1.01f;
            const bool fast = __all(Ma <= BOUND_MAX && Mb <= BOUND_MAX && vmx <= 1e30f);
            const unsigned sa = (fast && h) ? pk2(-Ma, 0.f) : q1a[0], sb = (fast && h) ? pk2(-Mb, 0.f) : q1b[0];
            const u32x4 a0 = {sa, q1a[1], 0u, 0u}, a1 = {h ? sa : 0u, 0u, q1a[0], q1a[1]}, b0 = {sb, q1b[1], 0u, 0u}, b1 = {h ? sb : 0u, 0u, q1b[0], q1b[1]};
            f32x16 Oa = {}, Ob = {};
            WAITV(6); lpost(C_AP + w, AP0); lwait_all(C_AP, 4, AP0, lane);
            __builtin_amdgcn_s_setprio(ATT_PRIO);
            STAMP(5); STAMPC(12);
            if (fast) att_fast(as, Q0a, BC(a0), BC(a1), Q0b, BC(b0), BC(b1), Oa, Ob);
            else att_safe(as, Q0a, BC(a0), BC(a1), Q0b, BC(b0), BC(b1), Oa, Ob);
            STAMP(6); STAMPC(13);
            const __amdgpu_buffer_rsrc_t ro = mkrsrc(ws + WS_OT, 64 * 40 * 1024);
#pragma unroll
            for (int q = 0; q < 2; ++q) {
                const f32x16& O = q ? Ob : Oa;
                const float inv = 1.f / __shfl(O[8], r + 32);
                const unsigned base = ((2 * ti + q) * 40 + 5 * g) * 1024;
                __builtin_amdgcn_raw_buffer_store_b128(__builtin_bit_cast(u32x4, pack8(O, 0, inv)), ro, base + w * 1024 + lane * 16, 0, 16);
                if (!h) {
                    u32x2 t = {pk2(O[8] * inv, O[9] * inv), pk2(O[10] * inv, O[11] * inv)};
                    __builtin_amdgcn_raw_buffer_store_b64(t, ro, base + 4096 + ((w >> 1) * 32 + r) * 16 + (w & 1) * 8, 0, 16);
                }
            }
            if constexpr (WO) {
                WAITV(0);
                if (lane < 2) __hip_atomic_store((gu32*)(ws + WS_OF) + (2 * ti + lane) * 32 + head, tag, RLX_AGENT);
            }
            STAMP(7);
        }
    } else {
        LANE_CONSTS();
        const unsigned xvo = (lane >> 4) * 1280 + (lane & 15) * 16;
        const int v = w - 4;
        __builtin_amdgcn_s_setprio(3);
        if constexpr (PRO) {
            const int kvm = 2 * ti + (g & 1); const bool isV = (g >> 1) & 1;
            const char* wkvb = isV ? p.wv : p.wk;
            if (haskv && v < 2) {
#pragma unroll
                for (int n = 0; n < 13; ++n) {
                    const unsigned o = v * 12800 + (n < 12 ? n * 1024 : 11776);
                    glds16(o + lane * 16, wkvb, L_WKV + o);
                }
            }
            BLOCK_BARRIER();
            if (v >= 2) {
                const int v2 = v - 2;
                unsigned wo5[5];
#pragma unroll
                for (int i = 0; i < 5; ++i) { const unsigned c = (v2 + 2 * i) * 64 + lane, row = c / 10u, col = c - row * 10u; wo5[i] = row * 1280 + col * 16; }
#pragma unroll
                for (int n = 0; n < 20; ++n) glds16(wo5[n % 5], wqb + (n / 5) * 64 * 1280, L_WQ + (v2 + 2 * n) * 1024);
                u32x4 wst[3][10];
                auto wst_issue = [&](auto I) {
                    constexpr int rr = I.value / 10, i = I.value % 10;
                    GLOAD16N(wst[rr][i], wo5[i % 5], wqb + (4 + 2 * rr + i / 5) * 64 * 1280);
                };
                static_for<0, 10>(wst_issue);
                WAITV(10); lpost(C_PP + v, 1);
                GET_TAG();
                STAMP(2);
                static_for<10, 30>(wst_issue);
                static_for<0, 3>([&](auto R) {
                    constexpr int rr = R.value;
                    waitv5<20 - 10 * rr>(wst[rr][0], wst[rr][1], wst[rr][2], wst[rr][3], wst[rr][4]); waitv5<63>(wst[rr][5], wst[rr][6], wst[rr][7], wst[rr][8], wst[rr][9]);
                    lwait_all(C_AP, 4, 3 + rr, lane);
#pragma unroll
                    for (int i = 0; i < 10; ++i) *LP(u32x4, L_WQ + (rr & 1) * 20480 + (v2 + 2 * i) * 1024 + lane * 16) = wst[rr][i];
                    lpost(C_WP + v, rr + 1);
                });
            } else {
              if (haskv) {
                f32x16 acc = {};
                const unsigned wa = L_WKV + (v * 320 + 8 * h + tq) * 40 + tb * 32 + (tb ? 0 : tp * 8);
                unsigned xa[4];
#pragma unroll
                for (int s = 0; s < 4; ++s) xa[s] = L_PANEL + kmt * 40960 + v * 20480 + h * 512 + ((r ^ ((2 * s + h) & 7)) << 4);
                WAITV(0);
                GET_TAG();
                STAMP(2);
                bf16x8 wf[20], xf[20];
#pragma unroll
                for (int s = 0; s < 20; ++s) wf[s] = ldstr2(wa + s * 640, wa + s * 640 + 160);
                lwait_all(C_AP, 4, 1, lane);
#pragma unroll
                for (int s = 0; s < 20; ++s) xf[s] = ldsf(xa[s & 3] + s * 1024);
                SBAR();
                if (isV) {
#pragma unroll
                    for (int s = 0; s < 20; ++s) acc = MFMA(xf[s], wf[s], acc);
                } else {
#pragma unroll
                    for (int s = 0; s < 20; ++s) acc = MFMA(wf[s], xf[s], acc);
                }
                if (v) {
#pragma unroll
                    for (int e = 0; e < 4; ++e) *LP(f32x4, L_WKV + 12800 + e * 1024 + lane * 16) = f32x4{acc[4 * e], acc[4 * e + 1], acc[4 * e + 2], acc[4 * e + 3]};
                    lpost(C_KVP, 1);
                } else {
                    lwait(C_KVP, 1);
#pragma unroll
                    for (int e = 0; e < 4; ++e) {
                        const f32x4 t = *LP(const f32x4, L_WKV + 12800 + e * 1024 + lane * 16);
                        acc[4 * e] += t[0]; acc[4 * e + 1] += t[1]; acc[4 * e + 2] += t[2]; acc[4 * e + 3] += t[3];
                    }
                    const __amdgpu_buffer_rsrc_t rk = mkrsrc(ws + WS_KV, 32 * KVD);
                    const unsigned base = ti * KVD, ke = g & 1;
                    const u32x4 f0 = __builtin_bit_cast(u32x4, pack8(acc, 0, 1.f)), f1 = __builtin_bit_cast(u32x4, pack8(acc, 8, 1.f));
                    float mx;
                    if (!isV) {
                        __builtin_amdgcn_raw_buffer_store_b128(f0, rk, base + ke * 1024 + lane * 16, 0, 16);
                        const u32x2 k1 = {f1[0], f1[1]};
                        if (!h) __builtin_amdgcn_raw_buffer_store_b64(k1, rk, base + KV_K1 + r * 16 + ke * 8, 0, 16);
                        lpost(C_KVFIN, 1);
                        float n2 = 0.f;
#pragma unroll
                        for (int e = 0; e < 4; ++e) n2 += bflo(f0[e]) * bflo(f0[e]) + bfhi(f0[e]) * bfhi(f0[e]);
                        if (!h) n2 += bflo(f1[0]) * bflo(f1[0]) + bfhi(f1[0]) * bfhi(f1[0]) + bflo(f1[1]) * bflo(f1[1]) + bfhi(f1[1]) * bfhi(f1[1]);
                        n2 += __shfl_xor(n2, 32);
                        mx = (n2 == n2) ? n2 : INFINITY;
                    } else {
                        const unsigned cst = r == 20 ? 0x3f803f80u : 0u;
                        const u32x4 c4 = {cst, cst, cst, cst};
                        if (r <= 21) {
                            __builtin_amdgcn_raw_buffer_store_b128(r >= 20 ? c4 : f0, rk, base + KV_V + ke * KV_VT + h * 352 + r * 16, 0, 16);
                            __builtin_amdgcn_raw_buffer_store_b128(r >= 20 ? c4 : f1, rk, base + KV_V + ke * KV_VT + 704 + h * 352 + r * 16, 0, 16);
                        }
                        lpost(C_KVFIN, 1);
                        float vm = 0.f;
#pragma unroll
                        for (int e = 0; e < 16; ++e) { const float a = fabsf(acc[e]); vm = (a <= vm) ? vm : ((a == a) ? a : INFINITY); }
                        mx = r < 20 ? vm : 0.f;
                    }
                    mx = wave_max(mx);
                    WAITV(0);
                    STAMP(15);
                    if (lane < 8) __hip_atomic_store((gu64*)(ws + WS_KF + lane * 1024 + (isV ? 512 : 0)) + kvm, ((unsigned long long)tag << 32) | __float_as_uint(mx), RLX_AGENT);
                }
            }
              else { WAITV(0); GET_TAG(); }
              STAMP(3);
            }
            if (haskv) lwait(C_KVFIN, 1);
#pragma unroll
            for (int n = 0; n < 2; ++n) {
                unsigned L = n * 64 + lane; L = (j * 4 + v) * 120 + (L < 120u ? L : 119u);
                const unsigned m3 = L >= 7680u, rem = L - (m3 ? 7680u : 0u), row = rem / 12u;
                l2touch((m3 ? p.w3 : p.w1) + row * 3072 + (xc & 1) * 1536 + (rem - row * 12u) * 128);
            }
            lwait_all(C_AP, 4, GU_DELAY, lane);
            STAMP(4);
            {
                const char* wsrc = ((v >> 1) ? p.w3 : p.w1) + g * 384 + (size_t)(v & 1) * 8 * 3072;
                unsigned gvo[3];
#pragma unroll
                for (int n = 0; n < 3; ++n) {
                    const unsigned c = n * 64 + lane, row = c / 24u, col = (c - row * 24u) ^ ((row & 2) << 1);
                    gvo[n] = row * 3072 + col * 16 - n * 1024;
                }
                const unsigned gdst = L_GU + (v >> 1) * 6176 + (v & 1) * 3072;
                auto gdma = [&](auto S) { constexpr int s = S.value; glds16x3v(gvo[0], gvo[1], gvo[2], wsrc + (size_t)s * 16 * 3072, gdst + (s % 3) * GSL); };
                static_for<0, 3>(gdma);
                f32x16 acc[2][3] = {};
                unsigned bn[3], pa[4], pb[4];
#pragma unroll
                for (int n = 0; n < 3; ++n) bn[n] = L_GU + tb * 6176 + (8 * h + tq) * 384 + (((3 * v + n) * 32 + (tp >> 1) * 16) ^ ((unsigned)(tq >> 1) << 6)) + (tp & 1) * 8;
#pragma unroll
                for (int s = 0; s < 4; ++s) { pa[s] = L_PANEL + h * 512 + ((r ^ ((2 * s + h) & 7)) << 4); pb[s] = pa[s] + 40960; PIN(pb[s]); }
                bf16x8 wf[2][3], xa[2], xb2[2];
                unsigned vaddr = L_MISC + (C_GP + v) * 4, vone = 1u; PIN(vaddr); PIN(vone);
                auto rdw = [&](auto S, auto N) { constexpr int s = S.value, n = N.value; const unsigned o = bn[n] + (s % 3) * GSL; wf[s & 1][n] = ldstr2(o, o + 1536); };
                auto rdx = [&](auto S) { constexpr int s = S.value; xa[s & 1] = ldsf(pa[s & 3] + s * 1024); xb2[s & 1] = ldsf(pb[s & 3] + s * 1024); };
                WAITV(6); linc(vaddr, vone); lwait_all(C_GP, 4, 1u, lane);
                rdx(IC<0>{}); rdw(IC<0>{}, IC<0>{}); rdw(IC<0>{}, IC<1>{}); rdw(IC<0>{}, IC<2>{});
                WAITV(3); linc(vaddr, vone);
                static_for<0, 40>([&](auto S) {
                    constexpr int s = S.value; constexpr bool nx = s + 1 < 40;
                    unsigned pk = 0;
                    MPRIO(GU_HI, 0); acc[0][0] = MFMA(wf[s & 1][0], xa[s & 1], acc[0][0]); SBAR();
                    if constexpr (nx) rdx(IC<s + 1>{});
                    SBAR();
                    MPRIO(GU_HI, 1); acc[1][0] = MFMA(wf[s & 1][0], xb2[s & 1], acc[1][0]); SBAR();
                    if constexpr (nx) pk = lpeek(C_GP, lane);
                    SBAR();
                    MPRIO(GU_HI, 2); acc[0][1] = MFMA(wf[s & 1][1], xa[s & 1], acc[0][1]); SBAR();
                    MPRIO(GU_HI, 3); acc[1][1] = MFMA(wf[s & 1][1], xb2[s & 1], acc[1][1]); SBAR();
                    if constexpr (nx) { lcheck(pk, C_GP, s + 2, lane); rdw(IC<s + 1>{}, IC<0>{}); rdw(IC<s + 1>{}, IC<1>{}); rdw(IC<s + 1>{}, IC<2>{}); }
                    SBAR();
                    MPRIO(GU_HI, 4); acc[0][2] = MFMA(wf[s & 1][2], xa[s & 1], acc[0][2]); SBAR();
                    if constexpr (s + 3 < 40) gdma(IC<s + 3>{});
                    SBAR();
                    MPRIO(GU_HI, 5); acc[1][2] = MFMA(wf[s & 1][2], xb2[s & 1], acc[1][2]); SBAR();
                    if constexpr (s + 3 < 40) WAITV(3); else WAITV(0);
                    linc(vaddr, vone); SBAR();
                });
                __builtin_amdgcn_s_setprio(3);
                STAMP(6);
                if constexpr (DN) {
#pragma unroll
                    for (int n = 0; n < 2; ++n) {
                        unsigned L = (j * 4 + v) * 85 + n * 64 + lane; L = L < 10880u ? L : 10879u;
                        const unsigned row = L / 5u;
                        l2touch((row < 1536u ? p.w2 + row * 1280 : p.wo + (row - 1536u) * 1280) + (xc & 1) * 640 + (L - row * 5u) * 128);
                    }
                }
                const __amdgpu_buffer_rsrc_t rh = mkrsrc(ws + WS_HT, 64 * 96 * 1024);
#pragma unroll
                for (int m = 0; m < 2; ++m)
#pragma unroll
                    for (int n = 0; n < 3; ++n) {
                        f32x16 hv;
#pragma unroll
                        for (int e = 0; e < 8; ++e) {
                            const float gg = acc[m][n][e];
                            hv[e] = gg * __builtin_amdgcn_rcpf(1.f + __builtin_amdgcn_exp2f(-1.4426950408889634f * gg)) * acc[m][n][e + 8];
                        }
                        __builtin_amdgcn_raw_buffer_store_b128(__builtin_bit_cast(u32x4, pack8(hv, 0, 1.f)), rh, ((2 * ti + m) * 96 + g * 12 + 3 * v + n) * 1024 + lane * 16, 0, 16);
                    }
                if constexpr (DN) {
                    WAITV(0);
                    if (lane < 2) __hip_atomic_store((gu32*)(ws + WS_HF) + (2 * ti + lane) * 32 + g * 4 + v, tag, RLX_AGENT);
                }
                STAMP(7);
            }
        } else {
            BLOCK_BARRIER();
        }
        if constexpr (DN || WO) {
            LANE_CONSTS();
            const unsigned bq = ((tb * 32 + (tp >> 1) * 16) ^ ((unsigned)(tq >> 1) << 6)) + (tp & 1) * 8;
            const unsigned drow = lane >> 3, dcol = ((lane & 7) ^ (((lane >> 4) & 1) << 2)) * 16;
            const int G = xc >> 1, r0 = G < 2 ? 6 * G : 12 + 5 * (G - 2), nr = G < 2 ? 6 : 5;
            if constexpr (FUSED) {
                if (b == 255 && v == 0) {
                    gu32* f = (gu32*)(ws + WS_HF) + (lane >> 3) * 64 + (lane & 7) * 4;
                    _Pragma("clang loop unroll(disable)") for (unsigned spins = 0; spins < (1u << 14); ++spins) {
                        bool ok = true;
#pragma unroll
                        for (int n = 0; n < 4; ++n) ok &= __hip_atomic_load(f + n * 512, RLX_AGENT) == tag;
                        if (__all(ok)) break;
                        TMO(spins, 1u << 14, 303, tag);
                        __builtin_amdgcn_s_sleep(32);
                    }
                    if (lane == 0) __hip_atomic_store((gu32*)(ws + WS_EP), tag, RLX_AGENT);
                }
            }
            if (j < 5 * nr) {
                const int dr = r0 + j / 5, dc = (xc & 1) * 5 + j % 5;
                const int mfirst = dr < 20 ? 3 * dr : 60 + 2 * (dr - 20), nm = dr < 20 ? 3 : 2;
                const unsigned dvo = drow * 1280 + dcol;
                const unsigned myring = L_PANEL + v * 20480;
                f32x16 acc0 = {}, acc1 = {};
                if constexpr (PRO) { lwait_all(C_GP, 4, 42, lane); lwait_all(C_AP, 4, 8, lane); }
                if constexpr (DN) {
                    f32x16 da[3][2] = {};
                    const int drot = (j % 5) * 4;
                    const char* bsrc = p.w2 + (size_t)v * 384 * 1280 + dc * 128;
                    const char* asrc = ws + WS_HT + ((size_t)mfirst * 96 + v * 24) * 1024;
                    const unsigned am1 = lane * 16 + 98304, am2 = lane * 16 + (nm == 3 ? 196608 : 98304);
                    u32x4 aq[8][3];
                    auto bissue = [&](auto K) {
                        constexpr int k = K.value;
                        int kr = k + drot; kr = kr >= 24 ? kr - 24 : kr;
                        const char* sb = bsrc + (size_t)kr * 16 * 1280;
                        glds16(dvo, sb, myring + (k % 10) * 2048); glds16(dvo + 8 * 1280, sb, myring + (k % 10) * 2048 + 1024);
                    };
                    auto aissue = [&](auto K) {
                        constexpr int k = K.value;
                        int kr = k + drot; kr = kr >= 24 ? kr - 24 : kr;
                        const char* sb = asrc + (size_t)kr * 1024;
                        GLOAD16S(aq[k & 7][0], lane * 16, sb); GLOAD16S(aq[k & 7][1], am1, sb); GLOAD16S(aq[k & 7][2], am2, sb);
                    };
                    static_for<0, 8>(bissue);
                    if constexpr (PRO) {
                        gu32* f = (gu32*)(ws + WS_HF) + (mfirst + ((lane >> 3) < nm ? (lane >> 3) : 0)) * 32 + 8 * v + (lane & 7);
                        _Pragma("clang loop unroll(disable)") for (unsigned spins = 0; spins < (1u << 14); ++spins) {
                            if (__all(__hip_atomic_load(f, RLX_AGENT) == tag)) break;
                            TMO(spins, 1u << 14, 302, tag);
                            __builtin_amdgcn_s_sleep(6);
                        }
                        CFENCE();
                    }
                    STAMP(8);
                    static_for<0, 8>(aissue);
                    const unsigned ba0 = myring + (4 * h + tq) * 128 + bq, ba1 = ba0 ^ 64;
                    bf16x8 w0[2], w1[2];
                    WAITVN(24);
                    w0[0] = ldstr2(ba0, ba0 + 1024); w1[0] = ldstr2(ba1, ba1 + 1024);
                    static_for<0, 24>([&](auto K) {
                        constexpr int k = K.value;
                        constexpr int yk = k <= 7 ? 21 + 2 * k : (k < 16 ? 35 : 5 * (23 - k));
                        constexpr int n = k + 1 <= 7 ? yk : yk - 2;
                        waitv3<(n < 0 ? 0 : n)>(aq[k & 7][0], aq[k & 7][1], aq[k & 7][2]);
                        constexpr unsigned o = ((k + 1) % 10) * 2048;
#define DXF(m) __builtin_bit_cast(bf16x8, aq[k & 7][m])
                        SBAR();
                        MPRIO(DN_HI, 0); da[0][0] = MFMA(w0[k & 1], DXF(0), da[0][0]); SBAR();
                        if constexpr (k + 1 < 24) w0[(k + 1) & 1] = ldstr2(ba0 + o, ba0 + o + 1024);
                        SBAR();
                        MPRIO(DN_HI, 1); da[0][1] = MFMA(w1[k & 1], DXF(0), da[0][1]); SBAR();
                        if constexpr (k + 1 < 24) w1[(k + 1) & 1] = ldstr2(ba1 + o, ba1 + o + 1024);
                        SBAR();
                        MPRIO(DN_HI, 2); da[1][0] = MFMA(w0[k & 1], DXF(1), da[1][0]); SBAR();
                        if constexpr (k + 8 < 24) bissue(IC<k + 8>{});
                        SBAR();
                        MPRIO(DN_HI, 3); da[1][1] = MFMA(w1[k & 1], DXF(1), da[1][1]); SBAR();
                        MPRIO(DN_HI, 4); da[2][0] = MFMA(w0[k & 1], DXF(2), da[2][0]); SBAR();
                        MPRIO(DN_HI, 5); da[2][1] = MFMA(w1[k & 1], DXF(2), da[2][1]); SBAR();
                        if constexpr (k + 8 < 24) aissue(IC<k + 8>{});
                    });
                    __builtin_amdgcn_s_setprio(3);
                    STAMP(9);
                    if constexpr (WO) {
#pragma unroll
                        for (int u = 0; u < 20; ++u) glds16(dvo + u * 8 * 1280, p.wo + (size_t)v * 160 * 1280 + dc * 128, myring + u * 1024);
                    }
#pragma unroll
                    for (int n = 0; n < 2; ++n) {
#pragma unroll
                        for (int m = 0; m < 3; ++m) {
                            if (m != v) {
                                const unsigned dst = L_GU + (m * 3 + (v > m ? v - 1 : v)) * 4096 + lane * 16;
#pragma unroll
                                for (int e = 0; e < 4; ++e) *LP(f32x4, dst + e * 1024) = f32x4{da[m][n][4 * e], da[m][n][4 * e + 1], da[m][n][4 * e + 2], da[m][n][4 * e + 3]};
                            }
                        }
                        lpost(C_DP + v, 2 * n + 1); lwait_all(C_DP, 4, 2 * n + 1, lane);
                        if (v < 3) {
                            f32x16 t = v == 0 ? da[0][n] : (v == 1 ? da[1][n] : da[2][n]);
#pragma unroll
                            for (int o = 0; o < 3; ++o)
#pragma unroll
                                for (int e = 0; e < 4; ++e) {
                                    const f32x4 u = *LP(const f32x4, L_GU + (v * 3 + o) * 4096 + e * 1024 + lane * 16);
                                    t[4 * e] += u[0]; t[4 * e + 1] += u[1]; t[4 * e + 2] += u[2]; t[4 * e + 3] += u[3];
                                }
                            if (n) acc1 = t; else acc0 = t;
                        }
                        if (n == 0) { lpost(C_DP + v, 2); lwait_all(C_DP, 4, 2, lane); }
                    }
                    if constexpr (!WO) {
                        if (v < 3) {
                            float* pt = (float*)(ws + WS_PT) + (size_t)(b * 3 + v) * 2048 + lane;
#pragma unroll
                            for (int e = 0; e < 16; ++e) { pt[e * 64] = acc0[e]; pt[1024 + e * 64] = acc1[e]; }
                        }
                    }
                } else {
#pragma unroll
                    for (int u = 0; u < 20; ++u) glds16(dvo + u * 8 * 1280, p.wo + (size_t)v * 160 * 1280 + dc * 128, myring + u * 1024);
                    if (v < 3) {
                        const float* pt = (const float*)(ws + WS_PT) + (size_t)(b * 3 + v) * 2048 + lane;
#pragma unroll
                        for (int e = 0; e < 16; ++e) { acc0[e] = pt[e * 64]; acc1[e] = pt[1024 + e * 64]; }
                    }
                }
                if constexpr (WO) { WAITV(0); lpost(C_DP + v, 4); }
                if (v < nm) {
                    const int m = mfirst + v;
                    const unsigned ba0 = L_PANEL + (4 * h + tq) * 128 + bq, ba1 = ba0 ^ 64;
                    if constexpr (WO) {
                        if constexpr (ATT) gwait(ws, WS_OF + m * 128, 32, tag, lane);
                        STAMP(10);
                        const __amdgpu_buffer_rsrc_t ro = mkrsrc(ws + WS_OT + (size_t)m * 40960, 40960);
                        u32x4 of[40];
#pragma unroll
                        for (int s = 0; s < 40; ++s) of[s] = __builtin_amdgcn_raw_buffer_load_b128(ro, lane * 16, s * 1024, 16);
                        const char* xrow = p.x + (size_t)(m * 32 + r) * 1280 + dc * 128 + h * 16;
                        u32x4 xres[4];
#pragma unroll
                        for (int e = 0; e < 4; ++e) xres[e] = *(const u32x4*)(xrow + e * 32);
                        lwait_all(C_DP, 4, 4, lane);
                        const unsigned bl0 = L_PANEL + (40 * h + 16 + tq) * 128 + bq, bl1 = bl0 ^ 64;
#pragma unroll
                        for (int s = 0; s < 40; ++s) {
                            const int gg = s / 5, jj = s % 5;
                            const bf16x8 xf = __builtin_bit_cast(bf16x8, of[s]);
                            if (jj < 4) {
                                const unsigned o = (80 * gg + 20 * jj) * 128;
                                acc0 = MFMA(ldstr2(ba0 + o, ba0 + o + 1024), xf, acc0);
                                acc1 = MFMA(ldstr2(ba1 + o, ba1 + o + 1024), xf, acc1);
                            } else {
                                const unsigned o = 80 * gg * 128;
                                acc0 = MFMA(ldstr2(bl0 + o, bl0 + o + 2560), xf, acc0);
                                acc1 = MFMA(ldstr2(bl1 + o, bl1 + o + 2560), xf, acc1);
                            }
                        }
                        char* orow = p.out + (size_t)(m * 32 + r) * 1280 + dc * 128 + h * 16;
#pragma unroll
                        for (int n = 0; n < 2; ++n) {
                            const f32x16& a = n ? acc1 : acc0;
#pragma unroll
                            for (int qq = 0; qq < 2; ++qq) {
                                float lo[4], hi[4];
#pragma unroll
                                for (int e = 0; e < 4; ++e) {
                                    auto sw = __builtin_amdgcn_permlane32_swap(__float_as_uint(a[8 * qq + e]), __float_as_uint(a[8 * qq + 4 + e]), false, false);
                                    lo[e] = __uint_as_float(sw[0]); hi[e] = __uint_as_float(sw[1]);
                                }
                                const u32x4 xv = xres[2 * n + qq];
                                const u32x4 ov = {pk2(lo[0] + bflo(xv[0]), lo[1] + bfhi(xv[0])), pk2(lo[2] + bflo(xv[1]), lo[3] + bfhi(xv[1])),
                                                  pk2(hi[0] + bflo(xv[2]), hi[1] + bfhi(xv[2])), pk2(hi[2] + bflo(xv[3]), hi[3] + bfhi(xv[3]))};
                                *(u32x4*)(orow + (2 * n + qq) * 32) = ov;
                            }
                        }
                        STAMP(11);
                    }
                }
            }
        }
    }
}

template <int PH>
static void launch(const Params& p, hipStream_t stream) {
    (void)hipFuncSetAttribute((const void*)mega<PH>, hipFuncAttributeMaxDynamicSharedMemorySize, L_TOTAL);
    mega<PH><<<256, 512, L_TOTAL, stream>>>(p);
}

extern "C" void kernel_launch(void* const* d_in, const int* in_sizes, int n_in, void* d_out, int out_size, void* d_ws, size_t ws_size, hipStream_t stream) {
    Params p;
    p.x = (const char*)d_in[0]; p.wq = (const char*)d_in[1]; p.wk = (const char*)d_in[2]; p.wv = (const char*)d_in[3]; p.wo = (const char*)d_in[4];
    p.w1 = (const char*)d_in[6]; p.w3 = (const char*)d_in[7]; p.w2 = (const char*)d_in[8];
    p.out = (char*)d_out; p.ws = (char*)d_ws;
    launch<27>(p, stream);
}
```

```cpp
#include <hip/hip_runtime.h>
#include <hip/hip_bf16.h>
#include <stdint.h>

#define AS1 __attribute__((address_space(1)))
#define AS3 __attribute__((address_space(3)))
typedef __bf16 bf16x2 __attribute__((ext_vector_type(2)));
typedef __bf16 bf16x8 __attribute__((ext_vector_type(8)));
typedef short s16x4 __attribute__((ext_vector_type(4)));
typedef short s16x8 __attribute__((ext_vector_type(8)));
typedef float f32x2 __attribute__((ext_vector_type(2)));
typedef float f32x4 __attribute__((ext_vector_type(4)));
typedef float f32x16 __attribute__((ext_vector_type(16)));
typedef unsigned u32x2 __attribute__((ext_vector_type(2)));
typedef unsigned u32x4 __attribute__((ext_vector_type(4)));
typedef AS1 unsigned gu32;
typedef AS1 unsigned long long gu64;

#define ATT_PRIO 2
#define GEM_PRIO 0
#define GU_HI 0
#define DN_HI 0
#define MPRIO(mask, i) __builtin_amdgcn_s_setprio(((mask) >> (i)) & 1 ? 3 : 0)
#define CODE_PF 66000
#define GU_DELAY 2
#define GU_ROT 4
#define GU_GQ 5
#define SPLIT_MODE 0

constexpr int SEQ = 2048, DM = 640, FF = 1536;
constexpr unsigned WS_KF = 0;
constexpr unsigned WS_HF = 16384;
constexpr unsigned WS_OF = 32768;
constexpr unsigned WS_EP = 49152;
constexpr unsigned KVD = 5376;
constexpr unsigned KV_K1 = 2048, KV_V = 2560, KV_VT = 1408;
constexpr unsigned WS_KV = 65536;
constexpr unsigned WS_HT = WS_KV + 176128;
constexpr unsigned WS_OT = WS_HT + 64 * 96 * 1024;
constexpr unsigned WS_QT = WS_OT + 64 * 40 * 1024;
constexpr unsigned WS_PT = WS_QT + 64 * 32 * 2048;
constexpr unsigned WS_ST = WS_PT + 256 * 3 * 32 * 256;
constexpr unsigned L_PANEL = 0;
constexpr unsigned L_KV = 81920;
constexpr unsigned L_WQ = 81920;
constexpr unsigned L_GU = 124928;
constexpr unsigned GSL = 12320;
constexpr unsigned L_WKV = 124928;
constexpr unsigned L_MISC = 161920;
constexpr unsigned L_TOTAL = 163840;
constexpr unsigned CHUNK = 2 * KVD;
constexpr int NCHUNK = 16;
enum { C_AP = 0  , C_GP = 4  , C_DP = 8  , C_PP = 12  , C_WP = 16  , C_KVP = 20, C_KVFIN, C_KVRDY, C_KN, C_VM, C_N };

struct Params { const char* x; const char* wq; const char* wk; const char* wv; const char* wo; const char* w1; const char* w3; const char* w2; char* out; char* ws; };

#define SBAR() __builtin_amdgcn_sched_barrier(0)
#define PIN(x) asm volatile("" : "+v"(x))
#define CFENCE() asm volatile("" ::: "memory")
#define WAITV(n) asm volatile("s_waitcnt vmcnt(" #n ")" ::: "memory")
#define MFMA(a, b, c) __builtin_amdgcn_mfma_f32_32x32x16_bf16((a), (b), (c), 0, 0, 0)
#define RLX_AGENT __ATOMIC_RELAXED, __HIP_MEMORY_SCOPE_AGENT
#define RLX_WG __ATOMIC_RELAXED, __HIP_MEMORY_SCOPE_WORKGROUP

#define TMO(spins, lim, code, tgt) do {} while (0)
#define STAMP(id) do {} while (0)
#define STAMPC(id) do {} while (0)

#define LP(T, off) ((AS3 T*)(uintptr_t)(unsigned)(off))
__device__ __forceinline__ bf16x8 ldsf(unsigned off) { return *LP(const bf16x8, off); }
__device__ __forceinline__ s16x4 ldstr(unsigned off) { return __builtin_amdgcn_ds_read_tr16_b64_v4i16(LP(s16x4, off)); }
__device__ __forceinline__ bf16x8 ldstr2(unsigned off0, unsigned off1) {
    s16x4 a = ldstr(off0), b = ldstr(off1);
    return __builtin_bit_cast(bf16x8, __builtin_shufflevector(a, b, 0, 1, 2, 3, 4, 5, 6, 7));
}
__device__ __forceinline__ unsigned pk2(float lo, float hi) { bf16x2 v = {(__bf16)lo, (__bf16)hi}; return __builtin_bit_cast(unsigned, v); }
__device__ __forceinline__ float bflo(unsigned u) { return __uint_as_float(u << 16); }
__device__ __forceinline__ float bfhi(unsigned u) { return __uint_as_float(u & 0xffff0000u); }
__device__ __forceinline__ float rbf(float f) { return (float)(__bf16)f; }

__device__ __forceinline__ void glds16(unsigned voff, const char* sbase, unsigned ldsdst) {
    unsigned keep;
    asm volatile("s_mov_b32 %0, m0\n\ts_mov_b32 m0, %3\n\ts_nop 2\n\tglobal_load_lds_dwordx4 %1, %2\n\ts_mov_b32 m0, %0"
                 : "=&s"(keep) : "v"(voff), "s"(sbase), "s"(ldsdst) : "memory");
}
__device__ __forceinline__ void glds16_sc1(unsigned voff, const char* sbase, unsigned ldsdst) {
    unsigned keep;
    asm volatile("s_mov_b32 %0, m0\n\ts_mov_b32 m0, %3\n\ts_nop 2\n\tglobal_load_lds_dwordx4 %1, %2 sc1\n\ts_mov_b32 m0, %0"
                 : "=&s"(keep) : "v"(voff), "s"(sbase), "s"(ldsdst) : "memory");
}
__device__ __forceinline__ void glds16x3_sc1(unsigned voff, const char* sbase, unsigned ldsdst) {
    unsigned keep;
    asm volatile("s_mov_b32 %0, m0\n\ts_mov_b32 m0, %3\n\ts_nop 2\n\tglobal_load_lds_dwordx4 %1, %2 sc1\n\tglobal_load_lds_dwordx4 %1, %2 offset:1024 sc1\n\tglobal_load_lds_dwordx4 %1, %2 offset:2048 sc1\n\ts_mov_b32 m0, %0"
                 : "=&s"(keep) : "v"(voff), "s"(sbase), "s"(ldsdst) : "memory");
}
__device__ __forceinline__ void glds16x3v(unsigned v0, unsigned v1, unsigned v2, const char* sbase, unsigned ldsdst) {
    unsigned keep;
    asm volatile("s_mov_b32 %0, m0\n\ts_mov_b32 m0, %5\n\ts_nop 0\n\tglobal_load_lds_dwordx4 %1, %4\n\tglobal_load_lds_dwordx4 %2, %4 offset:1024\n\tglobal_load_lds_dwordx4 %3, %4 offset:2048\n\ts_mov_b32 m0, %0"
                 : "=&s"(keep) : "v"(v0), "v"(v1), "v"(v2), "s"(sbase), "s"(ldsdst) : "memory");
}
__device__ __forceinline__ void l2touch(const char* ga) {
    unsigned keep;
    asm volatile("s_mov_b32 %0, m0\n\ts_mov_b32 m0, %2\n\ts_nop 0\n\tglobal_load_lds_dword %1, off\n\ts_mov_b32 m0, %0" : "=&s"(keep) : "v"(ga), "s"(L_MISC + 1024) : "memory");
}
#define GLOAD16(dst, voff, sbase, imm) asm volatile("s_nop 4\n\tglobal_load_dwordx4 %0, %1, %2 offset:%3" : "=v"(dst) : "v"(voff), "s"(sbase), "i"(imm) : "memory")

__device__ __forceinline__ AS3 unsigned* lcnt(int c) { return LP(unsigned, L_MISC) + c; }
__device__ __forceinline__ void larrive(int c, int lane) { CFENCE(); if (lane == 0) __hip_atomic_fetch_add(lcnt(c), 1u, RLX_WG); CFENCE(); }
__device__ __forceinline__ void lset(int c, unsigned v, int lane) { CFENCE(); if (lane == 0) __hip_atomic_store(lcnt(c), v, RLX_WG); CFENCE(); }
__device__ __forceinline__ void lwait(int c, unsigned target) {
    CFENCE();
    _Pragma("clang loop unroll(disable)") for (unsigned spins = 0; spins < (1u << 22); ++spins) {
        if ((unsigned)__builtin_amdgcn_readfirstlane(__hip_atomic_load(lcnt(c), RLX_WG)) >= target) break;
        TMO(spins, 1u << 16, 100 + c, target);
        __builtin_amdgcn_s_sleep(1);
    }
    CFENCE();
}
__device__ __forceinline__ void lwait_all(int c, int n, unsigned target, int lane) {
    CFENCE();
    AS3 unsigned* q = lcnt(c) + (lane < n ? lane : 0);
    _Pragma("clang loop unroll(disable)") for (unsigned spins = 0; spins < (1u << 16); ++spins) {
        if (__all(__hip_atomic_load(q, RLX_WG) >= target)) break;
        TMO(spins, 1u << 16, 200 + c, target);
        __builtin_amdgcn_s_sleep(1);
    }
    CFENCE();
}
__device__ __forceinline__ void lpost(int c, unsigned v) { CFENCE(); *(volatile AS3 unsigned*)lcnt(c) = v; CFENCE(); }
__device__ __forceinline__ void linc(unsigned vaddr, unsigned vone) {
    asm volatile("s_mov_b64 exec, 1\n\tds_add_u32 %0, %1\n\ts_mov_b64 exec, -1" :: "v"(vaddr), "v"(vone) : "memory");
}
__device__ __forceinline__ unsigned lpeek(int c, int lane) { return *((volatile AS3 unsigned*)lcnt(c) + (lane & 3)); }
__device__ __forceinline__ void lcheck(unsigned pk, int c, unsigned target, int lane) {
    if (__builtin_expect(__any(pk < target), 0)) lwait_all(c, 4, target, lane);
    CFENCE();
}
__device__ __forceinline__ void gwait(const char* ws, unsigned off, int n, unsigned tag, int lane) {
    gu32* f = (gu32*)(ws + off) + (lane < n ? lane : 0);
    _Pragma("clang loop unroll(disable)") for (unsigned spins = 0; spins < (1u << 14); ++spins) {
        unsigned v = __hip_atomic_load(f, RLX_AGENT);
        if (__all(v == tag)) break;
        TMO(spins, 1u << 14, 300, off);
        __builtin_amdgcn_s_sleep(6);
    }
    CFENCE();
}
__device__ __forceinline__ float gwait64(const char* ws, unsigned off, unsigned tag, int lane) {
    gu64* f = (gu64*)(ws + off) + lane;
    unsigned long long v = 0;
    _Pragma("clang loop unroll(disable)") for (unsigned spins = 0; spins < (1u << 14); ++spins) {
        v = __hip_atomic_load(f, RLX_AGENT);
        if (__all((unsigned)(v >> 32) == tag)) break;
        __builtin_amdgcn_s_sleep(6);
    }
    CFENCE();
    return __uint_as_float((unsigned)v);
}
__device__ __forceinline__ __amdgpu_buffer_rsrc_t mkrsrc(const char* base, unsigned bytes) {
    return __builtin_amdgcn_make_buffer_rsrc((void*)base, 0, bytes, 0x00020000);
}
__device__ __forceinline__ float dpp_sum16(float v) {
    v += __int_as_float(__builtin_amdgcn_update_dpp(0, __float_as_int(v), 0x128, 0xf, 0xf, false));
    v += __int_as_float(__builtin_amdgcn_update_dpp(0, __float_as_int(v), 0x124, 0xf, 0xf, false));
    v += __int_as_float(__builtin_amdgcn_update_dpp(0, __float_as_int(v), 0x122, 0xf, 0xf, false));
    v += __int_as_float(__builtin_amdgcn_update_dpp(0, __float_as_int(v), 0x121, 0xf, 0xf, false));
    return v;
}
__device__ __forceinline__ float wave_max(float v) {
    int x = __float_as_int(v);
    x = max(x, __builtin_amdgcn_update_dpp(0, x, 0x128, 0xf, 0xf, false)); x = max(x, __builtin_amdgcn_update_dpp(0, x, 0x124, 0xf, 0xf, false));
    x = max(x, __builtin_amdgcn_update_dpp(0, x, 0x122, 0xf, 0xf, false)); x = max(x, __builtin_amdgcn_update_dpp(0, x, 0x121, 0xf, 0xf, false));
    const int a = max(__builtin_amdgcn_readlane(x, 0), __builtin_amdgcn_readlane(x, 16)), b = max(__builtin_amdgcn_readlane(x, 32), __builtin_amdgcn_readlane(x, 48));
    return __int_as_float(max(a, b));
}
__device__ __forceinline__ bf16x8 pack8(const f32x16& a, int b, float sc) {
    u32x4 r = {pk2(a[b] * sc, a[b + 1] * sc), pk2(a[b + 2] * sc, a[b + 3] * sc), pk2(a[b + 4] * sc, a[b + 5] * sc), pk2(a[b + 6] * sc, a[b + 7] * sc)};
    return __builtin_bit_cast(bf16x8, r);
}

template <int I> struct IC { static constexpr int value = I; };
template <int B, int E, class F> __device__ __forceinline__ void static_for(F&& f) { if constexpr (B < E) { f(IC<B>{}); static_for<B + 1, E>(f); } }
#define WAITVN(n) asm volatile("s_waitcnt vmcnt(%0)" :: "i"(n) : "memory")
__device__ __forceinline__ void GLOAD16S(u32x4& dst, unsigned voff, const char* sbase) { asm volatile("global_load_dwordx4 %0, %1, %2 sc1" : "=v"(dst) : "v"(voff), "s"(sbase) : "memory"); }
__device__ __forceinline__ void GLOAD16N(u32x4& dst, unsigned voff, const char* sbase) { asm volatile("global_load_dwordx4 %0, %1, %2" : "=v"(dst) : "v"(voff), "s"(sbase) : "memory"); }
__device__ __forceinline__ void GLOAD16N_IF(u32x4& dst, unsigned voff, const char* sbase, int cond) {
    asm volatile("s_cmp_eq_u32 %3, 0\n\ts_cbranch_scc1 1f\n\tglobal_load_dwordx4 %0, %1, %2\n1:" : "=v"(dst) : "v"(voff), "s"(sbase), "s"(cond) : "memory", "scc"); }
template <int N> __device__ __forceinline__ void waitv5(u32x4& a, u32x4& b, u32x4& c, u32x4& d, u32x4& e) { asm volatile("s_waitcnt vmcnt(%5)" : "+v"(a), "+v"(b), "+v"(c), "+v"(d), "+v"(e) : "i"(N) : "memory"); }
__device__ __forceinline__ void gload8x2_sc1(unsigned long long& a, unsigned long long& b, unsigned voff, const char* sbase) {
    asm volatile("global_load_dwordx2 %0, %2, %3 sc1\n\tglobal_load_dwordx2 %1, %2, %3 offset:512 sc1" : "=&v"(a), "=&v"(b) : "v"(voff), "s"(sbase) : "memory"); }
__device__ __forceinline__ void waitv8x2(unsigned long long& a, unsigned long long& b) { asm volatile("s_waitcnt vmcnt(0)" : "+v"(a), "+v"(b) :: "memory"); }
__device__ __forceinline__ void PIN2(u32x4& a, u32x4& b) { asm volatile("" : "+v"(a), "+v"(b) :: "memory"); }
__device__ __forceinline__ u32x4 scale8(unsigned x0, unsigned x1, unsigned x2, unsigned x3, unsigned clo, unsigned chi, float c) {
    unsigned o0, o1, o2, o3; float t0, t1, t2, t3, t4, t5, t6, t7;
    asm("v_dot2_f32_bf16 %4, %12, %16, %18\n\tv_dot2_f32_bf16 %5, %12, %17, %18\n\tv_dot2_f32_bf16 %6, %13, %16, %18\n\tv_dot2_f32_bf16 %7, %13, %17, %18\n\t"
        "v_dot2_f32_bf16 %8, %14, %16, %18\n\tv_dot2_f32_bf16 %9, %14, %17, %18\n\tv_dot2_f32_bf16 %10, %15, %16, %18\n\tv_dot2_f32_bf16 %11, %15, %17, %18\n\t"
        "v_cvt_pk_bf16_f32 %0, %4, %5\n\tv_cvt_pk_bf16_f32 %1, %6, %7\n\tv_cvt_pk_bf16_f32 %2, %8, %9\n\tv_cvt_pk_bf16_f32 %3, %10, %11"
        : "=&v"(o0), "=&v"(o1), "=&v"(o2), "=&v"(o3), "=&v"(t0), "=&v"(t1), "=&v"(t2), "=&v"(t3), "=&v"(t4), "=&v"(t5), "=&v"(t6), "=&v"(t7)
        : "v"(x0), "v"(x1), "v"(x2), "v"(x3), "v"(clo), "v"(chi), "v"(c));
    return u32x4{o0, o1, o2, o3};
}
template <int N> __device__ __forceinline__ void waitv3(u32x4& a, u32x4& b, u32x4& c) { asm volatile("s_waitcnt vmcnt(%3)" : "+v"(a), "+v"(b), "+v"(c) : "i"(N) : "memory"); }

constexpr float QSCALE = 0.22360679774997896f * 1.4426950408889634f;
constexpr float BOUND_MAX = 50.f;

struct AttSync { int lane; const char* kv; unsigned dvoff; unsigned wo; int w; };
__device__ __forceinline__ unsigned kvslot(int c) { return L_KV + ((c + 2) & 3) * CHUNK; }
__device__ __forceinline__ void att_dma(const AttSync& a, int c) { glds16x3_sc1(a.dvoff, a.kv + (size_t)c * CHUNK, kvslot(c) + a.wo); }
constexpr unsigned AP0 = 16;
#define RING_X(c) do { WAITV(3); lpost(C_AP + as.w, AP0 + (c) + 1); } while (0)
#define RP_A
#define RP_B
#define RING_Y(c, pk) do { RP_A lcheck(pk, C_AP, AP0 + (c) + 1, lane); const int cn_ = (c) + 3 < NCHUNK ? (c) + 3 : NCHUNK - 1;   \
                           glds16x3_sc1(as.dvoff, as.kv + (size_t)cn_ * CHUNK, kvslot((c) + 3) + as.wo); RP_B } while (0)

__device__ __forceinline__ void att_fast(const AttSync& as, bf16x8 Q0a, bf16x8 Q1a0, bf16x8 Q1a1, bf16x8 Q0b, bf16x8 Q1b0, bf16x8 Q1b1, f32x16& Oa, f32x16& Ob) {
    const int lane = as.lane, r = lane & 31, h = lane >> 5;
    const unsigned ak0 = lane * 16, ak1 = h ? KV_V + 20 * 16 : KV_K1 + r * 16;
    const unsigned av = KV_V + h * 352 + (r <= 20 ? r : 21) * 16;
    const f32x16 zero = {};
    f32x16 Sa, Sb = {};
    bf16x8 K0[2], K1[2], V0[2], V1[2];
    u32x4 Pa0, Pa1, Pb0 = {}, Pb1 = {};
    unsigned pk = 0;
    unsigned long long rp0 = 0, rp1 = 0;
    V0[1] = bf16x8{}; V1[1] = bf16x8{};
    unsigned vk0 = L_KV + ak0, vk1 = L_KV + ak1, vv = L_KV + av;
    PIN(vk0); PIN(vk1); PIN(vv);
    K0[0] = ldsf(vk0 + 2 * CHUNK); K1[0] = ldsf(vk1 + 2 * CHUNK); K0[1] = ldsf(vk0 + 2 * CHUNK + 1024); V1[0] = ldsf(vv + 2 * CHUNK + 704); V0[0] = ldsf(vv + 2 * CHUNK);
    Sa = MFMA(K0[0], Q0a, zero); Sa = MFMA(K1[0], Q1a0, Sa);
#define EX4(S, i) do { S[i] = __builtin_amdgcn_exp2f(S[i]); S[i + 1] = __builtin_amdgcn_exp2f(S[i + 1]); S[i + 2] = __builtin_amdgcn_exp2f(S[i + 2]); S[i + 3] = __builtin_amdgcn_exp2f(S[i + 3]); } while (0)
#define GAP(MF, LD, T, k, PW, j, S, i) do { MF; SBAR(); LD; PW[j] = pk2(T[k], T[k + 1]); PW[j + 1] = pk2(T[k + 2], T[k + 3]); PIN(PW); SBAR(); EX4(S, i); PIN(S); SBAR(); } while (0)
#define BC(x) __builtin_bit_cast(bf16x8, x)
#define STEP(e, d, QB1, QA1N, KN, L3, LK0, LK1, LV) do { \
        GAP(Ob = MFMA(V0[e ^ 1], BC(Pb0), Ob), (void)0,                  Sb, 12, Pb1, 2, Sa, 0); \
        GAP(Sb = MFMA(K0[e], Q0b, zero),       (void)0,                  Sa, 0, Pa0, 0, Sa, 4); \
        GAP(Sb = MFMA(K1[d], QB1, Sb),         L3,                       Sa, 4, Pa0, 2, Sa, 8); \
        GAP(Ob = MFMA(V1[e ^ 1], BC(Pb1), Ob), K0[e] = ldsf(LK0),        Sa, 8, Pa1, 0, Sa, 12); \
        GAP(Oa = MFMA(V0[e], BC(Pa0), Oa),     LK1,                      Sa, 12, Pa1, 2, Sb, 0); \
        GAP(Sa = MFMA(K0[e ^ 1], Q0a, zero),   V1[e ^ 1] = ldsf((LV) + 704), Sb, 0, Pb0, 0, Sb, 4); \
        GAP(Sa = MFMA(K1[KN], QA1N, Sa),       V0[e ^ 1] = ldsf(LV),     Sb, 4, Pb0, 2, Sb, 8); \
        GAP(Oa = MFMA(V1[e], BC(Pa1), Oa),     (void)0,                  Sb, 8, Pb1, 0, Sb, 12); \
    } while (0)
#define CHUNK4(c, B, N) do { \
        STEP(0, 0, Q1b0, Q1a1, 0, (void)0, vk0 + (B) + KVD, K1[1] = ldsf(vk1 + (B) + KVD), vv + (B) + KV_VT); \
        RING_X(c); \
        STEP(1, 0, Q1b1, Q1a0, 1, pk = lpeek(C_AP, lane), vk0 + (B) + KVD + 1024, (void)0, vv + (B) + KVD); \
        RING_Y(c, pk); \
        STEP(0, 1, Q1b0, Q1a1, 1, (void)0, vk0 + (N), K1[0] = ldsf(vk1 + (N)), vv + (B) + KVD + KV_VT); \
        STEP(1, 1, Q1b1, Q1a0, 0, (void)0, vk0 + (N) + 1024, (void)0, vv + (N)); \
    } while (0)
    for (int c = 0; c < NCHUNK; c += 4) {
        CHUNK4(c, 2 * CHUNK, 3 * CHUNK); CHUNK4(c + 1, 3 * CHUNK, 0); CHUNK4(c + 2, 0, CHUNK); CHUNK4(c + 3, CHUNK, 2 * CHUNK);
    }
#undef CHUNK4
    Pb1[2] = pk2(Sb[12], Sb[13]); Pb1[3] = pk2(Sb[14], Sb[15]);
    Ob = MFMA(V0[1], BC(Pb0), Ob); Ob = MFMA(V1[1], BC(Pb1), Ob);
#undef STEP
#undef GAP
#undef EX4
}

__device__ __forceinline__ void att_safe(const AttSync& as, bf16x8 Q0a, bf16x8 Q1a0, bf16x8 Q1a1, bf16x8 Q0b, bf16x8 Q1b0, bf16x8 Q1b1, f32x16& Oa, f32x16& Ob) {
    const int lane = as.lane, r = lane & 31, h = lane >> 5;
    const unsigned ak0 = lane * 16, ak1 = h ? KV_V + 20 * 16 : KV_K1 + r * 16;
    const unsigned av = KV_V + h * 352 + (r <= 20 ? r : 21) * 16;
    const f32x16 zero = {};
    float ma = -INFINITY, mb = -INFINITY;
    unsigned long long rp0 = 0, rp1 = 0;
    for (int c = 0; c < NCHUNK; ++c) {
        const unsigned b0 = kvslot(c);
        for (int t = 0; t < 4; ++t) {
            if (t == 1) RING_X(c);
            if (t == 2) RING_Y(c, 0u);
            const unsigned base = b0 + (t >> 1) * KVD; const int e = t & 1;
            bf16x8 K0 = ldsf(base + ak0 + e * 1024), K1 = ldsf(base + ak1), V0 = ldsf(base + av + e * KV_VT), V1 = ldsf(base + av + e * KV_VT + 704);
#pragma unroll
            for (int q = 0; q < 2; ++q) {
                f32x16 S = MFMA(K0, q ? Q0b : Q0a, zero); S = MFMA(K1, q ? (e ? Q1b1 : Q1b0) : (e ? Q1a1 : Q1a0), S);
                float mx = S[0];
#pragma unroll
                for (int i = 1; i < 16; ++i) mx = fmaxf(mx, S[i]);
                mx = fmaxf(mx, __shfl_xor(mx, 32));
                float& m = q ? mb : ma; f32x16& O = q ? Ob : Oa;
                const float mn = fmaxf(m, mx);
                const float al = __builtin_amdgcn_exp2f(m - mn);
                m = mn;
#pragma unroll
                for (int i = 0; i < 16; ++i) { O[i] *= al; S[i] = __builtin_amdgcn_exp2f(S[i] - mn); }
                O = MFMA(V0, pack8(S, 0, 1.f), O); O = MFMA(V1, pack8(S, 8, 1.f), O);
            }
        }
    }
}

__device__ __forceinline__ void xload4(u32x4 (&x)[5], const char* xb, unsigned vo) {
    asm volatile("global_load_dwordx4 %0, %5, %6\n\tglobal_load_dwordx4 %1, %5, %6 offset:256\n\tglobal_load_dwordx4 %2, %5, %6 offset:512\n\t"
                 "global_load_dwordx4 %3, %5, %6 offset:768\n\tglobal_load_dwordx4 %4, %5, %6 offset:1024"
                 : "=&v"(x[0]), "=&v"(x[1]), "=&v"(x[2]), "=&v"(x[3]), "=&v"(x[4]) : "v"(vo), "s"(xb) : "memory");
}
__device__ __forceinline__ void ln_rows(const u32x4 (&xr)[5], int row0, int lane) {
    f32x2 xv[20];
    f32x2 s0 = {0.f, 0.f}, s1 = {0.f, 0.f}, q0 = {0.f, 0.f}, q1 = {0.f, 0.f};
#pragma unroll
    for (int i = 0; i < 20; i += 2) {
        const unsigned a = xr[i >> 2][i & 3], b = xr[i >> 2][(i & 3) + 1];
        xv[i] = f32x2{bflo(a), bfhi(a)}; xv[i + 1] = f32x2{bflo(b), bfhi(b)};
        s0 += xv[i]; q0 = __builtin_elementwise_fma(xv[i], xv[i], q0);
        s1 += xv[i + 1]; q1 = __builtin_elementwise_fma(xv[i + 1], xv[i + 1], q1);
    }
    s0 += s1; q0 += q1;
    const float s = dpp_sum16(s0[0] + s0[1]), ss = dpp_sum16(q0[0] + q0[1]);
    const float mean = s * (1.f / 640.f);
    const float var = fmaxf(ss * (1.f / 640.f) - mean * mean, 0.f);
    const float rs = __builtin_amdgcn_rcpf(rbf(__builtin_amdgcn_sqrtf(rbf(rbf(var) + 1e-5f)))), mo = -rbf(mean) * rs;
    const f32x2 rs2 = {rs, rs}, mo2 = {mo, mo};
    const int row = row0 + (lane >> 4), c = lane & 15;
    const unsigned wa = L_PANEL + (row >> 5) * 40960 + (c >> 1) * 1024 + (c & 1) * 512 + (((row & 31) ^ (c & 7)) << 4);
#pragma unroll
    for (int u = 0; u < 5; ++u) {
        u32x4 o;
#pragma unroll
        for (int e = 0; e < 4; ++e) { const f32x2 y = __builtin_elementwise_fma(xv[4 * u + e], rs2, mo2); o[e] = pk2(y[0], y[1]); }
        *LP(u32x4, wa + u * 8192) = o;
    }
}
#define BLOCK_BARRIER() asm volatile("s_waitcnt lgkmcnt(0)\n\ts_barrier" ::: "memory")

template <int PH>
__global__ void __launch_bounds__(512, 2) mega(Params p) {
    constexpr bool PRO = PH & 1, ATT = PH & 2, DN = PH & 8, WO = PH & 16;
    const int w = __builtin_amdgcn_readfirstlane(threadIdx.x >> 6);
    const int b = blockIdx.x, xc = b & 7, j = b >> 3;
    const int ti = (xc >> 1) * 8 + (j >> 2), g = (xc & 1) * 4 + (j & 3);
    unsigned tag = 1u, epv = 0u;
    constexpr bool FUSED = PH == 27;
    if constexpr (FUSED) asm volatile("global_load_dword %0, %1, %2 sc1" : "=v"(epv) : "v"(0u), "s"(p.ws + WS_EP) : "memory");
#define GET_TAG() do { if constexpr (FUSED) { asm volatile("s_nop 0\n\tv_readfirstlane_b32 %0, %1\n\ts_nop 3" : "=s"(tag) : "v"(epv) : "memory"); tag += 0x9E3779B1u; } } while (0)
    char* ws = p.ws;
    STAMP(0);
    {
        const char* pc; asm volatile("s_getpc_b64 %0" : "=s"(pc));
        const unsigned line = (unsigned)(j * 8) * 64u + threadIdx.x;
        unsigned keep;
        if (line * 128u < CODE_PF)
            asm volatile("s_mov_b32 %0, m0\n\ts_mov_b32 m0, %3\n\ts_nop 2\n\tglobal_load_lds_dword %1, %2\n\ts_mov_b32 m0, %0" : "=&s"(keep) : "v"(line * 128u), "s"(pc), "s"(L_MISC + 1024) : "memory");
    }
    if (threadIdx.x < C_N) *lcnt(threadIdx.x) = (threadIdx.x == C_WP || threadIdx.x == C_WP + 1 || threadIdx.x == C_PP || threadIdx.x == C_PP + 1) ? 100u : 0u;
    const bool haskv = (g >> 2) == (ti & 1);
    const char* wqb = p.wq + g * 160;
    const int kmt = g & 1;
    const char* xb = p.x + (size_t)ti * 64 * 1280;
#define LANE_CONSTS() int lane = __builtin_amdgcn_mbcnt_hi(~0u, __builtin_amdgcn_mbcnt_lo(~0u, 0u)); PIN(lane); \
    const int r = lane & 31, h = lane >> 5, tq = (lane & 15) >> 2, tp = lane & 3, tb = (lane >> 4) & 1; (void)r; (void)h; (void)tq; (void)tp; (void)tb

    if (w < 4) {
        LANE_CONSTS();
        const unsigned xvo = (lane >> 4) * 1280 + (lane & 15) * 16;
        bf16x8 Q0a, Q0b; u32x2 q1a, q1b;
        const int head = 4 * g + w;
        const AttSync as{lane, ws + WS_KV, (unsigned)((w < 3 ? w * 3072 : CHUNK - 3072) + lane * 16), (unsigned)(w < 3 ? w * 3072 : CHUNK - 3072), w};
        __builtin_amdgcn_s_setprio(3);
        unsigned long long fk = 0, fv = 0; bool early = false;
        if constexpr (PRO) {
            u32x4 x0[5], x1[5], x2[5], x3[5];
            const int ra = kmt * 32 + w * 8, rb = (kmt ^ 1) * 32 + w * 8;
            xload4(x0, xb + (size_t)ra * 1280, xvo); xload4(x1, xb + (size_t)(ra + 4) * 1280, xvo);
            xload4(x2, xb + (size_t)rb * 1280, xvo); xload4(x3, xb + (size_t)(rb + 4) * 1280, xvo);
            BLOCK_BARRIER();
            waitv5<15>(x0[0], x0[1], x0[2], x0[3], x0[4]);
            GET_TAG();
            STAMP(1);
            ln_rows(x0, ra, lane);
            waitv5<10>(x1[0], x1[1], x1[2], x1[3], x1[4]);
            ln_rows(x1, ra + 4, lane);
            lpost(C_AP + w, 1);
            waitv5<5>(x2[0], x2[1], x2[2], x2[3], x2[4]);
            ln_rows(x2, rb, lane);
            waitv5<0>(x3[0], x3[1], x3[2], x3[3], x3[4]);
            ln_rows(x3, rb + 4, lane);
            lpost(C_AP + w, 2);
            unsigned pa[4];
#pragma unroll
            for (int s = 0; s < 4; ++s) pa[s] = L_PANEL + h * 512 + ((r ^ ((2 * s + h) & 7)) << 4);
            f32x16 qa = {}, qb = {};
            const unsigned wa = L_WQ + (8 * h + tq) * 160 + w * 40 + tb * 32 + (tb ? 0 : tp * 8);
            lwait_all(C_AP, 4, 2, lane); lwait_all(C_PP, 4, 1, lane);
            STAMP(2);
            bf16x8 fw[2][4], fa[2][4], fb[2][4];
#pragma unroll
            for (int s4 = 0; s4 < 4; ++s4) { fw[0][s4] = ldstr2(wa + s4 * 2560, wa + s4 * 2560 + 640); fa[0][s4] = ldsf(pa[s4] + s4 * 1024); fb[0][s4] = ldsf(pa[s4] + 40960 + s4 * 1024); }
            unsigned pk = 0;
            static_for<0, 10>([&](auto S) {
                constexpr int st = S.value, nx = st + 1;
                if constexpr (st == 3 || st == 5 || st == 7) lcheck(pk, C_WP, (st - 1) / 2, lane);
                if constexpr (st == 6 && ATT) gload8x2_sc1(fk, fv, lane * 8, ws + WS_KF + xc * 1024);
                if constexpr (st == 8 && ATT) {
                    waitv8x2(fk, fv);
                    early = __all((unsigned)(fk >> 32) == tag && (unsigned)(fv >> 32) == tag);
                    if (early) { lwait_all(C_AP, 4, 6, lane); att_dma(as, 0); att_dma(as, 1); }
                    STAMP(8);
                }
#pragma unroll
                for (int s4 = 0; s4 < 4; ++s4) {
                    qa = MFMA(fw[st & 1][s4], fa[st & 1][s4], qa); SBAR();
                    if constexpr (nx < 10) { const unsigned o = wa + (nx & 3) * 10240 + s4 * 2560; fw[nx & 1][s4] = ldstr2(o, o + 640); SBAR(); }
                    qb = MFMA(fw[st & 1][s4], fb[st & 1][s4], qb); SBAR();
                    if constexpr (nx < 10) { fa[nx & 1][s4] = ldsf(pa[s4] + (nx * 4 + s4) * 1024); fb[nx & 1][s4] = ldsf(pa[s4] + 40960 + (nx * 4 + s4) * 1024); SBAR(); }
                    if (s4 == 2 && (st == 2 || st == 4 || st == 6)) { pk = lpeek(C_WP, lane); SBAR(); }
                }
                if constexpr (!(st & 1) && st <= 6) lpost(C_AP + w, 3 + st / 2);
            });
            lpost(C_AP + w, 8);
            Q0a = pack8(qa, 0, QSCALE); Q0b = pack8(qb, 0, QSCALE);
            q1a = u32x2{pk2(qa[8] * QSCALE, qa[9] * QSCALE), pk2(qa[10] * QSCALE, qa[11] * QSCALE)};
            q1b = u32x2{pk2(qb[8] * QSCALE, qb[9] * QSCALE), pk2(qb[10] * QSCALE, qb[11] * QSCALE)};
            if (h) { q1a = u32x2{0u, 0u}; q1b = u32x2{0u, 0u}; }
            if constexpr (!ATT) {
                char* qt = ws + WS_QT + (size_t)((2 * ti) * 32 + head) * 2048 + lane * 16;
                *(bf16x8*)qt = Q0a; *(u32x2*)(qt + 1024) = q1a; *(bf16x8*)(qt + 65536) = Q0b; *(u32x2*)(qt + 65536 + 1024) = q1b;
            }
            STAMP(3);
        } else {
            BLOCK_BARRIER();
            if constexpr (ATT) {
                const char* qt = ws + WS_QT + (size_t)((2 * ti) * 32 + head) * 2048 + lane * 16;
                Q0a = *(const bf16x8*)qt; q1a = *(const u32x2*)(qt + 1024); Q0b = *(const bf16x8*)(qt + 65536); q1b = *(const u32x2*)(qt + 65536 + 1024);
            }
        }
        if constexpr (ATT) {
            float na = 0.f, nb = 0.f;
            {
                const u32x4 a0 = __builtin_bit_cast(u32x4, Q0a), b0 = __builtin_bit_cast(u32x4, Q0b);
#pragma unroll
                for (int e = 0; e < 4; ++e) { na += bflo(a0[e]) * bflo(a0[e]) + bfhi(a0[e]) * bfhi(a0[e]); nb += bflo(b0[e]) * bflo(b0[e]) + bfhi(b0[e]) * bfhi(b0[e]); }
#pragma unroll
                for (int e = 0; e < 2; ++e) { na += bflo(q1a[e]) * bflo(q1a[e]) + bfhi(q1a[e]) * bfhi(q1a[e]); nb += bflo(q1b[e]) * bflo(q1b[e]) + bfhi(q1b[e]) * bfhi(q1b[e]); }
                na += __shfl_xor(na, 32); nb += __shfl_xor(nb, 32);
            }
            if (!early) {
                gu64* f = (gu64*)(ws + WS_KF + xc * 1024) + lane;
                _Pragma("clang loop unroll(disable)") for (unsigned spins = 0; spins < (1u << 14); ++spins) {
                    fk = __hip_atomic_load(f, RLX_AGENT); fv = __hip_atomic_load(f + 64, RLX_AGENT);
                    if (__all((unsigned)(fk >> 32) == tag && (unsigned)(fv >> 32) == tag)) break;
                    TMO(spins, 1u << 14, 301, tag);
                    __builtin_amdgcn_s_sleep(4);
                }
                if constexpr (PRO) lwait_all(C_AP, 4, 6, lane);
                att_dma(as, 0); att_dma(as, 1);
            }
            STAMP(4);
            if constexpr (PRO) lwait_all(C_AP, 4, 8, lane);
            att_dma(as, 2);
            const float kn2 = wave_max(__uint_as_float((unsigned)fk)), vmx = wave_max(__uint_as_float((unsigned)fv));
            const float Ma = __builtin_sqrtf(na * kn2) * 1.01f, Mb = __builtin_sqrtf(nb * kn2) * 1.01f;
            const bool fast = __all(Ma <= BOUND_MAX && Mb <= BOUND_MAX && vmx <= 1e30f);
            const unsigned sa = (fast && h) ? pk2(-Ma, 0.f) : q1a[0], sb = (fast && h) ? pk2(-Mb, 0.f) : q1b[0];
            const u32x4 a0 = {sa, q1a[1], 0u, 0u}, a1 = {h ? sa : 0u, 0u, q1a[0], q1a[1]}, b0 = {sb, q1b[1], 0u, 0u}, b1 = {h ? sb : 0u, 0u, q1b[0], q1b[1]};
            f32x16 Oa = {}, Ob = {};
            WAITV(6); lpost(C_AP + w, AP0); lwait_all(C_AP, 4, AP0, lane);
            __builtin_amdgcn_s_setprio(ATT_PRIO);
            STAMP(5); STAMPC(12);
            if (fast) att_fast(as, Q0a, BC(a0), BC(a1), Q0b, BC(b0), BC(b1), Oa, Ob);
            else att_safe(as, Q0a, BC(a0), BC(a1), Q0b, BC(b0), BC(b1), Oa, Ob);
            STAMP(6); STAMPC(13);
            const __amdgpu_buffer_rsrc_t ro = mkrsrc(ws + WS_OT, 64 * 40 * 1024);
#pragma unroll
            for (int q = 0; q < 2; ++q) {
                const f32x16& O = q ? Ob : Oa;
                const float inv = 1.f / __shfl(O[8], r + 32);
                const unsigned base = ((2 * ti + q) * 40 + 5 * g) * 1024;
                __builtin_amdgcn_raw_buffer_store_b128(__builtin_bit_cast(u32x4, pack8(O, 0, inv)), ro, base + w * 1024 + lane * 16, 0, 16);
                if (!h) {
                    u32x2 t = {pk2(O[8] * inv, O[9] * inv), pk2(O[10] * inv, O[11] * inv)};
                    __builtin_amdgcn_raw_buffer_store_b64(t, ro, base + 4096 + ((w >> 1) * 32 + r) * 16 + (w & 1) * 8, 0, 16);
                }
            }
            if constexpr (WO) {
                WAITV(0);
                if (lane < 2) __hip_atomic_store((gu32*)(ws + WS_OF) + (2 * ti + lane) * 32 + head, tag, RLX_AGENT);
            }
            STAMP(7);
        }
    } else {
        LANE_CONSTS();
        const unsigned xvo = (lane >> 4) * 1280 + (lane & 15) * 16;
        const int v = w - 4;
        __builtin_amdgcn_s_setprio(3);
        if constexpr (PRO) {
            const int kvm = 2 * ti + (g & 1); const bool isV = (g >> 1) & 1;
            const char* wkvb = isV ? p.wv : p.wk;
            if (haskv && v < 2) {
#pragma unroll
                for (int n = 0; n < 13; ++n) {
                    const unsigned o = v * 12800 + (n < 12 ? n * 1024 : 11776);
                    glds16(o + lane * 16, wkvb, L_WKV + o);
                }
            }
            BLOCK_BARRIER();
            if (v >= 2) {
                const int v2 = v - 2;
                unsigned wo5[5];
#pragma unroll
                for (int i = 0; i < 5; ++i) { const unsigned c = (v2 + 2 * i) * 64 + lane, row = c / 10u, col = c - row * 10u; wo5[i] = row * 1280 + col * 16; }
#pragma unroll
                for (int n = 0; n < 20; ++n) glds16(wo5[n % 5], wqb + (n / 5) * 64 * 1280, L_WQ + (v2 + 2 * n) * 1024);
                u32x4 wst[3][10];
                auto wst_issue = [&](auto I) {
                    constexpr int rr = I.value / 10, i = I.value % 10;
                    GLOAD16N(wst[rr][i], wo5[i % 5], wqb + (4 + 2 * rr + i / 5) * 64 * 1280);
                };
                static_for<0, 10>(wst_issue);
                WAITV(10); lpost(C_PP + v, 1);
                GET_TAG();
                STAMP(2);
                static_for<10, 30>(wst_issue);
                static_for<0, 3>([&](auto R) {
                    constexpr int rr = R.value;
                    waitv5<20 - 10 * rr>(wst[rr][0], wst[rr][1], wst[rr][2], wst[rr][3], wst[rr][4]); waitv5<63>(wst[rr][5], wst[rr][6], wst[rr][7], wst[rr][8], wst[rr][9]);
                    lwait_all(C_AP, 4, 3 + rr, lane);
#pragma unroll
                    for (int i = 0; i < 10; ++i) *LP(u32x4, L_WQ + (rr & 1) * 20480 + (v2 + 2 * i) * 1024 + lane * 16) = wst[rr][i];
                    lpost(C_WP + v, rr + 1);
                });
            } else {
              if (haskv) {
                f32x16 acc = {};
                const unsigned wa = L_WKV + (v * 320 + 8 * h + tq) * 40 + tb * 32 + (tb ? 0 : tp * 8);
                unsigned xa[4];
#pragma unroll
                for (int s = 0; s < 4; ++s) xa[s] = L_PANEL + kmt * 40960 + v * 20480 + h * 512 + ((r ^ ((2 * s + h) & 7)) << 4);
                WAITV(0);
                GET_TAG();
                STAMP(2);
                bf16x8 wf[20], xf[20];
#pragma unroll
                for (int s = 0; s < 20; ++s) wf[s] = ldstr2(wa + s * 640, wa + s * 640 + 160);
                lwait_all(C_AP, 4, 1, lane);
#pragma unroll
                for (int s = 0; s < 20; ++s) xf[s] = ldsf(xa[s & 3] + s * 1024);
                SBAR();
                if (isV) {
#pragma unroll
                    for (int s = 0; s < 20; ++s) acc = MFMA(xf[s], wf[s], acc);
                } else {
#pragma unroll
                    for (int s = 0; s < 20; ++s) acc = MFMA(wf[s], xf[s], acc);
                }
                if (v) {
#pragma unroll
                    for (int e = 0; e < 4; ++e) *LP(f32x4, L_WKV + 12800 + e * 1024 + lane * 16) = f32x4{acc[4 * e], acc[4 * e + 1], acc[4 * e + 2], acc[4 * e + 3]};
                    lpost(C_KVP, 1);
                } else {
                    lwait(C_KVP, 1);
#pragma unroll
                    for (int e = 0; e < 4; ++e) {
                        const f32x4 t = *LP(const f32x4, L_WKV + 12800 + e * 1024 + lane * 16);
                        acc[4 * e] += t[0]; acc[4 * e + 1] += t[1]; acc[4 * e + 2] += t[2]; acc[4 * e + 3] += t[3];
                    }
                    const __amdgpu_buffer_rsrc_t rk = mkrsrc(ws + WS_KV, 32 * KVD);
                    const unsigned base = ti * KVD, ke = g & 1;
                    const u32x4 f0 = __builtin_bit_cast(u32x4, pack8(acc, 0, 1.f)), f1 = __builtin_bit_cast(u32x4, pack8(acc, 8, 1.f));
                    float mx;
                    if (!isV) {
                        __builtin_amdgcn_raw_buffer_store_b128(f0, rk, base + ke * 1024 + lane * 16, 0, 16);
                        const u32x2 k1 = {f1[0], f1[1]};
                        if (!h) __builtin_amdgcn_raw_buffer_store_b64(k1, rk, base + KV_K1 + r * 16 + ke * 8, 0, 16);
                        lpost(C_KVFIN, 1);
                        float n2 = 0.f;
#pragma unroll
                        for (int e = 0; e < 4; ++e) n2 += bflo(f0[e]) * bflo(f0[e]) + bfhi(f0[e]) * bfhi(f0[e]);
                        if (!h) n2 += bflo(f1[0]) * bflo(f1[0]) + bfhi(f1[0]) * bfhi(f1[0]) + bflo(f1[1]) * bflo(f1[1]) + bfhi(f1[1]) * bfhi(f1[1]);
                        n2 += __shfl_xor(n2, 32);
                        mx = (n2 == n2) ? n2 : INFINITY;
                    } else {
                        const unsigned cst = r == 20 ? 0x3f803f80u : 0u;
                        const u32x4 c4 = {cst, cst, cst, cst};
                        if (r <= 21) {
                            __builtin_amdgcn_raw_buffer_store_b128(r >= 20 ? c4 : f0, rk, base + KV_V + ke * KV_VT + h * 352 + r * 16, 0, 16);
                            __builtin_amdgcn_raw_buffer_store_b128(r >= 20 ? c4 : f1, rk, base + KV_V + ke * KV_VT + 704 + h * 352 + r * 16, 0, 16);
                        }
                        lpost(C_KVFIN, 1);
                        float vm = 0.f;
#pragma unroll
                        for (int e = 0; e < 16; ++e) { const float a = fabsf(acc[e]); vm = (a <= vm) ? vm : ((a == a) ? a : INFINITY); }
                        mx = r < 20 ? vm : 0.f;
                    }
                    mx = wave_max(mx);
                    WAITV(0);
                    STAMP(15);
                    if (lane < 8) __hip_atomic_store((gu64*)(ws + WS_KF + lane * 1024 + (isV ? 512 : 0)) + kvm, ((unsigned long long)tag << 32) | __float_as_uint(mx), RLX_AGENT);
                }
            }
              else { WAITV(0); GET_TAG(); }
              STAMP(3);
            }
            if (haskv) lwait(C_KVFIN, 1);
#pragma unroll
            for (int n = 0; n < 2; ++n) {
                unsigned L = n * 64 + lane; L = (j * 4 + v) * 120 + (L < 120u ? L : 119u);
                const unsigned m3 = L >= 7680u, rem = L - (m3 ? 7680u : 0u), row = rem / 12u;
                l2touch((m3 ? p.w3 : p.w1) + row * 3072 + (xc & 1) * 1536 + (rem - row * 12u) * 128);
            }
            lwait_all(C_AP, 4, GU_DELAY, lane);
            STAMP(4);
            {
                const char* wsrc = ((v >> 1) ? p.w3 : p.w1) + g * 384 + (size_t)(v & 1) * 8 * 3072;
                unsigned gvo[3];
#pragma unroll
                for (int n = 0; n < 3; ++n) {
                    const unsigned c = n * 64 + lane, row = c / 24u, col = (c - row * 24u) ^ ((row & 2) << 1);
                    gvo[n] = row * 3072 + col * 16 - n * 1024;
                }
                const unsigned gdst = L_GU + (v >> 1) * 6176 + (v & 1) * 3072;
                auto gdma = [&](auto S) { constexpr int s = S.value; glds16x3v(gvo[0], gvo[1], gvo[2], wsrc + (size_t)s * 16 * 3072, gdst + (s % 3) * GSL); };
                static_for<0, 3>(gdma);
                f32x16 acc[2][3] = {};
                unsigned bn[3], pa[4], pb[4];
#pragma unroll
                for (int n = 0; n < 3; ++n) bn[n] = L_GU + tb * 6176 + (8 * h + tq) * 384 + (((3 * v + n) * 32 + (tp >> 1) * 16) ^ ((unsigned)(tq >> 1) << 6)) + (tp & 1) * 8;
#pragma unroll
                for (int s = 0; s < 4; ++s) { pa[s] = L_PANEL + h * 512 + ((r ^ ((2 * s + h) & 7)) << 4); pb[s] = pa[s] + 40960; PIN(pb[s]); }
                bf16x8 wf[2][3], xa[2], xb2[2];
                unsigned vaddr = L_MISC + (C_GP + v) * 4, vone = 1u; PIN(vaddr); PIN(vone);
                auto rdw = [&](auto S, auto N) { constexpr int s = S.value, n = N.value; const unsigned o = bn[n] + (s % 3) * GSL; wf[s & 1][n] = ldstr2(o, o + 1536); };
                auto rdx = [&](auto S) { constexpr int s = S.value; xa[s & 1] = ldsf(pa[s & 3] + s * 1024); xb2[s & 1] = ldsf(pb[s & 3] + s * 1024); };
                WAITV(6); linc(vaddr, vone); lwait_all(C_GP, 4, 1u, lane);
                rdx(IC<0>{}); rdw(IC<0>{}, IC<0>{}); rdw(IC<0>{}, IC<1>{}); rdw(IC<0>{}, IC<2>{});
                WAITV(3); linc(vaddr, vone);
                static_for<0, 40>([&](auto S) {
                    constexpr int s = S.value; constexpr bool nx = s + 1 < 40;
                    unsigned pk = 0;
                    MPRIO(GU_HI, 0); acc[0][0] = MFMA(wf[s & 1][0], xa[s & 1], acc[0][0]); SBAR();
                    if constexpr (nx) rdx(IC<s + 1>{});
                    SBAR();
                    MPRIO(GU_HI, 1); acc[1][0] = MFMA(wf[s & 1][0], xb2[s & 1], acc[1][0]); SBAR();
                    if constexpr (nx) pk = lpeek(C_GP, lane);
                    SBAR();
                    MPRIO(GU_HI, 2); acc[0][1] = MFMA(wf[s & 1][1], xa[s & 1], acc[0][1]); SBAR();
                    MPRIO(GU_HI, 3); acc[1][1] = MFMA(wf[s & 1][1], xb2[s & 1], acc[1][1]); SBAR();
                    if constexpr (nx) { lcheck(pk, C_GP, s + 2, lane); rdw(IC<s + 1>{}, IC<0>{}); rdw(IC<s + 1>{}, IC<1>{}); rdw(IC<s + 1>{}, IC<2>{}); }
                    SBAR();
                    MPRIO(GU_HI, 4); acc[0][2] = MFMA(wf[s & 1][2], xa[s & 1], acc[0][2]); SBAR();
                    if constexpr (s + 3 < 40) gdma(IC<s + 3>{});
                    SBAR();
                    MPRIO(GU_HI, 5); acc[1][2] = MFMA(wf[s & 1][2], xb2[s & 1], acc[1][2]); SBAR();
                    if constexpr (s + 3 < 40) WAITV(3); else WAITV(0);
                    linc(vaddr, vone); SBAR();
                });
                __builtin_amdgcn_s_setprio(3);
                STAMP(6);
                if constexpr (DN) {
#pragma unroll
                    for (int n = 0; n < 2; ++n) {
                        unsigned L = (j * 4 + v) * 85 + n * 64 + lane; L = L < 10880u ? L : 10879u;
                        const unsigned row = L / 5u;
                        l2touch((row < 1536u ? p.w2 + row * 1280 : p.wo + (row - 1536u) * 1280) + (xc & 1) * 640 + (L - row * 5u) * 128);
                    }
                }
                const __amdgpu_buffer_rsrc_t rh = mkrsrc(ws + WS_HT, 64 * 96 * 1024);
#pragma unroll
                for (int m = 0; m < 2; ++m)
#pragma unroll
                    for (int n = 0; n < 3; ++n) {
                        f32x16 hv;
#pragma unroll
                        for (int e = 0; e < 8; ++e) {
                            const float gg = acc[m][n][e];
                            hv[e] = gg * __builtin_amdgcn_rcpf(1.f + __builtin_amdgcn_exp2f(-1.4426950408889634f * gg)) * acc[m][n][e + 8];
                        }
                        __builtin_amdgcn_raw_buffer_store_b128(__builtin_bit_cast(u32x4, pack8(hv, 0, 1.f)), rh, ((2 * ti + m) * 96 + g * 12 + 3 * v + n) * 1024 + lane * 16, 0, 16);
                    }
                if constexpr (DN) {
                    WAITV(0);
                    if (lane < 2) __hip_atomic_store((gu32*)(ws + WS_HF) + (2 * ti + lane) * 32 + g * 4 + v, tag, RLX_AGENT);
                }
                STAMP(7);
            }
        } else {
            BLOCK_BARRIER();
        }
        if constexpr (DN || WO) {
            LANE_CONSTS();
            const unsigned bq = ((tb * 32 + (tp >> 1) * 16) ^ ((unsigned)(tq >> 1) << 6)) + (tp & 1) * 8;
            const unsigned drow = lane >> 3, dcol = ((lane & 7) ^ (((lane >> 4) & 1) << 2)) * 16;
            const int G = xc >> 1, r0 = G < 2 ? 6 * G : 12 + 5 * (G - 2), nr = G < 2 ? 6 : 5;
            if constexpr (FUSED) {
                if (b == 255 && v == 0) {
                    gu32* f = (gu32*)(ws + WS_HF) + (lane >> 3) * 64 + (lane & 7) * 4;
                    _Pragma("clang loop unroll(disable)") for (unsigned spins = 0; spins < (1u << 14); ++spins) {
                        bool ok = true;
#pragma unroll
                        for (int n = 0; n < 4; ++n) ok &= __hip_atomic_load(f + n * 512, RLX_AGENT) == tag;
                        if (__all(ok)) break;
                        TMO(spins, 1u << 14, 303, tag);
                        __builtin_amdgcn_s_sleep(32);
                    }
                    if (lane == 0) __hip_atomic_store((gu32*)(ws + WS_EP), tag, RLX_AGENT);
                }
            }
            if (j < 5 * nr) {
                const int dr = r0 + j / 5, dc = (xc & 1) * 5 + j % 5;
                const int mfirst = dr < 20 ? 3 * dr : 60 + 2 * (dr - 20), nm = dr < 20 ? 3 : 2;
                const unsigned dvo = drow * 1280 + dcol;
                const unsigned myring = L_PANEL + v * 20480;
                f32x16 acc0 = {}, acc1 = {};
                if constexpr (PRO) { lwait_all(C_GP, 4, 42, lane); lwait_all(C_AP, 4, 8, lane); }
                if constexpr (DN) {
                    f32x16 da[3][2] = {};
                    const int drot = (j % 5) * 4;
                    const char* bsrc = p.w2 + (size_t)v * 384 * 1280 + dc * 128;
                    const char* asrc = ws + WS_HT + ((size_t)mfirst * 96 + v * 24) * 1024;
                    const unsigned am1 = lane * 16 + 98304, am2 = lane * 16 + (nm == 3 ? 196608 : 98304);
                    u32x4 aq[8][3];
                    auto bissue = [&](auto K) {
                        constexpr int k = K.value;
                        int kr = k + drot; kr = kr >= 24 ? kr - 24 : kr;
                        const char* sb = bsrc + (size_t)kr * 16 * 1280;
                        glds16(dvo, sb, myring + (k % 10) * 2048); glds16(dvo + 8 * 1280, sb, myring + (k % 10) * 2048 + 1024);
                    };
                    auto aissue = [&](auto K) {
                        constexpr int k = K.value;
                        int kr = k + drot; kr = kr >= 24 ? kr - 24 : kr;
                        const char* sb = asrc + (size_t)kr * 1024;
                        GLOAD16S(aq[k & 7][0], lane * 16, sb); GLOAD16S(aq[k & 7][1], am1, sb); GLOAD16S(aq[k & 7][2], am2, sb);
                    };
                    static_for<0, 8>(bissue);
                    if constexpr (PRO) {
                        gu32* f = (gu32*)(ws + WS_HF) + (mfirst + ((lane >> 3) < nm ? (lane >> 3) : 0)) * 32 + 8 * v + (lane & 7);
                        _Pragma("clang loop unroll(disable)") for (unsigned spins = 0; spins < (1u << 14); ++spins) {
                            if (__all(__hip_atomic_load(f, RLX_AGENT) == tag)) break;
                            TMO(spins, 1u << 14, 302, tag);
                            __builtin_amdgcn_s_sleep(6);
                        }
                        CFENCE();
                    }
                    STAMP(8);
                    static_for<0, 8>(aissue);
                    const unsigned ba0 = myring + (4 * h + tq) * 128 + bq, ba1 = ba0 ^ 64;
                    bf16x8 w0[2], w1[2];
                    WAITVN(24);
                    w0[0] = ldstr2(ba0, ba0 + 1024); w1[0] = ldstr2(ba1, ba1 + 1024);
                    static_for<0, 24>([&](auto K) {
                        constexpr int k = K.value;
                        constexpr int yk = k <= 7 ? 21 + 2 * k : (k < 16 ? 35 : 5 * (23 - k));
                        constexpr int n = k + 1 <= 7 ? yk : yk - 2;
                        waitv3<(n < 0 ? 0 : n)>(aq[k & 7][0], aq[k & 7][1], aq[k & 7][2]);
                        constexpr unsigned o = ((k + 1) % 10) * 2048;
#define DXF(m) __builtin_bit_cast(bf16x8, aq[k & 7][m])
                        SBAR();
                        MPRIO(DN_HI, 0); da[0][0] = MFMA(w0[k & 1], DXF(0), da[0][0]); SBAR();
                        if constexpr (k + 1 < 24) w0[(k + 1) & 1] = ldstr2(ba0 + o, ba0 + o + 1024);
                        SBAR();
                        MPRIO(DN_HI, 1); da[0][1] = MFMA(w1[k & 1], DXF(0), da[0][1]); SBAR();
                        if constexpr (k + 1 < 24) w1[(k + 1) & 1] = ldstr2(ba1 + o, ba1 + o + 1024);
                        SBAR();
                        MPRIO(DN_HI, 2); da[1][0] = MFMA(w0[k & 1], DXF(1), da[1][0]); SBAR();
                        if constexpr (k + 8 < 24) bissue(IC<k + 8>{});
                        SBAR();
                        MPRIO(DN_HI, 3); da[1][1] = MFMA(w1[k & 1], DXF(1), da[1][1]); SBAR();
                        MPRIO(DN_HI, 4); da[2][0] = MFMA(w0[k & 1], DXF(2), da[2][0]); SBAR();
                        MPRIO(DN_HI, 5); da[2][1] = MFMA(w1[k & 1], DXF(2), da[2][1]); SBAR();
                        if constexpr (k + 8 < 24) aissue(IC<k + 8>{});
                    });
                    __builtin_amdgcn_s_setprio(3);
                    STAMP(9);
                    if constexpr (WO) {
#pragma unroll
                        for (int u = 0; u < 20; ++u) glds16(dvo + u * 8 * 1280, p.wo + (size_t)v * 160 * 1280 + dc * 128, myring + u * 1024);
                    }
#pragma unroll
                    for (int n = 0; n < 2; ++n) {
#pragma unroll
                        for (int m = 0; m < 3; ++m) {
                            if (m != v) {
                                const unsigned dst = L_GU + (m * 3 + (v > m ? v - 1 : v)) * 4096 + lane * 16;
#pragma unroll
                                for (int e = 0; e < 4; ++e) *LP(f32x4, dst + e * 1024) = f32x4{da[m][n][4 * e], da[m][n][4 * e + 1], da[m][n][4 * e + 2], da[m][n][4 * e + 3]};
                            }
                        }
                        lpost(C_DP + v, 2 * n + 1); lwait_all(C_DP, 4, 2 * n + 1, lane);
                        if (v < 3) {
                            f32x16 t = v == 0 ? da[0][n] : (v == 1 ? da[1][n] : da[2][n]);
#pragma unroll
                            for (int o = 0; o < 3; ++o)
#pragma unroll
                                for (int e = 0; e < 4; ++e) {
                                    const f32x4 u = *LP(const f32x4, L_GU + (v * 3 + o) * 4096 + e * 1024 + lane * 16);
                                    t[4 * e] += u[0]; t[4 * e + 1] += u[1]; t[4 * e + 2] += u[2]; t[4 * e + 3] += u[3];
                                }
                            if (n) acc1 = t; else acc0 = t;
                        }
                        if (n == 0) { lpost(C_DP + v, 2); lwait_all(C_DP, 4, 2, lane); }
                    }
                    if constexpr (!WO) {
                        if (v < 3) {
                            float* pt = (float*)(ws + WS_PT) + (size_t)(b * 3 + v) * 2048 + lane;
#pragma unroll
                            for (int e = 0; e < 16; ++e) { pt[e * 64] = acc0[e]; pt[1024 + e * 64] = acc1[e]; }
                        }
                    }
                } else {
#pragma unroll
                    for (int u = 0; u < 20; ++u) glds16(dvo + u * 8 * 1280, p.wo + (size_t)v * 160 * 1280 + dc * 128, myring + u * 1024);
                    if (v < 3) {
                        const float* pt = (const float*)(ws + WS_PT) + (size_t)(b * 3 + v) * 2048 + lane;
#pragma unroll
                        for (int e = 0; e < 16; ++e) { acc0[e] = pt[e * 64]; acc1[e] = pt[1024 + e * 64]; }
                    }
                }
                if constexpr (WO) { WAITV(0); lpost(C_DP + v, 4); }
                if (v < nm) {
                    const int m = mfirst + v;
                    const unsigned ba0 = L_PANEL + (4 * h + tq) * 128 + bq, ba1 = ba0 ^ 64;
                    if constexpr (WO) {
                        if constexpr (ATT) gwait(ws, WS_OF + m * 128, 32, tag, lane);
                        STAMP(10);
                        const __amdgpu_buffer_rsrc_t ro = mkrsrc(ws + WS_OT + (size_t)m * 40960, 40960);
                        u32x4 of[40];
#pragma unroll
                        for (int s = 0; s < 40; ++s) of[s] = __builtin_amdgcn_raw_buffer_load_b128(ro, lane * 16, s * 1024, 16);
                        const char* xrow = p.x + (size_t)(m * 32 + r) * 1280 + dc * 128 + h * 16;
                        u32x4 xres[4];
#pragma unroll
                        for (int e = 0; e < 4; ++e) xres[e] = *(const u32x4*)(xrow + e * 32);
                        lwait_all(C_DP, 4, 4, lane);
                        const unsigned bl0 = L_PANEL + (40 * h + 16 + tq) * 128 + bq, bl1 = bl0 ^ 64;
#pragma unroll
                        for (int s = 0; s < 40; ++s) {
                            const int gg = s / 5, jj = s % 5;
                            const bf16x8 xf = __builtin_bit_cast(bf16x8, of[s]);
                            if (jj < 4) {
                                const unsigned o = (80 * gg + 20 * jj) * 128;
                                acc0 = MFMA(ldstr2(ba0 + o, ba0 + o + 1024), xf, acc0);
                                acc1 = MFMA(ldstr2(ba1 + o, ba1 + o + 1024), xf, acc1);
                            } else {
                                const unsigned o = 80 * gg * 128;
                                acc0 = MFMA(ldstr2(bl0 + o, bl0 + o + 2560), xf, acc0);
                                acc1 = MFMA(ldstr2(bl1 + o, bl1 + o + 2560), xf, acc1);
                            }
                        }
                        char* orow = p.out + (size_t)(m * 32 + r) * 1280 + dc * 128 + h * 16;
#pragma unroll
                        for (int n = 0; n < 2; ++n) {
                            const f32x16& a = n ? acc1 : acc0;
#pragma unroll
                            for (int qq = 0; qq < 2; ++qq) {
                                float lo[4], hi[4];
#pragma unroll
                                for (int e = 0; e < 4; ++e) {
                                    auto sw = __builtin_amdgcn_permlane32_swap(__float_as_uint(a[8 * qq + e]), __float_as_uint(a[8 * qq + 4 + e]), false, false);
                                    lo[e] = __uint_as_float(sw[0]); hi[e] = __uint_as_float(sw[1]);
                                }
                                const u32x4 xv = xres[2 * n + qq];
                                const u32x4 ov = {pk2(lo[0] + bflo(xv[0]), lo[1] + bfhi(xv[0])), pk2(lo[2] + bflo(xv[1]), lo[3] + bfhi(xv[1])),
                                                  pk2(hi[0] + bflo(xv[2]), hi[1] + bfhi(xv[2])), pk2(hi[2] + bflo(xv[3]), hi[3] + bfhi(xv[3]))};
                                *(u32x4*)(orow + (2 * n + qq) * 32) = ov;
                            }
                        }
                        STAMP(11);
                    }
                }
            }
        }
    }
}

template <int PH>
static void launch(const Params& p, hipStream_t stream) {
    (void)hipFuncSetAttribute((const void*)mega<PH>, hipFuncAttributeMaxDynamicSharedMemorySize, L_TOTAL);
    mega<PH><<<256, 512, L_TOTAL, stream>>>(p);
}

extern "C" void kernel_launch(void* const* d_in, const int* in_sizes, int n_in, void* d_out, int out_size, void* d_ws, size_t ws_size, hipStream_t stream) {
    Params p;
    p.x = (const char*)d_in[0]; p.wq = (const char*)d_in[1]; p.wk = (const char*)d_in[2]; p.wv = (const char*)d_in[3]; p.wo = (const char*)d_in[4];
    p.w1 = (const char*)d_in[6]; p.w3 = (const char*)d_in[7]; p.w2 = (const char*)d_in[8];
    p.out = (char*)d_out; p.ws = (char*)d_ws;
    launch<27>(p, stream);
}
```

```cpp
#include <hip/hip_runtime.h>
#include <hip/hip_bf16.h>
#include <stdint.h>

#define AS1 __attribute__((address_space(1)))
#define AS3 __attribute__((address_space(3)))
typedef __bf16 bf16x2 __attribute__((ext_vector_type(2)));
typedef __bf16 bf16x8 __attribute__((ext_vector_type(8)));
typedef short s16x4 __attribute__((ext_vector_type(4)));
typedef short s16x8 __attribute__((ext_vector_type(8)));
typedef float f32x2 __attribute__((ext_vector_type(2)));
typedef float f32x4 __attribute__((ext_vector_type(4)));
typedef float f32x16 __attribute__((ext_vector_type(16)));
typedef unsigned u32x2 __attribute__((ext_vector_type(2)));
typedef unsigned u32x4 __attribute__((ext_vector_type(4)));
typedef AS1 unsigned gu32;
typedef AS1 unsigned long long gu64;

#define ATT_PRIO 2
#define GEM_PRIO 0
#define GU_HI 0
#define DN_HI 0
#define MPRIO(mask, i) __builtin_amdgcn_s_setprio(((mask) >> (i)) & 1 ? 3 : 0)
#define GU_DELAY 2
#define GU_ROT 4
#define GU_GQ 5
#define SPLIT_MODE 0

constexpr int SEQ = 2048, DM = 640, FF = 1536;
constexpr unsigned WS_KF = 0;
constexpr unsigned WS_HF = 16384;
constexpr unsigned WS_OF = 32768;
constexpr unsigned WS_EP = 49152;
constexpr unsigned KVD = 5376;
constexpr unsigned KV_K1 = 2048, KV_V = 2560, KV_VT = 1408;
constexpr unsigned WS_KV = 65536;
constexpr unsigned WS_HT = WS_KV + 176128;
constexpr unsigned WS_OT = WS_HT + 64 * 96 * 1024;
constexpr unsigned WS_QT = WS_OT + 64 * 40 * 1024;
constexpr unsigned WS_PT = WS_QT + 64 * 32 * 2048;
constexpr unsigned WS_ST = WS_PT + 256 * 3 * 32 * 256;
constexpr unsigned L_PANEL = 0;
constexpr unsigned L_KV = 81920;
constexpr unsigned L_WQ = 81920;
constexpr unsigned L_GU = 124928;
constexpr unsigned GSL = 12320;
constexpr unsigned L_WKV = 124928;
constexpr unsigned L_MISC = 161920;
constexpr unsigned L_TOTAL = 163840;
constexpr unsigned CHUNK = 2 * KVD;
constexpr int NCHUNK = 16;
enum { C_AP = 0  , C_GP = 4  , C_DP = 8  , C_PP = 12  , C_WP = 16  , C_KVP = 20, C_KVFIN, C_KVRDY, C_KN, C_VM, C_N };

struct Params { const char* x; const char* wq; const char* wk; const char* wv; const char* wo; const char* w1; const char* w3; const char* w2; char* out; char* ws; };

#define SBAR() __builtin_amdgcn_sched_barrier(0)
#define PIN(x) asm volatile("" : "+v"(x))
#define CFENCE() asm volatile("" ::: "memory")
#define WAITV(n) asm volatile("s_waitcnt vmcnt(" #n ")" ::: "memory")
#define MFMA(a, b, c) __builtin_amdgcn_mfma_f32_32x32x16_bf16((a), (b), (c), 0, 0, 0)
#define RLX_AGENT __ATOMIC_RELAXED, __HIP_MEMORY_SCOPE_AGENT
#define RLX_WG __ATOMIC_RELAXED, __HIP_MEMORY_SCOPE_WORKGROUP

#define TMO(spins, lim, code, tgt) do {} while (0)
#define STAMP(id) do {} while (0)
#define STAMPC(id) do {} while (0)

#define LP(T, off) ((AS3 T*)(uintptr_t)(unsigned)(off))
__device__ __forceinline__ bf16x8 ldsf(unsigned off) { return *LP(const bf16x8, off); }
__device__ __forceinline__ s16x4 ldstr(unsigned off) { return __builtin_amdgcn_ds_read_tr16_b64_v4i16(LP(s16x4, off)); }
__device__ __forceinline__ bf16x8 ldstr2(unsigned off0, unsigned off1) {
    s16x4 a = ldstr(off0), b = ldstr(off1);
    return __builtin_bit_cast(bf16x8, __builtin_shufflevector(a, b, 0, 1, 2, 3, 4, 5, 6, 7));
}
__device__ __forceinline__ unsigned pk2(float lo, float hi) { bf16x2 v = {(__bf16)lo, (__bf16)hi}; return __builtin_bit_cast(unsigned, v); }
__device__ __forceinline__ float bflo(unsigned u) { return __uint_as_float(u << 16); }
__device__ __forceinline__ float bfhi(unsigned u) { return __uint_as_float(u & 0xffff0000u); }
__device__ __forceinline__ float rbf(float f) { return (float)(__bf16)f; }

__device__ __forceinline__ void glds16(unsigned voff, const char* sbase, unsigned ldsdst) {
    unsigned keep;
    asm volatile("s_mov_b32 %0, m0\n\ts_mov_b32 m0, %3\n\ts_nop 2\n\tglobal_load_lds_dwordx4 %1, %2\n\ts_mov_b32 m0, %0"
                 : "=&s"(keep) : "v"(voff), "s"(sbase), "s"(ldsdst) : "memory");
}
__device__ __forceinline__ void glds16_sc1(unsigned voff, const char* sbase, unsigned ldsdst) {
    unsigned keep;
    asm volatile("s_mov_b32 %0, m0\n\ts_mov_b32 m0, %3\n\ts_nop 2\n\tglobal_load_lds_dwordx4 %1, %2 sc1\n\ts_mov_b32 m0, %0"
                 : "=&s"(keep) : "v"(voff), "s"(sbase), "s"(ldsdst) : "memory");
}
__device__ __forceinline__ void glds16x3_sc1(unsigned voff, const char* sbase, unsigned ldsdst) {
    unsigned keep;
    asm volatile("s_mov_b32 %0, m0\n\ts_mov_b32 m0, %3\n\ts_nop 2\n\tglobal_load_lds_dwordx4 %1, %2 sc1\n\tglobal_load_lds_dwordx4 %1, %2 offset:1024 sc1\n\tglobal_load_lds_dwordx4 %1, %2 offset:2048 sc1\n\ts_mov_b32 m0, %0"
                 : "=&s"(keep) : "v"(voff), "s"(sbase), "s"(ldsdst) : "memory");
}
__device__ __forceinline__ void glds16x3v(unsigned v0, unsigned v1, unsigned v2, const char* sbase, unsigned ldsdst) {
    unsigned keep;
    asm volatile("s_mov_b32 %0, m0\n\ts_mov_b32 m0, %5\n\ts_nop 0\n\tglobal_load_lds_dwordx4 %1, %4\n\tglobal_load_lds_dwordx4 %2, %4 offset:1024\n\tglobal_load_lds_dwordx4 %3, %4 offset:2048\n\ts_mov_b32 m0, %0"
                 : "=&s"(keep) : "v"(v0), "v"(v1), "v"(v2), "s"(sbase), "s"(ldsdst) : "memory");
}
__device__ __forceinline__ void l2touch(const char* ga) {
    unsigned keep;
    asm volatile("s_mov_b32 %0, m0\n\ts_mov_b32 m0, %2\n\ts_nop 0\n\tglobal_load_lds_dword %1, off\n\ts_mov_b32 m0, %0" : "=&s"(keep) : "v"(ga), "s"(L_MISC + 1024) : "memory");
}
#define GLOAD16(dst, voff, sbase, imm) asm volatile("s_nop 4\n\tglobal_load_dwordx4 %0, %1, %2 offset:%3" : "=v"(dst) : "v"(voff), "s"(sbase), "i"(imm) : "memory")

__device__ __forceinline__ AS3 unsigned* lcnt(int c) { return LP(unsigned, L_MISC) + c; }
__device__ __forceinline__ void larrive(int c, int lane) { CFENCE(); if (lane == 0) __hip_atomic_fetch_add(lcnt(c), 1u, RLX_WG); CFENCE(); }
__device__ __forceinline__ void lset(int c, unsigned v, int lane) { CFENCE(); if (lane == 0) __hip_atomic_store(lcnt(c), v, RLX_WG); CFENCE(); }
__device__ __forceinline__ void lwait(int c, unsigned target) {
    CFENCE();
    _Pragma("clang loop unroll(disable)") for (unsigned spins = 0; spins < (1u << 22); ++spins) {
        if ((unsigned)__builtin_amdgcn_readfirstlane(__hip_atomic_load(lcnt(c), RLX_WG)) >= target) break;
        TMO(spins, 1u << 16, 100 + c, target);
        __builtin_amdgcn_s_sleep(1);
    }
    CFENCE();
}
__device__ __forceinline__ void lwait_all(int c, int n, unsigned target, int lane) {
    CFENCE();
    AS3 unsigned* q = lcnt(c) + (lane < n ? lane : 0);
    _Pragma("clang loop unroll(disable)") for (unsigned spins = 0; spins < (1u << 16); ++spins) {
        if (__all(__hip_atomic_load(q, RLX_WG) >= target)) break;
        TMO(spins, 1u << 16, 200 + c, target);
        __builtin_amdgcn_s_sleep(1);
    }
    CFENCE();
}
__device__ __forceinline__ void lpost(int c, unsigned v) { CFENCE(); *(volatile AS3 unsigned*)lcnt(c) = v; CFENCE(); }
__device__ __forceinline__ void linc(unsigned vaddr, unsigned vone) {
    asm volatile("s_mov_b64 exec, 1\n\tds_add_u32 %0, %1\n\ts_mov_b64 exec, -1" :: "v"(vaddr), "v"(vone) : "memory");
}
__device__ __forceinline__ unsigned lpeek(int c, int lane) { return *((volatile AS3 unsigned*)lcnt(c) + (lane & 3)); }
__device__ __forceinline__ void lcheck(unsigned pk, int c, unsigned target, int lane) {
    if (__builtin_expect(__any(pk < target), 0)) lwait_all(c, 4, target, lane);
    CFENCE();
}
__device__ __forceinline__ void gwait(const char* ws, unsigned off, int n, unsigned tag, int lane) {
    gu32* f = (gu32*)(ws + off) + (lane < n ? lane : 0);
    _Pragma("clang loop unroll(disable)") for (unsigned spins = 0; spins < (1u << 14); ++spins) {
        unsigned v = __hip_atomic_load(f, RLX_AGENT);
        if (__all(v == tag)) break;
        TMO(spins, 1u << 14, 300, off);
        __builtin_amdgcn_s_sleep(6);
    }
    CFENCE();
}
__device__ __forceinline__ float gwait64(const char* ws, unsigned off, unsigned tag, int lane) {
    gu64* f = (gu64*)(ws + off) + lane;
    unsigned long long v = 0;
    _Pragma("clang loop unroll(disable)") for (unsigned spins = 0; spins < (1u << 14); ++spins) {
        v = __hip_atomic_load(f, RLX_AGENT);
        if (__all((unsigned)(v >> 32) == tag)) break;
        __builtin_amdgcn_s_sleep(6);
    }
    CFENCE();
    return __uint_as_float((unsigned)v);
}
__device__ __forceinline__ __amdgpu_buffer_rsrc_t mkrsrc(const char* base, unsigned bytes) {
    return __builtin_amdgcn_make_buffer_rsrc((void*)base, 0, bytes, 0x00020000);
}
__device__ __forceinline__ float dpp_sum16(float v) {
    v += __int_as_float(__builtin_amdgcn_update_dpp(0, __float_as_int(v), 0x128, 0xf, 0xf, false));
    v += __int_as_float(__builtin_amdgcn_update_dpp(0, __float_as_int(v), 0x124, 0xf, 0xf, false));
    v += __int_as_float(__builtin_amdgcn_update_dpp(0, __float_as_int(v), 0x122, 0xf, 0xf, false));
    v += __int_as_float(__builtin_amdgcn_update_dpp(0, __float_as_int(v), 0x121, 0xf, 0xf, false));
    return v;
}
__device__ __forceinline__ float wave_max(float v) {
    int x = __float_as_int(v);
    x = max(x, __builtin_amdgcn_update_dpp(0, x, 0x128, 0xf, 0xf, false)); x = max(x, __builtin_amdgcn_update_dpp(0, x, 0x124, 0xf, 0xf, false));
    x = max(x, __builtin_amdgcn_update_dpp(0, x, 0x122, 0xf, 0xf, false)); x = max(x, __builtin_amdgcn_update_dpp(0, x, 0x121, 0xf, 0xf, false));
    const int a = max(__builtin_amdgcn_readlane(x, 0), __builtin_amdgcn_readlane(x, 16)), b = max(__builtin_amdgcn_readlane(x, 32), __builtin_amdgcn_readlane(x, 48));
    return __int_as_float(max(a, b));
}
__device__ __forceinline__ bf16x8 pack8(const f32x16& a, int b, float sc) {
    u32x4 r = {pk2(a[b] * sc, a[b + 1] * sc), pk2(a[b + 2] * sc, a[b + 3] * sc), pk2(a[b + 4] * sc, a[b + 5] * sc), pk2(a[b + 6] * sc, a[b + 7] * sc)};
    return __builtin_bit_cast(bf16x8, r);
}

template <int I> struct IC { static constexpr int value = I; };
template <int B, int E, class F> __device__ __forceinline__ void static_for(F&& f) { if constexpr (B < E) { f(IC<B>{}); static_for<B + 1, E>(f); } }
#define WAITVN(n) asm volatile("s_waitcnt vmcnt(%0)" :: "i"(n) : "memory")
__device__ __forceinline__ void GLOAD16S(u32x4& dst, unsigned voff, const char* sbase) { asm volatile("global_load_dwordx4 %0, %1, %2 sc1" : "=v"(dst) : "v"(voff), "s"(sbase) : "memory"); }
__device__ __forceinline__ void GLOAD16N(u32x4& dst, unsigned voff, const char* sbase) { asm volatile("global_load_dwordx4 %0, %1, %2" : "=v"(dst) : "v"(voff), "s"(sbase) : "memory"); }
__device__ __forceinline__ void GLOAD16N_IF(u32x4& dst, unsigned voff, const char* sbase, int cond) {
    asm volatile("s_cmp_eq_u32 %3, 0\n\ts_cbranch_scc1 1f\n\tglobal_load_dwordx4 %0, %1, %2\n1:" : "=v"(dst) : "v"(voff), "s"(sbase), "s"(cond) : "memory", "scc"); }
template <int N> __device__ __forceinline__ void waitv5(u32x4& a, u32x4& b, u32x4& c, u32x4& d, u32x4& e) { asm volatile("s_waitcnt vmcnt(%5)" : "+v"(a), "+v"(b), "+v"(c), "+v"(d), "+v"(e) : "i"(N) : "memory"); }
__device__ __forceinline__ void gload8x2_sc1(unsigned long long& a, unsigned long long& b, unsigned voff, const char* sbase) {
    asm volatile("global_load_dwordx2 %0, %2, %3 sc1\n\tglobal_load_dwordx2 %1, %2, %3 offset:512 sc1" : "=&v"(a), "=&v"(b) : "v"(voff), "s"(sbase) : "memory"); }
__device__ __forceinline__ void waitv8x2(unsigned long long& a, unsigned long long& b) { asm volatile("s_waitcnt vmcnt(0)" : "+v"(a), "+v"(b) :: "memory"); }
__device__ __forceinline__ void PIN2(u32x4& a, u32x4& b) { asm volatile("" : "+v"(a), "+v"(b) :: "memory"); }
__device__ __forceinline__ u32x4 scale8(unsigned x0, unsigned x1, unsigned x2, unsigned x3, unsigned clo, unsigned chi, float c) {
    unsigned o0, o1, o2, o3; float t0, t1, t2, t3, t4, t5, t6, t7;
    asm("v_dot2_f32_bf16 %4, %12, %16, %18\n\tv_dot2_f32_bf16 %5, %12, %17, %18\n\tv_dot2_f32_bf16 %6, %13, %16, %18\n\tv_dot2_f32_bf16 %7, %13, %17, %18\n\t"
        "v_dot2_f32_bf16 %8, %14, %16, %18\n\tv_dot2_f32_bf16 %9, %14, %17, %18\n\tv_dot2_f32_bf16 %10, %15, %16, %18\n\tv_dot2_f32_bf16 %11, %15, %17, %18\n\t"
        "v_cvt_pk_bf16_f32 %0, %4, %5\n\tv_cvt_pk_bf16_f32 %1, %6, %7\n\tv_cvt_pk_bf16_f32 %2, %8, %9\n\tv_cvt_pk_bf16_f32 %3, %10, %11"
        : "=&v"(o0), "=&v"(o1), "=&v"(o2), "=&v"(o3), "=&v"(t0), "=&v"(t1), "=&v"(t2), "=&v"(t3), "=&v"(t4), "=&v"(t5), "=&v"(t6), "=&v"(t7)
        : "v"(x0), "v"(x1), "v"(x2), "v"(x3), "v"(clo), "v"(chi), "v"(c));
    return u32x4{o0, o1, o2, o3};
}
template <int N> __device__ __forceinline__ void waitv3(u32x4& a, u32x4& b, u32x4& c) { asm volatile("s_waitcnt vmcnt(%3)" : "+v"(a), "+v"(b), "+v"(c) : "i"(N) : "memory"); }

constexpr float QSCALE = 0.22360679774997896f * 1.4426950408889634f;
constexpr float BOUND_MAX = 50.f;

struct AttSync { int lane; const char* kv; unsigned dvoff; unsigned wo; int w; };
__device__ __forceinline__ unsigned kvslot(int c) { return L_KV + ((c + 2) & 3) * CHUNK; }
__device__ __forceinline__ void att_dma(const AttSync& a, int c) { glds16x3_sc1(a.dvoff, a.kv + (size_t)c * CHUNK, kvslot(c) + a.wo); }
constexpr unsigned AP0 = 16;
#define RING_X(c) do { WAITV(3); lpost(C_AP + as.w, AP0 + (c) + 1); } while (0)
#define RP_A
#define RP_B
#define RING_Y(c, pk) do { RP_A lcheck(pk, C_AP, AP0 + (c) + 1, lane); const int cn_ = (c) + 3 < NCHUNK ? (c) + 3 : NCHUNK - 1;   \
                           glds16x3_sc1(as.dvoff, as.kv + (size_t)cn_ * CHUNK, kvslot((c) + 3) + as.wo); RP_B } while (0)

__device__ __forceinline__ void att_fast(const AttSync& as, bf16x8 Q0a, bf16x8 Q1a0, bf16x8 Q1a1, bf16x8 Q0b, bf16x8 Q1b0, bf16x8 Q1b1, f32x16& Oa, f32x16& Ob) {
    const int lane = as.lane, r = lane & 31, h = lane >> 5;
    const unsigned ak0 = lane * 16, ak1 = h ? KV_V + 20 * 16 : KV_K1 + r * 16;
    const unsigned av = KV_V + h * 352 + (r <= 20 ? r : 21) * 16;
    const f32x16 zero = {};
    f32x16 Sa, Sb = {};
    bf16x8 K0[2], K1[2], V0[2], V1[2];
    u32x4 Pa0, Pa1, Pb0 = {}, Pb1 = {};
    unsigned pk = 0;
    unsigned long long rp0 = 0, rp1 = 0;
    V0[1] = bf16x8{}; V1[1] = bf16x8{};
    unsigned vk0 = L_KV + ak0, vk1 = L_KV + ak1, vv = L_KV + av;
    PIN(vk0); PIN(vk1); PIN(vv);
    K0[0] = ldsf(vk0 + 2 * CHUNK); K1[0] = ldsf(vk1 + 2 * CHUNK); K0[1] = ldsf(vk0 + 2 * CHUNK + 1024); V1[0] = ldsf(vv + 2 * CHUNK + 704); V0[0] = ldsf(vv + 2 * CHUNK);
    Sa = MFMA(K0[0], Q0a, zero); Sa = MFMA(K1[0], Q1a0, Sa);
#define EX4(S, i) do { S[i] = __builtin_amdgcn_exp2f(S[i]); S[i + 1] = __builtin_amdgcn_exp2f(S[i + 1]); S[i + 2] = __builtin_amdgcn_exp2f(S[i + 2]); S[i + 3] = __builtin_amdgcn_exp2f(S[i + 3]); } while (0)
#define GAP(MF, LD, T, k, PW, j, S, i) do { MF; SBAR(); LD; PW[j] = pk2(T[k], T[k + 1]); PW[j + 1] = pk2(T[k + 2], T[k + 3]); PIN(PW); SBAR(); EX4(S, i); PIN(S); SBAR(); } while (0)
#define BC(x) __builtin_bit_cast(bf16x8, x)
#define STEP(e, d, QB1, QA1N, KN, L3, LK0, LK1, LV) do { \
        GAP(Ob = MFMA(V0[e ^ 1], BC(Pb0), Ob), (void)0,                  Sb, 12, Pb1, 2, Sa, 0); \
        GAP(Sb = MFMA(K0[e], Q0b, zero),       (void)0,                  Sa, 0, Pa0, 0, Sa, 4); \
        GAP(Sb = MFMA(K1[d], QB1, Sb),         L3,                       Sa, 4, Pa0, 2, Sa, 8); \
        GAP(Ob = MFMA(V1[e ^ 1], BC(Pb1), Ob), K0[e] = ldsf(LK0),        Sa, 8, Pa1, 0, Sa, 12); \
        GAP(Oa = MFMA(V0[e], BC(Pa0), Oa),     LK1,                      Sa, 12, Pa1, 2, Sb, 0); \
        GAP(Sa = MFMA(K0[e ^ 1], Q0a, zero),   V1[e ^ 1] = ldsf((LV) + 704), Sb, 0, Pb0, 0, Sb, 4); \
        GAP(Sa = MFMA(K1[KN], QA1N, Sa),       V0[e ^ 1] = ldsf(LV),     Sb, 4, Pb0, 2, Sb, 8); \
        GAP(Oa = MFMA(V1[e], BC(Pa1), Oa),     (void)0,                  Sb, 8, Pb1, 0, Sb, 12); \
    } while (0)
#define CHUNK4(c, B, N) do { \
        STEP(0, 0, Q1b0, Q1a1, 0, (void)0, vk0 + (B) + KVD, K1[1] = ldsf(vk1 + (B) + KVD), vv + (B) + KV_VT); \
        RING_X(c); \
        STEP(1, 0, Q1b1, Q1a0, 1, pk = lpeek(C_AP, lane), vk0 + (B) + KVD + 1024, (void)0, vv + (B) + KVD); \
        RING_Y(c, pk); \
        STEP(0, 1, Q1b0, Q1a1, 1, (void)0, vk0 + (N), K1[0] = ldsf(vk1 + (N)), vv + (B) + KVD + KV_VT); \
        STEP(1, 1, Q1b1, Q1a0, 0, (void)0, vk0 + (N) + 1024, (void)0, vv + (N)); \
    } while (0)
    for (int c = 0; c < NCHUNK; c += 4) {
        CHUNK4(c, 2 * CHUNK, 3 * CHUNK); CHUNK4(c + 1, 3 * CHUNK, 0); CHUNK4(c + 2, 0, CHUNK); CHUNK4(c + 3, CHUNK, 2 * CHUNK);
    }
#undef CHUNK4
    Pb1[2] = pk2(Sb[12], Sb[13]); Pb1[3] = pk2(Sb[14], Sb[15]);
    Ob = MFMA(V0[1], BC(Pb0), Ob); Ob = MFMA(V1[1], BC(Pb1), Ob);
#undef STEP
#undef GAP
#undef EX4
}

__device__ __forceinline__ void att_safe(const AttSync& as, bf16x8 Q0a, bf16x8 Q1a0, bf16x8 Q1a1, bf16x8 Q0b, bf16x8 Q1b0, bf16x8 Q1b1, f32x16& Oa, f32x16& Ob) {
    const int lane = as.lane, r = lane & 31, h = lane >> 5;
    const unsigned ak0 = lane * 16, ak1 = h ? KV_V + 20 * 16 : KV_K1 + r * 16;
    const unsigned av = KV_V + h * 352 + (r <= 20 ? r : 21) * 16;
    const f32x16 zero = {};
    float ma = -INFINITY, mb = -INFINITY;
    unsigned long long rp0 = 0, rp1 = 0;
    for (int c = 0; c < NCHUNK; ++c) {
        const unsigned b0 = kvslot(c);
        for (int t = 0; t < 4; ++t) {
            if (t == 1) RING_X(c);
            if (t == 2) RING_Y(c, 0u);
            const unsigned base = b0 + (t >> 1) * KVD; const int e = t & 1;
            bf16x8 K0 = ldsf(base + ak0 + e * 1024), K1 = ldsf(base + ak1), V0 = ldsf(base + av + e * KV_VT), V1 = ldsf(base + av + e * KV_VT + 704);
#pragma unroll
            for (int q = 0; q < 2; ++q) {
                f32x16 S = MFMA(K0, q ? Q0b : Q0a, zero); S = MFMA(K1, q ? (e ? Q1b1 : Q1b0) : (e ? Q1a1 : Q1a0), S);
                float mx = S[0];
#pragma unroll
                for (int i = 1; i < 16; ++i) mx = fmaxf(mx, S[i]);
                mx = fmaxf(mx, __shfl_xor(mx, 32));
                float& m = q ? mb : ma; f32x16& O = q ? Ob : Oa;
                const float mn = fmaxf(m, mx);
                const float al = __builtin_amdgcn_exp2f(m - mn);
                m = mn;
#pragma unroll
                for (int i = 0; i < 16; ++i) { O[i] *= al; S[i] = __builtin_amdgcn_exp2f(S[i] - mn); }
                O = MFMA(V0, pack8(S, 0, 1.f), O); O = MFMA(V1, pack8(S, 8, 1.f), O);
            }
        }
    }
}

__device__ __forceinline__ void xload4(u32x4 (&x)[5], const char* xb, unsigned vo) {
    asm volatile("global_load_dwordx4 %0, %5, %6\n\tglobal_load_dwordx4 %1, %5, %6 offset:256\n\tglobal_load_dwordx4 %2, %5, %6 offset:512\n\t"
                 "global_load_dwordx4 %3, %5, %6 offset:768\n\tglobal_load_dwordx4 %4, %5, %6 offset:1024"
                 : "=&v"(x[0]), "=&v"(x[1]), "=&v"(x[2]), "=&v"(x[3]), "=&v"(x[4]) : "v"(vo), "s"(xb) : "memory");
}
__device__ __forceinline__ void ln_rows(const u32x4 (&xr)[5], int row0, int lane) {
    f32x2 xv[20];
    f32x2 s0 = {0.f, 0.f}, s1 = {0.f, 0.f}, q0 = {0.f, 0.f}, q1 = {0.f, 0.f};
#pragma unroll
    for (int i = 0; i < 20; i += 2) {
        const unsigned a = xr[i >> 2][i & 3], b = xr[i >> 2][(i & 3) + 1];
        xv[i] = f32x2{bflo(a), bfhi(a)}; xv[i + 1] = f32x2{bflo(b), bfhi(b)};
        s0 += xv[i]; q0 = __builtin_elementwise_fma(xv[i], xv[i], q0);
        s1 += xv[i + 1]; q1 = __builtin_elementwise_fma(xv[i + 1], xv[i + 1], q1);
    }
    s0 += s1; q0 += q1;
    const float s = dpp_sum16(s0[0] + s0[1]), ss = dpp_sum16(q0[0] + q0[1]);
    const float mean = s * (1.f / 640.f);
    const float var = fmaxf(ss * (1.f / 640.f) - mean * mean, 0.f);
    const float rs = __builtin_amdgcn_rcpf(rbf(__builtin_amdgcn_sqrtf(rbf(rbf(var) + 1e-5f)))), mo = -rbf(mean) * rs;
    const f32x2 rs2 = {rs, rs}, mo2 = {mo, mo};
    const int row = row0 + (lane >> 4), c = lane & 15;
    const unsigned wa = L_PANEL + (row >> 5) * 40960 + (c >> 1) * 1024 + (c & 1) * 512 + (((row & 31) ^ (c & 7)) << 4);
#pragma unroll
    for (int u = 0; u < 5; ++u) {
        u32x4 o;
#pragma unroll
        for (int e = 0; e < 4; ++e) { const f32x2 y = __builtin_elementwise_fma(xv[4 * u + e], rs2, mo2); o[e] = pk2(y[0], y[1]); }
        *LP(u32x4, wa + u * 8192) = o;
    }
}
#define BLOCK_BARRIER() asm volatile("s_waitcnt lgkmcnt(0)\n\ts_barrier" ::: "memory")

template <int PH>
__global__ void __launch_bounds__(512, 2) mega(Params p) {
    constexpr bool PRO = PH & 1, ATT = PH & 2, DN = PH & 8, WO = PH & 16;
    const int w = __builtin_amdgcn_readfirstlane(threadIdx.x >> 6);
    const int b = blockIdx.x, xc = b & 7, j = b >> 3;
    const int ti = (xc >> 1) * 8 + (j >> 2), g = (xc & 1) * 4 + (j & 3);
    unsigned tag = 1u, epv = 0u;
    constexpr bool FUSED = PH == 27;
    if constexpr (FUSED) asm volatile("global_load_dword %0, %1, %2 sc1" : "=v"(epv) : "v"(0u), "s"(p.ws + WS_EP) : "memory");
#define GET_TAG() do { if constexpr (FUSED) { asm volatile("s_nop 0\n\tv_readfirstlane_b32 %0, %1\n\ts_nop 3" : "=s"(tag) : "v"(epv) : "memory"); tag += 0x9E3779B1u; } } while (0)
    char* ws = p.ws;
    STAMP(0);
    {
        constexpr unsigned CODE_PF = 61440;
        const char* pc; asm volatile("s_getpc_b64 %0" : "=s"(pc));
        const unsigned line = (unsigned)(j * 8) * 64u + threadIdx.x;
        unsigned keep;
        if (line * 128u < CODE_PF)
            asm volatile("s_mov_b32 %0, m0\n\ts_mov_b32 m0, %3\n\ts_nop 2\n\tglobal_load_lds_dword %1, %2\n\ts_mov_b32 m0, %0" : "=&s"(keep) : "v"(line * 128u), "s"(pc), "s"(L_MISC + 1024) : "memory");
    }
    if (threadIdx.x < C_N) *lcnt(threadIdx.x) = (threadIdx.x == C_WP || threadIdx.x == C_WP + 1 || threadIdx.x == C_PP || threadIdx.x == C_PP + 1) ? 100u : 0u;
    const bool haskv = (g >> 2) == (ti & 1);
    const char* wqb = p.wq + g * 160;
    const int kmt = g & 1;
    const char* xb = p.x + (size_t)ti * 64 * 1280;
#define LANE_CONSTS() int lane = __builtin_amdgcn_mbcnt_hi(~0u, __builtin_amdgcn_mbcnt_lo(~0u, 0u)); PIN(lane); \
    const int r = lane & 31, h = lane >> 5, tq = (lane & 15) >> 2, tp = lane & 3, tb = (lane >> 4) & 1; (void)r; (void)h; (void)tq; (void)tp; (void)tb

    if (w < 4) {
        LANE_CONSTS();
        const unsigned xvo = (lane >> 4) * 1280 + (lane & 15) * 16;
        bf16x8 Q0a, Q0b; u32x2 q1a, q1b;
        const int head = 4 * g + w;
        const AttSync as{lane, ws + WS_KV, (unsigned)((w < 3 ? w * 3072 : CHUNK - 3072) + lane * 16), (unsigned)(w < 3 ? w * 3072 : CHUNK - 3072), w};
        __builtin_amdgcn_s_setprio(3);
        unsigned long long fk = 0, fv = 0; bool early = false;
        if constexpr (PRO) {
            u32x4 x0[5], x1[5], x2[5], x3[5];
            const int ra = kmt * 32 + w * 8, rb = (kmt ^ 1) * 32 + w * 8;
            xload4(x0, xb + (size_t)ra * 1280, xvo); xload4(x1, xb + (size_t)(ra + 4) * 1280, xvo);
            xload4(x2, xb + (size_t)rb * 1280, xvo); xload4(x3, xb + (size_t)(rb + 4) * 1280, xvo);
            BLOCK_BARRIER();
            waitv5<15>(x0[0], x0[1], x0[2], x0[3], x0[4]);
            GET_TAG();
            STAMP(1);
            ln_rows(x0, ra, lane);
            waitv5<10>(x1[0], x1[1], x1[2], x1[3], x1[4]);
            ln_rows(x1, ra + 4, lane);
            lpost(C_AP + w, 1);
            waitv5<5>(x2[0], x2[1], x2[2], x2[3], x2[4]);
            ln_rows(x2, rb, lane);
            waitv5<0>(x3[0], x3[1], x3[2], x3[3], x3[4]);
            ln_rows(x3, rb + 4, lane);
            lpost(C_AP + w, 2);
            unsigned pa[4];
#pragma unroll
            for (int s = 0; s < 4; ++s) pa[s] = L_PANEL + h * 512 + ((r ^ ((2 * s + h) & 7)) << 4);
            f32x16 qa = {}, qb = {};
            const unsigned wa = L_WQ + (8 * h + tq) * 160 + w * 40 + tb * 32 + (tb ? 0 : tp * 8);
            lwait_all(C_AP, 4, 2, lane); lwait_all(C_PP, 4, 1, lane);
            STAMP(2);
            bf16x8 fw[2][4], fa[2][4], fb[2][4];
#pragma unroll
            for (int s4 = 0; s4 < 4; ++s4) { fw[0][s4] = ldstr2(wa + s4 * 2560, wa + s4 * 2560 + 640); fa[0][s4] = ldsf(pa[s4] + s4 * 1024); fb[0][s4] = ldsf(pa[s4] + 40960 + s4 * 1024); }
            unsigned pk = 0;
            static_for<0, 10>([&](auto S) {
                constexpr int st = S.value, nx = st + 1;
                if constexpr (st == 3 || st == 5 || st == 7) lcheck(pk, C_WP, (st - 1) / 2, lane);
                if constexpr (st == 6 && ATT) gload8x2_sc1(fk, fv, lane * 8, ws + WS_KF + xc * 1024);
                if constexpr (st == 8 && ATT) {
                    waitv8x2(fk, fv);
                    early = __all((unsigned)(fk >> 32) == tag && (unsigned)(fv >> 32) == tag);
                    if (early) { lwait_all(C_AP, 4, 6, lane); att_dma(as, 0); att_dma(as, 1); }
                    STAMP(8);
                }
#pragma unroll
                for (int s4 = 0; s4 < 4; ++s4) {
                    qa = MFMA(fw[st & 1][s4], fa[st & 1][s4], qa); SBAR();
                    if constexpr (nx < 10) { const unsigned o = wa + (nx & 3) * 10240 + s4 * 2560; fw[nx & 1][s4] = ldstr2(o, o + 640); SBAR(); }
                    qb = MFMA(fw[st & 1][s4], fb[st & 1][s4], qb); SBAR();
                    if constexpr (nx < 10) { fa[nx & 1][s4] = ldsf(pa[s4] + (nx * 4 + s4) * 1024); fb[nx & 1][s4] = ldsf(pa[s4] + 40960 + (nx * 4 + s4) * 1024); SBAR(); }
                    if (s4 == 2 && (st == 2 || st == 4 || st == 6)) { pk = lpeek(C_WP, lane); SBAR(); }
                }
                if constexpr (!(st & 1) && st <= 6) lpost(C_AP + w, 3 + st / 2);
            });
            lpost(C_AP + w, 8);
            Q0a = pack8(qa, 0, QSCALE); Q0b = pack8(qb, 0, QSCALE);
            q1a = u32x2{pk2(qa[8] * QSCALE, qa[9] * QSCALE), pk2(qa[10] * QSCALE, qa[11] * QSCALE)};
            q1b = u32x2{pk2(qb[8] * QSCALE, qb[9] * QSCALE), pk2(qb[10] * QSCALE, qb[11] * QSCALE)};
            if (h) { q1a = u32x2{0u, 0u}; q1b = u32x2{0u, 0u}; }
            if constexpr (!ATT) {
                char* qt = ws + WS_QT + (size_t)((2 * ti) * 32 + head) * 2048 + lane * 16;
                *(bf16x8*)qt = Q0a; *(u32x2*)(qt + 1024) = q1a; *(bf16x8*)(qt + 65536) = Q0b; *(u32x2*)(qt + 65536 + 1024) = q1b;
            }
            STAMP(3);
        } else {
            BLOCK_BARRIER();
            if constexpr (ATT) {
                const char* qt = ws + WS_QT + (size_t)((2 * ti) * 32 + head) * 2048 + lane * 16;
                Q0a = *(const bf16x8*)qt; q1a = *(const u32x2*)(qt + 1024); Q0b = *(const bf16x8*)(qt + 65536); q1b = *(const u32x2*)(qt + 65536 + 1024);
            }
        }
        if constexpr (ATT) {
            float na = 0.f, nb = 0.f;
            {
                const u32x4 a0 = __builtin_bit_cast(u32x4, Q0a), b0 = __builtin_bit_cast(u32x4, Q0b);
#pragma unroll
                for (int e = 0; e < 4; ++e) { na += bflo(a0[e]) * bflo(a0[e]) + bfhi(a0[e]) * bfhi(a0[e]); nb += bflo(b0[e]) * bflo(b0[e]) + bfhi(b0[e]) * bfhi(b0[e]); }
#pragma unroll
                for (int e = 0; e < 2; ++e) { na += bflo(q1a[e]) * bflo(q1a[e]) + bfhi(q1a[e]) * bfhi(q1a[e]); nb += bflo(q1b[e]) * bflo(q1b[e]) + bfhi(q1b[e]) * bfhi(q1b[e]); }
                na += __shfl_xor(na, 32); nb += __shfl_xor(nb, 32);
            }
            if (!early) {
                gu64* f = (gu64*)(ws + WS_KF + xc * 1024) + lane;
                _Pragma("clang loop unroll(disable)") for (unsigned spins = 0; spins < (1u << 14); ++spins) {
                    fk = __hip_atomic_load(f, RLX_AGENT); fv = __hip_atomic_load(f + 64, RLX_AGENT);
                    if (__all((unsigned)(fk >> 32) == tag && (unsigned)(fv >> 32) == tag)) break;
                    TMO(spins, 1u << 14, 301, tag);
                    __builtin_amdgcn_s_sleep(4);
                }
                if constexpr (PRO) lwait_all(C_AP, 4, 6, lane);
                att_dma(as, 0); att_dma(as, 1);
            }
            STAMP(4);
            if constexpr (PRO) lwait_all(C_AP, 4, 8, lane);
            att_dma(as, 2);
            const float kn2 = wave_max(__uint_as_float((unsigned)fk)), vmx = wave_max(__uint_as_float((unsigned)fv));
            const float Ma = __builtin_sqrtf(na * kn2) * 1.01f, Mb = __builtin_sqrtf(nb * kn2) * 1.01f;
            const bool fast = __all(Ma <= BOUND_MAX && Mb <= BOUND_MAX && vmx <= 1e30f);
            const unsigned sa = (fast && h) ? pk2(-Ma, 0.f) : q1a[0], sb = (fast && h) ? pk2(-Mb, 0.f) : q1b[0];
            const u32x4 a0 = {sa, q1a[1], 0u, 0u}, a1 = {h ? sa : 0u, 0u, q1a[0], q1a[1]}, b0 = {sb, q1b[1], 0u, 0u}, b1 = {h ? sb : 0u, 0u, q1b[0], q1b[1]};
            f32x16 Oa = {}, Ob = {};
            WAITV(6); lpost(C_AP + w, AP0); lwait_all(C_AP, 4, AP0, lane);
            __builtin_amdgcn_s_setprio(ATT_PRIO);
            STAMP(5); STAMPC(12);
            if (fast) att_fast(as, Q0a, BC(a0), BC(a1), Q0b, BC(b0), BC(b1), Oa, Ob);
            else att_safe(as, Q0a, BC(a0), BC(a1), Q0b, BC(b0), BC(b1), Oa, Ob);
            STAMP(6); STAMPC(13);
            const __amdgpu_buffer_rsrc_t ro = mkrsrc(ws + WS_OT, 64 * 40 * 1024);
#pragma unroll
            for (int q = 0; q < 2; ++q) {
                const f32x16& O = q ? Ob : Oa;
                const float inv = 1.f / __shfl(O[8], r + 32);
                const unsigned base = ((2 * ti + q) * 40 + 5 * g) * 1024;
                __builtin_amdgcn_raw_buffer_store_b128(__builtin_bit_cast(u32x4, pack8(O, 0, inv)), ro, base + w * 1024 + lane * 16, 0, 16);
                if (!h) {
                    u32x2 t = {pk2(O[8] * inv, O[9] * inv), pk2(O[10] * inv, O[11] * inv)};
                    __builtin_amdgcn_raw_buffer_store_b64(t, ro, base + 4096 + ((w >> 1) * 32 + r) * 16 + (w & 1) * 8, 0, 16);
                }
            }
            if constexpr (WO) {
                WAITV(0);
                if (lane < 2) __hip_atomic_store((gu32*)(ws + WS_OF) + (2 * ti + lane) * 32 + head, tag, RLX_AGENT);
            }
            STAMP(7);
        }
    } else {
        LANE_CONSTS();
        const unsigned xvo = (lane >> 4) * 1280 + (lane & 15) * 16;
        const int v = w - 4;
        __builtin_amdgcn_s_setprio(3);
        if constexpr (PRO) {
            const int kvm = 2 * ti + (g & 1); const bool isV = (g >> 1) & 1;
            const char* wkvb = isV ? p.wv : p.wk;
            if (haskv && v < 2) {
#pragma unroll
                for (int n = 0; n < 13; ++n) {
                    const unsigned o = v * 12800 + (n < 12 ? n * 1024 : 11776);
                    glds16(o + lane * 16, wkvb, L_WKV + o);
                }
            }
            BLOCK_BARRIER();
            if (v >= 2) {
                const int v2 = v - 2;
                unsigned wo5[5];
#pragma unroll
                for (int i = 0; i < 5; ++i) { const unsigned c = (v2 + 2 * i) * 64 + lane, row = c / 10u, col = c - row * 10u; wo5[i] = row * 1280 + col * 16; }
#pragma unroll
                for (int n = 0; n < 20; ++n) glds16(wo5[n % 5], wqb + (n / 5) * 64 * 1280, L_WQ + (v2 + 2 * n) * 1024);
                u32x4 wst[3][10];
                auto wst_issue = [&](auto I) {
                    constexpr int rr = I.value / 10, i = I.value % 10;
                    GLOAD16N(wst[rr][i], wo5[i % 5], wqb + (4 + 2 * rr + i / 5) * 64 * 1280);
                };
                static_for<0, 10>(wst_issue);
                WAITV(10); lpost(C_PP + v, 1);
                GET_TAG();
                STAMP(2);
                static_for<10, 30>(wst_issue);
                static_for<0, 3>([&](auto R) {
                    constexpr int rr = R.value;
                    waitv5<20 - 10 * rr>(wst[rr][0], wst[rr][1], wst[rr][2], wst[rr][3], wst[rr][4]); waitv5<63>(wst[rr][5], wst[rr][6], wst[rr][7], wst[rr][8], wst[rr][9]);
                    lwait_all(C_AP, 4, 3 + rr, lane);
#pragma unroll
                    for (int i = 0; i < 10; ++i) *LP(u32x4, L_WQ + (rr & 1) * 20480 + (v2 + 2 * i) * 1024 + lane * 16) = wst[rr][i];
                    lpost(C_WP + v, rr + 1);
                });
            } else {
              if (haskv) {
                f32x16 acc = {};
                const unsigned wa = L_WKV + (v * 320 + 8 * h + tq) * 40 + tb * 32 + (tb ? 0 : tp * 8);
                unsigned xa[4];
#pragma unroll
                for (int s = 0; s < 4; ++s) xa[s] = L_PANEL + kmt * 40960 + v * 20480 + h * 512 + ((r ^ ((2 * s + h) & 7)) << 4);
                WAITV(0);
                GET_TAG();
                STAMP(2);
                bf16x8 wf[20], xf[20];
#pragma unroll
                for (int s = 0; s < 20; ++s) wf[s] = ldstr2(wa + s * 640, wa + s * 640 + 160);
                lwait_all(C_AP, 4, 1, lane);
#pragma unroll
                for (int s = 0; s < 20; ++s) xf[s] = ldsf(xa[s & 3] + s * 1024);
                SBAR();
                if (isV) {
#pragma unroll
                    for (int s = 0; s < 20; ++s) acc = MFMA(xf[s], wf[s], acc);
                } else {
#pragma unroll
                    for (int s = 0; s < 20; ++s) acc = MFMA(wf[s], xf[s], acc);
                }
                if (v) {
#pragma unroll
                    for (int e = 0; e < 4; ++e) *LP(f32x4, L_WKV + 12800 + e * 1024 + lane * 16) = f32x4{acc[4 * e], acc[4 * e + 1], acc[4 * e + 2], acc[4 * e + 3]};
                    lpost(C_KVP, 1);
                } else {
                    lwait(C_KVP, 1);
#pragma unroll
                    for (int e = 0; e < 4; ++e) {
                        const f32x4 t = *LP(const f32x4, L_WKV + 12800 + e * 1024 + lane * 16);
                        acc[4 * e] += t[0]; acc[4 * e + 1] += t[1]; acc[4 * e + 2] += t[2]; acc[4 * e + 3] += t[3];
                    }
                    const __amdgpu_buffer_rsrc_t rk = mkrsrc(ws + WS_KV, 32 * KVD);
                    const unsigned base = ti * KVD, ke = g & 1;
                    const u32x4 f0 = __builtin_bit_cast(u32x4, pack8(acc, 0, 1.f)), f1 = __builtin_bit_cast(u32x4, pack8(acc, 8, 1.f));
                    float mx;
                    if (!isV) {
                        __builtin_amdgcn_raw_buffer_store_b128(f0, rk, base + ke * 1024 + lane * 16, 0, 16);
                        const u32x2 k1 = {f1[0], f1[1]};
                        if (!h) __builtin_amdgcn_raw_buffer_store_b64(k1, rk, base + KV_K1 + r * 16 + ke * 8, 0, 16);
                        lpost(C_KVFIN, 1);
                        float n2 = 0.f;
#pragma unroll
                        for (int e = 0; e < 4; ++e) n2 += bflo(f0[e]) * bflo(f0[e]) + bfhi(f0[e]) * bfhi(f0[e]);
                        if (!h) n2 += bflo(f1[0]) * bflo(f1[0]) + bfhi(f1[0]) * bfhi(f1[0]) + bflo(f1[1]) * bflo(f1[1]) + bfhi(f1[1]) * bfhi(f1[1]);
                        n2 += __shfl_xor(n2, 32);
                        mx = (n2 == n2) ? n2 : INFINITY;
                    } else {
                        const unsigned cst = r == 20 ? 0x3f803f80u : 0u;
                        const u32x4 c4 = {cst, cst, cst, cst};
                        if (r <= 21) {
                            __builtin_amdgcn_raw_buffer_store_b128(r >= 20 ? c4 : f0, rk, base + KV_V + ke * KV_VT + h * 352 + r * 16, 0, 16);
                            __builtin_amdgcn_raw_buffer_store_b128(r >= 20 ? c4 : f1, rk, base + KV_V + ke * KV_VT + 704 + h * 352 + r * 16, 0, 16);
                        }
                        lpost(C_KVFIN, 1);
                        float vm = 0.f;
#pragma unroll
                        for (int e = 0; e < 16; ++e) { const float a = fabsf(acc[e]); vm = (a <= vm) ? vm : ((a == a) ? a : INFINITY); }
                        mx = r < 20 ? vm : 0.f;
                    }
                    mx = wave_max(mx);
                    WAITV(0);
                    STAMP(15);
                    if (lane < 8) __hip_atomic_store((gu64*)(ws + WS_KF + lane * 1024 + (isV ? 512 : 0)) + kvm, ((unsigned long long)tag << 32) | __float_as_uint(mx), RLX_AGENT);
                }
            }
              else { WAITV(0); GET_TAG(); }
              STAMP(3);
            }
            if (haskv) lwait(C_KVFIN, 1);
#pragma unroll
            for (int n = 0; n < 2; ++n) {
                unsigned L = n * 64 + lane; L = (j * 4 + v) * 120 + (L < 120u ? L : 119u);
                const unsigned m3 = L >= 7680u, rem = L - (m3 ? 7680u : 0u), row = rem / 12u;
                l2touch((m3 ? p.w3 : p.w1) + row * 3072 + (xc & 1) * 1536 + (rem - row * 12u) * 128);
            }
            lwait_all(C_AP, 4, GU_DELAY, lane);
            STAMP(4);
            {
                const char* wsrc = ((v >> 1) ? p.w3 : p.w1) + g * 384 + (size_t)(v & 1) * 8 * 3072;
                unsigned gvo[3];
#pragma unroll
                for (int n = 0; n < 3; ++n) {
                    const unsigned c = n * 64 + lane, row = c / 24u, col = (c - row * 24u) ^ ((row & 2) << 1);
                    gvo[n] = row * 3072 + col * 16 - n * 1024;
                }
                const unsigned gdst = L_GU + (v >> 1) * 6176 + (v & 1) * 3072;
                auto gdma = [&](auto S) { constexpr int s = S.value; glds16x3v(gvo[0], gvo[1], gvo[2], wsrc + (size_t)s * 16 * 3072, gdst + (s % 3) * GSL); };
                static_for<0, 3>(gdma);
                f32x16 acc[2][3] = {};
                unsigned bn[3], pa[4], pb[4];
#pragma unroll
                for (int n = 0; n < 3; ++n) bn[n] = L_GU + tb * 6176 + (8 * h + tq) * 384 + (((3 * v + n) * 32 + (tp >> 1) * 16) ^ ((unsigned)(tq >> 1) << 6)) + (tp & 1) * 8;
#pragma unroll
                for (int s = 0; s < 4; ++s) { pa[s] = L_PANEL + h * 512 + ((r ^ ((2 * s + h) & 7)) << 4); pb[s] = pa[s] + 40960; PIN(pb[s]); }
                bf16x8 wf[2][3], xa[2], xb2[2];
                unsigned vaddr = L_MISC + (C_GP + v) * 4, vone = 1u; PIN(vaddr); PIN(vone);
                auto rdw = [&](auto S, auto N) { constexpr int s = S.value, n = N.value; const unsigned o = bn[n] + (s % 3) * GSL; wf[s & 1][n] = ldstr2(o, o + 1536); };
                auto rdx = [&](auto S) { constexpr int s = S.value; xa[s & 1] = ldsf(pa[s & 3] + s * 1024); xb2[s & 1] = ldsf(pb[s & 3] + s * 1024); };
                WAITV(6); linc(vaddr, vone); lwait_all(C_GP, 4, 1u, lane);
                rdx(IC<0>{}); rdw(IC<0>{}, IC<0>{}); rdw(IC<0>{}, IC<1>{}); rdw(IC<0>{}, IC<2>{});
                WAITV(3); linc(vaddr, vone);
                static_for<0, 40>([&](auto S) {
                    constexpr int s = S.value; constexpr bool nx = s + 1 < 40;
                    unsigned pk = 0;
                    MPRIO(GU_HI, 0); acc[0][0] = MFMA(wf[s & 1][0], xa[s & 1], acc[0][0]); SBAR();
                    if constexpr (nx) rdx(IC<s + 1>{});
                    SBAR();
                    MPRIO(GU_HI, 1); acc[1][0] = MFMA(wf[s & 1][0], xb2[s & 1], acc[1][0]); SBAR();
                    if constexpr (nx) pk = lpeek(C_GP, lane);
                    SBAR();
                    MPRIO(GU_HI, 2); acc[0][1] = MFMA(wf[s & 1][1], xa[s & 1], acc[0][1]); SBAR();
                    MPRIO(GU_HI, 3); acc[1][1] = MFMA(wf[s & 1][1], xb2[s & 1], acc[1][1]); SBAR();
                    if constexpr (nx) { lcheck(pk, C_GP, s + 2, lane); rdw(IC<s + 1>{}, IC<0>{}); rdw(IC<s + 1>{}, IC<1>{}); rdw(IC<s + 1>{}, IC<2>{}); }
                    SBAR();
                    MPRIO(GU_HI, 4); acc[0][2] = MFMA(wf[s & 1][2], xa[s & 1], acc[0][2]); SBAR();
                    if constexpr (s + 3 < 40) gdma(IC<s + 3>{});
                    SBAR();
                    MPRIO(GU_HI, 5); acc[1][2] = MFMA(wf[s & 1][2], xb2[s & 1], acc[1][2]); SBAR();
                    if constexpr (s + 3 < 40) WAITV(3); else WAITV(0);
                    linc(vaddr, vone); SBAR();
                });
                __builtin_amdgcn_s_setprio(3);
                STAMP(6);
                const __amdgpu_buffer_rsrc_t rh = mkrsrc(ws + WS_HT, 64 * 96 * 1024);
#pragma unroll
                for (int m = 0; m < 2; ++m)
#pragma unroll
                    for (int n = 0; n < 3; ++n) {
                        f32x16 hv;
#pragma unroll
                        for (int e = 0; e < 8; ++e) {
                            const float gg = acc[m][n][e];
                            hv[e] = gg * __builtin_amdgcn_rcpf(1.f + __builtin_amdgcn_exp2f(-1.4426950408889634f * gg)) * acc[m][n][e + 8];
                        }
                        __builtin_amdgcn_raw_buffer_store_b128(__builtin_bit_cast(u32x4, pack8(hv, 0, 1.f)), rh, ((2 * ti + m) * 96 + g * 12 + 3 * v + n) * 1024 + lane * 16, 0, 16);
                    }
                if constexpr (DN) {
                    WAITV(0);
                    if (lane < 2) __hip_atomic_store((gu32*)(ws + WS_HF) + (2 * ti + lane) * 32 + g * 4 + v, tag, RLX_AGENT);
                }
                if constexpr (DN) {
#pragma unroll
                    for (int n = 0; n < 2; ++n) {
                        unsigned L = (j * 4 + v) * 85 + n * 64 + lane; L = L < 10880u ? L : 10879u;
                        const unsigned row = L / 5u;
                        l2touch((row < 1536u ? p.w2 + row * 1280 : p.wo + (row - 1536u) * 1280) + (xc & 1) * 640 + (L - row * 5u) * 128);
                    }
                }
                STAMP(7);
            }
        } else {
            BLOCK_BARRIER();
        }
        if constexpr (DN || WO) {
            LANE_CONSTS();
            const unsigned bq = ((tb * 32 + (tp >> 1) * 16) ^ ((unsigned)(tq >> 1) << 6)) + (tp & 1) * 8;
            const unsigned drow = lane >> 3, dcol = ((lane & 7) ^ (((lane >> 4) & 1) << 2)) * 16;
            const int G = xc >> 1, r0 = G < 2 ? 6 * G : 12 + 5 * (G - 2), nr = G < 2 ? 6 : 5;
            if constexpr (FUSED) {
                if (b == 255 && v == 0) {
                    gu32* f = (gu32*)(ws + WS_HF) + (lane >> 3) * 64 + (lane & 7) * 4;
                    _Pragma("clang loop unroll(disable)") for (unsigned spins = 0; spins < (1u << 14); ++spins) {
                        bool ok = true;
#pragma unroll
                        for (int n = 0; n < 4; ++n) ok &= __hip_atomic_load(f + n * 512, RLX_AGENT) == tag;
                        if (__all(ok)) break;
                        TMO(spins, 1u << 14, 303, tag);
                        __builtin_amdgcn_s_sleep(32);
                    }
                    if (lane == 0) __hip_atomic_store((gu32*)(ws + WS_EP), tag, RLX_AGENT);
                }
            }
            if (j < 5 * nr) {
                const int dr = r0 + j / 5, dc = (xc & 1) * 5 + j % 5;
                const int mfirst = dr < 20 ? 3 * dr : 60 + 2 * (dr - 20), nm = dr < 20 ? 3 : 2;
                const unsigned dvo = drow * 1280 + dcol;
                const unsigned myring = L_PANEL + v * 20480;
                f32x16 acc0 = {}, acc1 = {};
                if constexpr (PRO) { lwait_all(C_GP, 4, 42, lane); lwait_all(C_AP, 4, 8, lane); }
                if constexpr (DN) {
                    f32x16 da[3][2] = {};
                    const int drot = (j % 5) * 4;
                    const char* bsrc = p.w2 + (size_t)v * 384 * 1280 + dc * 128;
                    const char* asrc = ws + WS_HT + ((size_t)mfirst * 96 + v * 24) * 1024;
                    const unsigned am1 = lane * 16 + 98304, am2 = lane * 16 + (nm == 3 ? 196608 : 98304);
                    u32x4 aq[8][3];
                    auto bissue = [&](auto K) {
                        constexpr int k = K.value;
                        int kr = k + drot; kr = kr >= 24 ? kr - 24 : kr;
                        const char* sb = bsrc + (size_t)kr * 16 * 1280;
                        glds16(dvo, sb, myring + (k % 10) * 2048); glds16(dvo + 8 * 1280, sb, myring + (k % 10) * 2048 + 1024);
                    };
                    auto woissue = [&](auto U) {
                        constexpr int u = U.value;
                        const char* sb = p.wo + (size_t)(v * 160 + u * 16) * 1280 + dc * 128;
                        glds16(dvo, sb, myring + u * 2048); glds16(dvo + 8 * 1280, sb, myring + u * 2048 + 1024);
                    };
                    auto aissue = [&](auto K) {
                        constexpr int k = K.value;
                        int kr = k + drot; kr = kr >= 24 ? kr - 24 : kr;
                        const char* sb = asrc + (size_t)kr * 1024;
                        GLOAD16S(aq[k & 7][0], lane * 16, sb); GLOAD16S(aq[k & 7][1], am1, sb); GLOAD16S(aq[k & 7][2], am2, sb);
                    };
                    static_for<0, 8>(bissue);
                    if constexpr (PRO) {
                        gu32* f = (gu32*)(ws + WS_HF) + (mfirst + ((lane >> 3) < nm ? (lane >> 3) : 0)) * 32 + 8 * v + (lane & 7);
                        _Pragma("clang loop unroll(disable)") for (unsigned spins = 0; spins < (1u << 14); ++spins) {
                            if (__all(__hip_atomic_load(f, RLX_AGENT) == tag)) break;
                            TMO(spins, 1u << 14, 302, tag);
                            __builtin_amdgcn_s_sleep(6);
                        }
                        CFENCE();
                    }
                    STAMP(8);
                    static_for<0, 8>(aissue);
                    const unsigned ba0 = myring + (4 * h + tq) * 128 + bq, ba1 = ba0 ^ 64;
                    bf16x8 w0[2], w1[2];
                    WAITVN(24);
                    w0[0] = ldstr2(ba0, ba0 + 1024); w1[0] = ldstr2(ba1, ba1 + 1024);
                    static_for<0, 24>([&](auto K) {
                        constexpr int k = K.value;
                        constexpr int yk = (k <= 7 ? 21 + 2 * k : (k < 16 ? 35 : 5 * (23 - k))) + (WO && k > 16 ? 2 * (k - 16) : 0);
                        constexpr int n = (k + 1 <= 7 || k == 23) ? yk : yk - 2;
                        waitv3<(n < 0 ? 0 : n)>(aq[k & 7][0], aq[k & 7][1], aq[k & 7][2]);
                        constexpr unsigned o = ((k + 1) % 10) * 2048;
#define DXF(m) __builtin_bit_cast(bf16x8, aq[k & 7][m])
                        SBAR();
                        MPRIO(DN_HI, 0); da[0][0] = MFMA(w0[k & 1], DXF(0), da[0][0]); SBAR();
                        if constexpr (k + 1 < 24) w0[(k + 1) & 1] = ldstr2(ba0 + o, ba0 + o + 1024);
                        SBAR();
                        MPRIO(DN_HI, 1); da[0][1] = MFMA(w1[k & 1], DXF(0), da[0][1]); SBAR();
                        if constexpr (k + 1 < 24) w1[(k + 1) & 1] = ldstr2(ba1 + o, ba1 + o + 1024);
                        SBAR();
                        MPRIO(DN_HI, 2); da[1][0] = MFMA(w0[k & 1], DXF(1), da[1][0]); SBAR();
                        if constexpr (k + 8 < 24) bissue(IC<k + 8>{});
                        else if constexpr (WO) woissue(IC<(k + 8) % 10>{});
                        SBAR();
                        MPRIO(DN_HI, 3); da[1][1] = MFMA(w1[k & 1], DXF(1), da[1][1]); SBAR();
                        MPRIO(DN_HI, 4); da[2][0] = MFMA(w0[k & 1], DXF(2), da[2][0]); SBAR();
                        MPRIO(DN_HI, 5); da[2][1] = MFMA(w1[k & 1], DXF(2), da[2][1]); SBAR();
                        if constexpr (k + 8 < 24) aissue(IC<k + 8>{});
                    });
                    __builtin_amdgcn_s_setprio(3);
                    STAMP(9);
                    if constexpr (WO) { CFENCE(); woissue(IC<2>{}); woissue(IC<3>{}); }
#pragma unroll
                    for (int n = 0; n < 2; ++n) {
#pragma unroll
                        for (int m = 0; m < 3; ++m) {
                            if (m != v) {
                                const unsigned dst = L_GU + (m * 3 + (v > m ? v - 1 : v)) * 4096 + lane * 16;
#pragma unroll
                                for (int e = 0; e < 4; ++e) *LP(f32x4, dst + e * 1024) = f32x4{da[m][n][4 * e], da[m][n][4 * e + 1], da[m][n][4 * e + 2], da[m][n][4 * e + 3]};
                            }
                        }
                        lpost(C_DP + v, 2 * n + 1); lwait_all(C_DP, 4, 2 * n + 1, lane);
                        if (v < 3) {
                            f32x16 t = v == 0 ? da[0][n] : (v == 1 ? da[1][n] : da[2][n]);
#pragma unroll
                            for (int o = 0; o < 3; ++o)
#pragma unroll
                                for (int e = 0; e < 4; ++e) {
                                    const f32x4 u = *LP(const f32x4, L_GU + (v * 3 + o) * 4096 + e * 1024 + lane * 16);
                                    t[4 * e] += u[0]; t[4 * e + 1] += u[1]; t[4 * e + 2] += u[2]; t[4 * e + 3] += u[3];
                                }
                            if (n) acc1 = t; else acc0 = t;
                        }
                        if (n == 0) { lpost(C_DP + v, 2); lwait_all(C_DP, 4, 2, lane); }
                    }
                    if constexpr (!WO) {
                        if (v < 3) {
                            float* pt = (float*)(ws + WS_PT) + (size_t)(b * 3 + v) * 2048 + lane;
#pragma unroll
                            for (int e = 0; e < 16; ++e) { pt[e * 64] = acc0[e]; pt[1024 + e * 64] = acc1[e]; }
                        }
                    }
                } else {
#pragma unroll
                    for (int u = 0; u < 20; ++u) glds16(dvo + u * 8 * 1280, p.wo + (size_t)v * 160 * 1280 + dc * 128, myring + u * 1024);
                    if (v < 3) {
                        const float* pt = (const float*)(ws + WS_PT) + (size_t)(b * 3 + v) * 2048 + lane;
#pragma unroll
                        for (int e = 0; e < 16; ++e) { acc0[e] = pt[e * 64]; acc1[e] = pt[1024 + e * 64]; }
                    }
                }
                if constexpr (WO) { WAITV(0); lpost(C_DP + v, 4); }
                if (v < nm) {
                    const int m = mfirst + v;
                    const unsigned ba0 = L_PANEL + (4 * h + tq) * 128 + bq, ba1 = ba0 ^ 64;
                    if constexpr (WO) {
                        const char* xrow = p.x + (size_t)(m * 32 + r) * 1280 + dc * 128 + h * 16;
                        u32x4 xres[4];
#pragma unroll
                        for (int e = 0; e < 4; ++e) xres[e] = *(const u32x4*)(xrow + e * 32);
                        lwait_all(C_DP, 4, 4, lane);
                        const __amdgpu_buffer_rsrc_t ro = mkrsrc(ws + WS_OT + (size_t)m * 40960, 40960);
                        gu32* of_ = (gu32*)(ws + WS_OF) + m * 32 + (lane & 31);
                        int hf = 0;
                        _Pragma("clang loop unroll(disable)") for (int it = 0; it < 2; ++it) {
                            if constexpr (ATT) {
                                _Pragma("clang loop unroll(disable)") for (unsigned spins = 0; spins < (1u << 14); ++spins) {
                                    const unsigned ok = (unsigned)__ballot(__hip_atomic_load(of_, RLX_AGENT) == tag);
                                    const bool lo = (ok & 0xffffu) == 0xffffu, hi = (ok >> 16) == 0xffffu;
                                    if (it == 0) { if (lo || hi) { hf = lo ? 0 : 1; break; } }
                                    else if (hf ? hi : lo) break;
                                    TMO(spins, 1u << 14, 300, m);
                                    __builtin_amdgcn_s_sleep(3);
                                }
                                CFENCE();
                            }
                            if (it == 0) STAMP(10);
                            u32x4 of[20];
#pragma unroll
                            for (int s = 0; s < 20; ++s) of[s] = __builtin_amdgcn_raw_buffer_load_b128(ro, lane * 16 + s * 1024, hf * 20480, 16);
                            const unsigned a0 = ba0 + hf * 40960, a1 = ba1 + hf * 40960, l0 = a0 + (36 * h + 16) * 128, l1 = a1 + (36 * h + 16) * 128;
                            bf16x8 w0[2], w1[2];
                            w0[0] = ldstr2(a0, a0 + 1024); w1[0] = ldstr2(a1, a1 + 1024);
                            static_for<0, 20>([&](auto S) {
                                constexpr int s = S.value, n = s + 1, gg = n / 5, jj = n % 5;
                                const bf16x8 xf = __builtin_bit_cast(bf16x8, of[s]);
                                if constexpr (n < 20) {
                                    if constexpr (jj < 4) {
                                        constexpr unsigned o = (80 * gg + 20 * jj) * 128;
                                        w0[n & 1] = ldstr2(a0 + o, a0 + o + 1024); w1[n & 1] = ldstr2(a1 + o, a1 + o + 1024);
                                    } else {
                                        constexpr unsigned o = 80 * gg * 128;
                                        w0[n & 1] = ldstr2(l0 + o, l0 + o + 2560); w1[n & 1] = ldstr2(l1 + o, l1 + o + 2560);
                                    }
                                }
                                SBAR();
                                acc0 = MFMA(w0[s & 1], xf, acc0); acc1 = MFMA(w1[s & 1], xf, acc1);
                                SBAR();
                            });
                            hf ^= 1;
                        }
                        char* orow = p.out + (size_t)(m * 32 + r) * 1280 + dc * 128 + h * 16;
#pragma unroll
                        for (int n = 0; n < 2; ++n) {
                            const f32x16& a = n ? acc1 : acc0;
#pragma unroll
                            for (int qq = 0; qq < 2; ++qq) {
                                float lo[4], hi[4];
#pragma unroll
                                for (int e = 0; e < 4; ++e) {
                                    auto sw = __builtin_amdgcn_permlane32_swap(__float_as_uint(a[8 * qq + e]), __float_as_uint(a[8 * qq + 4 + e]), false, false);
                                    lo[e] = __uint_as_float(sw[0]); hi[e] = __uint_as_float(sw[1]);
                                }
                                const u32x4 xv = xres[2 * n + qq];
                                const u32x4 ov = {pk2(lo[0] + bflo(xv[0]), lo[1] + bfhi(xv[0])), pk2(lo[2] + bflo(xv[1]), lo[3] + bfhi(xv[1])),
                                                  pk2(hi[0] + bflo(xv[2]), hi[1] + bfhi(xv[2])), pk2(hi[2] + bflo(xv[3]), hi[3] + bfhi(xv[3]))};
                                *(u32x4*)(orow + (2 * n + qq) * 32) = ov;
                            }
                        }
                        STAMP(11);
                    }
                }
            }
        }
    }
}

template <int PH>
static void launch(const Params& p, hipStream_t stream) {
    (void)hipFuncSetAttribute((const void*)mega<PH>, hipFuncAttributeMaxDynamicSharedMemorySize, L_TOTAL);
    mega<PH><<<256, 512, L_TOTAL, stream>>>(p);
}

extern "C" void kernel_launch(void* const* d_in, const int* in_sizes, int n_in, void* d_out, int out_size, void* d_ws, size_t ws_size, hipStream_t stream) {
    Params p;
    p.x = (const char*)d_in[0]; p.wq = (const char*)d_in[1]; p.wk = (const char*)d_in[2]; p.wv = (const char*)d_in[3]; p.wo = (const char*)d_in[4];
    p.w1 = (const char*)d_in[6]; p.w3 = (const char*)d_in[7]; p.w2 = (const char*)d_in[8];
    p.out = (char*)d_out; p.ws = (char*)d_ws;
    launch<27>(p, stream);
}
```

```cpp
#include <hip/hip_runtime.h>
#include <hip/hip_bf16.h>
#include <stdint.h>

#define AS1 __attribute__((address_space(1)))
#define AS3 __attribute__((address_space(3)))
typedef __bf16 bf16x2 __attribute__((ext_vector_type(2)));
typedef __bf16 bf16x8 __attribute__((ext_vector_type(8)));
typedef short s16x4 __attribute__((ext_vector_type(4)));
typedef short s16x8 __attribute__((ext_vector_type(8)));
typedef float f32x2 __attribute__((ext_vector_type(2)));
typedef float f32x4 __attribute__((ext_vector_type(4)));
typedef float f32x16 __attribute__((ext_vector_type(16)));
typedef unsigned u32x2 __attribute__((ext_vector_type(2)));
typedef unsigned u32x4 __attribute__((ext_vector_type(4)));
typedef AS1 unsigned gu32;
typedef AS1 unsigned long long gu64;

#define ATT_PRIO 2
#define GEM_PRIO 0
#define GU_HI 0
#define DN_HI 0
#define MPRIO(mask, i) __builtin_amdgcn_s_setprio(((mask) >> (i)) & 1 ? 3 : 0)
#define GU_DELAY 8
#define GU_ROT 4
#define GU_GQ 5
#define OF_SLEEP 3
#define HF_SLEEP 6
#define SPLIT_MODE 0

constexpr int SEQ = 2048, DM = 640, FF = 1536;
constexpr unsigned WS_KF = 0;
constexpr unsigned WS_HF = 16384;
constexpr unsigned WS_OF = 32768;
constexpr unsigned WS_EP = 49152;
constexpr unsigned KVD = 5376;
constexpr unsigned KV_K1 = 2048, KV_V = 2560, KV_VT = 1408;
constexpr unsigned WS_KV = 65536;
constexpr unsigned WS_HT = WS_KV + 176128;
constexpr unsigned WS_OT = WS_HT + 64 * 96 * 1024;
constexpr unsigned WS_QT = WS_OT + 64 * 40 * 1024;
constexpr unsigned WS_PT = WS_QT + 64 * 32 * 2048;
constexpr unsigned WS_ST = WS_PT + 256 * 3 * 32 * 256;
constexpr unsigned L_PANEL = 0;
constexpr unsigned L_KV = 81920;
constexpr unsigned L_WQ = 81920;
constexpr unsigned L_GU = 124928;
constexpr unsigned GSL = 12320;
constexpr unsigned L_WKV = 124928;
constexpr unsigned L_MISC = 161920;
constexpr unsigned L_TOTAL = 163840;
constexpr unsigned CHUNK = 2 * KVD;
constexpr int NCHUNK = 16;
enum { C_AP = 0  , C_GP = 4  , C_DP = 8  , C_PP = 12  , C_WP = 16  , C_KVP = 20, C_KVFIN, C_KVRDY, C_KN, C_VM, C_N };

struct Params { const char* x; const char* wq; const char* wk; const char* wv; const char* wo; const char* w1; const char* w3; const char* w2; char* out; char* ws; };

#define SBAR() __builtin_amdgcn_sched_barrier(0)
#define PIN(x) asm volatile("" : "+v"(x))
#define CFENCE() asm volatile("" ::: "memory")
#define WAITV(n) asm volatile("s_waitcnt vmcnt(" #n ")" ::: "memory")
#define MFMA(a, b, c) __builtin_amdgcn_mfma_f32_32x32x16_bf16((a), (b), (c), 0, 0, 0)
#define RLX_AGENT __ATOMIC_RELAXED, __HIP_MEMORY_SCOPE_AGENT
#define RLX_WG __ATOMIC_RELAXED, __HIP_MEMORY_SCOPE_WORKGROUP

#define TMO(spins, lim, code, tgt) do {} while (0)
#define STAMP(id) do {} while (0)
#define STAMPC(id) do {} while (0)

#define LP(T, off) ((AS3 T*)(uintptr_t)(unsigned)(off))
__device__ __forceinline__ bf16x8 ldsf(unsigned off) { return *LP(const bf16x8, off); }
__device__ __forceinline__ s16x4 ldstr(unsigned off) { return __builtin_amdgcn_ds_read_tr16_b64_v4i16(LP(s16x4, off)); }
__device__ __forceinline__ bf16x8 ldstr2(unsigned off0, unsigned off1) {
    s16x4 a = ldstr(off0), b = ldstr(off1);
    return __builtin_bit_cast(bf16x8, __builtin_shufflevector(a, b, 0, 1, 2, 3, 4, 5, 6, 7));
}
__device__ __forceinline__ unsigned pk2(float lo, float hi) { bf16x2 v = {(__bf16)lo, (__bf16)hi}; return __builtin_bit_cast(unsigned, v); }
__device__ __forceinline__ float bflo(unsigned u) { return __uint_as_float(u << 16); }
__device__ __forceinline__ float bfhi(unsigned u) { return __uint_as_float(u & 0xffff0000u); }
__device__ __forceinline__ float rbf(float f) { return (float)(__bf16)f; }

__device__ __forceinline__ void glds16(unsigned voff, const char* sbase, unsigned ldsdst) {
    unsigned keep;
    asm volatile("s_mov_b32 %0, m0\n\ts_mov_b32 m0, %3\n\ts_nop 2\n\tglobal_load_lds_dwordx4 %1, %2\n\ts_mov_b32 m0, %0"
                 : "=&s"(keep) : "v"(voff), "s"(sbase), "s"(ldsdst) : "memory");
}
__device__ __forceinline__ void glds16_sc1(unsigned voff, const char* sbase, unsigned ldsdst) {
    unsigned keep;
    asm volatile("s_mov_b32 %0, m0\n\ts_mov_b32 m0, %3\n\ts_nop 2\n\tglobal_load_lds_dwordx4 %1, %2 sc1\n\ts_mov_b32 m0, %0"
                 : "=&s"(keep) : "v"(voff), "s"(sbase), "s"(ldsdst) : "memory");
}
__device__ __forceinline__ void glds16x3_sc1(unsigned voff, const char* sbase, unsigned ldsdst) {
    unsigned keep;
    asm volatile("s_mov_b32 %0, m0\n\ts_mov_b32 m0, %3\n\ts_nop 2\n\tglobal_load_lds_dwordx4 %1, %2 sc1\n\tglobal_load_lds_dwordx4 %1, %2 offset:1024 sc1\n\tglobal_load_lds_dwordx4 %1, %2 offset:2048 sc1\n\ts_mov_b32 m0, %0"
                 : "=&s"(keep) : "v"(voff), "s"(sbase), "s"(ldsdst) : "memory");
}
__device__ __forceinline__ void glds16x3v(unsigned v0, unsigned v1, unsigned v2, const char* sbase, unsigned ldsdst) {
    unsigned keep;
    asm volatile("s_mov_b32 %0, m0\n\ts_mov_b32 m0, %5\n\ts_nop 0\n\tglobal_load_lds_dwordx4 %1, %4\n\tglobal_load_lds_dwordx4 %2, %4 offset:1024\n\tglobal_load_lds_dwordx4 %3, %4 offset:2048\n\ts_mov_b32 m0, %0"
                 : "=&s"(keep) : "v"(v0), "v"(v1), "v"(v2), "s"(sbase), "s"(ldsdst) : "memory");
}
__device__ __forceinline__ void l2touch(const char* ga) {
    unsigned keep;
    asm volatile("s_mov_b32 %0, m0\n\ts_mov_b32 m0, %2\n\ts_nop 0\n\tglobal_load_lds_dword %1, off\n\ts_mov_b32 m0, %0" : "=&s"(keep) : "v"(ga), "s"(L_MISC + 1024) : "memory");
}
#define GLOAD16(dst, voff, sbase, imm) asm volatile("s_nop 4\n\tglobal_load_dwordx4 %0, %1, %2 offset:%3" : "=v"(dst) : "v"(voff), "s"(sbase), "i"(imm) : "memory")

__device__ __forceinline__ AS3 unsigned* lcnt(int c) { return LP(unsigned, L_MISC) + c; }
__device__ __forceinline__ void larrive(int c, int lane) { CFENCE(); if (lane == 0) __hip_atomic_fetch_add(lcnt(c), 1u, RLX_WG); CFENCE(); }
__device__ __forceinline__ void lset(int c, unsigned v, int lane) { CFENCE(); if (lane == 0) __hip_atomic_store(lcnt(c), v, RLX_WG); CFENCE(); }
__device__ __forceinline__ void lwait(int c, unsigned target) {
    CFENCE();
    _Pragma("clang loop unroll(disable)") for (unsigned spins = 0; spins < (1u << 22); ++spins) {
        if ((unsigned)__builtin_amdgcn_readfirstlane(__hip_atomic_load(lcnt(c), RLX_WG)) >= target) break;
        TMO(spins, 1u << 16, 100 + c, target);
        __builtin_amdgcn_s_sleep(1);
    }
    CFENCE();
}
__device__ __forceinline__ void lwait_all(int c, int n, unsigned target, int lane) {
    CFENCE();
    AS3 unsigned* q = lcnt(c) + (lane < n ? lane : 0);
    _Pragma("clang loop unroll(disable)") for (unsigned spins = 0; spins < (1u << 16); ++spins) {
        if (__all(__hip_atomic_load(q, RLX_WG) >= target)) break;
        TMO(spins, 1u << 16, 200 + c, target);
        __builtin_amdgcn_s_sleep(1);
    }
    CFENCE();
}
__device__ __forceinline__ void lpost(int c, unsigned v) { CFENCE(); *(volatile AS3 unsigned*)lcnt(c) = v; CFENCE(); }
__device__ __forceinline__ void linc(unsigned vaddr, unsigned vone) {
    asm volatile("s_mov_b64 exec, 1\n\tds_add_u32 %0, %1\n\ts_mov_b64 exec, -1" :: "v"(vaddr), "v"(vone) : "memory");
}
__device__ __forceinline__ unsigned lpeek(int c, int lane) { return *((volatile AS3 unsigned*)lcnt(c) + (lane & 3)); }
__device__ __forceinline__ void lcheck(unsigned pk, int c, unsigned target, int lane) {
    if (__builtin_expect(__any(pk < target), 0)) lwait_all(c, 4, target, lane);
    CFENCE();
}
__device__ __forceinline__ void gwait(const char* ws, unsigned off, int n, unsigned tag, int lane) {
    gu32* f = (gu32*)(ws + off) + (lane < n ? lane : 0);
    _Pragma("clang loop unroll(disable)") for (unsigned spins = 0; spins < (1u << 14); ++spins) {
        unsigned v = __hip_atomic_load(f, RLX_AGENT);
        if (__all(v == tag)) break;
        TMO(spins, 1u << 14, 300, off);
        __builtin_amdgcn_s_sleep(6);
    }
    CFENCE();
}
__device__ __forceinline__ float gwait64(const char* ws, unsigned off, unsigned tag, int lane) {
    gu64* f = (gu64*)(ws + off) + lane;
    unsigned long long v = 0;
    _Pragma("clang loop unroll(disable)") for (unsigned spins = 0; spins < (1u << 14); ++spins) {
        v = __hip_atomic_load(f, RLX_AGENT);
        if (__all((unsigned)(v >> 32) == tag)) break;
        __builtin_amdgcn_s_sleep(6);
    }
    CFENCE();
    return __uint_as_float((unsigned)v);
}
__device__ __forceinline__ __amdgpu_buffer_rsrc_t mkrsrc(const char* base, unsigned bytes) {
    return __builtin_amdgcn_make_buffer_rsrc((void*)base, 0, bytes, 0x00020000);
}
__device__ __forceinline__ float dpp_sum16(float v) {
    v += __int_as_float(__builtin_amdgcn_update_dpp(0, __float_as_int(v), 0x128, 0xf, 0xf, false));
    v += __int_as_float(__builtin_amdgcn_update_dpp(0, __float_as_int(v), 0x124, 0xf, 0xf, false));
    v += __int_as_float(__builtin_amdgcn_update_dpp(0, __float_as_int(v), 0x122, 0xf, 0xf, false));
    v += __int_as_float(__builtin_amdgcn_update_dpp(0, __float_as_int(v), 0x121, 0xf, 0xf, false));
    return v;
}
__device__ __forceinline__ float wave_max(float v) {
    int x = __float_as_int(v);
    x = max(x, __builtin_amdgcn_update_dpp(0, x, 0x128, 0xf, 0xf, false)); x = max(x, __builtin_amdgcn_update_dpp(0, x, 0x124, 0xf, 0xf, false));
    x = max(x, __builtin_amdgcn_update_dpp(0, x, 0x122, 0xf, 0xf, false)); x = max(x, __builtin_amdgcn_update_dpp(0, x, 0x121, 0xf, 0xf, false));
    const int a = max(__builtin_amdgcn_readlane(x, 0), __builtin_amdgcn_readlane(x, 16)), b = max(__builtin_amdgcn_readlane(x, 32), __builtin_amdgcn_readlane(x, 48));
    return __int_as_float(max(a, b));
}
__device__ __forceinline__ bf16x8 pack8(const f32x16& a, int b, float sc) {
    u32x4 r = {pk2(a[b] * sc, a[b + 1] * sc), pk2(a[b + 2] * sc, a[b + 3] * sc), pk2(a[b + 4] * sc, a[b + 5] * sc), pk2(a[b + 6] * sc, a[b + 7] * sc)};
    return __builtin_bit_cast(bf16x8, r);
}

template <int I> struct IC { static constexpr int value = I; };
template <int B, int E, class F> __device__ __forceinline__ void static_for(F&& f) { if constexpr (B < E) { f(IC<B>{}); static_for<B + 1, E>(f); } }
#define WAITVN(n) asm volatile("s_waitcnt vmcnt(%0)" :: "i"(n) : "memory")
__device__ __forceinline__ void GLOAD16S(u32x4& dst, unsigned voff, const char* sbase) { asm volatile("global_load_dwordx4 %0, %1, %2 sc1" : "=v"(dst) : "v"(voff), "s"(sbase) : "memory"); }
__device__ __forceinline__ void GLOAD16N(u32x4& dst, unsigned voff, const char* sbase) { asm volatile("global_load_dwordx4 %0, %1, %2" : "=v"(dst) : "v"(voff), "s"(sbase) : "memory"); }
__device__ __forceinline__ void GLOAD16N_IF(u32x4& dst, unsigned voff, const char* sbase, int cond) {
    asm volatile("s_cmp_eq_u32 %3, 0\n\ts_cbranch_scc1 1f\n\tglobal_load_dwordx4 %0, %1, %2\n1:" : "=v"(dst) : "v"(voff), "s"(sbase), "s"(cond) : "memory", "scc"); }
template <int N> __device__ __forceinline__ void waitv5(u32x4& a, u32x4& b, u32x4& c, u32x4& d, u32x4& e) { asm volatile("s_waitcnt vmcnt(%5)" : "+v"(a), "+v"(b), "+v"(c), "+v"(d), "+v"(e) : "i"(N) : "memory"); }
__device__ __forceinline__ void gload8x2_sc1(unsigned long long& a, unsigned long long& b, unsigned voff, const char* sbase) {
    asm volatile("global_load_dwordx2 %0, %2, %3 sc1\n\tglobal_load_dwordx2 %1, %2, %3 offset:512 sc1" : "=&v"(a), "=&v"(b) : "v"(voff), "s"(sbase) : "memory"); }
__device__ __forceinline__ void waitv8x2(unsigned long long& a, unsigned long long& b) { asm volatile("s_waitcnt vmcnt(0)" : "+v"(a), "+v"(b) :: "memory"); }
__device__ __forceinline__ void PIN2(u32x4& a, u32x4& b) { asm volatile("" : "+v"(a), "+v"(b) :: "memory"); }
__device__ __forceinline__ u32x4 scale8(unsigned x0, unsigned x1, unsigned x2, unsigned x3, unsigned clo, unsigned chi, float c) {
    unsigned o0, o1, o2, o3; float t0, t1, t2, t3, t4, t5, t6, t7;
    asm("v_dot2_f32_bf16 %4, %12, %16, %18\n\tv_dot2_f32_bf16 %5, %12, %17, %18\n\tv_dot2_f32_bf16 %6, %13, %16, %18\n\tv_dot2_f32_bf16 %7, %13, %17, %18\n\t"
        "v_dot2_f32_bf16 %8, %14, %16, %18\n\tv_dot2_f32_bf16 %9, %14, %17, %18\n\tv_dot2_f32_bf16 %10, %15, %16, %18\n\tv_dot2_f32_bf16 %11, %15, %17, %18\n\t"
        "v_cvt_pk_bf16_f32 %0, %4, %5\n\tv_cvt_pk_bf16_f32 %1, %6, %7\n\tv_cvt_pk_bf16_f32 %2, %8, %9\n\tv_cvt_pk_bf16_f32 %3, %10, %11"
        : "=&v"(o0), "=&v"(o1), "=&v"(o2), "=&v"(o3), "=&v"(t0), "=&v"(t1), "=&v"(t2), "=&v"(t3), "=&v"(t4), "=&v"(t5), "=&v"(t6), "=&v"(t7)
        : "v"(x0), "v"(x1), "v"(x2), "v"(x3), "v"(clo), "v"(chi), "v"(c));
    return u32x4{o0, o1, o2, o3};
}
template <int N> __device__ __forceinline__ void waitv3(u32x4& a, u32x4& b, u32x4& c) { asm volatile("s_waitcnt vmcnt(%3)" : "+v"(a), "+v"(b), "+v"(c) : "i"(N) : "memory"); }

constexpr float QSCALE = 0.22360679774997896f * 1.4426950408889634f;
constexpr float BOUND_MAX = 50.f;

struct AttSync { int lane; const char* kv; unsigned dvoff; unsigned wo; int w; };
__device__ __forceinline__ unsigned kvslot(int c) { return L_KV + ((c + 2) & 3) * CHUNK; }
__device__ __forceinline__ void att_dma(const AttSync& a, int c) { glds16x3_sc1(a.dvoff, a.kv + (size_t)c * CHUNK, kvslot(c) + a.wo); }
constexpr unsigned AP0 = 16;
#define RING_X(c) do { WAITV(3); lpost(C_AP + as.w, AP0 + (c) + 1); } while (0)
#define RP_A
#define RP_B
#define RING_Y(c, pk) do { RP_A lcheck(pk, C_AP, AP0 + (c) + 1, lane); const int cn_ = (c) + 3 < NCHUNK ? (c) + 3 : NCHUNK - 1;   \
                           glds16x3_sc1(as.dvoff, as.kv + (size_t)cn_ * CHUNK, kvslot((c) + 3) + as.wo); RP_B } while (0)

__device__ __forceinline__ void att_fast(const AttSync& as, bf16x8 Q0a, bf16x8 Q1a0, bf16x8 Q1a1, bf16x8 Q0b, bf16x8 Q1b0, bf16x8 Q1b1, f32x16& Oa, f32x16& Ob) {
    const int lane = as.lane, r = lane & 31, h = lane >> 5;
    const unsigned ak0 = lane * 16, ak1 = h ? KV_V + 20 * 16 : KV_K1 + r * 16;
    const unsigned av = KV_V + h * 352 + (r <= 20 ? r : 21) * 16;
    const f32x16 zero = {};
    f32x16 Sa, Sb = {};
    bf16x8 K0[2], K1[2], V0[2], V1[2];
    u32x4 Pa0, Pa1, Pb0 = {}, Pb1 = {};
    unsigned pk = 0;
    unsigned long long rp0 = 0, rp1 = 0;
    V0[1] = bf16x8{}; V1[1] = bf16x8{};
    unsigned vk0 = L_KV + ak0, vk1 = L_KV + ak1, vv = L_KV + av;
    PIN(vk0); PIN(vk1); PIN(vv);
    K0[0] = ldsf(vk0 + 2 * CHUNK); K1[0] = ldsf(vk1 + 2 * CHUNK); K0[1] = ldsf(vk0 + 2 * CHUNK + 1024); V1[0] = ldsf(vv + 2 * CHUNK + 704); V0[0] = ldsf(vv + 2 * CHUNK);
    Sa = MFMA(K0[0], Q0a, zero); Sa = MFMA(K1[0], Q1a0, Sa);
#define EX4(S, i) do { S[i] = __builtin_amdgcn_exp2f(S[i]); S[i + 1] = __builtin_amdgcn_exp2f(S[i + 1]); S[i + 2] = __builtin_amdgcn_exp2f(S[i + 2]); S[i + 3] = __builtin_amdgcn_exp2f(S[i + 3]); } while (0)
#define GAP(MF, LD, T, k, PW, j, S, i) do { MF; SBAR(); LD; PW[j] = pk2(T[k], T[k + 1]); PW[j + 1] = pk2(T[k + 2], T[k + 3]); PIN(PW); SBAR(); EX4(S, i); PIN(S); SBAR(); } while (0)
#define BC(x) __builtin_bit_cast(bf16x8, x)
#define STEP(e, d, QB1, QA1N, KN, L3, LK0, LK1, LV) do { \
        GAP(Ob = MFMA(V0[e ^ 1], BC(Pb0), Ob), (void)0,                  Sb, 12, Pb1, 2, Sa, 0); \
        GAP(Sb = MFMA(K0[e], Q0b, zero),       (void)0,                  Sa, 0, Pa0, 0, Sa, 4); \
        GAP(Sb = MFMA(K1[d], QB1, Sb),         L3,                       Sa, 4, Pa0, 2, Sa, 8); \
        GAP(Ob = MFMA(V1[e ^ 1], BC(Pb1), Ob), K0[e] = ldsf(LK0),        Sa, 8, Pa1, 0, Sa, 12); \
        GAP(Oa = MFMA(V0[e], BC(Pa0), Oa),     LK1,                      Sa, 12, Pa1, 2, Sb, 0); \
        GAP(Sa = MFMA(K0[e ^ 1], Q0a, zero),   V1[e ^ 1] = ldsf((LV) + 704), Sb, 0, Pb0, 0, Sb, 4); \
        GAP(Sa = MFMA(K1[KN], QA1N, Sa),       V0[e ^ 1] = ldsf(LV),     Sb, 4, Pb0, 2, Sb, 8); \
        GAP(Oa = MFMA(V1[e], BC(Pa1), Oa),     (void)0,                  Sb, 8, Pb1, 0, Sb, 12); \
    } while (0)
#define CHUNK4(c, B, N) do { \
        STEP(0, 0, Q1b0, Q1a1, 0, (void)0, vk0 + (B) + KVD, K1[1] = ldsf(vk1 + (B) + KVD), vv + (B) + KV_VT); \
        RING_X(c); \
        STEP(1, 0, Q1b1, Q1a0, 1, pk = lpeek(C_AP, lane), vk0 + (B) + KVD + 1024, (void)0, vv + (B) + KVD); \
        RING_Y(c, pk); \
        STEP(0, 1, Q1b0, Q1a1, 1, (void)0, vk0 + (N), K1[0] = ldsf(vk1 + (N)), vv + (B) + KVD + KV_VT); \
        STEP(1, 1, Q1b1, Q1a0, 0, (void)0, vk0 + (N) + 1024, (void)0, vv + (N)); \
    } while (0)
    for (int c = 0; c < NCHUNK; c += 4) {
        CHUNK4(c, 2 * CHUNK, 3 * CHUNK); CHUNK4(c + 1, 3 * CHUNK, 0); CHUNK4(c + 2, 0, CHUNK); CHUNK4(c + 3, CHUNK, 2 * CHUNK);
    }
#undef CHUNK4
    Pb1[2] = pk2(Sb[12], Sb[13]); Pb1[3] = pk2(Sb[14], Sb[15]);
    Ob = MFMA(V0[1], BC(Pb0), Ob); Ob = MFMA(V1[1], BC(Pb1), Ob);
#undef STEP
#undef GAP
#undef EX4
}

__device__ __forceinline__ void att_safe(const AttSync& as, bf16x8 Q0a, bf16x8 Q1a0, bf16x8 Q1a1, bf16x8 Q0b, bf16x8 Q1b0, bf16x8 Q1b1, f32x16& Oa, f32x16& Ob) {
    const int lane = as.lane, r = lane & 31, h = lane >> 5;
    const unsigned ak0 = lane * 16, ak1 = h ? KV_V + 20 * 16 : KV_K1 + r * 16;
    const unsigned av = KV_V + h * 352 + (r <= 20 ? r : 21) * 16;
    const f32x16 zero = {};
    float ma = -INFINITY, mb = -INFINITY;
    unsigned long long rp0 = 0, rp1 = 0;
    for (int c = 0; c < NCHUNK; ++c) {
        const unsigned b0 = kvslot(c);
        for (int t = 0; t < 4; ++t) {
            if (t == 1) RING_X(c);
            if (t == 2) RING_Y(c, 0u);
            const unsigned base = b0 + (t >> 1) * KVD; const int e = t & 1;
            bf16x8 K0 = ldsf(base + ak0 + e * 1024), K1 = ldsf(base + ak1), V0 = ldsf(base + av + e * KV_VT), V1 = ldsf(base + av + e * KV_VT + 704);
#pragma unroll
            for (int q = 0; q < 2; ++q) {
                f32x16 S = MFMA(K0, q ? Q0b : Q0a, zero); S = MFMA(K1, q ? (e ? Q1b1 : Q1b0) : (e ? Q1a1 : Q1a0), S);
                float mx = S[0];
#pragma unroll
                for (int i = 1; i < 16; ++i) mx = fmaxf(mx, S[i]);
                mx = fmaxf(mx, __shfl_xor(mx, 32));
                float& m = q ? mb : ma; f32x16& O = q ? Ob : Oa;
                const float mn = fmaxf(m, mx);
                const float al = __builtin_amdgcn_exp2f(m - mn);
                m = mn;
#pragma unroll
                for (int i = 0; i < 16; ++i) { O[i] *= al; S[i] = __builtin_amdgcn_exp2f(S[i] - mn); }
                O = MFMA(V0, pack8(S, 0, 1.f), O); O = MFMA(V1, pack8(S, 8, 1.f), O);
            }
        }
    }
}

__device__ __forceinline__ void xload4(u32x4 (&x)[5], const char* xb, unsigned vo) {
    asm volatile("global_load_dwordx4 %0, %5, %6\n\tglobal_load_dwordx4 %1, %5, %6 offset:256\n\tglobal_load_dwordx4 %2, %5, %6 offset:512\n\t"
                 "global_load_dwordx4 %3, %5, %6 offset:768\n\tglobal_load_dwordx4 %4, %5, %6 offset:1024"
                 : "=&v"(x[0]), "=&v"(x[1]), "=&v"(x[2]), "=&v"(x[3]), "=&v"(x[4]) : "v"(vo), "s"(xb) : "memory");
}
__device__ __forceinline__ void ln_rows(const u32x4 (&xr)[5], int row0, int lane) {
    f32x2 xv[20];
    f32x2 s0 = {0.f, 0.f}, s1 = {0.f, 0.f}, q0 = {0.f, 0.f}, q1 = {0.f, 0.f};
#pragma unroll
    for (int i = 0; i < 20; i += 2) {
        const unsigned a = xr[i >> 2][i & 3], b = xr[i >> 2][(i & 3) + 1];
        xv[i] = f32x2{bflo(a), bfhi(a)}; xv[i + 1] = f32x2{bflo(b), bfhi(b)};
        s0 += xv[i]; q0 = __builtin_elementwise_fma(xv[i], xv[i], q0);
        s1 += xv[i + 1]; q1 = __builtin_elementwise_fma(xv[i + 1], xv[i + 1], q1);
    }
    s0 += s1; q0 += q1;
    const float s = dpp_sum16(s0[0] + s0[1]), ss = dpp_sum16(q0[0] + q0[1]);
    const float mean = s * (1.f / 640.f);
    const float var = fmaxf(ss * (1.f / 640.f) - mean * mean, 0.f);
    const float rs = __builtin_amdgcn_rcpf(rbf(__builtin_amdgcn_sqrtf(rbf(rbf(var) + 1e-5f)))), mo = -rbf(mean) * rs;
    const f32x2 rs2 = {rs, rs}, mo2 = {mo, mo};
    const int row = row0 + (lane >> 4), c = lane & 15;
    const unsigned wa = L_PANEL + (row >> 5) * 40960 + (c >> 1) * 1024 + (c & 1) * 512 + (((row & 31) ^ (c & 7)) << 4);
#pragma unroll
    for (int u = 0; u < 5; ++u) {
        u32x4 o;
#pragma unroll
        for (int e = 0; e < 4; ++e) { const f32x2 y = __builtin_elementwise_fma(xv[4 * u + e], rs2, mo2); o[e] = pk2(y[0], y[1]); }
        *LP(u32x4, wa + u * 8192) = o;
    }
}
#define BLOCK_BARRIER() asm volatile("s_waitcnt lgkmcnt(0)\n\ts_barrier" ::: "memory")

template <int PH>
__global__ void __launch_bounds__(512, 2) mega(Params p) {
    constexpr bool PRO = PH & 1, ATT = PH & 2, DN = PH & 8, WO = PH & 16;
    const int w = __builtin_amdgcn_readfirstlane(threadIdx.x >> 6);
    const int b = blockIdx.x, xc = b & 7, j = b >> 3;
    const int ti = (xc >> 1) * 8 + (j >> 2), g = (xc & 1) * 4 + (j & 3);
    unsigned tag = 1u, epv = 0u;
    constexpr bool FUSED = PH == 27;
    if constexpr (FUSED) asm volatile("global_load_dword %0, %1, %2 sc1" : "=v"(epv) : "v"(0u), "s"(p.ws + WS_EP) : "memory");
#define GET_TAG() do { if constexpr (FUSED) { asm volatile("s_nop 0\n\tv_readfirstlane_b32 %0, %1\n\ts_nop 3" : "=s"(tag) : "v"(epv) : "memory"); tag += 0x9E3779B1u; } } while (0)
    char* ws = p.ws;
    STAMP(0);
    {
        constexpr unsigned CODE_PF = 61440;
        const char* pc; asm volatile("s_getpc_b64 %0" : "=s"(pc));
        const unsigned line = (unsigned)(j * 8) * 64u + threadIdx.x;
        unsigned keep;
        if (line * 128u < CODE_PF)
            asm volatile("s_mov_b32 %0, m0\n\ts_mov_b32 m0, %3\n\ts_nop 2\n\tglobal_load_lds_dword %1, %2\n\ts_mov_b32 m0, %0" : "=&s"(keep) : "v"(line * 128u), "s"(pc), "s"(L_MISC + 1024) : "memory");
    }
    if (threadIdx.x < C_N) *lcnt(threadIdx.x) = (threadIdx.x == C_WP || threadIdx.x == C_WP + 1 || threadIdx.x == C_PP || threadIdx.x == C_PP + 1) ? 100u : 0u;
    const bool haskv = (g >> 2) == (ti & 1);
    const char* wqb = p.wq + g * 160;
    const int kmt = g & 1;
    const char* xb = p.x + (size_t)ti * 64 * 1280;
#define LANE_CONSTS() int lane = __builtin_amdgcn_mbcnt_hi(~0u, __builtin_amdgcn_mbcnt_lo(~0u, 0u)); PIN(lane); \
    const int r = lane & 31, h = lane >> 5, tq = (lane & 15) >> 2, tp = lane & 3, tb = (lane >> 4) & 1; (void)r; (void)h; (void)tq; (void)tp; (void)tb

    if (w < 4) {
        LANE_CONSTS();
        const unsigned xvo = (lane >> 4) * 1280 + (lane & 15) * 16;
        bf16x8 Q0a, Q0b; u32x2 q1a, q1b;
        const int head = 4 * g + w;
        const AttSync as{lane, ws + WS_KV, (unsigned)((w < 3 ? w * 3072 : CHUNK - 3072) + lane * 16), (unsigned)(w < 3 ? w * 3072 : CHUNK - 3072), w};
        __builtin_amdgcn_s_setprio(3);
        unsigned long long fk = 0, fv = 0; bool early = false;
        if constexpr (PRO) {
            u32x4 x0[5], x1[5], x2[5], x3[5];
            const int ra = kmt * 32 + w * 8, rb = (kmt ^ 1) * 32 + w * 8;
            xload4(x0, xb + (size_t)ra * 1280, xvo); xload4(x1, xb + (size_t)(ra + 4) * 1280, xvo);
            xload4(x2, xb + (size_t)rb * 1280, xvo); xload4(x3, xb + (size_t)(rb + 4) * 1280, xvo);
            BLOCK_BARRIER();
            waitv5<15>(x0[0], x0[1], x0[2], x0[3], x0[4]);
            GET_TAG();
            STAMP(1);
            ln_rows(x0, ra, lane);
            waitv5<10>(x1[0], x1[1], x1[2], x1[3], x1[4]);
            ln_rows(x1, ra + 4, lane);
            lpost(C_AP + w, 1);
            waitv5<5>(x2[0], x2[1], x2[2], x2[3], x2[4]);
            ln_rows(x2, rb, lane);
            waitv5<0>(x3[0], x3[1], x3[2], x3[3], x3[4]);
            ln_rows(x3, rb + 4, lane);
            lpost(C_AP + w, 2);
            unsigned pa[4];
#pragma unroll
            for (int s = 0; s < 4; ++s) pa[s] = L_PANEL + h * 512 + ((r ^ ((2 * s + h) & 7)) << 4);
            f32x16 qa = {}, qb = {};
            const unsigned wa = L_WQ + (8 * h + tq) * 160 + w * 40 + tb * 32 + (tb ? 0 : tp * 8);
            lwait_all(C_AP, 4, 2, lane); lwait_all(C_PP, 4, 1, lane);
            STAMP(2);
            bf16x8 fw[2][4], fa[2][4], fb[2][4];
#pragma unroll
            for (int s4 = 0; s4 < 4; ++s4) { fw[0][s4] = ldstr2(wa + s4 * 2560, wa + s4 * 2560 + 640); fa[0][s4] = ldsf(pa[s4] + s4 * 1024); fb[0][s4] = ldsf(pa[s4] + 40960 + s4 * 1024); }
            unsigned pk = 0;
            static_for<0, 10>([&](auto S) {
                constexpr int st = S.value, nx = st + 1;
                if constexpr (st == 3 || st == 5 || st == 7) lcheck(pk, C_WP, (st - 1) / 2, lane);
                if constexpr (st == 6 && ATT) gload8x2_sc1(fk, fv, lane * 8, ws + WS_KF + xc * 1024);
                if constexpr (st == 8 && ATT) {
                    waitv8x2(fk, fv);
                    early = __all((unsigned)(fk >> 32) == tag && (unsigned)(fv >> 32) == tag);
                    if (early) { lwait_all(C_AP, 4, 6, lane); att_dma(as, 0); att_dma(as, 1); }
                    STAMP(8);
                }
#pragma unroll
                for (int s4 = 0; s4 < 4; ++s4) {
                    qa = MFMA(fw[st & 1][s4], fa[st & 1][s4], qa); SBAR();
                    if constexpr (nx < 10) { const unsigned o = wa + (nx & 3) * 10240 + s4 * 2560; fw[nx & 1][s4] = ldstr2(o, o + 640); SBAR(); }
                    qb = MFMA(fw[st & 1][s4], fb[st & 1][s4], qb); SBAR();
                    if constexpr (nx < 10) { fa[nx & 1][s4] = ldsf(pa[s4] + (nx * 4 + s4) * 1024); fb[nx & 1][s4] = ldsf(pa[s4] + 40960 + (nx * 4 + s4) * 1024); SBAR(); }
                    if (s4 == 2 && (st == 2 || st == 4 || st == 6)) { pk = lpeek(C_WP, lane); SBAR(); }
                }
                if constexpr (!(st & 1) && st <= 6) lpost(C_AP + w, 3 + st / 2);
            });
            lpost(C_AP + w, 8);
            Q0a = pack8(qa, 0, QSCALE); Q0b = pack8(qb, 0, QSCALE);
            q1a = u32x2{pk2(qa[8] * QSCALE, qa[9] * QSCALE), pk2(qa[10] * QSCALE, qa[11] * QSCALE)};
            q1b = u32x2{pk2(qb[8] * QSCALE, qb[9] * QSCALE), pk2(qb[10] * QSCALE, qb[11] * QSCALE)};
            if (h) { q1a = u32x2{0u, 0u}; q1b = u32x2{0u, 0u}; }
            if constexpr (!ATT) {
                char* qt = ws + WS_QT + (size_t)((2 * ti) * 32 + head) * 2048 + lane * 16;
                *(bf16x8*)qt = Q0a; *(u32x2*)(qt + 1024) = q1a; *(bf16x8*)(qt + 65536) = Q0b; *(u32x2*)(qt + 65536 + 1024) = q1b;
            }
            STAMP(3);
        } else {
            BLOCK_BARRIER();
            if constexpr (ATT) {
                const char* qt = ws + WS_QT + (size_t)((2 * ti) * 32 + head) * 2048 + lane * 16;
                Q0a = *(const bf16x8*)qt; q1a = *(const u32x2*)(qt + 1024); Q0b = *(const bf16x8*)(qt + 65536); q1b = *(const u32x2*)(qt + 65536 + 1024);
            }
        }
        if constexpr (ATT) {
            float na = 0.f, nb = 0.f;
            {
                const u32x4 a0 = __builtin_bit_cast(u32x4, Q0a), b0 = __builtin_bit_cast(u32x4, Q0b);
#pragma unroll
                for (int e = 0; e < 4; ++e) { na += bflo(a0[e]) * bflo(a0[e]) + bfhi(a0[e]) * bfhi(a0[e]); nb += bflo(b0[e]) * bflo(b0[e]) + bfhi(b0[e]) * bfhi(b0[e]); }
#pragma unroll
                for (int e = 0; e < 2; ++e) { na += bflo(q1a[e]) * bflo(q1a[e]) + bfhi(q1a[e]) * bfhi(q1a[e]); nb += bflo(q1b[e]) * bflo(q1b[e]) + bfhi(q1b[e]) * bfhi(q1b[e]); }
                na += __shfl_xor(na, 32); nb += __shfl_xor(nb, 32);
            }
            if (!early) {
                gu64* f = (gu64*)(ws + WS_KF + xc * 1024) + lane;
                _Pragma("clang loop unroll(disable)") for (unsigned spins = 0; spins < (1u << 14); ++spins) {
                    fk = __hip_atomic_load(f, RLX_AGENT); fv = __hip_atomic_load(f + 64, RLX_AGENT);
                    if (__all((unsigned)(fk >> 32) == tag && (unsigned)(fv >> 32) == tag)) break;
                    TMO(spins, 1u << 14, 301, tag);
                    __builtin_amdgcn_s_sleep(4);
                }
                if constexpr (PRO) lwait_all(C_AP, 4, 6, lane);
                att_dma(as, 0); att_dma(as, 1);
            }
            STAMP(4);
            if constexpr (PRO) lwait_all(C_AP, 4, 8, lane);
            att_dma(as, 2);
            const float kn2 = wave_max(__uint_as_float((unsigned)fk)), vmx = wave_max(__uint_as_float((unsigned)fv));
            const float Ma = __builtin_sqrtf(na * kn2) * 1.01f, Mb = __builtin_sqrtf(nb * kn2) * 1.01f;
            const bool fast = __all(Ma <= BOUND_MAX && Mb <= BOUND_MAX && vmx <= 1e30f);
            const unsigned sa = (fast && h) ? pk2(-Ma, 0.f) : q1a[0], sb = (fast && h) ? pk2(-Mb, 0.f) : q1b[0];
            const u32x4 a0 = {sa, q1a[1], 0u, 0u}, a1 = {h ? sa : 0u, 0u, q1a[0], q1a[1]}, b0 = {sb, q1b[1], 0u, 0u}, b1 = {h ? sb : 0u, 0u, q1b[0], q1b[1]};
            f32x16 Oa = {}, Ob = {};
            WAITV(6); lpost(C_AP + w, AP0); lwait_all(C_AP, 4, AP0, lane);
            __builtin_amdgcn_s_setprio(ATT_PRIO);
            STAMP(5); STAMPC(12);
            if (fast) att_fast(as, Q0a, BC(a0), BC(a1), Q0b, BC(b0), BC(b1), Oa, Ob);
            else att_safe(as, Q0a, BC(a0), BC(a1), Q0b, BC(b0), BC(b1), Oa, Ob);
            STAMP(6); STAMPC(13);
            const __amdgpu_buffer_rsrc_t ro = mkrsrc(ws + WS_OT, 64 * 40 * 1024);
#pragma unroll
            for (int q = 0; q < 2; ++q) {
                const f32x16& O = q ? Ob : Oa;
                const float inv = 1.f / __shfl(O[8], r + 32);
                const unsigned base = ((2 * ti + q) * 40 + 5 * g) * 1024;
                __builtin_amdgcn_raw_buffer_store_b128(__builtin_bit_cast(u32x4, pack8(O, 0, inv)), ro, base + w * 1024 + lane * 16, 0, 16);
                if (!h) {
                    u32x2 t = {pk2(O[8] * inv, O[9] * inv), pk2(O[10] * inv, O[11] * inv)};
                    __builtin_amdgcn_raw_buffer_store_b64(t, ro, base + 4096 + ((w >> 1) * 32 + r) * 16 + (w & 1) * 8, 0, 16);
                }
            }
            if constexpr (WO) {
                WAITV(0);
                if (lane < 2) __hip_atomic_store((gu32*)(ws + WS_OF) + (2 * ti + lane) * 32 + head, tag, RLX_AGENT);
            }
            STAMP(7);
        }
    } else {
        LANE_CONSTS();
        const unsigned xvo = (lane >> 4) * 1280 + (lane & 15) * 16;
        const int v = w - 4;
        __builtin_amdgcn_s_setprio(3);
        if constexpr (PRO) {
            const int kvm = 2 * ti + (g & 1); const bool isV = (g >> 1) & 1;
            const char* wkvb = isV ? p.wv : p.wk;
            if (haskv && v < 2) {
#pragma unroll
                for (int n = 0; n < 13; ++n) {
                    const unsigned o = v * 12800 + (n < 12 ? n * 1024 : 11776);
                    glds16(o + lane * 16, wkvb, L_WKV + o);
                }
            }
            BLOCK_BARRIER();
            if (v >= 2) {
                const int v2 = v - 2;
                unsigned wo5[5];
#pragma unroll
                for (int i = 0; i < 5; ++i) { const unsigned c = (v2 + 2 * i) * 64 + lane, row = c / 10u, col = c - row * 10u; wo5[i] = row * 1280 + col * 16; }
#pragma unroll
                for (int n = 0; n < 20; ++n) glds16(wo5[n % 5], wqb + (n / 5) * 64 * 1280, L_WQ + (v2 + 2 * n) * 1024);
                u32x4 wst[3][10];
                auto wst_issue = [&](auto I) {
                    constexpr int rr = I.value / 10, i = I.value % 10;
                    GLOAD16N(wst[rr][i], wo5[i % 5], wqb + (4 + 2 * rr + i / 5) * 64 * 1280);
                };
                static_for<0, 10>(wst_issue);
                WAITV(10); lpost(C_PP + v, 1);
                GET_TAG();
                STAMP(2);
                static_for<10, 30>(wst_issue);
                static_for<0, 3>([&](auto R) {
                    constexpr int rr = R.value;
                    waitv5<20 - 10 * rr>(wst[rr][0], wst[rr][1], wst[rr][2], wst[rr][3], wst[rr][4]); waitv5<63>(wst[rr][5], wst[rr][6], wst[rr][7], wst[rr][8], wst[rr][9]);
                    lwait_all(C_AP, 4, 3 + rr, lane);
#pragma unroll
                    for (int i = 0; i < 10; ++i) *LP(u32x4, L_WQ + (rr & 1) * 20480 + (v2 + 2 * i) * 1024 + lane * 16) = wst[rr][i];
                    lpost(C_WP + v, rr + 1);
                });
            } else {
              if (haskv) {
                f32x16 acc = {};
                const unsigned wa = L_WKV + (v * 320 + 8 * h + tq) * 40 + tb * 32 + (tb ? 0 : tp * 8);
                unsigned xa[4];
#pragma unroll
                for (int s = 0; s < 4; ++s) xa[s] = L_PANEL + kmt * 40960 + v * 20480 + h * 512 + ((r ^ ((2 * s + h) & 7)) << 4);
                WAITV(0);
                GET_TAG();
                STAMP(2);
                bf16x8 wf[20], xf[20];
#pragma unroll
                for (int s = 0; s < 20; ++s) wf[s] = ldstr2(wa + s * 640, wa + s * 640 + 160);
                lwait_all(C_AP, 4, 1, lane);
#pragma unroll
                for (int s = 0; s < 20; ++s) xf[s] = ldsf(xa[s & 3] + s * 1024);
                SBAR();
                if (isV) {
#pragma unroll
                    for (int s = 0; s < 20; ++s) acc = MFMA(xf[s], wf[s], acc);
                } else {
#pragma unroll
                    for (int s = 0; s < 20; ++s) acc = MFMA(wf[s], xf[s], acc);
                }
                if (v) {
#pragma unroll
                    for (int e = 0; e < 4; ++e) *LP(f32x4, L_WKV + 12800 + e * 1024 + lane * 16) = f32x4{acc[4 * e], acc[4 * e + 1], acc[4 * e + 2], acc[4 * e + 3]};
                    lpost(C_KVP, 1);
                } else {
                    lwait(C_KVP, 1);
#pragma unroll
                    for (int e = 0; e < 4; ++e) {
                        const f32x4 t = *LP(const f32x4, L_WKV + 12800 + e * 1024 + lane * 16);
                        acc[4 * e] += t[0]; acc[4 * e + 1] += t[1]; acc[4 * e + 2] += t[2]; acc[4 * e + 3] += t[3];
                    }
                    const __amdgpu_buffer_rsrc_t rk = mkrsrc(ws + WS_KV, 32 * KVD);
                    const unsigned base = ti * KVD, ke = g & 1;
                    const u32x4 f0 = __builtin_bit_cast(u32x4, pack8(acc, 0, 1.f)), f1 = __builtin_bit_cast(u32x4, pack8(acc, 8, 1.f));
                    float mx;
                    if (!isV) {
                        __builtin_amdgcn_raw_buffer_store_b128(f0, rk, base + ke * 1024 + lane * 16, 0, 16);
                        const u32x2 k1 = {f1[0], f1[1]};
                        if (!h) __builtin_amdgcn_raw_buffer_store_b64(k1, rk, base + KV_K1 + r * 16 + ke * 8, 0, 16);
                        lpost(C_KVFIN, 1);
                        float n2 = 0.f;
#pragma unroll
                        for (int e = 0; e < 4; ++e) n2 += bflo(f0[e]) * bflo(f0[e]) + bfhi(f0[e]) * bfhi(f0[e]);
                        if (!h) n2 += bflo(f1[0]) * bflo(f1[0]) + bfhi(f1[0]) * bfhi(f1[0]) + bflo(f1[1]) * bflo(f1[1]) + bfhi(f1[1]) * bfhi(f1[1]);
                        n2 += __shfl_xor(n2, 32);
                        mx = (n2 == n2) ? n2 : INFINITY;
                    } else {
                        const unsigned cst = r == 20 ? 0x3f803f80u : 0u;
                        const u32x4 c4 = {cst, cst, cst, cst};
                        if (r <= 21) {
                            __builtin_amdgcn_raw_buffer_store_b128(r >= 20 ? c4 : f0, rk, base + KV_V + ke * KV_VT + h * 352 + r * 16, 0, 16);
                            __builtin_amdgcn_raw_buffer_store_b128(r >= 20 ? c4 : f1, rk, base + KV_V + ke * KV_VT + 704 + h * 352 + r * 16, 0, 16);
                        }
                        lpost(C_KVFIN, 1);
                        float vm = 0.f;
#pragma unroll
                        for (int e = 0; e < 16; ++e) { const float a = fabsf(acc[e]); vm = (a <= vm) ? vm : ((a == a) ? a : INFINITY); }
                        mx = r < 20 ? vm : 0.f;
                    }
                    mx = wave_max(mx);
                    WAITV(0);
                    STAMP(15);
                    if (lane < 8) __hip_atomic_store((gu64*)(ws + WS_KF + lane * 1024 + (isV ? 512 : 0)) + kvm, ((unsigned long long)tag << 32) | __float_as_uint(mx), RLX_AGENT);
                }
            }
              else { WAITV(0); GET_TAG(); }
              STAMP(3);
            }
            if (haskv) lwait(C_KVFIN, 1);
#pragma unroll
            for (int n = 0; n < 2; ++n) {
                unsigned L = n * 64 + lane; L = (j * 4 + v) * 120 + (L < 120u ? L : 119u);
                const unsigned m3 = L >= 7680u, rem = L - (m3 ? 7680u : 0u), row = rem / 12u;
                l2touch((m3 ? p.w3 : p.w1) + row * 3072 + (xc & 1) * 1536 + (rem - row * 12u) * 128);
            }
            lwait_all(C_AP, 4, GU_DELAY, lane);
            STAMP(4);
            {
                const char* wsrc = ((v >> 1) ? p.w3 : p.w1) + g * 384 + (size_t)(v & 1) * 8 * 3072;
                unsigned gvo[3];
#pragma unroll
                for (int n = 0; n < 3; ++n) {
                    const unsigned c = n * 64 + lane, row = c / 24u, col = (c - row * 24u) ^ ((row & 2) << 1);
                    gvo[n] = row * 3072 + col * 16 - n * 1024;
                }
                const unsigned gdst = L_GU + (v >> 1) * 6176 + (v & 1) * 3072;
                auto gdma = [&](auto S) { constexpr int s = S.value; glds16x3v(gvo[0], gvo[1], gvo[2], wsrc + (size_t)s * 16 * 3072, gdst + (s % 3) * GSL); };
                static_for<0, 3>(gdma);
                f32x16 acc[2][3] = {};
                unsigned bn[3], pa[4], pb[4];
#pragma unroll
                for (int n = 0; n < 3; ++n) bn[n] = L_GU + tb * 6176 + (8 * h + tq) * 384 + (((3 * v + n) * 32 + (tp >> 1) * 16) ^ ((unsigned)(tq >> 1) << 6)) + (tp & 1) * 8;
#pragma unroll
                for (int s = 0; s < 4; ++s) { pa[s] = L_PANEL + h * 512 + ((r ^ ((2 * s + h) & 7)) << 4); pb[s] = pa[s] + 40960; PIN(pb[s]); }
                bf16x8 wf[2][3], xa[2], xb2[2];
                unsigned vaddr = L_MISC + (C_GP + v) * 4, vone = 1u; PIN(vaddr); PIN(vone);
                auto rdw = [&](auto S, auto N) { constexpr int s = S.value, n = N.value; const unsigned o = bn[n] + (s % 3) * GSL; wf[s & 1][n] = ldstr2(o, o + 1536); };
                auto rdx = [&](auto S) { constexpr int s = S.value; xa[s & 1] = ldsf(pa[s & 3] + s * 1024); xb2[s & 1] = ldsf(pb[s & 3] + s * 1024); };
                WAITV(6); linc(vaddr, vone); lwait_all(C_GP, 4, 1u, lane);
                rdx(IC<0>{}); rdw(IC<0>{}, IC<0>{}); rdw(IC<0>{}, IC<1>{}); rdw(IC<0>{}, IC<2>{});
                WAITV(3); linc(vaddr, vone);
                static_for<0, 40>([&](auto S) {
                    constexpr int s = S.value; constexpr bool nx = s + 1 < 40;
                    unsigned pk = 0;
                    MPRIO(GU_HI, 0); acc[0][0] = MFMA(wf[s & 1][0], xa[s & 1], acc[0][0]); SBAR();
                    if constexpr (nx) rdx(IC<s + 1>{});
                    SBAR();
                    MPRIO(GU_HI, 1); acc[1][0] = MFMA(wf[s & 1][0], xb2[s & 1], acc[1][0]); SBAR();
                    if constexpr (nx) pk = lpeek(C_GP, lane);
                    SBAR();
                    MPRIO(GU_HI, 2); acc[0][1] = MFMA(wf[s & 1][1], xa[s & 1], acc[0][1]); SBAR();
                    MPRIO(GU_HI, 3); acc[1][1] = MFMA(wf[s & 1][1], xb2[s & 1], acc[1][1]); SBAR();
                    if constexpr (nx) { lcheck(pk, C_GP, s + 2, lane); rdw(IC<s + 1>{}, IC<0>{}); rdw(IC<s + 1>{}, IC<1>{}); rdw(IC<s + 1>{}, IC<2>{}); }
                    SBAR();
                    MPRIO(GU_HI, 4); acc[0][2] = MFMA(wf[s & 1][2], xa[s & 1], acc[0][2]); SBAR();
                    if constexpr (s + 3 < 40) gdma(IC<s + 3>{});
                    SBAR();
                    MPRIO(GU_HI, 5); acc[1][2] = MFMA(wf[s & 1][2], xb2[s & 1], acc[1][2]); SBAR();
                    if constexpr (s + 3 < 40) WAITV(3); else WAITV(0);
                    linc(vaddr, vone); SBAR();
                });
                __builtin_amdgcn_s_setprio(3);
                STAMP(6);
                const __amdgpu_buffer_rsrc_t rh = mkrsrc(ws + WS_HT, 64 * 96 * 1024);
#pragma unroll
                for (int m = 0; m < 2; ++m)
#pragma unroll
                    for (int n = 0; n < 3; ++n) {
                        f32x16 hv;
#pragma unroll
                        for (int e = 0; e < 8; ++e) {
                            const float gg = acc[m][n][e];
                            hv[e] = gg * __builtin_amdgcn_rcpf(1.f + __builtin_amdgcn_exp2f(-1.4426950408889634f * gg)) * acc[m][n][e + 8];
                        }
                        __builtin_amdgcn_raw_buffer_store_b128(__builtin_bit_cast(u32x4, pack8(hv, 0, 1.f)), rh, ((2 * ti + m) * 96 + g * 12 + 3 * v + n) * 1024 + lane * 16, 0, 16);
                    }
                if constexpr (DN) {
                    WAITV(0);
                    if (lane < 2) __hip_atomic_store((gu32*)(ws + WS_HF) + (2 * ti + lane) * 32 + g * 4 + v, tag, RLX_AGENT);
                }
                if constexpr (DN) {
#pragma unroll
                    for (int n = 0; n < 2; ++n) {
                        unsigned L = (j * 4 + v) * 85 + n * 64 + lane; L = L < 10880u ? L : 10879u;
                        const unsigned row = L / 5u;
                        l2touch((row < 1536u ? p.w2 + row * 1280 : p.wo + (row - 1536u) * 1280) + (xc & 1) * 640 + (L - row * 5u) * 128);
                    }
                }
                STAMP(7);
            }
        } else {
            BLOCK_BARRIER();
        }
        if constexpr (DN || WO) {
            LANE_CONSTS();
            const unsigned bq = ((tb * 32 + (tp >> 1) * 16) ^ ((unsigned)(tq >> 1) << 6)) + (tp & 1) * 8;
            const unsigned drow = lane >> 3, dcol = ((lane & 7) ^ (((lane >> 4) & 1) << 2)) * 16;
            const int G = xc >> 1, r0 = G < 2 ? 6 * G : 12 + 5 * (G - 2), nr = G < 2 ? 6 : 5;
            if constexpr (FUSED) {
                if (b == 255 && v == 0) {
                    gu32* f = (gu32*)(ws + WS_HF) + (lane >> 3) * 64 + (lane & 7) * 4;
                    _Pragma("clang loop unroll(disable)") for (unsigned spins = 0; spins < (1u << 14); ++spins) {
                        bool ok = true;
#pragma unroll
                        for (int n = 0; n < 4; ++n) ok &= __hip_atomic_load(f + n * 512, RLX_AGENT) == tag;
                        if (__all(ok)) break;
                        TMO(spins, 1u << 14, 303, tag);
                        __builtin_amdgcn_s_sleep(32);
                    }
                    if (lane == 0) __hip_atomic_store((gu32*)(ws + WS_EP), tag, RLX_AGENT);
                }
            }
            if (j < 5 * nr) {
                const int dr = r0 + j / 5, dc = (xc & 1) * 5 + j % 5;
                const int mfirst = dr < 20 ? 3 * dr : 60 + 2 * (dr - 20), nm = dr < 20 ? 3 : 2;
                const unsigned dvo = drow * 1280 + dcol;
                const unsigned myring = L_PANEL + v * 20480;
                f32x16 acc0 = {}, acc1 = {};
                if constexpr (PRO) { lwait_all(C_GP, 4, 42, lane); lwait_all(C_AP, 4, 8, lane); }
                if constexpr (DN) {
                    f32x16 da[3][2] = {};
                    const int drot = (j % 5) * 4;
                    const char* bsrc = p.w2 + (size_t)v * 384 * 1280 + dc * 128;
                    const char* asrc = ws + WS_HT + ((size_t)mfirst * 96 + v * 24) * 1024;
                    const unsigned am1 = lane * 16 + 98304, am2 = lane * 16 + (nm == 3 ? 196608 : 98304);
                    u32x4 aq[8][3];
                    auto bissue = [&](auto K) {
                        constexpr int k = K.value;
                        int kr = k + drot; kr = kr >= 24 ? kr - 24 : kr;
                        const char* sb = bsrc + (size_t)kr * 16 * 1280;
                        glds16(dvo, sb, myring + (k % 10) * 2048); glds16(dvo + 8 * 1280, sb, myring + (k % 10) * 2048 + 1024);
                    };
                    auto woissue = [&](auto U) {
                        constexpr int u = U.value;
                        const char* sb = p.wo + (size_t)(v * 160 + u * 16) * 1280 + dc * 128;
                        glds16(dvo, sb, myring + u * 2048); glds16(dvo + 8 * 1280, sb, myring + u * 2048 + 1024);
                    };
                    auto aissue = [&](auto K) {
                        constexpr int k = K.value;
                        int kr = k + drot; kr = kr >= 24 ? kr - 24 : kr;
                        const char* sb = asrc + (size_t)kr * 1024;
                        GLOAD16S(aq[k & 7][0], lane * 16, sb); GLOAD16S(aq[k & 7][1], am1, sb); GLOAD16S(aq[k & 7][2], am2, sb);
                    };
                    static_for<0, 8>(bissue);
                    if constexpr (PRO) {
                        gu32* f = (gu32*)(ws + WS_HF) + (mfirst + ((lane >> 3) < nm ? (lane >> 3) : 0)) * 32 + 8 * v + (lane & 7);
                        _Pragma("clang loop unroll(disable)") for (unsigned spins = 0; spins < (1u << 14); ++spins) {
                            if (__all(__hip_atomic_load(f, RLX_AGENT) == tag)) break;
                            TMO(spins, 1u << 14, 302, tag);
                            __builtin_amdgcn_s_sleep(HF_SLEEP);
                        }
                        CFENCE();
                    }
                    STAMP(8);
                    static_for<0, 8>(aissue);
                    const unsigned ba0 = myring + (4 * h + tq) * 128 + bq, ba1 = ba0 ^ 64;
                    bf16x8 w0[2], w1[2];
                    WAITVN(24);
                    w0[0] = ldstr2(ba0, ba0 + 1024); w1[0] = ldstr2(ba1, ba1 + 1024);
                    static_for<0, 24>([&](auto K) {
                        constexpr int k = K.value;
                        constexpr int yk = (k <= 7 ? 21 + 2 * k : (k < 16 ? 35 : 5 * (23 - k))) + (WO && k > 16 ? 2 * (k - 16) : 0);
                        constexpr int n = (k + 1 <= 7 || k == 23) ? yk : yk - 2;
                        waitv3<(n < 0 ? 0 : n)>(aq[k & 7][0], aq[k & 7][1], aq[k & 7][2]);
                        constexpr unsigned o = ((k + 1) % 10) * 2048;
#define DXF(m) __builtin_bit_cast(bf16x8, aq[k & 7][m])
                        SBAR();
                        MPRIO(DN_HI, 0); da[0][0] = MFMA(w0[k & 1], DXF(0), da[0][0]); SBAR();
                        if constexpr (k + 1 < 24) w0[(k + 1) & 1] = ldstr2(ba0 + o, ba0 + o + 1024);
                        SBAR();
                        MPRIO(DN_HI, 1); da[0][1] = MFMA(w1[k & 1], DXF(0), da[0][1]); SBAR();
                        if constexpr (k + 1 < 24) w1[(k + 1) & 1] = ldstr2(ba1 + o, ba1 + o + 1024);
                        SBAR();
                        MPRIO(DN_HI, 2); da[1][0] = MFMA(w0[k & 1], DXF(1), da[1][0]); SBAR();
                        if constexpr (k + 8 < 24) bissue(IC<k + 8>{});
                        else if constexpr (WO) woissue(IC<(k + 8) % 10>{});
                        SBAR();
                        MPRIO(DN_HI, 3); da[1][1] = MFMA(w1[k & 1], DXF(1), da[1][1]); SBAR();
                        MPRIO(DN_HI, 4); da[2][0] = MFMA(w0[k & 1], DXF(2), da[2][0]); SBAR();
                        MPRIO(DN_HI, 5); da[2][1] = MFMA(w1[k & 1], DXF(2), da[2][1]); SBAR();
                        if constexpr (k + 8 < 24) aissue(IC<k + 8>{});
                    });
                    __builtin_amdgcn_s_setprio(3);
                    STAMP(9);
                    if constexpr (WO) { CFENCE(); woissue(IC<2>{}); woissue(IC<3>{}); }
#pragma unroll
                    for (int n = 0; n < 2; ++n) {
#pragma unroll
                        for (int m = 0; m < 3; ++m) {
                            if (m != v) {
                                const unsigned dst = L_GU + (m * 3 + (v > m ? v - 1 : v)) * 4096 + lane * 16;
#pragma unroll
                                for (int e = 0; e < 4; ++e) *LP(f32x4, dst + e * 1024) = f32x4{da[m][n][4 * e], da[m][n][4 * e + 1], da[m][n][4 * e + 2], da[m][n][4 * e + 3]};
                            }
                        }
                        lpost(C_DP + v, 2 * n + 1); lwait_all(C_DP, 4, 2 * n + 1, lane);
                        if (v < 3) {
                            f32x16 t = v == 0 ? da[0][n] : (v == 1 ? da[1][n] : da[2][n]);
#pragma unroll
                            for (int o = 0; o < 3; ++o)
#pragma unroll
                                for (int e = 0; e < 4; ++e) {
                                    const f32x4 u = *LP(const f32x4, L_GU + (v * 3 + o) * 4096 + e * 1024 + lane * 16);
                                    t[4 * e] += u[0]; t[4 * e + 1] += u[1]; t[4 * e + 2] += u[2]; t[4 * e + 3] += u[3];
                                }
                            if (n) acc1 = t; else acc0 = t;
                        }
                        if (n == 0) { lpost(C_DP + v, 2); lwait_all(C_DP, 4, 2, lane); }
                    }
                    if constexpr (!WO) {
                        if (v < 3) {
                            float* pt = (float*)(ws + WS_PT) + (size_t)(b * 3 + v) * 2048 + lane;
#pragma unroll
                            for (int e = 0; e < 16; ++e) { pt[e * 64] = acc0[e]; pt[1024 + e * 64] = acc1[e]; }
                        }
                    }
                } else {
#pragma unroll
                    for (int u = 0; u < 20; ++u) glds16(dvo + u * 8 * 1280, p.wo + (size_t)v * 160 * 1280 + dc * 128, myring + u * 1024);
                    if (v < 3) {
                        const float* pt = (const float*)(ws + WS_PT) + (size_t)(b * 3 + v) * 2048 + lane;
#pragma unroll
                        for (int e = 0; e < 16; ++e) { acc0[e] = pt[e * 64]; acc1[e] = pt[1024 + e * 64]; }
                    }
                }
                if constexpr (WO) { WAITV(0); lpost(C_DP + v, 4); }
                if (v < nm) {
                    const int m = mfirst + v;
                    const unsigned ba0 = L_PANEL + (4 * h + tq) * 128 + bq, ba1 = ba0 ^ 64;
                    if constexpr (WO) {
                        const char* xrow = p.x + (size_t)(m * 32 + r) * 1280 + dc * 128 + h * 16;
                        u32x4 xres[4];
#pragma unroll
                        for (int e = 0; e < 4; ++e) xres[e] = *(const u32x4*)(xrow + e * 32);
                        lwait_all(C_DP, 4, 4, lane);
                        const __amdgpu_buffer_rsrc_t ro = mkrsrc(ws + WS_OT + (size_t)m * 40960, 40960);
                        gu32* of_ = (gu32*)(ws + WS_OF) + m * 32 + (lane & 31);
                        int hf = 0;
                        f32x16 t0[2] = {}, t1[2] = {};
#pragma unroll
                        for (int it = 0; it < 2; ++it) {
                            if constexpr (ATT) {
                                _Pragma("clang loop unroll(disable)") for (unsigned spins = 0; spins < (1u << 14); ++spins) {
                                    const unsigned ok = (unsigned)__ballot(__hip_atomic_load(of_, RLX_AGENT) == tag);
                                    const bool lo = (ok & 0xffffu) == 0xffffu, hi = (ok >> 16) == 0xffffu;
                                    if (it == 0) { if (lo || hi) { hf = lo ? 0 : 1; break; } }
                                    else if (hf ? hi : lo) break;
                                    TMO(spins, 1u << 14, 300, m);
                                    __builtin_amdgcn_s_sleep(OF_SLEEP);
                                }
                                CFENCE();
                            }
                            if (it == 0) STAMP(10);
                            u32x4 of[20];
#pragma unroll
                            for (int s = 0; s < 20; ++s) of[s] = __builtin_amdgcn_raw_buffer_load_b128(ro, lane * 16 + s * 1024, hf * 20480, 16);
                            const unsigned a0 = ba0 + hf * 40960, a1 = ba1 + hf * 40960, l0 = a0 + (36 * h + 16) * 128, l1 = a1 + (36 * h + 16) * 128;
                            bf16x8 w0[2], w1[2];
                            w0[0] = ldstr2(a0, a0 + 1024); w1[0] = ldstr2(a1, a1 + 1024);
                            static_for<0, 20>([&](auto S) {
                                constexpr int s = S.value, n = s + 1, gg = n / 5, jj = n % 5;
                                const bf16x8 xf = __builtin_bit_cast(bf16x8, of[s]);
                                if constexpr (n < 20) {
                                    if constexpr (jj < 4) {
                                        constexpr unsigned o = (80 * gg + 20 * jj) * 128;
                                        w0[n & 1] = ldstr2(a0 + o, a0 + o + 1024); w1[n & 1] = ldstr2(a1 + o, a1 + o + 1024);
                                    } else {
                                        constexpr unsigned o = 80 * gg * 128;
                                        w0[n & 1] = ldstr2(l0 + o, l0 + o + 2560); w1[n & 1] = ldstr2(l1 + o, l1 + o + 2560);
                                    }
                                }
                                SBAR();
                                t0[it] = MFMA(w0[s & 1], xf, t0[it]); t1[it] = MFMA(w1[s & 1], xf, t1[it]);
                                SBAR();
                            });
                            hf ^= 1;
                        }
                        acc0 += t0[0] + t0[1]; acc1 += t1[0] + t1[1];
                        char* orow = p.out + (size_t)(m * 32 + r) * 1280 + dc * 128 + h * 16;
#pragma unroll
                        for (int n = 0; n < 2; ++n) {
                            const f32x16& a = n ? acc1 : acc0;
#pragma unroll
                            for (int qq = 0; qq < 2; ++qq) {
                                float lo[4], hi[4];
#pragma unroll
                                for (int e = 0; e < 4; ++e) {
                                    auto sw = __builtin_amdgcn_permlane32_swap(__float_as_uint(a[8 * qq + e]), __float_as_uint(a[8 * qq + 4 + e]), false, false);
                                    lo[e] = __uint_as_float(sw[0]); hi[e] = __uint_as_float(sw[1]);
                                }
                                const u32x4 xv = xres[2 * n + qq];
                                const u32x4 ov = {pk2(lo[0] + bflo(xv[0]), lo[1] + bfhi(xv[0])), pk2(lo[2] + bflo(xv[1]), lo[3] + bfhi(xv[1])),
                                                  pk2(hi[0] + bflo(xv[2]), hi[1] + bfhi(xv[2])), pk2(hi[2] + bflo(xv[3]), hi[3] + bfhi(xv[3]))};
                                *(u32x4*)(orow + (2 * n + qq) * 32) = ov;
                            }
                        }
                        STAMP(11);
                    }
                }
            }
        }
    }
}

template <int PH>
static void launch(const Params& p, hipStream_t stream) {
    (void)hipFuncSetAttribute((const void*)mega<PH>, hipFuncAttributeMaxDynamicSharedMemorySize, L_TOTAL);
    mega<PH><<<256, 512, L_TOTAL, stream>>>(p);
}

extern "C" void kernel_launch(void* const* d_in, const int* in_sizes, int n_in, void* d_out, int out_size, void* d_ws, size_t ws_size, hipStream_t stream) {
    Params p;
    p.x = (const char*)d_in[0]; p.wq = (const char*)d_in[1]; p.wk = (const char*)d_in[2]; p.wv = (const char*)d_in[3]; p.wo = (const char*)d_in[4];
    p.w1 = (const char*)d_in[6]; p.w3 = (const char*)d_in[7]; p.w2 = (const char*)d_in[8];
    p.out = (char*)d_out; p.ws = (char*)d_ws;
    launch<27>(p, stream);
}
```

```cpp
#include <hip/hip_runtime.h>
#include <hip/hip_bf16.h>
#include <stdint.h>

#define AS1 __attribute__((address_space(1)))
#define AS3 __attribute__((address_space(3)))
typedef __bf16 bf16x2 __attribute__((ext_vector_type(2)));
typedef __bf16 bf16x8 __attribute__((ext_vector_type(8)));
typedef short s16x4 __attribute__((ext_vector_type(4)));
typedef short s16x8 __attribute__((ext_vector_type(8)));
typedef float f32x2 __attribute__((ext_vector_type(2)));
typedef float f32x4 __attribute__((ext_vector_type(4)));
typedef float f32x16 __attribute__((ext_vector_type(16)));
typedef unsigned u32x2 __attribute__((ext_vector_type(2)));
typedef unsigned u32x4 __attribute__((ext_vector_type(4)));
typedef AS1 unsigned gu32;
typedef AS1 unsigned long long gu64;

#define ATT_PRIO 2
#define GEM_PRIO 0
#define GU_HI 0
#define DN_HI 0
#define MPRIO(mask, i) __builtin_amdgcn_s_setprio(((mask) >> (i)) & 1 ? 3 : 0)
#define GU_DELAY 8
#define GU_ROT 4
#define GU_GQ 5
#define OF_SLEEP 3
#define HF_SLEEP 6
#define DN_DQ 6
#define SPLIT_MODE 0

constexpr int SEQ = 2048, DM = 640, FF = 1536;
constexpr unsigned WS_KF = 0;
constexpr unsigned WS_HF = 16384;
constexpr unsigned WS_OF = 32768;
constexpr unsigned WS_EP = 49152;
constexpr unsigned KVD = 5376;
constexpr unsigned KV_K1 = 2048, KV_V = 2560, KV_VT = 1408;
constexpr unsigned WS_KV = 65536;
constexpr unsigned WS_HT = WS_KV + 176128;
constexpr unsigned WS_OT = WS_HT + 64 * 96 * 1024;
constexpr unsigned WS_QT = WS_OT + 64 * 40 * 1024;
constexpr unsigned WS_PT = WS_QT + 64 * 32 * 2048;
constexpr unsigned WS_ST = WS_PT + 256 * 3 * 32 * 256;
constexpr unsigned L_PANEL = 0;
constexpr unsigned L_KV = 81920;
constexpr unsigned L_WQ = 81920;
constexpr unsigned L_GU = 124928;
constexpr unsigned GSL = 12320;
constexpr unsigned L_WKV = 124928;
constexpr unsigned L_MISC = 161920;
constexpr unsigned L_TOTAL = 163840;
constexpr unsigned CHUNK = 2 * KVD;
constexpr int NCHUNK = 16;
enum { C_AP = 0  , C_GP = 4  , C_DP = 8  , C_PP = 12  , C_WP = 16  , C_KVP = 20, C_KVFIN, C_KVRDY, C_KN, C_VM, C_N };

struct Params { const char* x; const char* wq; const char* wk; const char* wv; const char* wo; const char* w1; const char* w3; const char* w2; char* out; char* ws; };

#define SBAR() __builtin_amdgcn_sched_barrier(0)
#define PIN(x) asm volatile("" : "+v"(x))
#define CFENCE() asm volatile("" ::: "memory")
#define WAITV(n) asm volatile("s_waitcnt vmcnt(" #n ")" ::: "memory")
#define MFMA(a, b, c) __builtin_amdgcn_mfma_f32_32x32x16_bf16((a), (b), (c), 0, 0, 0)
#define RLX_AGENT __ATOMIC_RELAXED, __HIP_MEMORY_SCOPE_AGENT
#define RLX_WG __ATOMIC_RELAXED, __HIP_MEMORY_SCOPE_WORKGROUP

#define TMO(spins, lim, code, tgt) do {} while (0)
#define STAMP(id) do {} while (0)
#define STAMPC(id) do {} while (0)

#define LP(T, off) ((AS3 T*)(uintptr_t)(unsigned)(off))
__device__ __forceinline__ bf16x8 ldsf(unsigned off) { return *LP(const bf16x8, off); }
__device__ __forceinline__ s16x4 ldstr(unsigned off) { return __builtin_amdgcn_ds_read_tr16_b64_v4i16(LP(s16x4, off)); }
__device__ __forceinline__ bf16x8 ldstr2(unsigned off0, unsigned off1) {
    s16x4 a = ldstr(off0), b = ldstr(off1);
    return __builtin_bit_cast(bf16x8, __builtin_shufflevector(a, b, 0, 1, 2, 3, 4, 5, 6, 7));
}
__device__ __forceinline__ unsigned pk2(float lo, float hi) { bf16x2 v = {(__bf16)lo, (__bf16)hi}; return __builtin_bit_cast(unsigned, v); }
__device__ __forceinline__ float bflo(unsigned u) { return __uint_as_float(u << 16); }
__device__ __forceinline__ float bfhi(unsigned u) { return __uint_as_float(u & 0xffff0000u); }
__device__ __forceinline__ float rbf(float f) { return (float)(__bf16)f; }

__device__ __forceinline__ void glds16(unsigned voff, const char* sbase, unsigned ldsdst) {
    unsigned keep;
    asm volatile("s_mov_b32 %0, m0\n\ts_mov_b32 m0, %3\n\ts_nop 2\n\tglobal_load_lds_dwordx4 %1, %2\n\ts_mov_b32 m0, %0"
                 : "=&s"(keep) : "v"(voff), "s"(sbase), "s"(ldsdst) : "memory");
}
__device__ __forceinline__ void glds16_sc1(unsigned voff, const char* sbase, unsigned ldsdst) {
    unsigned keep;
    asm volatile("s_mov_b32 %0, m0\n\ts_mov_b32 m0, %3\n\ts_nop 2\n\tglobal_load_lds_dwordx4 %1, %2 sc1\n\ts_mov_b32 m0, %0"
                 : "=&s"(keep) : "v"(voff), "s"(sbase), "s"(ldsdst) : "memory");
}
__device__ __forceinline__ void glds16x3_sc1(unsigned voff, const char* sbase, unsigned ldsdst) {
    unsigned keep;
    asm volatile("s_mov_b32 %0, m0\n\ts_mov_b32 m0, %3\n\ts_nop 2\n\tglobal_load_lds_dwordx4 %1, %2 sc1\n\tglobal_load_lds_dwordx4 %1, %2 offset:1024 sc1\n\tglobal_load_lds_dwordx4 %1, %2 offset:2048 sc1\n\ts_mov_b32 m0, %0"
                 : "=&s"(keep) : "v"(voff), "s"(sbase), "s"(ldsdst) : "memory");
}
__device__ __forceinline__ void glds16x3v(unsigned v0, unsigned v1, unsigned v2, const char* sbase, unsigned ldsdst) {
    unsigned keep;
    asm volatile("s_mov_b32 %0, m0\n\ts_mov_b32 m0, %5\n\ts_nop 0\n\tglobal_load_lds_dwordx4 %1, %4\n\tglobal_load_lds_dwordx4 %2, %4 offset:1024\n\tglobal_load_lds_dwordx4 %3, %4 offset:2048\n\ts_mov_b32 m0, %0"
                 : "=&s"(keep) : "v"(v0), "v"(v1), "v"(v2), "s"(sbase), "s"(ldsdst) : "memory");
}
__device__ __forceinline__ void l2touch(const char* ga) {
    unsigned keep;
    asm volatile("s_mov_b32 %0, m0\n\ts_mov_b32 m0, %2\n\ts_nop 0\n\tglobal_load_lds_dword %1, off\n\ts_mov_b32 m0, %0" : "=&s"(keep) : "v"(ga), "s"(L_MISC + 1024) : "memory");
}
#define GLOAD16(dst, voff, sbase, imm) asm volatile("s_nop 4\n\tglobal_load_dwordx4 %0, %1, %2 offset:%3" : "=v"(dst) : "v"(voff), "s"(sbase), "i"(imm) : "memory")

__device__ __forceinline__ AS3 unsigned* lcnt(int c) { return LP(unsigned, L_MISC) + c; }
__device__ __forceinline__ void larrive(int c, int lane) { CFENCE(); if (lane == 0) __hip_atomic_fetch_add(lcnt(c), 1u, RLX_WG); CFENCE(); }
__device__ __forceinline__ void lset(int c, unsigned v, int lane) { CFENCE(); if (lane == 0) __hip_atomic_store(lcnt(c), v, RLX_WG); CFENCE(); }
__device__ __forceinline__ void lwait(int c, unsigned target) {
    CFENCE();
    _Pragma("clang loop unroll(disable)") for (unsigned spins = 0; spins < (1u << 22); ++spins) {
        if ((unsigned)__builtin_amdgcn_readfirstlane(__hip_atomic_load(lcnt(c), RLX_WG)) >= target) break;
        TMO(spins, 1u << 16, 100 + c, target);
        __builtin_amdgcn_s_sleep(1);
    }
    CFENCE();
}
__device__ __forceinline__ void lwait_all(int c, int n, unsigned target, int lane) {
    CFENCE();
    AS3 unsigned* q = lcnt(c) + (lane < n ? lane : 0);
    _Pragma("clang loop unroll(disable)") for (unsigned spins = 0; spins < (1u << 16); ++spins) {
        if (__all(__hip_atomic_load(q, RLX_WG) >= target)) break;
        TMO(spins, 1u << 16, 200 + c, target);
        __builtin_amdgcn_s_sleep(1);
    }
    CFENCE();
}
__device__ __forceinline__ void lpost(int c, unsigned v) { CFENCE(); *(volatile AS3 unsigned*)lcnt(c) = v; CFENCE(); }
__device__ __forceinline__ void linc(unsigned vaddr, unsigned vone) {
    asm volatile("s_mov_b64 exec, 1\n\tds_add_u32 %0, %1\n\ts_mov_b64 exec, -1" :: "v"(vaddr), "v"(vone) : "memory");
}
__device__ __forceinline__ unsigned lpeek(int c, int lane) { return *((volatile AS3 unsigned*)lcnt(c) + (lane & 3)); }
__device__ __forceinline__ void lcheck(unsigned pk, int c, unsigned target, int lane) {
    if (__builtin_expect(__any(pk < target), 0)) lwait_all(c, 4, target, lane);
    CFENCE();
}
__device__ __forceinline__ void gwait(const char* ws, unsigned off, int n, unsigned tag, int lane) {
    gu32* f = (gu32*)(ws + off) + (lane < n ? lane : 0);
    _Pragma("clang loop unroll(disable)") for (unsigned spins = 0; spins < (1u << 14); ++spins) {
        unsigned v = __hip_atomic_load(f, RLX_AGENT);
        if (__all(v == tag)) break;
        TMO(spins, 1u << 14, 300, off);
        __builtin_amdgcn_s_sleep(6);
    }
    CFENCE();
}
__device__ __forceinline__ float gwait64(const char* ws, unsigned off, unsigned tag, int lane) {
    gu64* f = (gu64*)(ws + off) + lane;
    unsigned long long v = 0;
    _Pragma("clang loop unroll(disable)") for (unsigned spins = 0; spins < (1u << 14); ++spins) {
        v = __hip_atomic_load(f, RLX_AGENT);
        if (__all((unsigned)(v >> 32) == tag)) break;
        __builtin_amdgcn_s_sleep(6);
    }
    CFENCE();
    return __uint_as_float((unsigned)v);
}
__device__ __forceinline__ __amdgpu_buffer_rsrc_t mkrsrc(const char* base, unsigned bytes) {
    return __builtin_amdgcn_make_buffer_rsrc((void*)base, 0, bytes, 0x00020000);
}
__device__ __forceinline__ float dpp_sum16(float v) {
    v += __int_as_float(__builtin_amdgcn_update_dpp(0, __float_as_int(v), 0x128, 0xf, 0xf, false));
    v += __int_as_float(__builtin_amdgcn_update_dpp(0, __float_as_int(v), 0x124, 0xf, 0xf, false));
    v += __int_as_float(__builtin_amdgcn_update_dpp(0, __float_as_int(v), 0x122, 0xf, 0xf, false));
    v += __int_as_float(__builtin_amdgcn_update_dpp(0, __float_as_int(v), 0x121, 0xf, 0xf, false));
    return v;
}
__device__ __forceinline__ float wave_max(float v) {
    int x = __float_as_int(v);
    x = max(x, __builtin_amdgcn_update_dpp(0, x, 0x128, 0xf, 0xf, false)); x = max(x, __builtin_amdgcn_update_dpp(0, x, 0x124, 0xf, 0xf, false));
    x = max(x, __builtin_amdgcn_update_dpp(0, x, 0x122, 0xf, 0xf, false)); x = max(x, __builtin_amdgcn_update_dpp(0, x, 0x121, 0xf, 0xf, false));
    const int a = max(__builtin_amdgcn_readlane(x, 0), __builtin_amdgcn_readlane(x, 16)), b = max(__builtin_amdgcn_readlane(x, 32), __builtin_amdgcn_readlane(x, 48));
    return __int_as_float(max(a, b));
}
__device__ __forceinline__ bf16x8 pack8(const f32x16& a, int b, float sc) {
    u32x4 r = {pk2(a[b] * sc, a[b + 1] * sc), pk2(a[b + 2] * sc, a[b + 3] * sc), pk2(a[b + 4] * sc, a[b + 5] * sc), pk2(a[b + 6] * sc, a[b + 7] * sc)};
    return __builtin_bit_cast(bf16x8, r);
}

template <int I> struct IC { static constexpr int value = I; };
template <int B, int E, class F> __device__ __forceinline__ void static_for(F&& f) { if constexpr (B < E) { f(IC<B>{}); static_for<B + 1, E>(f); } }
#define WAITVN(n) asm volatile("s_waitcnt vmcnt(%0)" :: "i"(n) : "memory")
__device__ __forceinline__ void GLOAD16S(u32x4& dst, unsigned voff, const char* sbase) { asm volatile("global_load_dwordx4 %0, %1, %2 sc1" : "=v"(dst) : "v"(voff), "s"(sbase) : "memory"); }
__device__ __forceinline__ void GLOAD16N(u32x4& dst, unsigned voff, const char* sbase) { asm volatile("global_load_dwordx4 %0, %1, %2" : "=v"(dst) : "v"(voff), "s"(sbase) : "memory"); }
__device__ __forceinline__ void GLOAD16N_IF(u32x4& dst, unsigned voff, const char* sbase, int cond) {
    asm volatile("s_cmp_eq_u32 %3, 0\n\ts_cbranch_scc1 1f\n\tglobal_load_dwordx4 %0, %1, %2\n1:" : "=v"(dst) : "v"(voff), "s"(sbase), "s"(cond) : "memory", "scc"); }
template <int N> __device__ __forceinline__ void waitv5(u32x4& a, u32x4& b, u32x4& c, u32x4& d, u32x4& e) { asm volatile("s_waitcnt vmcnt(%5)" : "+v"(a), "+v"(b), "+v"(c), "+v"(d), "+v"(e) : "i"(N) : "memory"); }
__device__ __forceinline__ void gload8x2_sc1(unsigned long long& a, unsigned long long& b, unsigned voff, const char* sbase) {
    asm volatile("global_load_dwordx2 %0, %2, %3 sc1\n\tglobal_load_dwordx2 %1, %2, %3 offset:512 sc1" : "=&v"(a), "=&v"(b) : "v"(voff), "s"(sbase) : "memory"); }
__device__ __forceinline__ void waitv8x2(unsigned long long& a, unsigned long long& b) { asm volatile("s_waitcnt vmcnt(0)" : "+v"(a), "+v"(b) :: "memory"); }
__device__ __forceinline__ void PIN2(u32x4& a, u32x4& b) { asm volatile("" : "+v"(a), "+v"(b) :: "memory"); }
__device__ __forceinline__ u32x4 scale8(unsigned x0, unsigned x1, unsigned x2, unsigned x3, unsigned clo, unsigned chi, float c) {
    unsigned o0, o1, o2, o3; float t0, t1, t2, t3, t4, t5, t6, t7;
    asm("v_dot2_f32_bf16 %4, %12, %16, %18\n\tv_dot2_f32_bf16 %5, %12, %17, %18\n\tv_dot2_f32_bf16 %6, %13, %16, %18\n\tv_dot2_f32_bf16 %7, %13, %17, %18\n\t"
        "v_dot2_f32_bf16 %8, %14, %16, %18\n\tv_dot2_f32_bf16 %9, %14, %17, %18\n\tv_dot2_f32_bf16 %10, %15, %16, %18\n\tv_dot2_f32_bf16 %11, %15, %17, %18\n\t"
        "v_cvt_pk_bf16_f32 %0, %4, %5\n\tv_cvt_pk_bf16_f32 %1, %6, %7\n\tv_cvt_pk_bf16_f32 %2, %8, %9\n\tv_cvt_pk_bf16_f32 %3, %10, %11"
        : "=&v"(o0), "=&v"(o1), "=&v"(o2), "=&v"(o3), "=&v"(t0), "=&v"(t1), "=&v"(t2), "=&v"(t3), "=&v"(t4), "=&v"(t5), "=&v"(t6), "=&v"(t7)
        : "v"(x0), "v"(x1), "v"(x2), "v"(x3), "v"(clo), "v"(chi), "v"(c));
    return u32x4{o0, o1, o2, o3};
}
template <int N> __device__ __forceinline__ void waitv3(u32x4& a, u32x4& b, u32x4& c) { asm volatile("s_waitcnt vmcnt(%3)" : "+v"(a), "+v"(b), "+v"(c) : "i"(N) : "memory"); }

constexpr int dn_younger(int k, int D, bool wo) {
    int n = 0, la = 0, lb = 0;
    for (int i = 0; i < D; ++i) { n += 2; if (i == k + 1) lb = n; }
    for (int i = 0; i < D; ++i) { n += 3; if (i == k) la = n; }
    for (int s = 0; s < k; ++s) {
        if (s + D < 24) { n += 2; if (s + D == k + 1) lb = n; }
        if (wo && s >= 16) n += 2;
        if (s + D < 24) { n += 3; if (s + D == k) la = n; }
    }
    return n - (la > lb ? la : lb);
}
constexpr float QSCALE = 0.22360679774997896f * 1.4426950408889634f;
constexpr float BOUND_MAX = 50.f;

struct AttSync { int lane; const char* kv; unsigned dvoff; unsigned wo; int w; };
__device__ __forceinline__ unsigned kvslot(int c) { return L_KV + ((c + 2) & 3) * CHUNK; }
__device__ __forceinline__ void att_dma(const AttSync& a, int c) { glds16x3_sc1(a.dvoff, a.kv + (size_t)c * CHUNK, kvslot(c) + a.wo); }
constexpr unsigned AP0 = 16;
#define RING_X(c) do { WAITV(3); lpost(C_AP + as.w, AP0 + (c) + 1); } while (0)
#define RP_A
#define RP_B
#define RING_Y(c, pk) do { RP_A lcheck(pk, C_AP, AP0 + (c) + 1, lane); const int cn_ = (c) + 3 < NCHUNK ? (c) + 3 : NCHUNK - 1;   \
                           glds16x3_sc1(as.dvoff, as.kv + (size_t)cn_ * CHUNK, kvslot((c) + 3) + as.wo); RP_B } while (0)

__device__ __forceinline__ void att_fast(const AttSync& as, bf16x8 Q0a, bf16x8 Q1a0, bf16x8 Q1a1, bf16x8 Q0b, bf16x8 Q1b0, bf16x8 Q1b1, f32x16& Oa, f32x16& Ob) {
    const int lane = as.lane, r = lane & 31, h = lane >> 5;
    const unsigned ak0 = lane * 16, ak1 = h ? KV_V + 20 * 16 : KV_K1 + r * 16;
    const unsigned av = KV_V + h * 352 + (r <= 20 ? r : 21) * 16;
    const f32x16 zero = {};
    f32x16 Sa, Sb = {};
    bf16x8 K0[2], K1[2], V0[2], V1[2];
    u32x4 Pa0, Pa1, Pb0 = {}, Pb1 = {};
    unsigned pk = 0;
    unsigned long long rp0 = 0, rp1 = 0;
    V0[1] = bf16x8{}; V1[1] = bf16x8{};
    unsigned vk0 = L_KV + ak0, vk1 = L_KV + ak1, vv = L_KV + av;
    PIN(vk0); PIN(vk1); PIN(vv);
    K0[0] = ldsf(vk0 + 2 * CHUNK); K1[0] = ldsf(vk1 + 2 * CHUNK); K0[1] = ldsf(vk0 + 2 * CHUNK + 1024); V1[0] = ldsf(vv + 2 * CHUNK + 704); V0[0] = ldsf(vv + 2 * CHUNK);
    Sa = MFMA(K0[0], Q0a, zero); Sa = MFMA(K1[0], Q1a0, Sa);
#define EX4(S, i) do { S[i] = __builtin_amdgcn_exp2f(S[i]); S[i + 1] = __builtin_amdgcn_exp2f(S[i + 1]); S[i + 2] = __builtin_amdgcn_exp2f(S[i + 2]); S[i + 3] = __builtin_amdgcn_exp2f(S[i + 3]); } while (0)
#define GAP(MF, LD, T, k, PW, j, S, i) do { MF; SBAR(); LD; PW[j] = pk2(T[k], T[k + 1]); PW[j + 1] = pk2(T[k + 2], T[k + 3]); PIN(PW); SBAR(); EX4(S, i); PIN(S); SBAR(); } while (0)
#define BC(x) __builtin_bit_cast(bf16x8, x)
#define STEP(e, d, QB1, QA1N, KN, L3, LK0, LK1, LV) do { \
        GAP(Ob = MFMA(V0[e ^ 1], BC(Pb0), Ob), (void)0,                  Sb, 12, Pb1, 2, Sa, 0); \
        GAP(Sb = MFMA(K0[e], Q0b, zero),       (void)0,                  Sa, 0, Pa0, 0, Sa, 4); \
        GAP(Sb = MFMA(K1[d], QB1, Sb),         L3,                       Sa, 4, Pa0, 2, Sa, 8); \
        GAP(Ob = MFMA(V1[e ^ 1], BC(Pb1), Ob), K0[e] = ldsf(LK0),        Sa, 8, Pa1, 0, Sa, 12); \
        GAP(Oa = MFMA(V0[e], BC(Pa0), Oa),     LK1,                      Sa, 12, Pa1, 2, Sb, 0); \
        GAP(Sa = MFMA(K0[e ^ 1], Q0a, zero),   V1[e ^ 1] = ldsf((LV) + 704), Sb, 0, Pb0, 0, Sb, 4); \
        GAP(Sa = MFMA(K1[KN], QA1N, Sa),       V0[e ^ 1] = ldsf(LV),     Sb, 4, Pb0, 2, Sb, 8); \
        GAP(Oa = MFMA(V1[e], BC(Pa1), Oa),     (void)0,                  Sb, 8, Pb1, 0, Sb, 12); \
    } while (0)
#define CHUNK4(c, B, N) do { \
        STEP(0, 0, Q1b0, Q1a1, 0, (void)0, vk0 + (B) + KVD, K1[1] = ldsf(vk1 + (B) + KVD), vv + (B) + KV_VT); \
        RING_X(c); \
        STEP(1, 0, Q1b1, Q1a0, 1, pk = lpeek(C_AP, lane), vk0 + (B) + KVD + 1024, (void)0, vv + (B) + KVD); \
        RING_Y(c, pk); \
        STEP(0, 1, Q1b0, Q1a1, 1, (void)0, vk0 + (N), K1[0] = ldsf(vk1 + (N)), vv + (B) + KVD + KV_VT); \
        STEP(1, 1, Q1b1, Q1a0, 0, (void)0, vk0 + (N) + 1024, (void)0, vv + (N)); \
    } while (0)
    for (int c = 0; c < NCHUNK; c += 4) {
        CHUNK4(c, 2 * CHUNK, 3 * CHUNK); CHUNK4(c + 1, 3 * CHUNK, 0); CHUNK4(c + 2, 0, CHUNK); CHUNK4(c + 3, CHUNK, 2 * CHUNK);
    }
#undef CHUNK4
    Pb1[2] = pk2(Sb[12], Sb[13]); Pb1[3] = pk2(Sb[14], Sb[15]);
    Ob = MFMA(V0[1], BC(Pb0), Ob); Ob = MFMA(V1[1], BC(Pb1), Ob);
#undef STEP
#undef GAP
#undef EX4
}

__device__ __forceinline__ void att_safe(const AttSync& as, bf16x8 Q0a, bf16x8 Q1a0, bf16x8 Q1a1, bf16x8 Q0b, bf16x8 Q1b0, bf16x8 Q1b1, f32x16& Oa, f32x16& Ob) {
    const int lane = as.lane, r = lane & 31, h = lane >> 5;
    const unsigned ak0 = lane * 16, ak1 = h ? KV_V + 20 * 16 : KV_K1 + r * 16;
    const unsigned av = KV_V + h * 352 + (r <= 20 ? r : 21) * 16;
    const f32x16 zero = {};
    float ma = -INFINITY, mb = -INFINITY;
    unsigned long long rp0 = 0, rp1 = 0;
    for (int c = 0; c < NCHUNK; ++c) {
        const unsigned b0 = kvslot(c);
        for (int t = 0; t < 4; ++t) {
            if (t == 1) RING_X(c);
            if (t == 2) RING_Y(c, 0u);
            const unsigned base = b0 + (t >> 1) * KVD; const int e = t & 1;
            bf16x8 K0 = ldsf(base + ak0 + e * 1024), K1 = ldsf(base + ak1), V0 = ldsf(base + av + e * KV_VT), V1 = ldsf(base + av + e * KV_VT + 704);
#pragma unroll
            for (int q = 0; q < 2; ++q) {
                f32x16 S = MFMA(K0, q ? Q0b : Q0a, zero); S = MFMA(K1, q ? (e ? Q1b1 : Q1b0) : (e ? Q1a1 : Q1a0), S);
                float mx = S[0];
#pragma unroll
                for (int i = 1; i < 16; ++i) mx = fmaxf(mx, S[i]);
                mx = fmaxf(mx, __shfl_xor(mx, 32));
                float& m = q ? mb : ma; f32x16& O = q ? Ob : Oa;
                const float mn = fmaxf(m, mx);
                const float al = __builtin_amdgcn_exp2f(m - mn);
                m = mn;
#pragma unroll
                for (int i = 0; i < 16; ++i) { O[i] *= al; S[i] = __builtin_amdgcn_exp2f(S[i] - mn); }
                O = MFMA(V0, pack8(S, 0, 1.f), O); O = MFMA(V1, pack8(S, 8, 1.f), O);
            }
        }
    }
}

__device__ __forceinline__ void xload4(u32x4 (&x)[5], const char* xb, unsigned vo) {
    asm volatile("global_load_dwordx4 %0, %5, %6\n\tglobal_load_dwordx4 %1, %5, %6 offset:256\n\tglobal_load_dwordx4 %2, %5, %6 offset:512\n\t"
                 "global_load_dwordx4 %3, %5, %6 offset:768\n\tglobal_load_dwordx4 %4, %5, %6 offset:1024"
                 : "=&v"(x[0]), "=&v"(x[1]), "=&v"(x[2]), "=&v"(x[3]), "=&v"(x[4]) : "v"(vo), "s"(xb) : "memory");
}
__device__ __forceinline__ void ln_rows(const u32x4 (&xr)[5], int row0, int lane) {
    f32x2 xv[20];
    f32x2 s0 = {0.f, 0.f}, s1 = {0.f, 0.f}, q0 = {0.f, 0.f}, q1 = {0.f, 0.f};
#pragma unroll
    for (int i = 0; i < 20; i += 2) {
        const unsigned a = xr[i >> 2][i & 3], b = xr[i >> 2][(i & 3) + 1];
        xv[i] = f32x2{bflo(a), bfhi(a)}; xv[i + 1] = f32x2{bflo(b), bfhi(b)};
        s0 += xv[i]; q0 = __builtin_elementwise_fma(xv[i], xv[i], q0);
        s1 += xv[i + 1]; q1 = __builtin_elementwise_fma(xv[i + 1], xv[i + 1], q1);
    }
    s0 += s1; q0 += q1;
    const float s = dpp_sum16(s0[0] + s0[1]), ss = dpp_sum16(q0[0] + q0[1]);
    const float mean = s * (1.f / 640.f);
    const float var = fmaxf(ss * (1.f / 640.f) - mean * mean, 0.f);
    const float rs = __builtin_amdgcn_rcpf(rbf(__builtin_amdgcn_sqrtf(rbf(rbf(var) + 1e-5f)))), mo = -rbf(mean) * rs;
    const f32x2 rs2 = {rs, rs}, mo2 = {mo, mo};
    const int row = row0 + (lane >> 4), c = lane & 15;
    const unsigned wa = L_PANEL + (row >> 5) * 40960 + (c >> 1) * 1024 + (c & 1) * 512 + (((row & 31) ^ (c & 7)) << 4);
#pragma unroll
    for (int u = 0; u < 5; ++u) {
        u32x4 o;
#pragma unroll
        for (int e = 0; e < 4; ++e) { const f32x2 y = __builtin_elementwise_fma(xv[4 * u + e], rs2, mo2); o[e] = pk2(y[0], y[1]); }
        *LP(u32x4, wa + u * 8192) = o;
    }
}
#define BLOCK_BARRIER() asm volatile("s_waitcnt lgkmcnt(0)\n\ts_barrier" ::: "memory")

template <int PH>
__global__ void __launch_bounds__(512, 2) mega(Params p) {
    constexpr bool PRO = PH & 1, ATT = PH & 2, DN = PH & 8, WO = PH & 16;
    const int w = __builtin_amdgcn_readfirstlane(threadIdx.x >> 6);
    const int b = blockIdx.x, xc = b & 7, j = b >> 3;
    const int ti = (xc >> 1) * 8 + (j >> 2), g = (xc & 1) * 4 + (j & 3);
    unsigned tag = 1u, epv = 0u;
    constexpr bool FUSED = PH == 27;
    if constexpr (FUSED) asm volatile("global_load_dword %0, %1, %2 sc1" : "=v"(epv) : "v"(0u), "s"(p.ws + WS_EP) : "memory");
#define GET_TAG() do { if constexpr (FUSED) { asm volatile("s_nop 0\n\tv_readfirstlane_b32 %0, %1\n\ts_nop 3" : "=s"(tag) : "v"(epv) : "memory"); tag += 0x9E3779B1u; } } while (0)
    char* ws = p.ws;
    STAMP(0);
    {
        constexpr unsigned CODE_PF = 61440;
        const char* pc; asm volatile("s_getpc_b64 %0" : "=s"(pc));
        const unsigned line = (unsigned)(j * 8) * 64u + threadIdx.x;
        unsigned keep;
        if (line * 128u < CODE_PF)
            asm volatile("s_mov_b32 %0, m0\n\ts_mov_b32 m0, %3\n\ts_nop 2\n\tglobal_load_lds_dword %1, %2\n\ts_mov_b32 m0, %0" : "=&s"(keep) : "v"(line * 128u), "s"(pc), "s"(L_MISC + 1024) : "memory");
    }
    if (threadIdx.x < C_N) *lcnt(threadIdx.x) = (threadIdx.x == C_WP || threadIdx.x == C_WP + 1 || threadIdx.x == C_PP || threadIdx.x == C_PP + 1) ? 100u : 0u;
    const bool haskv = (g >> 2) == (ti & 1);
    const char* wqb = p.wq + g * 160;
    const int kmt = g & 1;
    const char* xb = p.x + (size_t)ti * 64 * 1280;
#define LANE_CONSTS() int lane = __builtin_amdgcn_mbcnt_hi(~0u, __builtin_amdgcn_mbcnt_lo(~0u, 0u)); PIN(lane); \
    const int r = lane & 31, h = lane >> 5, tq = (lane & 15) >> 2, tp = lane & 3, tb = (lane >> 4) & 1; (void)r; (void)h; (void)tq; (void)tp; (void)tb

    if (w < 4) {
        LANE_CONSTS();
        const unsigned xvo = (lane >> 4) * 1280 + (lane & 15) * 16;
        bf16x8 Q0a, Q0b; u32x2 q1a, q1b;
        const int head = 4 * g + w;
        const AttSync as{lane, ws + WS_KV, (unsigned)((w < 3 ? w * 3072 : CHUNK - 3072) + lane * 16), (unsigned)(w < 3 ? w * 3072 : CHUNK - 3072), w};
        __builtin_amdgcn_s_setprio(3);
        unsigned long long fk = 0, fv = 0; bool early = false;
        if constexpr (PRO) {
            u32x4 x0[5], x1[5], x2[5], x3[5];
            const int ra = kmt * 32 + w * 8, rb = (kmt ^ 1) * 32 + w * 8;
            xload4(x0, xb + (size_t)ra * 1280, xvo); xload4(x1, xb + (size_t)(ra + 4) * 1280, xvo);
            xload4(x2, xb + (size_t)rb * 1280, xvo); xload4(x3, xb + (size_t)(rb + 4) * 1280, xvo);
            BLOCK_BARRIER();
            waitv5<15>(x0[0], x0[1], x0[2], x0[3], x0[4]);
            GET_TAG();
            STAMP(1);
            ln_rows(x0, ra, lane);
            waitv5<10>(x1[0], x1[1], x1[2], x1[3], x1[4]);
            ln_rows(x1, ra + 4, lane);
            lpost(C_AP + w, 1);
            waitv5<5>(x2[0], x2[1], x2[2], x2[3], x2[4]);
            ln_rows(x2, rb, lane);
            waitv5<0>(x3[0], x3[1], x3[2], x3[3], x3[4]);
            ln_rows(x3, rb + 4, lane);
            lpost(C_AP + w, 2);
            unsigned pa[4];
#pragma unroll
            for (int s = 0; s < 4; ++s) pa[s] = L_PANEL + h * 512 + ((r ^ ((2 * s + h) & 7)) << 4);
            f32x16 qa = {}, qb = {};
            const unsigned wa = L_WQ + (8 * h + tq) * 160 + w * 40 + tb * 32 + (tb ? 0 : tp * 8);
            lwait_all(C_AP, 4, 2, lane); lwait_all(C_PP, 4, 1, lane);
            STAMP(2);
            bf16x8 fw[2][4], fa[2][4], fb[2][4];
#pragma unroll
            for (int s4 = 0; s4 < 4; ++s4) { fw[0][s4] = ldstr2(wa + s4 * 2560, wa + s4 * 2560 + 640); fa[0][s4] = ldsf(pa[s4] + s4 * 1024); fb[0][s4] = ldsf(pa[s4] + 40960 + s4 * 1024); }
            unsigned pk = 0;
            static_for<0, 10>([&](auto S) {
                constexpr int st = S.value, nx = st + 1;
                if constexpr (st == 3 || st == 5 || st == 7) lcheck(pk, C_WP, (st - 1) / 2, lane);
                if constexpr (st == 6 && ATT) gload8x2_sc1(fk, fv, lane * 8, ws + WS_KF + xc * 1024);
                if constexpr (st == 8 && ATT) {
                    waitv8x2(fk, fv);
                    early = __all((unsigned)(fk >> 32) == tag && (unsigned)(fv >> 32) == tag);
                    if (early) { lwait_all(C_AP, 4, 6, lane); att_dma(as, 0); att_dma(as, 1); }
                    STAMP(8);
                }
#pragma unroll
                for (int s4 = 0; s4 < 4; ++s4) {
                    qa = MFMA(fw[st & 1][s4], fa[st & 1][s4], qa); SBAR();
                    if constexpr (nx < 10) { const unsigned o = wa + (nx & 3) * 10240 + s4 * 2560; fw[nx & 1][s4] = ldstr2(o, o + 640); SBAR(); }
                    qb = MFMA(fw[st & 1][s4], fb[st & 1][s4], qb); SBAR();
                    if constexpr (nx < 10) { fa[nx & 1][s4] = ldsf(pa[s4] + (nx * 4 + s4) * 1024); fb[nx & 1][s4] = ldsf(pa[s4] + 40960 + (nx * 4 + s4) * 1024); SBAR(); }
                    if (s4 == 2 && (st == 2 || st == 4 || st == 6)) { pk = lpeek(C_WP, lane); SBAR(); }
                }
                if constexpr (!(st & 1) && st <= 6) lpost(C_AP + w, 3 + st / 2);
            });
            lpost(C_AP + w, 8);
            Q0a = pack8(qa, 0, QSCALE); Q0b = pack8(qb, 0, QSCALE);
            q1a = u32x2{pk2(qa[8] * QSCALE, qa[9] * QSCALE), pk2(qa[10] * QSCALE, qa[11] * QSCALE)};
            q1b = u32x2{pk2(qb[8] * QSCALE, qb[9] * QSCALE), pk2(qb[10] * QSCALE, qb[11] * QSCALE)};
            if (h) { q1a = u32x2{0u, 0u}; q1b = u32x2{0u, 0u}; }
            if constexpr (!ATT) {
                char* qt = ws + WS_QT + (size_t)((2 * ti) * 32 + head) * 2048 + lane * 16;
                *(bf16x8*)qt = Q0a; *(u32x2*)(qt + 1024) = q1a; *(bf16x8*)(qt + 65536) = Q0b; *(u32x2*)(qt + 65536 + 1024) = q1b;
            }
            STAMP(3);
        } else {
            BLOCK_BARRIER();
            if constexpr (ATT) {
                const char* qt = ws + WS_QT + (size_t)((2 * ti) * 32 + head) * 2048 + lane * 16;
                Q0a = *(const bf16x8*)qt; q1a = *(const u32x2*)(qt + 1024); Q0b = *(const bf16x8*)(qt + 65536); q1b = *(const u32x2*)(qt + 65536 + 1024);
            }
        }
        if constexpr (ATT) {
            float na = 0.f, nb = 0.f;
            {
                const u32x4 a0 = __builtin_bit_cast(u32x4, Q0a), b0 = __builtin_bit_cast(u32x4, Q0b);
#pragma unroll
                for (int e = 0; e < 4; ++e) { na += bflo(a0[e]) * bflo(a0[e]) + bfhi(a0[e]) * bfhi(a0[e]); nb += bflo(b0[e]) * bflo(b0[e]) + bfhi(b0[e]) * bfhi(b0[e]); }
#pragma unroll
                for (int e = 0; e < 2; ++e) { na += bflo(q1a[e]) * bflo(q1a[e]) + bfhi(q1a[e]) * bfhi(q1a[e]); nb += bflo(q1b[e]) * bflo(q1b[e]) + bfhi(q1b[e]) * bfhi(q1b[e]); }
                na += __shfl_xor(na, 32); nb += __shfl_xor(nb, 32);
            }
            if (!early) {
                gu64* f = (gu64*)(ws + WS_KF + xc * 1024) + lane;
                _Pragma("clang loop unroll(disable)") for (unsigned spins = 0; spins < (1u << 14); ++spins) {
                    fk = __hip_atomic_load(f, RLX_AGENT); fv = __hip_atomic_load(f + 64, RLX_AGENT);
                    if (__all((unsigned)(fk >> 32) == tag && (unsigned)(fv >> 32) == tag)) break;
                    TMO(spins, 1u << 14, 301, tag);
                    __builtin_amdgcn_s_sleep(4);
                }
                if constexpr (PRO) lwait_all(C_AP, 4, 6, lane);
                att_dma(as, 0); att_dma(as, 1);
            }
            STAMP(4);
            if constexpr (PRO) lwait_all(C_AP, 4, 8, lane);
            att_dma(as, 2);
            const float kn2 = wave_max(__uint_as_float((unsigned)fk)), vmx = wave_max(__uint_as_float((unsigned)fv));
            const float Ma = __builtin_sqrtf(na * kn2) * 1.01f, Mb = __builtin_sqrtf(nb * kn2) * 1.01f;
            const bool fast = __all(Ma <= BOUND_MAX && Mb <= BOUND_MAX && vmx <= 1e30f);
            const unsigned sa = (fast && h) ? pk2(-Ma, 0.f) : q1a[0], sb = (fast && h) ? pk2(-Mb, 0.f) : q1b[0];
            const u32x4 a0 = {sa, q1a[1], 0u, 0u}, a1 = {h ? sa : 0u, 0u, q1a[0], q1a[1]}, b0 = {sb, q1b[1], 0u, 0u}, b1 = {h ? sb : 0u, 0u, q1b[0], q1b[1]};
            f32x16 Oa = {}, Ob = {};
            WAITV(6); lpost(C_AP + w, AP0); lwait_all(C_AP, 4, AP0, lane);
            __builtin_amdgcn_s_setprio(ATT_PRIO);
            STAMP(5); STAMPC(12);
            if (fast) att_fast(as, Q0a, BC(a0), BC(a1), Q0b, BC(b0), BC(b1), Oa, Ob);
            else att_safe(as, Q0a, BC(a0), BC(a1), Q0b, BC(b0), BC(b1), Oa, Ob);
            STAMP(6); STAMPC(13);
            const __amdgpu_buffer_rsrc_t ro = mkrsrc(ws + WS_OT, 64 * 40 * 1024);
#pragma unroll
            for (int q = 0; q < 2; ++q) {
                const f32x16& O = q ? Ob : Oa;
                const float inv = 1.f / __shfl(O[8], r + 32);
                const unsigned base = ((2 * ti + q) * 40 + 5 * g) * 1024;
                __builtin_amdgcn_raw_buffer_store_b128(__builtin_bit_cast(u32x4, pack8(O, 0, inv)), ro, base + w * 1024 + lane * 16, 0, 16);
                if (!h) {
                    u32x2 t = {pk2(O[8] * inv, O[9] * inv), pk2(O[10] * inv, O[11] * inv)};
                    __builtin_amdgcn_raw_buffer_store_b64(t, ro, base + 4096 + ((w >> 1) * 32 + r) * 16 + (w & 1) * 8, 0, 16);
                }
            }
            if constexpr (WO) {
                WAITV(0);
                if (lane < 2) __hip_atomic_store((gu32*)(ws + WS_OF) + (2 * ti + lane) * 32 + head, tag, RLX_AGENT);
            }
            STAMP(7);
        }
    } else {
        LANE_CONSTS();
        const unsigned xvo = (lane >> 4) * 1280 + (lane & 15) * 16;
        const int v = w - 4;
        __builtin_amdgcn_s_setprio(3);
        if constexpr (PRO) {
            const int kvm = 2 * ti + (g & 1); const bool isV = (g >> 1) & 1;
            const char* wkvb = isV ? p.wv : p.wk;
            if (haskv && v < 2) {
#pragma unroll
                for (int n = 0; n < 13; ++n) {
                    const unsigned o = v * 12800 + (n < 12 ? n * 1024 : 11776);
                    glds16(o + lane * 16, wkvb, L_WKV + o);
                }
            }
            BLOCK_BARRIER();
            if (v >= 2) {
                const int v2 = v - 2;
                unsigned wo5[5];
#pragma unroll
                for (int i = 0; i < 5; ++i) { const unsigned c = (v2 + 2 * i) * 64 + lane, row = c / 10u, col = c - row * 10u; wo5[i] = row * 1280 + col * 16; }
#pragma unroll
                for (int n = 0; n < 20; ++n) glds16(wo5[n % 5], wqb + (n / 5) * 64 * 1280, L_WQ + (v2 + 2 * n) * 1024);
                u32x4 wst[3][10];
                auto wst_issue = [&](auto I) {
                    constexpr int rr = I.value / 10, i = I.value % 10;
                    GLOAD16N(wst[rr][i], wo5[i % 5], wqb + (4 + 2 * rr + i / 5) * 64 * 1280);
                };
                static_for<0, 10>(wst_issue);
                WAITV(10); lpost(C_PP + v, 1);
                GET_TAG();
                STAMP(2);
                static_for<10, 30>(wst_issue);
                static_for<0, 3>([&](auto R) {
                    constexpr int rr = R.value;
                    waitv5<20 - 10 * rr>(wst[rr][0], wst[rr][1], wst[rr][2], wst[rr][3], wst[rr][4]); waitv5<63>(wst[rr][5], wst[rr][6], wst[rr][7], wst[rr][8], wst[rr][9]);
                    lwait_all(C_AP, 4, 3 + rr, lane);
#pragma unroll
                    for (int i = 0; i < 10; ++i) *LP(u32x4, L_WQ + (rr & 1) * 20480 + (v2 + 2 * i) * 1024 + lane * 16) = wst[rr][i];
                    lpost(C_WP + v, rr + 1);
                });
            } else {
              if (haskv) {
                f32x16 acc = {};
                const unsigned wa = L_WKV + (v * 320 + 8 * h + tq) * 40 + tb * 32 + (tb ? 0 : tp * 8);
                unsigned xa[4];
#pragma unroll
                for (int s = 0; s < 4; ++s) xa[s] = L_PANEL + kmt * 40960 + v * 20480 + h * 512 + ((r ^ ((2 * s + h) & 7)) << 4);
                WAITV(0);
                GET_TAG();
                STAMP(2);
                bf16x8 wf[20], xf[20];
#pragma unroll
                for (int s = 0; s < 20; ++s) wf[s] = ldstr2(wa + s * 640, wa + s * 640 + 160);
                lwait_all(C_AP, 4, 1, lane);
#pragma unroll
                for (int s = 0; s < 20; ++s) xf[s] = ldsf(xa[s & 3] + s * 1024);
                SBAR();
                if (isV) {
#pragma unroll
                    for (int s = 0; s < 20; ++s) acc = MFMA(xf[s], wf[s], acc);
                } else {
#pragma unroll
                    for (int s = 0; s < 20; ++s) acc = MFMA(wf[s], xf[s], acc);
                }
                if (v) {
#pragma unroll
                    for (int e = 0; e < 4; ++e) *LP(f32x4, L_WKV + 12800 + e * 1024 + lane * 16) = f32x4{acc[4 * e], acc[4 * e + 1], acc[4 * e + 2], acc[4 * e + 3]};
                    lpost(C_KVP, 1);
                } else {
                    lwait(C_KVP, 1);
#pragma unroll
                    for (int e = 0; e < 4; ++e) {
                        const f32x4 t = *LP(const f32x4, L_WKV + 12800 + e * 1024 + lane * 16);
                        acc[4 * e] += t[0]; acc[4 * e + 1] += t[1]; acc[4 * e + 2] += t[2]; acc[4 * e + 3] += t[3];
                    }
                    const __amdgpu_buffer_rsrc_t rk = mkrsrc(ws + WS_KV, 32 * KVD);
                    const unsigned base = ti * KVD, ke = g & 1;
                    const u32x4 f0 = __builtin_bit_cast(u32x4, pack8(acc, 0, 1.f)), f1 = __builtin_bit_cast(u32x4, pack8(acc, 8, 1.f));
                    float mx;
                    if (!isV) {
                        __builtin_amdgcn_raw_buffer_store_b128(f0, rk, base + ke * 1024 + lane * 16, 0, 16);
                        const u32x2 k1 = {f1[0], f1[1]};
                        if (!h) __builtin_amdgcn_raw_buffer_store_b64(k1, rk, base + KV_K1 + r * 16 + ke * 8, 0, 16);
                        lpost(C_KVFIN, 1);
                        float n2 = 0.f;
#pragma unroll
                        for (int e = 0; e < 4; ++e) n2 += bflo(f0[e]) * bflo(f0[e]) + bfhi(f0[e]) * bfhi(f0[e]);
                        if (!h) n2 += bflo(f1[0]) * bflo(f1[0]) + bfhi(f1[0]) * bfhi(f1[0]) + bflo(f1[1]) * bflo(f1[1]) + bfhi(f1[1]) * bfhi(f1[1]);
                        n2 += __shfl_xor(n2, 32);
                        mx = (n2 == n2) ? n2 : INFINITY;
                    } else {
                        const unsigned cst = r == 20 ? 0x3f803f80u : 0u;
                        const u32x4 c4 = {cst, cst, cst, cst};
                        if (r <= 21) {
                            __builtin_amdgcn_raw_buffer_store_b128(r >= 20 ? c4 : f0, rk, base + KV_V + ke * KV_VT + h * 352 + r * 16, 0, 16);
                            __builtin_amdgcn_raw_buffer_store_b128(r >= 20 ? c4 : f1, rk, base + KV_V + ke * KV_VT + 704 + h * 352 + r * 16, 0, 16);
                        }
                        lpost(C_KVFIN, 1);
                        float vm = 0.f;
#pragma unroll
                        for (int e = 0; e < 16; ++e) { const float a = fabsf(acc[e]); vm = (a <= vm) ? vm : ((a == a) ? a : INFINITY); }
                        mx = r < 20 ? vm : 0.f;
                    }
                    mx = wave_max(mx);
                    WAITV(0);
                    STAMP(15);
                    if (lane < 8) __hip_atomic_store((gu64*)(ws + WS_KF + lane * 1024 + (isV ? 512 : 0)) + kvm, ((unsigned long long)tag << 32) | __float_as_uint(mx), RLX_AGENT);
                }
            }
              else { WAITV(0); GET_TAG(); }
              STAMP(3);
            }
            if (haskv) lwait(C_KVFIN, 1);
#pragma unroll
            for (int n = 0; n < 2; ++n) {
                unsigned L = n * 64 + lane; L = (j * 4 + v) * 120 + (L < 120u ? L : 119u);
                const unsigned m3 = L >= 7680u, rem = L - (m3 ? 7680u : 0u), row = rem / 12u;
                l2touch((m3 ? p.w3 : p.w1) + row * 3072 + (xc & 1) * 1536 + (rem - row * 12u) * 128);
            }
            lwait_all(C_AP, 4, GU_DELAY, lane);
            STAMP(4);
            {
                const char* wsrc = ((v >> 1) ? p.w3 : p.w1) + g * 384 + (size_t)(v & 1) * 8 * 3072;
                unsigned gvo[3];
#pragma unroll
                for (int n = 0; n < 3; ++n) {
                    const unsigned c = n * 64 + lane, row = c / 24u, col = (c - row * 24u) ^ ((row & 2) << 1);
                    gvo[n] = row * 3072 + col * 16 - n * 1024;
                }
                const unsigned gdst = L_GU + (v >> 1) * 6176 + (v & 1) * 3072;
                auto gdma = [&](auto S) { constexpr int s = S.value; glds16x3v(gvo[0], gvo[1], gvo[2], wsrc + (size_t)s * 16 * 3072, gdst + (s % 3) * GSL); };
                static_for<0, 3>(gdma);
                f32x16 acc[2][3] = {};
                unsigned bn[3], pa[4], pb[4];
#pragma unroll
                for (int n = 0; n < 3; ++n) bn[n] = L_GU + tb * 6176 + (8 * h + tq) * 384 + (((3 * v + n) * 32 + (tp >> 1) * 16) ^ ((unsigned)(tq >> 1) << 6)) + (tp & 1) * 8;
#pragma unroll
                for (int s = 0; s < 4; ++s) { pa[s] = L_PANEL + h * 512 + ((r ^ ((2 * s + h) & 7)) << 4); pb[s] = pa[s] + 40960; PIN(pb[s]); }
                bf16x8 wf[2][3], xa[2], xb2[2];
                unsigned vaddr = L_MISC + (C_GP + v) * 4, vone = 1u; PIN(vaddr); PIN(vone);
                auto rdw = [&](auto S, auto N) { constexpr int s = S.value, n = N.value; const unsigned o = bn[n] + (s % 3) * GSL; wf[s & 1][n] = ldstr2(o, o + 1536); };
                auto rdx = [&](auto S) { constexpr int s = S.value; xa[s & 1] = ldsf(pa[s & 3] + s * 1024); xb2[s & 1] = ldsf(pb[s & 3] + s * 1024); };
                WAITV(6); linc(vaddr, vone); lwait_all(C_GP, 4, 1u, lane);
                rdx(IC<0>{}); rdw(IC<0>{}, IC<0>{}); rdw(IC<0>{}, IC<1>{}); rdw(IC<0>{}, IC<2>{});
                WAITV(3); linc(vaddr, vone);
                static_for<0, 40>([&](auto S) {
                    constexpr int s = S.value; constexpr bool nx = s + 1 < 40;
                    unsigned pk = 0;
                    MPRIO(GU_HI, 0); acc[0][0] = MFMA(wf[s & 1][0], xa[s & 1], acc[0][0]); SBAR();
                    if constexpr (nx) rdx(IC<s + 1>{});
                    SBAR();
                    MPRIO(GU_HI, 1); acc[1][0] = MFMA(wf[s & 1][0], xb2[s & 1], acc[1][0]); SBAR();
                    if constexpr (nx) pk = lpeek(C_GP, lane);
                    SBAR();
                    MPRIO(GU_HI, 2); acc[0][1] = MFMA(wf[s & 1][1], xa[s & 1], acc[0][1]); SBAR();
                    MPRIO(GU_HI, 3); acc[1][1] = MFMA(wf[s & 1][1], xb2[s & 1], acc[1][1]); SBAR();
                    if constexpr (nx) { lcheck(pk, C_GP, s + 2, lane); rdw(IC<s + 1>{}, IC<0>{}); rdw(IC<s + 1>{}, IC<1>{}); rdw(IC<s + 1>{}, IC<2>{}); }
                    SBAR();
                    MPRIO(GU_HI, 4); acc[0][2] = MFMA(wf[s & 1][2], xa[s & 1], acc[0][2]); SBAR();
                    if constexpr (s + 3 < 40) gdma(IC<s + 3>{});
                    SBAR();
                    MPRIO(GU_HI, 5); acc[1][2] = MFMA(wf[s & 1][2], xb2[s & 1], acc[1][2]); SBAR();
                    if constexpr (s + 3 < 40) WAITV(3); else WAITV(0);
                    linc(vaddr, vone); SBAR();
                });
                __builtin_amdgcn_s_setprio(3);
                STAMP(6);
                const __amdgpu_buffer_rsrc_t rh = mkrsrc(ws + WS_HT, 64 * 96 * 1024);
#pragma unroll
                for (int m = 0; m < 2; ++m)
#pragma unroll
                    for (int n = 0; n < 3; ++n) {
                        f32x16 hv;
#pragma unroll
                        for (int e = 0; e < 8; ++e) {
                            const float gg = acc[m][n][e];
                            hv[e] = gg * __builtin_amdgcn_rcpf(1.f + __builtin_amdgcn_exp2f(-1.4426950408889634f * gg)) * acc[m][n][e + 8];
                        }
                        __builtin_amdgcn_raw_buffer_store_b128(__builtin_bit_cast(u32x4, pack8(hv, 0, 1.f)), rh, ((2 * ti + m) * 96 + g * 12 + 3 * v + n) * 1024 + lane * 16, 0, 16);
                    }
                if constexpr (DN) {
                    WAITV(0);
                    if (lane < 2) __hip_atomic_store((gu32*)(ws + WS_HF) + (2 * ti + lane) * 32 + g * 4 + v, tag, RLX_AGENT);
                }
                if constexpr (DN) {
#pragma unroll
                    for (int n = 0; n < 2; ++n) {
                        unsigned L = (j * 4 + v) * 85 + n * 64 + lane; L = L < 10880u ? L : 10879u;
                        const unsigned row = L / 5u;
                        l2touch((row < 1536u ? p.w2 + row * 1280 : p.wo + (row - 1536u) * 1280) + (xc & 1) * 640 + (L - row * 5u) * 128);
                    }
                }
                STAMP(7);
            }
        } else {
            BLOCK_BARRIER();
        }
        if constexpr (DN || WO) {
            LANE_CONSTS();
            const unsigned bq = ((tb * 32 + (tp >> 1) * 16) ^ ((unsigned)(tq >> 1) << 6)) + (tp & 1) * 8;
            const unsigned drow = lane >> 3, dcol = ((lane & 7) ^ (((lane >> 4) & 1) << 2)) * 16;
            const int G = xc >> 1, r0 = G < 2 ? 6 * G : 12 + 5 * (G - 2), nr = G < 2 ? 6 : 5;
            if constexpr (FUSED) {
                if (b == 255 && v == 0) {
                    gu32* f = (gu32*)(ws + WS_HF) + (lane >> 3) * 64 + (lane & 7) * 4;
                    _Pragma("clang loop unroll(disable)") for (unsigned spins = 0; spins < (1u << 14); ++spins) {
                        bool ok = true;
#pragma unroll
                        for (int n = 0; n < 4; ++n) ok &= __hip_atomic_load(f + n * 512, RLX_AGENT) == tag;
                        if (__all(ok)) break;
                        TMO(spins, 1u << 14, 303, tag);
                        __builtin_amdgcn_s_sleep(32);
                    }
                    if (lane == 0) __hip_atomic_store((gu32*)(ws + WS_EP), tag, RLX_AGENT);
                }
            }
            if (j < 5 * nr) {
                const int dr = r0 + j / 5, dc = (xc & 1) * 5 + j % 5;
                const int mfirst = dr < 20 ? 3 * dr : 60 + 2 * (dr - 20), nm = dr < 20 ? 3 : 2;
                const unsigned dvo = drow * 1280 + dcol;
                const unsigned myring = L_PANEL + v * 20480;
                f32x16 acc0 = {}, acc1 = {};
                if constexpr (PRO) { lwait_all(C_GP, 4, 42, lane); lwait_all(C_AP, 4, 8, lane); }
                if constexpr (DN) {
                    f32x16 da[3][2] = {};
                    const int drot = (j % 5) * 4;
                    const char* bsrc = p.w2 + (size_t)v * 384 * 1280 + dc * 128;
                    const char* asrc = ws + WS_HT + ((size_t)mfirst * 96 + v * 24) * 1024;
                    const unsigned am1 = lane * 16 + 98304, am2 = lane * 16 + (nm == 3 ? 196608 : 98304);
                    constexpr int DQ = DN_DQ;
                    u32x4 aq[DQ][3];
                    auto bissue = [&](auto K) {
                        constexpr int k = K.value;
                        int kr = k + drot; kr = kr >= 24 ? kr - 24 : kr;
                        const char* sb = bsrc + (size_t)kr * 16 * 1280;
                        glds16(dvo, sb, myring + (k % 10) * 2048); glds16(dvo + 8 * 1280, sb, myring + (k % 10) * 2048 + 1024);
                    };
                    auto woissue = [&](auto U) {
                        constexpr int u = U.value;
                        const char* sb = p.wo + (size_t)(v * 160 + u * 16) * 1280 + dc * 128;
                        glds16(dvo, sb, myring + u * 2048); glds16(dvo + 8 * 1280, sb, myring + u * 2048 + 1024);
                    };
                    auto aissue = [&](auto K) {
                        constexpr int k = K.value;
                        int kr = k + drot; kr = kr >= 24 ? kr - 24 : kr;
                        const char* sb = asrc + (size_t)kr * 1024;
                        GLOAD16S(aq[k % DQ][0], lane * 16, sb); GLOAD16S(aq[k % DQ][1], am1, sb); GLOAD16S(aq[k % DQ][2], am2, sb);
                    };
                    static_for<0, DQ>(bissue);
                    if constexpr (PRO) {
                        gu32* f = (gu32*)(ws + WS_HF) + (mfirst + ((lane >> 3) < nm ? (lane >> 3) : 0)) * 32 + 8 * v + (lane & 7);
                        _Pragma("clang loop unroll(disable)") for (unsigned spins = 0; spins < (1u << 14); ++spins) {
                            if (__all(__hip_atomic_load(f, RLX_AGENT) == tag)) break;
                            TMO(spins, 1u << 14, 302, tag);
                            __builtin_amdgcn_s_sleep(HF_SLEEP);
                        }
                        CFENCE();
                    }
                    STAMP(8);
                    static_for<0, DQ>(aissue);
                    const unsigned ba0 = myring + (4 * h + tq) * 128 + bq, ba1 = ba0 ^ 64;
                    bf16x8 w0[2], w1[2];
                    WAITVN(3 * DQ);
                    w0[0] = ldstr2(ba0, ba0 + 1024); w1[0] = ldstr2(ba1, ba1 + 1024);
                    static_for<0, 24>([&](auto K) {
                        constexpr int k = K.value;
                        waitv3<dn_younger(k, DQ, WO)>(aq[k % DQ][0], aq[k % DQ][1], aq[k % DQ][2]);
                        constexpr unsigned o = ((k + 1) % 10) * 2048;
#define DXF(m) __builtin_bit_cast(bf16x8, aq[k % DQ][m])
                        SBAR();
                        MPRIO(DN_HI, 0); da[0][0] = MFMA(w0[k & 1], DXF(0), da[0][0]); SBAR();
                        if constexpr (k + 1 < 24) w0[(k + 1) & 1] = ldstr2(ba0 + o, ba0 + o + 1024);
                        SBAR();
                        MPRIO(DN_HI, 1); da[0][1] = MFMA(w1[k & 1], DXF(0), da[0][1]); SBAR();
                        if constexpr (k + 1 < 24) w1[(k + 1) & 1] = ldstr2(ba1 + o, ba1 + o + 1024);
                        SBAR();
                        MPRIO(DN_HI, 2); da[1][0] = MFMA(w0[k & 1], DXF(1), da[1][0]); SBAR();
                        if constexpr (k + DQ < 24) bissue(IC<k + DQ>{});
                        if constexpr (WO && k >= 16) woissue(IC<(k + 8) % 10>{});
                        SBAR();
                        MPRIO(DN_HI, 3); da[1][1] = MFMA(w1[k & 1], DXF(1), da[1][1]); SBAR();
                        MPRIO(DN_HI, 4); da[2][0] = MFMA(w0[k & 1], DXF(2), da[2][0]); SBAR();
                        MPRIO(DN_HI, 5); da[2][1] = MFMA(w1[k & 1], DXF(2), da[2][1]); SBAR();
                        if constexpr (k + DQ < 24) aissue(IC<k + DQ>{});
                    });
                    __builtin_amdgcn_s_setprio(3);
                    STAMP(9);
                    if constexpr (WO) { CFENCE(); woissue(IC<2>{}); woissue(IC<3>{}); }
#pragma unroll
                    for (int n = 0; n < 2; ++n) {
#pragma unroll
                        for (int m = 0; m < 3; ++m) {
                            if (m != v) {
                                const unsigned dst = L_GU + (m * 3 + (v > m ? v - 1 : v)) * 4096 + lane * 16;
#pragma unroll
                                for (int e = 0; e < 4; ++e) *LP(f32x4, dst + e * 1024) = f32x4{da[m][n][4 * e], da[m][n][4 * e + 1], da[m][n][4 * e + 2], da[m][n][4 * e + 3]};
                            }
                        }
                        lpost(C_DP + v, 2 * n + 1); lwait_all(C_DP, 4, 2 * n + 1, lane);
                        if (v < 3) {
                            f32x16 t = v == 0 ? da[0][n] : (v == 1 ? da[1][n] : da[2][n]);
#pragma unroll
                            for (int o = 0; o < 3; ++o)
#pragma unroll
                                for (int e = 0; e < 4; ++e) {
                                    const f32x4 u = *LP(const f32x4, L_GU + (v * 3 + o) * 4096 + e * 1024 + lane * 16);
                                    t[4 * e] += u[0]; t[4 * e + 1] += u[1]; t[4 * e + 2] += u[2]; t[4 * e + 3] += u[3];
                                }
                            if (n) acc1 = t; else acc0 = t;
                        }
                        if (n == 0) { lpost(C_DP + v, 2); lwait_all(C_DP, 4, 2, lane); }
                    }
                    if constexpr (!WO) {
                        if (v < 3) {
                            float* pt = (float*)(ws + WS_PT) + (size_t)(b * 3 + v) * 2048 + lane;
#pragma unroll
                            for (int e = 0; e < 16; ++e) { pt[e * 64] = acc0[e]; pt[1024 + e * 64] = acc1[e]; }
                        }
                    }
                } else {
#pragma unroll
                    for (int u = 0; u < 20; ++u) glds16(dvo + u * 8 * 1280, p.wo + (size_t)v * 160 * 1280 + dc * 128, myring + u * 1024);
                    if (v < 3) {
                        const float* pt = (const float*)(ws + WS_PT) + (size_t)(b * 3 + v) * 2048 + lane;
#pragma unroll
                        for (int e = 0; e < 16; ++e) { acc0[e] = pt[e * 64]; acc1[e] = pt[1024 + e * 64]; }
                    }
                }
                if constexpr (WO) { WAITV(0); lpost(C_DP + v, 4); }
                if (v < nm) {
                    const int m = mfirst + v;
                    const unsigned ba0 = L_PANEL + (4 * h + tq) * 128 + bq, ba1 = ba0 ^ 64;
                    if constexpr (WO) {
                        const char* xrow = p.x + (size_t)(m * 32 + r) * 1280 + dc * 128 + h * 16;
                        u32x4 xres[4];
#pragma unroll
                        for (int e = 0; e < 4; ++e) xres[e] = *(const u32x4*)(xrow + e * 32);
                        lwait_all(C_DP, 4, 4, lane);
                        const __amdgpu_buffer_rsrc_t ro = mkrsrc(ws + WS_OT + (size_t)m * 40960, 40960);
                        gu32* of_ = (gu32*)(ws + WS_OF) + m * 32 + (lane & 31);
                        int hf = 0;
                        f32x16 t0[2] = {}, t1[2] = {};
#pragma unroll
                        for (int it = 0; it < 2; ++it) {
                            if constexpr (ATT) {
                                _Pragma("clang loop unroll(disable)") for (unsigned spins = 0; spins < (1u << 14); ++spins) {
                                    const unsigned ok = (unsigned)__ballot(__hip_atomic_load(of_, RLX_AGENT) == tag);
                                    const bool lo = (ok & 0xffffu) == 0xffffu, hi = (ok >> 16) == 0xffffu;
                                    if (it == 0) { if (lo || hi) { hf = lo ? 0 : 1; break; } }
                                    else if (hf ? hi : lo) break;
                                    TMO(spins, 1u << 14, 300, m);
                                    __builtin_amdgcn_s_sleep(OF_SLEEP);
                                }
                                CFENCE();
                            }
                            if (it == 0) STAMP(10);
                            u32x4 of[20];
#pragma unroll
                            for (int s = 0; s < 20; ++s) of[s] = __builtin_amdgcn_raw_buffer_load_b128(ro, lane * 16 + s * 1024, hf * 20480, 16);
                            const unsigned a0 = ba0 + hf * 40960, a1 = ba1 + hf * 40960, l0 = a0 + (36 * h + 16) * 128, l1 = a1 + (36 * h + 16) * 128;
                            bf16x8 w0[2], w1[2];
                            w0[0] = ldstr2(a0, a0 + 1024); w1[0] = ldstr2(a1, a1 + 1024);
                            static_for<0, 20>([&](auto S) {
                                constexpr int s = S.value, n = s + 1, gg = n / 5, jj = n % 5;
                                const bf16x8 xf = __builtin_bit_cast(bf16x8, of[s]);
                                if constexpr (n < 20) {
                                    if constexpr (jj < 4) {
                                        constexpr unsigned o = (80 * gg + 20 * jj) * 128;
                                        w0[n & 1] = ldstr2(a0 + o, a0 + o + 1024); w1[n & 1] = ldstr2(a1 + o, a1 + o + 1024);
                                    } else {
                                        constexpr unsigned o = 80 * gg * 128;
                                        w0[n & 1] = ldstr2(l0 + o, l0 + o + 2560); w1[n & 1] = ldstr2(l1 + o, l1 + o + 2560);
                                    }
                                }
                                SBAR();
                                t0[it] = MFMA(w0[s & 1], xf, t0[it]); t1[it] = MFMA(w1[s & 1], xf, t1[it]);
                                SBAR();
                            });
                            hf ^= 1;
                        }
                        acc0 += t0[0] + t0[1]; acc1 += t1[0] + t1[1];
                        char* orow = p.out + (size_t)(m * 32 + r) * 1280 + dc * 128 + h * 16;
#pragma unroll
                        for (int n = 0; n < 2; ++n) {
                            const f32x16& a = n ? acc1 : acc0;
#pragma unroll
                            for (int qq = 0; qq < 2; ++qq) {
                                float lo[4], hi[4];
#pragma unroll
                                for (int e = 0; e < 4; ++e) {
                                    auto sw = __builtin_amdgcn_permlane32_swap(__float_as_uint(a[8 * qq + e]), __float_as_uint(a[8 * qq + 4 + e]), false, false);
                                    lo[e] = __uint_as_float(sw[0]); hi[e] = __uint_as_float(sw[1]);
                                }
                                const u32x4 xv = xres[2 * n + qq];
                                const u32x4 ov = {pk2(lo[0] + bflo(xv[0]), lo[1] + bfhi(xv[0])), pk2(lo[2] + bflo(xv[1]), lo[3] + bfhi(xv[1])),
                                                  pk2(hi[0] + bflo(xv[2]), hi[1] + bfhi(xv[2])), pk2(hi[2] + bflo(xv[3]), hi[3] + bfhi(xv[3]))};
                                *(u32x4*)(orow + (2 * n + qq) * 32) = ov;
                            }
                        }
                        STAMP(11);
                    }
                }
            }
        }
    }
}

template <int PH>
static void launch(const Params& p, hipStream_t stream) {
    (void)hipFuncSetAttribute((const void*)mega<PH>, hipFuncAttributeMaxDynamicSharedMemorySize, L_TOTAL);
    mega<PH><<<256, 512, L_TOTAL, stream>>>(p);
}

extern "C" void kernel_launch(void* const* d_in, const int* in_sizes, int n_in, void* d_out, int out_size, void* d_ws, size_t ws_size, hipStream_t stream) {
    Params p;
    p.x = (const char*)d_in[0]; p.wq = (const char*)d_in[1]; p.wk = (const char*)d_in[2]; p.wv = (const char*)d_in[3]; p.wo = (const char*)d_in[4];
    p.w1 = (const char*)d_in[6]; p.w3 = (const char*)d_in[7]; p.w2 = (const char*)d_in[8];
    p.out = (char*)d_out; p.ws = (char*)d_ws;
    launch<27>(p, stream);
}
```

```cpp
#include <hip/hip_runtime.h>
#include <hip/hip_bf16.h>
#include <stdint.h>

#define AS1 __attribute__((address_space(1)))
#define AS3 __attribute__((address_space(3)))
typedef __bf16 bf16x2 __attribute__((ext_vector_type(2)));
typedef __bf16 bf16x8 __attribute__((ext_vector_type(8)));
typedef short s16x4 __attribute__((ext_vector_type(4)));
typedef short s16x8 __attribute__((ext_vector_type(8)));
typedef float f32x2 __attribute__((ext_vector_type(2)));
typedef float f32x4 __attribute__((ext_vector_type(4)));
typedef float f32x16 __attribute__((ext_vector_type(16)));
typedef unsigned u32x2 __attribute__((ext_vector_type(2)));
typedef unsigned u32x4 __attribute__((ext_vector_type(4)));
typedef AS1 unsigned gu32;
typedef AS1 unsigned long long gu64;

#define ATT_PRIO 2
#define GEM_PRIO 0
#define GU_HI 0
#define DN_HI 0
#define MPRIO(mask, i) __builtin_amdgcn_s_setprio(((mask) >> (i)) & 1 ? 3 : 0)
#define GU_DELAY 8
#define GU_ROT 4
#define GU_GQ 5
#define OF_SLEEP 3
#define HF_SLEEP 6
#define DN_DQ 8
#define SPLIT_MODE 0

constexpr int SEQ = 2048, DM = 640, FF = 1536;
constexpr unsigned WS_KF = 0;
constexpr unsigned WS_HF = 16384;
constexpr unsigned WS_OF = 32768;
constexpr unsigned WS_EP = 49152;
constexpr unsigned KVD = 5376;
constexpr unsigned KV_K1 = 2048, KV_V = 2560, KV_VT = 1408;
constexpr unsigned WS_KV = 65536;
constexpr unsigned WS_HT = WS_KV + 176128;
constexpr unsigned WS_OT = WS_HT + 64 * 96 * 1024;
constexpr unsigned WS_QT = WS_OT + 64 * 40 * 1024;
constexpr unsigned WS_PT = WS_QT + 64 * 32 * 2048;
constexpr unsigned WS_ST = WS_PT + 256 * 3 * 32 * 256;
constexpr unsigned L_PANEL = 0;
constexpr unsigned L_KV = 81920;
constexpr unsigned L_WQ = 81920;
constexpr unsigned L_GU = 124928;
constexpr unsigned GSL = 12320;
constexpr unsigned L_WKV = 124928;
constexpr unsigned L_MISC = 161920;
constexpr unsigned L_TOTAL = 163840;
constexpr unsigned CHUNK = 2 * KVD;
constexpr int NCHUNK = 16;
enum { C_AP = 0  , C_GP = 4  , C_DP = 8  , C_PP = 12  , C_WP = 16  , C_KVP = 20, C_KVFIN, C_TAG, C_N };

struct Params { const char* x; const char* wq; const char* wk; const char* wv; const char* wo; const char* w1; const char* w3; const char* w2; char* out; char* ws; };

#define SBAR() __builtin_amdgcn_sched_barrier(0)
#define PIN(x) asm volatile("" : "+v"(x))
#define CFENCE() asm volatile("" ::: "memory")
#define WAITV(n) asm volatile("s_waitcnt vmcnt(" #n ")" ::: "memory")
#define MFMA(a, b, c) __builtin_amdgcn_mfma_f32_32x32x16_bf16((a), (b), (c), 0, 0, 0)
#define RLX_AGENT __ATOMIC_RELAXED, __HIP_MEMORY_SCOPE_AGENT
#define RLX_WG __ATOMIC_RELAXED, __HIP_MEMORY_SCOPE_WORKGROUP

#define TMO(spins, lim, code, tgt) do {} while (0)
#define STAMP(id) do {} while (0)
#define STAMPC(id) do {} while (0)

#define LP(T, off) ((AS3 T*)(uintptr_t)(unsigned)(off))
__device__ __forceinline__ bf16x8 ldsf(unsigned off) { return *LP(const bf16x8, off); }
__device__ __forceinline__ s16x4 ldstr(unsigned off) { return __builtin_amdgcn_ds_read_tr16_b64_v4i16(LP(s16x4, off)); }
__device__ __forceinline__ bf16x8 ldstr2(unsigned off0, unsigned off1) {
    s16x4 a = ldstr(off0), b = ldstr(off1);
    return __builtin_bit_cast(bf16x8, __builtin_shufflevector(a, b, 0, 1, 2, 3, 4, 5, 6, 7));
}
__device__ __forceinline__ unsigned pk2(float lo, float hi) { bf16x2 v = {(__bf16)lo, (__bf16)hi}; return __builtin_bit_cast(unsigned, v); }
__device__ __forceinline__ float bflo(unsigned u) { return __uint_as_float(u << 16); }
__device__ __forceinline__ float bfhi(unsigned u) { return __uint_as_float(u & 0xffff0000u); }
__device__ __forceinline__ float rbf(float f) { return (float)(__bf16)f; }

__device__ __forceinline__ void glds16(unsigned voff, const char* sbase, unsigned ldsdst) {
    unsigned keep;
    asm volatile("s_mov_b32 %0, m0\n\ts_mov_b32 m0, %3\n\ts_nop 2\n\tglobal_load_lds_dwordx4 %1, %2\n\ts_mov_b32 m0, %0"
                 : "=&s"(keep) : "v"(voff), "s"(sbase), "s"(ldsdst) : "memory");
}
__device__ __forceinline__ void glds16_sc1(unsigned voff, const char* sbase, unsigned ldsdst) {
    unsigned keep;
    asm volatile("s_mov_b32 %0, m0\n\ts_mov_b32 m0, %3\n\ts_nop 2\n\tglobal_load_lds_dwordx4 %1, %2 sc1\n\ts_mov_b32 m0, %0"
                 : "=&s"(keep) : "v"(voff), "s"(sbase), "s"(ldsdst) : "memory");
}
__device__ __forceinline__ void glds16x3_sc1(unsigned voff, const char* sbase, unsigned ldsdst) {
    unsigned keep;
    asm volatile("s_mov_b32 %0, m0\n\ts_mov_b32 m0, %3\n\ts_nop 2\n\tglobal_load_lds_dwordx4 %1, %2 sc1\n\tglobal_load_lds_dwordx4 %1, %2 offset:1024 sc1\n\tglobal_load_lds_dwordx4 %1, %2 offset:2048 sc1\n\ts_mov_b32 m0, %0"
                 : "=&s"(keep) : "v"(voff), "s"(sbase), "s"(ldsdst) : "memory");
}
__device__ __forceinline__ void glds16x3v(unsigned v0, unsigned v1, unsigned v2, const char* sbase, unsigned ldsdst) {
    unsigned keep;
    asm volatile("s_mov_b32 %0, m0\n\ts_mov_b32 m0, %5\n\ts_nop 0\n\tglobal_load_lds_dwordx4 %1, %4\n\tglobal_load_lds_dwordx4 %2, %4 offset:1024\n\tglobal_load_lds_dwordx4 %3, %4 offset:2048\n\ts_mov_b32 m0, %0"
                 : "=&s"(keep) : "v"(v0), "v"(v1), "v"(v2), "s"(sbase), "s"(ldsdst) : "memory");
}
__device__ __forceinline__ void l2touch(const char* ga) {
    unsigned keep;
    asm volatile("s_mov_b32 %0, m0\n\ts_mov_b32 m0, %2\n\ts_nop 0\n\tglobal_load_lds_dword %1, off\n\ts_mov_b32 m0, %0" : "=&s"(keep) : "v"(ga), "s"(L_MISC + 1024) : "memory");
}
#define GLOAD16(dst, voff, sbase, imm) asm volatile("s_nop 4\n\tglobal_load_dwordx4 %0, %1, %2 offset:%3" : "=v"(dst) : "v"(voff), "s"(sbase), "i"(imm) : "memory")

__device__ __forceinline__ AS3 unsigned* lcnt(int c) { return LP(unsigned, L_MISC) + c; }
__device__ __forceinline__ void larrive(int c, int lane) { CFENCE(); if (lane == 0) __hip_atomic_fetch_add(lcnt(c), 1u, RLX_WG); CFENCE(); }
__device__ __forceinline__ void lset(int c, unsigned v, int lane) { CFENCE(); if (lane == 0) __hip_atomic_store(lcnt(c), v, RLX_WG); CFENCE(); }
__device__ __forceinline__ void lwait(int c, unsigned target) {
    CFENCE();
    _Pragma("clang loop unroll(disable)") for (unsigned spins = 0; spins < (1u << 22); ++spins) {
        if ((unsigned)__builtin_amdgcn_readfirstlane(__hip_atomic_load(lcnt(c), RLX_WG)) >= target) break;
        TMO(spins, 1u << 16, 100 + c, target);
        __builtin_amdgcn_s_sleep(1);
    }
    CFENCE();
}
__device__ __forceinline__ void lwait_all(int c, int n, unsigned target, int lane) {
    CFENCE();
    AS3 unsigned* q = lcnt(c) + (lane < n ? lane : 0);
    _Pragma("clang loop unroll(disable)") for (unsigned spins = 0; spins < (1u << 16); ++spins) {
        if (__all(__hip_atomic_load(q, RLX_WG) >= target)) break;
        TMO(spins, 1u << 16, 200 + c, target);
        __builtin_amdgcn_s_sleep(1);
    }
    CFENCE();
}
__device__ __forceinline__ void lpost(int c, unsigned v) { CFENCE(); *(volatile AS3 unsigned*)lcnt(c) = v; CFENCE(); }
__device__ __forceinline__ void linc(unsigned vaddr, unsigned vone) {
    asm volatile("s_mov_b64 exec, 1\n\tds_add_u32 %0, %1\n\ts_mov_b64 exec, -1" :: "v"(vaddr), "v"(vone) : "memory");
}
__device__ __forceinline__ unsigned lpeek(int c, int lane) { return *((volatile AS3 unsigned*)lcnt(c) + (lane & 3)); }
__device__ __forceinline__ void lcheck(unsigned pk, int c, unsigned target, int lane) {
    if (__builtin_expect(__any(pk < target), 0)) lwait_all(c, 4, target, lane);
    CFENCE();
}
__device__ __forceinline__ void gwait(const char* ws, unsigned off, int n, unsigned tag, int lane) {
    gu32* f = (gu32*)(ws + off) + (lane < n ? lane : 0);
    _Pragma("clang loop unroll(disable)") for (unsigned spins = 0; spins < (1u << 14); ++spins) {
        unsigned v = __hip_atomic_load(f, RLX_AGENT);
        if (__all(v == tag)) break;
        TMO(spins, 1u << 14, 300, off);
        __builtin_amdgcn_s_sleep(6);
    }
    CFENCE();
}
__device__ __forceinline__ float gwait64(const char* ws, unsigned off, unsigned tag, int lane) {
    gu64* f = (gu64*)(ws + off) + lane;
    unsigned long long v = 0;
    _Pragma("clang loop unroll(disable)") for (unsigned spins = 0; spins < (1u << 14); ++spins) {
        v = __hip_atomic_load(f, RLX_AGENT);
        if (__all((unsigned)(v >> 32) == tag)) break;
        __builtin_amdgcn_s_sleep(6);
    }
    CFENCE();
    return __uint_as_float((unsigned)v);
}
__device__ __forceinline__ __amdgpu_buffer_rsrc_t mkrsrc(const char* base, unsigned bytes) {
    return __builtin_amdgcn_make_buffer_rsrc((void*)base, 0, bytes, 0x00020000);
}
__device__ __forceinline__ float dpp_sum16(float v) {
    v += __int_as_float(__builtin_amdgcn_update_dpp(0, __float_as_int(v), 0x128, 0xf, 0xf, false));
    v += __int_as_float(__builtin_amdgcn_update_dpp(0, __float_as_int(v), 0x124, 0xf, 0xf, false));
    v += __int_as_float(__builtin_amdgcn_update_dpp(0, __float_as_int(v), 0x122, 0xf, 0xf, false));
    v += __int_as_float(__builtin_amdgcn_update_dpp(0, __float_as_int(v), 0x121, 0xf, 0xf, false));
    return v;
}
__device__ __forceinline__ float wave_max(float v) {
    int x = __float_as_int(v);
    x = max(x, __builtin_amdgcn_update_dpp(0, x, 0x128, 0xf, 0xf, false)); x = max(x, __builtin_amdgcn_update_dpp(0, x, 0x124, 0xf, 0xf, false));
    x = max(x, __builtin_amdgcn_update_dpp(0, x, 0x122, 0xf, 0xf, false)); x = max(x, __builtin_amdgcn_update_dpp(0, x, 0x121, 0xf, 0xf, false));
    const int a = max(__builtin_amdgcn_readlane(x, 0), __builtin_amdgcn_readlane(x, 16)), b = max(__builtin_amdgcn_readlane(x, 32), __builtin_amdgcn_readlane(x, 48));
    return __int_as_float(max(a, b));
}
__device__ __forceinline__ bf16x8 pack8(const f32x16& a, int b, float sc) {
    u32x4 r = {pk2(a[b] * sc, a[b + 1] * sc), pk2(a[b + 2] * sc, a[b + 3] * sc), pk2(a[b + 4] * sc, a[b + 5] * sc), pk2(a[b + 6] * sc, a[b + 7] * sc)};
    return __builtin_bit_cast(bf16x8, r);
}

template <int I> struct IC { static constexpr int value = I; };
template <int B, int E, class F> __device__ __forceinline__ void static_for(F&& f) { if constexpr (B < E) { f(IC<B>{}); static_for<B + 1, E>(f); } }
#define WAITVN(n) asm volatile("s_waitcnt vmcnt(%0)" :: "i"(n) : "memory")
__device__ __forceinline__ void GLOAD16S(u32x4& dst, unsigned voff, const char* sbase) { asm volatile("global_load_dwordx4 %0, %1, %2 sc1" : "=v"(dst) : "v"(voff), "s"(sbase) : "memory"); }
__device__ __forceinline__ void GLOAD16N(u32x4& dst, unsigned voff, const char* sbase) { asm volatile("global_load_dwordx4 %0, %1, %2" : "=v"(dst) : "v"(voff), "s"(sbase) : "memory"); }
__device__ __forceinline__ void GLOAD16N_IF(u32x4& dst, unsigned voff, const char* sbase, int cond) {
    asm volatile("s_cmp_eq_u32 %3, 0\n\ts_cbranch_scc1 1f\n\tglobal_load_dwordx4 %0, %1, %2\n1:" : "=v"(dst) : "v"(voff), "s"(sbase), "s"(cond) : "memory", "scc"); }
template <int N> __device__ __forceinline__ void waitv5(u32x4& a, u32x4& b, u32x4& c, u32x4& d, u32x4& e) { asm volatile("s_waitcnt vmcnt(%5)" : "+v"(a), "+v"(b), "+v"(c), "+v"(d), "+v"(e) : "i"(N) : "memory"); }
__device__ __forceinline__ void gload8x2_sc1(unsigned long long& a, unsigned long long& b, unsigned voff, const char* sbase) {
    asm volatile("global_load_dwordx2 %0, %2, %3 sc1\n\tglobal_load_dwordx2 %1, %2, %3 offset:512 sc1" : "=&v"(a), "=&v"(b) : "v"(voff), "s"(sbase) : "memory"); }
__device__ __forceinline__ void waitv8x2(unsigned long long& a, unsigned long long& b) { asm volatile("s_waitcnt vmcnt(0)" : "+v"(a), "+v"(b) :: "memory"); }
__device__ __forceinline__ void PIN2(u32x4& a, u32x4& b) { asm volatile("" : "+v"(a), "+v"(b) :: "memory"); }
__device__ __forceinline__ u32x4 scale8(unsigned x0, unsigned x1, unsigned x2, unsigned x3, unsigned clo, unsigned chi, float c) {
    unsigned o0, o1, o2, o3; float t0, t1, t2, t3, t4, t5, t6, t7;
    asm("v_dot2_f32_bf16 %4, %12, %16, %18\n\tv_dot2_f32_bf16 %5, %12, %17, %18\n\tv_dot2_f32_bf16 %6, %13, %16, %18\n\tv_dot2_f32_bf16 %7, %13, %17, %18\n\t"
        "v_dot2_f32_bf16 %8, %14, %16, %18\n\tv_dot2_f32_bf16 %9, %14, %17, %18\n\tv_dot2_f32_bf16 %10, %15, %16, %18\n\tv_dot2_f32_bf16 %11, %15, %17, %18\n\t"
        "v_cvt_pk_bf16_f32 %0, %4, %5\n\tv_cvt_pk_bf16_f32 %1, %6, %7\n\tv_cvt_pk_bf16_f32 %2, %8, %9\n\tv_cvt_pk_bf16_f32 %3, %10, %11"
        : "=&v"(o0), "=&v"(o1), "=&v"(o2), "=&v"(o3), "=&v"(t0), "=&v"(t1), "=&v"(t2), "=&v"(t3), "=&v"(t4), "=&v"(t5), "=&v"(t6), "=&v"(t7)
        : "v"(x0), "v"(x1), "v"(x2), "v"(x3), "v"(clo), "v"(chi), "v"(c));
    return u32x4{o0, o1, o2, o3};
}
template <int N> __device__ __forceinline__ void waitv3(u32x4& a, u32x4& b, u32x4& c) { asm volatile("s_waitcnt vmcnt(%3)" : "+v"(a), "+v"(b), "+v"(c) : "i"(N) : "memory"); }

constexpr int dn_younger(int k, int D, bool wo) {
    int n = 0, la = 0, lb = 0;
    for (int i = 0; i < D; ++i) { n += 2; if (i == k + 1) lb = n; }
    for (int i = 0; i < D; ++i) { n += 3; if (i == k) la = n; }
    for (int s = 0; s < k; ++s) {
        if (s + D < 24) { n += 2; if (s + D == k + 1) lb = n; }
        if (wo && s >= 16) n += 2;
        if (s + D < 24) { n += 3; if (s + D == k) la = n; }
    }
    return n - (la > lb ? la : lb);
}
constexpr float QSCALE = 0.22360679774997896f * 1.4426950408889634f;
constexpr float BOUND_MAX = 50.f;

struct AttSync { int lane; const char* kv; unsigned dvoff; unsigned wo; int w; };
__device__ __forceinline__ unsigned kvslot(int c) { return L_KV + ((c + 2) & 3) * CHUNK; }
__device__ __forceinline__ void att_dma(const AttSync& a, int c) { glds16x3_sc1(a.dvoff, a.kv + (size_t)c * CHUNK, kvslot(c) + a.wo); }
constexpr unsigned AP0 = 16;
#define RING_X(c) do { WAITV(3); lpost(C_AP + as.w, AP0 + (c) + 1); } while (0)
#define RP_A
#define RP_B
#define RING_Y(c, pk) do { RP_A lcheck(pk, C_AP, AP0 + (c) + 1, lane); const int cn_ = (c) + 3 < NCHUNK ? (c) + 3 : NCHUNK - 1;   \
                           glds16x3_sc1(as.dvoff, as.kv + (size_t)cn_ * CHUNK, kvslot((c) + 3) + as.wo); RP_B } while (0)

__device__ __forceinline__ void att_fast(const AttSync& as, bf16x8 Q0a, bf16x8 Q1a0, bf16x8 Q1a1, bf16x8 Q0b, bf16x8 Q1b0, bf16x8 Q1b1, f32x16& Oa, f32x16& Ob) {
    const int lane = as.lane, r = lane & 31, h = lane >> 5;
    const unsigned ak0 = lane * 16, ak1 = h ? KV_V + 20 * 16 : KV_K1 + r * 16;
    const unsigned av = KV_V + h * 352 + (r <= 20 ? r : 21) * 16;
    const f32x16 zero = {};
    f32x16 Sa, Sb = {};
    bf16x8 K0[2], K1[2], V0[2], V1[2];
    u32x4 Pa0, Pa1, Pb0 = {}, Pb1 = {};
    unsigned pk = 0;
    unsigned long long rp0 = 0, rp1 = 0;
    V0[1] = bf16x8{}; V1[1] = bf16x8{};
    unsigned vk0 = L_KV + ak0, vk1 = L_KV + ak1, vv = L_KV + av;
    PIN(vk0); PIN(vk1); PIN(vv);
    K0[0] = ldsf(vk0 + 2 * CHUNK); K1[0] = ldsf(vk1 + 2 * CHUNK); K0[1] = ldsf(vk0 + 2 * CHUNK + 1024); V1[0] = ldsf(vv + 2 * CHUNK + 704); V0[0] = ldsf(vv + 2 * CHUNK);
    Sa = MFMA(K0[0], Q0a, zero); Sa = MFMA(K1[0], Q1a0, Sa);
#define EX4(S, i) do { S[i] = __builtin_amdgcn_exp2f(S[i]); S[i + 1] = __builtin_amdgcn_exp2f(S[i + 1]); S[i + 2] = __builtin_amdgcn_exp2f(S[i + 2]); S[i + 3] = __builtin_amdgcn_exp2f(S[i + 3]); } while (0)
#define GAP(MF, LD, T, k, PW, j, S, i) do { MF; SBAR(); LD; PW[j] = pk2(T[k], T[k + 1]); PW[j + 1] = pk2(T[k + 2], T[k + 3]); PIN(PW); SBAR(); EX4(S, i); PIN(S); SBAR(); } while (0)
#define BC(x) __builtin_bit_cast(bf16x8, x)
#define STEP(e, d, QB1, QA1N, KN, L3, LK0, LK1, LV) do { \
        GAP(Ob = MFMA(V0[e ^ 1], BC(Pb0), Ob), (void)0,                  Sb, 12, Pb1, 2, Sa, 0); \
        GAP(Sb = MFMA(K0[e], Q0b, zero),       (void)0,                  Sa, 0, Pa0, 0, Sa, 4); \
        GAP(Sb = MFMA(K1[d], QB1, Sb),         L3,                       Sa, 4, Pa0, 2, Sa, 8); \
        GAP(Ob = MFMA(V1[e ^ 1], BC(Pb1), Ob), K0[e] = ldsf(LK0),        Sa, 8, Pa1, 0, Sa, 12); \
        GAP(Oa = MFMA(V0[e], BC(Pa0), Oa),     LK1,                      Sa, 12, Pa1, 2, Sb, 0); \
        GAP(Sa = MFMA(K0[e ^ 1], Q0a, zero),   V1[e ^ 1] = ldsf((LV) + 704), Sb, 0, Pb0, 0, Sb, 4); \
        GAP(Sa = MFMA(K1[KN], QA1N, Sa),       V0[e ^ 1] = ldsf(LV),     Sb, 4, Pb0, 2, Sb, 8); \
        GAP(Oa = MFMA(V1[e], BC(Pa1), Oa),     (void)0,                  Sb, 8, Pb1, 0, Sb, 12); \
    } while (0)
#define CHUNK4(c, B, N) do { \
        STEP(0, 0, Q1b0, Q1a1, 0, (void)0, vk0 + (B) + KVD, K1[1] = ldsf(vk1 + (B) + KVD), vv + (B) + KV_VT); \
        RING_X(c); \
        STEP(1, 0, Q1b1, Q1a0, 1, pk = lpeek(C_AP, lane), vk0 + (B) + KVD + 1024, (void)0, vv + (B) + KVD); \
        RING_Y(c, pk); \
        STEP(0, 1, Q1b0, Q1a1, 1, (void)0, vk0 + (N), K1[0] = ldsf(vk1 + (N)), vv + (B) + KVD + KV_VT); \
        STEP(1, 1, Q1b1, Q1a0, 0, (void)0, vk0 + (N) + 1024, (void)0, vv + (N)); \
    } while (0)
    for (int c = 0; c < NCHUNK; c += 4) {
        CHUNK4(c, 2 * CHUNK, 3 * CHUNK); CHUNK4(c + 1, 3 * CHUNK, 0); CHUNK4(c + 2, 0, CHUNK); CHUNK4(c + 3, CHUNK, 2 * CHUNK);
    }
#undef CHUNK4
    Pb1[2] = pk2(Sb[12], Sb[13]); Pb1[3] = pk2(Sb[14], Sb[15]);
    Ob = MFMA(V0[1], BC(Pb0), Ob); Ob = MFMA(V1[1], BC(Pb1), Ob);
#undef STEP
#undef GAP
#undef EX4
}

__device__ __forceinline__ void att_safe(const AttSync& as, bf16x8 Q0a, bf16x8 Q1a0, bf16x8 Q1a1, bf16x8 Q0b, bf16x8 Q1b0, bf16x8 Q1b1, f32x16& Oa, f32x16& Ob) {
    const int lane = as.lane, r = lane & 31, h = lane >> 5;
    const unsigned ak0 = lane * 16, ak1 = h ? KV_V + 20 * 16 : KV_K1 + r * 16;
    const unsigned av = KV_V + h * 352 + (r <= 20 ? r : 21) * 16;
    const f32x16 zero = {};
    float ma = -INFINITY, mb = -INFINITY;
    unsigned long long rp0 = 0, rp1 = 0;
    for (int c = 0; c < NCHUNK; ++c) {
        const unsigned b0 = kvslot(c);
        for (int t = 0; t < 4; ++t) {
            if (t == 1) RING_X(c);
            if (t == 2) RING_Y(c, 0u);
            const unsigned base = b0 + (t >> 1) * KVD; const int e = t & 1;
            bf16x8 K0 = ldsf(base + ak0 + e * 1024), K1 = ldsf(base + ak1), V0 = ldsf(base + av + e * KV_VT), V1 = ldsf(base + av + e * KV_VT + 704);
#pragma unroll
            for (int q = 0; q < 2; ++q) {
                f32x16 S = MFMA(K0, q ? Q0b : Q0a, zero); S = MFMA(K1, q ? (e ? Q1b1 : Q1b0) : (e ? Q1a1 : Q1a0), S);
                float mx = S[0];
#pragma unroll
                for (int i = 1; i < 16; ++i) mx = fmaxf(mx, S[i]);
                mx = fmaxf(mx, __shfl_xor(mx, 32));
                float& m = q ? mb : ma; f32x16& O = q ? Ob : Oa;
                const float mn = fmaxf(m, mx);
                const float al = __builtin_amdgcn_exp2f(m - mn);
                m = mn;
#pragma unroll
                for (int i = 0; i < 16; ++i) { O[i] *= al; S[i] = __builtin_amdgcn_exp2f(S[i] - mn); }
                O = MFMA(V0, pack8(S, 0, 1.f), O); O = MFMA(V1, pack8(S, 8, 1.f), O);
            }
        }
    }
}

__device__ __forceinline__ void xload8(u32x4 (&x)[5], u32x4 (&y)[5], const char* xb, unsigned vo, unsigned vo2, int skip) {
    asm volatile("s_cmp_lg_u32 %13, 0\n\ts_cbranch_scc1 1f\n\t"
                 "global_load_dwordx4 %0, %10, %12\n\tglobal_load_dwordx4 %1, %10, %12 offset:256\n\tglobal_load_dwordx4 %2, %10, %12 offset:512\n\t"
                 "global_load_dwordx4 %3, %10, %12 offset:768\n\tglobal_load_dwordx4 %4, %10, %12 offset:1024\n\t"
                 "global_load_dwordx4 %5, %11, %12\n\tglobal_load_dwordx4 %6, %11, %12 offset:256\n\tglobal_load_dwordx4 %7, %11, %12 offset:512\n\t"
                 "global_load_dwordx4 %8, %11, %12 offset:768\n\tglobal_load_dwordx4 %9, %11, %12 offset:1024\n1:"
                 : "=&v"(x[0]), "=&v"(x[1]), "=&v"(x[2]), "=&v"(x[3]), "=&v"(x[4]), "=&v"(y[0]), "=&v"(y[1]), "=&v"(y[2]), "=&v"(y[3]), "=&v"(y[4])
                 : "v"(vo), "v"(vo2), "s"(xb), "s"(skip) : "memory", "scc");
}
__device__ __forceinline__ void xload4(u32x4 (&x)[5], const char* xb, unsigned vo) {
    asm volatile("global_load_dwordx4 %0, %5, %6\n\tglobal_load_dwordx4 %1, %5, %6 offset:256\n\tglobal_load_dwordx4 %2, %5, %6 offset:512\n\t"
                 "global_load_dwordx4 %3, %5, %6 offset:768\n\tglobal_load_dwordx4 %4, %5, %6 offset:1024"
                 : "=&v"(x[0]), "=&v"(x[1]), "=&v"(x[2]), "=&v"(x[3]), "=&v"(x[4]) : "v"(vo), "s"(xb) : "memory");
}
__device__ __forceinline__ void ln_rows(const u32x4 (&xr)[5], int row0, int lane) {
    f32x2 xv[20];
    f32x2 s0 = {0.f, 0.f}, s1 = {0.f, 0.f}, q0 = {0.f, 0.f}, q1 = {0.f, 0.f};
#pragma unroll
    for (int i = 0; i < 20; i += 2) {
        const unsigned a = xr[i >> 2][i & 3], b = xr[i >> 2][(i & 3) + 1];
        xv[i] = f32x2{bflo(a), bfhi(a)}; xv[i + 1] = f32x2{bflo(b), bfhi(b)};
        s0 += xv[i]; q0 = __builtin_elementwise_fma(xv[i], xv[i], q0);
        s1 += xv[i + 1]; q1 = __builtin_elementwise_fma(xv[i + 1], xv[i + 1], q1);
    }
    s0 += s1; q0 += q1;
    const float s = dpp_sum16(s0[0] + s0[1]), ss = dpp_sum16(q0[0] + q0[1]);
    const float mean = s * (1.f / 640.f);
    const float var = fmaxf(ss * (1.f / 640.f) - mean * mean, 0.f);
    const float rs = __builtin_amdgcn_rcpf(rbf(__builtin_amdgcn_sqrtf(rbf(rbf(var) + 1e-5f)))), mo = -rbf(mean) * rs;
    const f32x2 rs2 = {rs, rs}, mo2 = {mo, mo};
    const int row = row0 + (lane >> 4), c = lane & 15;
    const unsigned wa = L_PANEL + (row >> 5) * 40960 + (c >> 1) * 1024 + (c & 1) * 512 + (((row & 31) ^ (c & 7)) << 4);
#pragma unroll
    for (int u = 0; u < 5; ++u) {
        u32x4 o;
#pragma unroll
        for (int e = 0; e < 4; ++e) { const f32x2 y = __builtin_elementwise_fma(xv[4 * u + e], rs2, mo2); o[e] = pk2(y[0], y[1]); }
        *LP(u32x4, wa + u * 8192) = o;
    }
}
#define BLOCK_BARRIER() asm volatile("s_waitcnt lgkmcnt(0)\n\ts_barrier" ::: "memory")

template <int PH>
__global__ void __launch_bounds__(512, 2) mega(Params p) {
    constexpr bool PRO = PH & 1, ATT = PH & 2, DN = PH & 8, WO = PH & 16;
    const int w = __builtin_amdgcn_readfirstlane(threadIdx.x >> 6);
    const int b = blockIdx.x, xc = b & 7, j = b >> 3;
    const int ti = (xc >> 1) * 8 + (j >> 2), g = (xc & 1) * 4 + (j & 3);
    constexpr bool FUSED = PH == 27;
    const int kmt = g & 1;
    const int ra = kmt * 32 + (w & 3) * 8, rb = (kmt ^ 1) * 32 + (w & 3) * 8;
    u32x4 x0[5], x1[5], x2[5], x3[5];
    if constexpr (PRO) {
        asm volatile("" :: "s"(p.x), "s"(p.ws), "s"(p.wq), "s"(p.wk));
        const char* xb = p.x + (size_t)ti * 64 * 1280;
        const unsigned xvo = ((threadIdx.x >> 4) & 3) * 1280 + (threadIdx.x & 15) * 16;
        xload8(x0, x1, xb + (size_t)ra * 1280, xvo, xvo + 5120, w >> 2);
        xload8(x2, x3, xb + (size_t)rb * 1280, xvo, xvo + 5120, w >> 2);
    }
    const char* pc; asm volatile("s_getpc_b64 %0" : "=s"(pc));
    unsigned tag = 1u;
    char* ws = p.ws;
    STAMP(0);
    if (threadIdx.x < C_N) *lcnt(threadIdx.x) = (threadIdx.x == C_WP || threadIdx.x == C_WP + 1 || threadIdx.x == C_PP || threadIdx.x == C_PP + 1) ? 100u : 0u;
    const bool haskv = (g >> 2) == (ti & 1);
    const char* wqb = p.wq + g * 160;
#define LANE_CONSTS() int lane = __builtin_amdgcn_mbcnt_hi(~0u, __builtin_amdgcn_mbcnt_lo(~0u, 0u)); PIN(lane); \
    const int r = lane & 31, h = lane >> 5, tq = (lane & 15) >> 2, tp = lane & 3, tb = (lane >> 4) & 1; (void)r; (void)h; (void)tq; (void)tp; (void)tb

    if (w >= 4) {
        LANE_CONSTS();
        const int v = w - 4;
        unsigned epv = 0u;
        if constexpr (FUSED) {
            asm volatile("global_load_dword %0, %1, %2 sc1" : "=v"(epv) : "v"(0u), "s"(p.ws + WS_EP) : "memory");
            constexpr unsigned CODE_PF = 65536;
            const unsigned line = (unsigned)(j * 4 + v) * 64u + lane;
            if (line * 128u < CODE_PF) l2touch(pc + line * 128u);
        }
#define GET_TAG() do { if constexpr (FUSED) { asm volatile("s_nop 0\n\tv_readfirstlane_b32 %0, %1\n\ts_nop 3" : "=s"(tag) : "v"(epv) : "memory"); tag += 0x9E3779B1u; } } while (0)
        __builtin_amdgcn_s_setprio(3);
        if constexpr (PRO) {
            const int kvm = 2 * ti + (g & 1); const bool isV = (g >> 1) & 1;
            const char* wkvb = isV ? p.wv : p.wk;
            if (haskv && v < 2) {
#pragma unroll
                for (int n = 0; n < 13; ++n) {
                    const unsigned o = v * 12800 + (n < 12 ? n * 1024 : 11776);
                    glds16(o + lane * 16, wkvb, L_WKV + o);
                }
            }
            BLOCK_BARRIER();
            if (v >= 2) {
                const int v2 = v - 2;
                unsigned wo5[5];
#pragma unroll
                for (int i = 0; i < 5; ++i) { const unsigned c = (v2 + 2 * i) * 64 + lane, row = c / 10u, col = c - row * 10u; wo5[i] = row * 1280 + col * 16; }
#pragma unroll
                for (int n = 0; n < 20; ++n) glds16(wo5[n % 5], wqb + (n / 5) * 64 * 1280, L_WQ + (v2 + 2 * n) * 1024);
                u32x4 wst[3][10];
                auto wst_issue = [&](auto I) {
                    constexpr int rr = I.value / 10, i = I.value % 10;
                    GLOAD16N(wst[rr][i], wo5[i % 5], wqb + (4 + 2 * rr + i / 5) * 64 * 1280);
                };
                static_for<0, 10>(wst_issue);
                WAITV(10);
                GET_TAG(); lpost(C_TAG, tag);
                lpost(C_PP + v, 1);
                STAMP(2);
                static_for<10, 30>(wst_issue);
                static_for<0, 3>([&](auto R) {
                    constexpr int rr = R.value;
                    waitv5<20 - 10 * rr>(wst[rr][0], wst[rr][1], wst[rr][2], wst[rr][3], wst[rr][4]); waitv5<63>(wst[rr][5], wst[rr][6], wst[rr][7], wst[rr][8], wst[rr][9]);
                    lwait_all(C_AP, 4, 3 + rr, lane);
#pragma unroll
                    for (int i = 0; i < 10; ++i) *LP(u32x4, L_WQ + (rr & 1) * 20480 + (v2 + 2 * i) * 1024 + lane * 16) = wst[rr][i];
                    lpost(C_WP + v, rr + 1);
                });
            } else {
              if (haskv) {
                f32x16 acc = {};
                const unsigned wa = L_WKV + (v * 320 + 8 * h + tq) * 40 + tb * 32 + (tb ? 0 : tp * 8);
                unsigned xa[4];
#pragma unroll
                for (int s = 0; s < 4; ++s) xa[s] = L_PANEL + kmt * 40960 + v * 20480 + h * 512 + ((r ^ ((2 * s + h) & 7)) << 4);
                WAITV(0);
                GET_TAG();
                STAMP(2);
                bf16x8 wf[20], xf[20];
#pragma unroll
                for (int s = 0; s < 20; ++s) wf[s] = ldstr2(wa + s * 640, wa + s * 640 + 160);
                lwait_all(C_AP, 4, 1, lane);
#pragma unroll
                for (int s = 0; s < 20; ++s) xf[s] = ldsf(xa[s & 3] + s * 1024);
                SBAR();
                if (isV) {
#pragma unroll
                    for (int s = 0; s < 20; ++s) acc = MFMA(xf[s], wf[s], acc);
                } else {
#pragma unroll
                    for (int s = 0; s < 20; ++s) acc = MFMA(wf[s], xf[s], acc);
                }
                if (v) {
#pragma unroll
                    for (int e = 0; e < 4; ++e) *LP(f32x4, L_WKV + 12800 + e * 1024 + lane * 16) = f32x4{acc[4 * e], acc[4 * e + 1], acc[4 * e + 2], acc[4 * e + 3]};
                    lpost(C_KVP, 1);
                } else {
                    lwait(C_KVP, 1);
#pragma unroll
                    for (int e = 0; e < 4; ++e) {
                        const f32x4 t = *LP(const f32x4, L_WKV + 12800 + e * 1024 + lane * 16);
                        acc[4 * e] += t[0]; acc[4 * e + 1] += t[1]; acc[4 * e + 2] += t[2]; acc[4 * e + 3] += t[3];
                    }
                    const __amdgpu_buffer_rsrc_t rk = mkrsrc(ws + WS_KV, 32 * KVD);
                    const unsigned base = ti * KVD, ke = g & 1;
                    const u32x4 f0 = __builtin_bit_cast(u32x4, pack8(acc, 0, 1.f)), f1 = __builtin_bit_cast(u32x4, pack8(acc, 8, 1.f));
                    float mx;
                    if (!isV) {
                        __builtin_amdgcn_raw_buffer_store_b128(f0, rk, base + ke * 1024 + lane * 16, 0, 16);
                        const u32x2 k1 = {f1[0], f1[1]};
                        if (!h) __builtin_amdgcn_raw_buffer_store_b64(k1, rk, base + KV_K1 + r * 16 + ke * 8, 0, 16);
                        lpost(C_KVFIN, 1);
                        float n2 = 0.f;
#pragma unroll
                        for (int e = 0; e < 4; ++e) n2 += bflo(f0[e]) * bflo(f0[e]) + bfhi(f0[e]) * bfhi(f0[e]);
                        if (!h) n2 += bflo(f1[0]) * bflo(f1[0]) + bfhi(f1[0]) * bfhi(f1[0]) + bflo(f1[1]) * bflo(f1[1]) + bfhi(f1[1]) * bfhi(f1[1]);
                        n2 += __shfl_xor(n2, 32);
                        mx = (n2 == n2) ? n2 : INFINITY;
                    } else {
                        const unsigned cst = r == 20 ? 0x3f803f80u : 0u;
                        const u32x4 c4 = {cst, cst, cst, cst};
                        if (r <= 21) {
                            __builtin_amdgcn_raw_buffer_store_b128(r >= 20 ? c4 : f0, rk, base + KV_V + ke * KV_VT + h * 352 + r * 16, 0, 16);
                            __builtin_amdgcn_raw_buffer_store_b128(r >= 20 ? c4 : f1, rk, base + KV_V + ke * KV_VT + 704 + h * 352 + r * 16, 0, 16);
                        }
                        lpost(C_KVFIN, 1);
                        float vm = 0.f;
#pragma unroll
                        for (int e = 0; e < 16; ++e) { const float a = fabsf(acc[e]); vm = (a <= vm) ? vm : ((a == a) ? a : INFINITY); }
                        mx = r < 20 ? vm : 0.f;
                    }
                    mx = wave_max(mx);
                    WAITV(0);
                    STAMP(15);
                    if (lane < 8) __hip_atomic_store((gu64*)(ws + WS_KF + lane * 1024 + (isV ? 512 : 0)) + kvm, ((unsigned long long)tag << 32) | __float_as_uint(mx), RLX_AGENT);
                }
            }
              else { WAITV(0); GET_TAG(); }
              STAMP(3);
            }
            if (haskv) lwait(C_KVFIN, 1);
#pragma unroll
            for (int n = 0; n < 2; ++n) {
                unsigned L = n * 64 + lane; L = (j * 4 + v) * 120 + (L < 120u ? L : 119u);
                const unsigned m3 = L >= 7680u, rem = L - (m3 ? 7680u : 0u), row = rem / 12u;
                l2touch((m3 ? p.w3 : p.w1) + row * 3072 + (xc & 1) * 1536 + (rem - row * 12u) * 128);
            }
            lwait_all(C_AP, 4, GU_DELAY, lane);
            STAMP(4);
            {
                const char* wsrc = ((v >> 1) ? p.w3 : p.w1) + g * 384 + (size_t)(v & 1) * 8 * 3072;
                unsigned gvo[3];
#pragma unroll
                for (int n = 0; n < 3; ++n) {
                    const unsigned c = n * 64 + lane, row = c / 24u, col = (c - row * 24u) ^ ((row & 2) << 1);
                    gvo[n] = row * 3072 + col * 16 - n * 1024;
                }
                const unsigned gdst = L_GU + (v >> 1) * 6176 + (v & 1) * 3072;
                auto gdma = [&](auto S) { constexpr int s = S.value; glds16x3v(gvo[0], gvo[1], gvo[2], wsrc + (size_t)s * 16 * 3072, gdst + (s % 3) * GSL); };
                static_for<0, 3>(gdma);
                f32x16 acc[2][3] = {};
                unsigned bn[3], pa[4], pb[4];
#pragma unroll
                for (int n = 0; n < 3; ++n) bn[n] = L_GU + tb * 6176 + (8 * h + tq) * 384 + (((3 * v + n) * 32 + (tp >> 1) * 16) ^ ((unsigned)(tq >> 1) << 6)) + (tp & 1) * 8;
#pragma unroll
                for (int s = 0; s < 4; ++s) { pa[s] = L_PANEL + h * 512 + ((r ^ ((2 * s + h) & 7)) << 4); pb[s] = pa[s] + 40960; PIN(pb[s]); }
                bf16x8 wf[2][3], xa[2], xb2[2];
                unsigned vaddr = L_MISC + (C_GP + v) * 4, vone = 1u; PIN(vaddr); PIN(vone);
                auto rdw = [&](auto S, auto N) { constexpr int s = S.value, n = N.value; const unsigned o = bn[n] + (s % 3) * GSL; wf[s & 1][n] = ldstr2(o, o + 1536); };
                auto rdx = [&](auto S) { constexpr int s = S.value; xa[s & 1] = ldsf(pa[s & 3] + s * 1024); xb2[s & 1] = ldsf(pb[s & 3] + s * 1024); };
                WAITV(6); linc(vaddr, vone); lwait_all(C_GP, 4, 1u, lane);
                rdx(IC<0>{}); rdw(IC<0>{}, IC<0>{}); rdw(IC<0>{}, IC<1>{}); rdw(IC<0>{}, IC<2>{});
                WAITV(3); linc(vaddr, vone);
                static_for<0, 40>([&](auto S) {
                    constexpr int s = S.value; constexpr bool nx = s + 1 < 40;
                    unsigned pk = 0;
                    MPRIO(GU_HI, 0); acc[0][0] = MFMA(wf[s & 1][0], xa[s & 1], acc[0][0]); SBAR();
                    if constexpr (nx) rdx(IC<s + 1>{});
                    SBAR();
                    MPRIO(GU_HI, 1); acc[1][0] = MFMA(wf[s & 1][0], xb2[s & 1], acc[1][0]); SBAR();
                    if constexpr (nx) pk = lpeek(C_GP, lane);
                    SBAR();
                    MPRIO(GU_HI, 2); acc[0][1] = MFMA(wf[s & 1][1], xa[s & 1], acc[0][1]); SBAR();
                    MPRIO(GU_HI, 3); acc[1][1] = MFMA(wf[s & 1][1], xb2[s & 1], acc[1][1]); SBAR();
                    if constexpr (nx) { lcheck(pk, C_GP, s + 2, lane); rdw(IC<s + 1>{}, IC<0>{}); rdw(IC<s + 1>{}, IC<1>{}); rdw(IC<s + 1>{}, IC<2>{}); }
                    SBAR();
                    MPRIO(GU_HI, 4); acc[0][2] = MFMA(wf[s & 1][2], xa[s & 1], acc[0][2]); SBAR();
                    if constexpr (s + 3 < 40) gdma(IC<s + 3>{});
                    SBAR();
                    MPRIO(GU_HI, 5); acc[1][2] = MFMA(wf[s & 1][2], xb2[s & 1], acc[1][2]); SBAR();
                    if constexpr (s + 3 < 40) WAITV(3); else WAITV(0);
                    linc(vaddr, vone); SBAR();
                });
                __builtin_amdgcn_s_setprio(3);
                STAMP(6);
                const __amdgpu_buffer_rsrc_t rh = mkrsrc(ws + WS_HT, 64 * 96 * 1024);
#pragma unroll
                for (int m = 0; m < 2; ++m)
#pragma unroll
                    for (int n = 0; n < 3; ++n) {
                        f32x16 hv;
#pragma unroll
                        for (int e = 0; e < 8; ++e) {
                            const float gg = acc[m][n][e];
                            hv[e] = gg * __builtin_amdgcn_rcpf(1.f + __builtin_amdgcn_exp2f(-1.4426950408889634f * gg)) * acc[m][n][e + 8];
                        }
                        __builtin_amdgcn_raw_buffer_store_b128(__builtin_bit_cast(u32x4, pack8(hv, 0, 1.f)), rh, ((2 * ti + m) * 96 + g * 12 + 3 * v + n) * 1024 + lane * 16, 0, 16);
                    }
                if constexpr (DN) {
                    WAITV(0);
                    if (lane < 2) __hip_atomic_store((gu32*)(ws + WS_HF) + (2 * ti + lane) * 32 + g * 4 + v, tag, RLX_AGENT);
                }
                if constexpr (DN) {
#pragma unroll
                    for (int n = 0; n < 2; ++n) {
                        unsigned L = (j * 4 + v) * 85 + n * 64 + lane; L = L < 10880u ? L : 10879u;
                        const unsigned row = L / 5u;
                        l2touch((row < 1536u ? p.w2 + row * 1280 : p.wo + (row - 1536u) * 1280) + (xc & 1) * 640 + (L - row * 5u) * 128);
                    }
                }
                STAMP(7);
            }
        } else {
            BLOCK_BARRIER();
        }
        if constexpr (DN || WO) {
            LANE_CONSTS();
            const unsigned bq = ((tb * 32 + (tp >> 1) * 16) ^ ((unsigned)(tq >> 1) << 6)) + (tp & 1) * 8;
            const unsigned drow = lane >> 3, dcol = ((lane & 7) ^ (((lane >> 4) & 1) << 2)) * 16;
            const int G = xc >> 1, r0 = G < 2 ? 6 * G : 12 + 5 * (G - 2), nr = G < 2 ? 6 : 5;
            if constexpr (FUSED) {
                if (b == 255 && v == 0) {
                    gu32* f = (gu32*)(ws + WS_HF) + (lane >> 3) * 64 + (lane & 7) * 4;
                    _Pragma("clang loop unroll(disable)") for (unsigned spins = 0; spins < (1u << 14); ++spins) {
                        bool ok = true;
#pragma unroll
                        for (int n = 0; n < 4; ++n) ok &= __hip_atomic_load(f + n * 512, RLX_AGENT) == tag;
                        if (__all(ok)) break;
                        TMO(spins, 1u << 14, 303, tag);
                        __builtin_amdgcn_s_sleep(32);
                    }
                    if (lane == 0) __hip_atomic_store((gu32*)(ws + WS_EP), tag, RLX_AGENT);
                }
            }
            if (j < 5 * nr) {
                const int dr = r0 + j / 5, dc = (xc & 1) * 5 + j % 5;
                const int mfirst = dr < 20 ? 3 * dr : 60 + 2 * (dr - 20), nm = dr < 20 ? 3 : 2;
                const unsigned dvo = drow * 1280 + dcol;
                const unsigned myring = L_PANEL + v * 20480;
                f32x16 acc0 = {}, acc1 = {};
                if constexpr (PRO) { lwait_all(C_GP, 4, 42, lane); lwait_all(C_AP, 4, 8, lane); }
                if constexpr (DN) {
                    f32x16 da[3][2] = {};
                    const int drot = (j % 5) * 4;
                    const char* bsrc = p.w2 + (size_t)v * 384 * 1280 + dc * 128;
                    const char* asrc = ws + WS_HT + ((size_t)mfirst * 96 + v * 24) * 1024;
                    const unsigned am1 = lane * 16 + 98304, am2 = lane * 16 + (nm == 3 ? 196608 : 98304);
                    constexpr int DQ = DN_DQ;
                    u32x4 aq[DQ][3];
                    auto bissue = [&](auto K) {
                        constexpr int k = K.value;
                        int kr = k + drot; kr = kr >= 24 ? kr - 24 : kr;
                        const char* sb = bsrc + (size_t)kr * 16 * 1280;
                        glds16(dvo, sb, myring + (k % 10) * 2048); glds16(dvo + 8 * 1280, sb, myring + (k % 10) * 2048 + 1024);
                    };
                    auto woissue = [&](auto U) {
                        constexpr int u = U.value;
                        const char* sb = p.wo + (size_t)(v * 160 + u * 16) * 1280 + dc * 128;
                        glds16(dvo, sb, myring + u * 2048); glds16(dvo + 8 * 1280, sb, myring + u * 2048 + 1024);
                    };
                    auto aissue = [&](auto K) {
                        constexpr int k = K.value;
                        int kr = k + drot; kr = kr >= 24 ? kr - 24 : kr;
                        const char* sb = asrc + (size_t)kr * 1024;
                        GLOAD16S(aq[k % DQ][0], lane * 16, sb); GLOAD16S(aq[k % DQ][1], am1, sb); GLOAD16S(aq[k % DQ][2], am2, sb);
                    };
                    static_for<0, DQ>(bissue);
                    if constexpr (PRO) {
                        gu32* f = (gu32*)(ws + WS_HF) + (mfirst + ((lane >> 3) < nm ? (lane >> 3) : 0)) * 32 + 8 * v + (lane & 7);
                        _Pragma("clang loop unroll(disable)") for (unsigned spins = 0; spins < (1u << 14); ++spins) {
                            if (__all(__hip_atomic_load(f, RLX_AGENT) == tag)) break;
                            TMO(spins, 1u << 14, 302, tag);
                            __builtin_amdgcn_s_sleep(HF_SLEEP);
                        }
                        CFENCE();
                    }
                    STAMP(8);
                    static_for<0, DQ>(aissue);
                    const unsigned ba0 = myring + (4 * h + tq) * 128 + bq, ba1 = ba0 ^ 64;
                    bf16x8 w0[2], w1[2];
                    WAITVN(3 * DQ);
                    w0[0] = ldstr2(ba0, ba0 + 1024); w1[0] = ldstr2(ba1, ba1 + 1024);
                    static_for<0, 24>([&](auto K) {
                        constexpr int k = K.value;
                        waitv3<dn_younger(k, DQ, WO)>(aq[k % DQ][0], aq[k % DQ][1], aq[k % DQ][2]);
                        constexpr unsigned o = ((k + 1) % 10) * 2048;
#define DXF(m) __builtin_bit_cast(bf16x8, aq[k % DQ][m])
                        SBAR();
                        MPRIO(DN_HI, 0); da[0][0] = MFMA(w0[k & 1], DXF(0), da[0][0]); SBAR();
                        if constexpr (k + 1 < 24) w0[(k + 1) & 1] = ldstr2(ba0 + o, ba0 + o + 1024);
                        SBAR();
                        MPRIO(DN_HI, 1); da[0][1] = MFMA(w1[k & 1], DXF(0), da[0][1]); SBAR();
                        if constexpr (k + 1 < 24) w1[(k + 1) & 1] = ldstr2(ba1 + o, ba1 + o + 1024);
                        SBAR();
                        MPRIO(DN_HI, 2); da[1][0] = MFMA(w0[k & 1], DXF(1), da[1][0]); SBAR();
                        if constexpr (k + DQ < 24) bissue(IC<k + DQ>{});
                        if constexpr (WO && k >= 16) woissue(IC<(k + 8) % 10>{});
                        SBAR();
                        MPRIO(DN_HI, 3); da[1][1] = MFMA(w1[k & 1], DXF(1), da[1][1]); SBAR();
                        MPRIO(DN_HI, 4); da[2][0] = MFMA(w0[k & 1], DXF(2), da[2][0]); SBAR();
                        MPRIO(DN_HI, 5); da[2][1] = MFMA(w1[k & 1], DXF(2), da[2][1]); SBAR();
                        if constexpr (k + DQ < 24) aissue(IC<k + DQ>{});
                    });
                    __builtin_amdgcn_s_setprio(3);
                    STAMP(9);
                    if constexpr (WO) { CFENCE(); woissue(IC<2>{}); woissue(IC<3>{}); }
#pragma unroll
                    for (int n = 0; n < 2; ++n) {
#pragma unroll
                        for (int m = 0; m < 3; ++m) {
                            if (m != v) {
                                const unsigned dst = L_GU + (m * 3 + (v > m ? v - 1 : v)) * 4096 + lane * 16;
#pragma unroll
                                for (int e = 0; e < 4; ++e) *LP(f32x4, dst + e * 1024) = f32x4{da[m][n][4 * e], da[m][n][4 * e + 1], da[m][n][4 * e + 2], da[m][n][4 * e + 3]};
                            }
                        }
                        lpost(C_DP + v, 2 * n + 1); lwait_all(C_DP, 4, 2 * n + 1, lane);
                        if (v < 3) {
                            f32x16 t = v == 0 ? da[0][n] : (v == 1 ? da[1][n] : da[2][n]);
#pragma unroll
                            for (int o = 0; o < 3; ++o)
#pragma unroll
                                for (int e = 0; e < 4; ++e) {
                                    const f32x4 u = *LP(const f32x4, L_GU + (v * 3 + o) * 4096 + e * 1024 + lane * 16);
                                    t[4 * e] += u[0]; t[4 * e + 1] += u[1]; t[4 * e + 2] += u[2]; t[4 * e + 3] += u[3];
                                }
                            if (n) acc1 = t; else acc0 = t;
                        }
                        if (n == 0) { lpost(C_DP + v, 2); lwait_all(C_DP, 4, 2, lane); }
                    }
                    if constexpr (!WO) {
                        if (v < 3) {
                            float* pt = (float*)(ws + WS_PT) + (size_t)(b * 3 + v) * 2048 + lane;
#pragma unroll
                            for (int e = 0; e < 16; ++e) { pt[e * 64] = acc0[e]; pt[1024 + e * 64] = acc1[e]; }
                        }
                    }
                } else {
#pragma unroll
                    for (int u = 0; u < 20; ++u) glds16(dvo + u * 8 * 1280, p.wo + (size_t)v * 160 * 1280 + dc * 128, myring + u * 1024);
                    if (v < 3) {
                        const float* pt = (const float*)(ws + WS_PT) + (size_t)(b * 3 + v) * 2048 + lane;
#pragma unroll
                        for (int e = 0; e < 16; ++e) { acc0[e] = pt[e * 64]; acc1[e] = pt[1024 + e * 64]; }
                    }
                }
                if constexpr (WO) { WAITV(0); lpost(C_DP + v, 4); }
                if (v < nm) {
                    const int m = mfirst + v;
                    const unsigned ba0 = L_PANEL + (4 * h + tq) * 128 + bq, ba1 = ba0 ^ 64;
                    if constexpr (WO) {
                        const char* xrow = p.x + (size_t)(m * 32 + r) * 1280 + dc * 128 + h * 16;
                        u32x4 xres[4];
#pragma unroll
                        for (int e = 0; e < 4; ++e) xres[e] = *(const u32x4*)(xrow + e * 32);
                        lwait_all(C_DP, 4, 4, lane);
                        const __amdgpu_buffer_rsrc_t ro = mkrsrc(ws + WS_OT + (size_t)m * 40960, 40960);
                        gu32* of_ = (gu32*)(ws + WS_OF) + m * 32 + (lane & 31);
                        int hf = 0;
                        f32x16 t0[2] = {}, t1[2] = {};
#pragma unroll
                        for (int it = 0; it < 2; ++it) {
                            if constexpr (ATT) {
                                _Pragma("clang loop unroll(disable)") for (unsigned spins = 0; spins < (1u << 14); ++spins) {
                                    const unsigned ok = (unsigned)__ballot(__hip_atomic_load(of_, RLX_AGENT) == tag);
                                    const bool lo = (ok & 0xffffu) == 0xffffu, hi = (ok >> 16) == 0xffffu;
                                    if (it == 0) { if (lo || hi) { hf = lo ? 0 : 1; break; } }
                                    else if (hf ? hi : lo) break;
                                    TMO(spins, 1u << 14, 300, m);
                                    __builtin_amdgcn_s_sleep(OF_SLEEP);
                                }
                                CFENCE();
                            }
                            if (it == 0) STAMP(10);
                            u32x4 of[20];
#pragma unroll
                            for (int s = 0; s < 20; ++s) of[s] = __builtin_amdgcn_raw_buffer_load_b128(ro, lane * 16 + s * 1024, hf * 20480, 16);
                            const unsigned a0 = ba0 + hf * 40960, a1 = ba1 + hf * 40960, l0 = a0 + (36 * h + 16) * 128, l1 = a1 + (36 * h + 16) * 128;
                            bf16x8 w0[2], w1[2];
                            w0[0] = ldstr2(a0, a0 + 1024); w1[0] = ldstr2(a1, a1 + 1024);
                            static_for<0, 20>([&](auto S) {
                                constexpr int s = S.value, n = s + 1, gg = n / 5, jj = n % 5;
                                const bf16x8 xf = __builtin_bit_cast(bf16x8, of[s]);
                                if constexpr (n < 20) {
                                    if constexpr (jj < 4) {
                                        constexpr unsigned o = (80 * gg + 20 * jj) * 128;
                                        w0[n & 1] = ldstr2(a0 + o, a0 + o + 1024); w1[n & 1] = ldstr2(a1 + o, a1 + o + 1024);
                                    } else {
                                        constexpr unsigned o = 80 * gg * 128;
                                        w0[n & 1] = ldstr2(l0 + o, l0 + o + 2560); w1[n & 1] = ldstr2(l1 + o, l1 + o + 2560);
                                    }
                                }
                                SBAR();
                                t0[it] = MFMA(w0[s & 1], xf, t0[it]); t1[it] = MFMA(w1[s & 1], xf, t1[it]);
                                SBAR();
                            });
                            hf ^= 1;
                        }
                        acc0 += t0[0] + t0[1]; acc1 += t1[0] + t1[1];
                        char* orow = p.out + (size_t)(m * 32 + r) * 1280 + dc * 128 + h * 16;
#pragma unroll
                        for (int n = 0; n < 2; ++n) {
                            const f32x16& a = n ? acc1 : acc0;
#pragma unroll
                            for (int qq = 0; qq < 2; ++qq) {
                                float lo[4], hi[4];
#pragma unroll
                                for (int e = 0; e < 4; ++e) {
                                    auto sw = __builtin_amdgcn_permlane32_swap(__float_as_uint(a[8 * qq + e]), __float_as_uint(a[8 * qq + 4 + e]), false, false);
                                    lo[e] = __uint_as_float(sw[0]); hi[e] = __uint_as_float(sw[1]);
                                }
                                const u32x4 xv = xres[2 * n + qq];
                                const u32x4 ov = {pk2(lo[0] + bflo(xv[0]), lo[1] + bfhi(xv[0])), pk2(lo[2] + bflo(xv[1]), lo[3] + bfhi(xv[1])),
                                                  pk2(hi[0] + bflo(xv[2]), hi[1] + bfhi(xv[2])), pk2(hi[2] + bflo(xv[3]), hi[3] + bfhi(xv[3]))};
                                *(u32x4*)(orow + (2 * n + qq) * 32) = ov;
                            }
                        }
                        STAMP(11);
                    }
                }
            }
        }
    } else {
        LANE_CONSTS();
        bf16x8 Q0a, Q0b; u32x2 q1a, q1b;
        const int head = 4 * g + w;
        const AttSync as{lane, ws + WS_KV, (unsigned)((w < 3 ? w * 3072 : CHUNK - 3072) + lane * 16), (unsigned)(w < 3 ? w * 3072 : CHUNK - 3072), w};
        __builtin_amdgcn_s_setprio(3);
        unsigned long long fk = 0, fv = 0; bool early = false;
        if constexpr (PRO) {
            BLOCK_BARRIER();
            waitv5<15>(x0[0], x0[1], x0[2], x0[3], x0[4]);
            STAMP(1);
            ln_rows(x0, ra, lane);
            waitv5<10>(x1[0], x1[1], x1[2], x1[3], x1[4]);
            ln_rows(x1, ra + 4, lane);
            lpost(C_AP + w, 1);
            waitv5<5>(x2[0], x2[1], x2[2], x2[3], x2[4]);
            ln_rows(x2, rb, lane);
            waitv5<0>(x3[0], x3[1], x3[2], x3[3], x3[4]);
            ln_rows(x3, rb + 4, lane);
            lpost(C_AP + w, 2);
            unsigned pa[4];
#pragma unroll
            for (int s = 0; s < 4; ++s) pa[s] = L_PANEL + h * 512 + ((r ^ ((2 * s + h) & 7)) << 4);
            f32x16 qa = {}, qb = {};
            const unsigned wa = L_WQ + (8 * h + tq) * 160 + w * 40 + tb * 32 + (tb ? 0 : tp * 8);
            lwait_all(C_AP, 4, 2, lane); lwait_all(C_PP, 4, 1, lane);
            if constexpr (FUSED) tag = __builtin_amdgcn_readfirstlane(*(volatile AS3 unsigned*)lcnt(C_TAG));
            STAMP(2);
            bf16x8 fw[2][4], fa[2][4], fb[2][4];
#pragma unroll
            for (int s4 = 0; s4 < 4; ++s4) { fw[0][s4] = ldstr2(wa + s4 * 2560, wa + s4 * 2560 + 640); fa[0][s4] = ldsf(pa[s4] + s4 * 1024); fb[0][s4] = ldsf(pa[s4] + 40960 + s4 * 1024); }
            unsigned pk = 0;
            static_for<0, 10>([&](auto S) {
                constexpr int st = S.value, nx = st + 1;
                if constexpr (st == 3 || st == 5 || st == 7) lcheck(pk, C_WP, (st - 1) / 2, lane);
                if constexpr (st == 6 && ATT) gload8x2_sc1(fk, fv, lane * 8, ws + WS_KF + xc * 1024);
                if constexpr (st == 8 && ATT) {
                    waitv8x2(fk, fv);
                    early = __all((unsigned)(fk >> 32) == tag && (unsigned)(fv >> 32) == tag);
                    if (early) { lwait_all(C_AP, 4, 6, lane); att_dma(as, 0); att_dma(as, 1); }
                    STAMP(8);
                }
#pragma unroll
                for (int s4 = 0; s4 < 4; ++s4) {
                    qa = MFMA(fw[st & 1][s4], fa[st & 1][s4], qa); SBAR();
                    if constexpr (nx < 10) { const unsigned o = wa + (nx & 3) * 10240 + s4 * 2560; fw[nx & 1][s4] = ldstr2(o, o + 640); SBAR(); }
                    qb = MFMA(fw[st & 1][s4], fb[st & 1][s4], qb); SBAR();
                    if constexpr (nx < 10) { fa[nx & 1][s4] = ldsf(pa[s4] + (nx * 4 + s4) * 1024); fb[nx & 1][s4] = ldsf(pa[s4] + 40960 + (nx * 4 + s4) * 1024); SBAR(); }
                    if (s4 == 2 && (st == 2 || st == 4 || st == 6)) { pk = lpeek(C_WP, lane); SBAR(); }
                }
                if constexpr (!(st & 1) && st <= 6) lpost(C_AP + w, 3 + st / 2);
            });
            lpost(C_AP + w, 8);
            Q0a = pack8(qa, 0, QSCALE); Q0b = pack8(qb, 0, QSCALE);
            q1a = u32x2{pk2(qa[8] * QSCALE, qa[9] * QSCALE), pk2(qa[10] * QSCALE, qa[11] * QSCALE)};
            q1b = u32x2{pk2(qb[8] * QSCALE, qb[9] * QSCALE), pk2(qb[10] * QSCALE, qb[11] * QSCALE)};
            if (h) { q1a = u32x2{0u, 0u}; q1b = u32x2{0u, 0u}; }
            if constexpr (!ATT) {
                char* qt = ws + WS_QT + (size_t)((2 * ti) * 32 + head) * 2048 + lane * 16;
                *(bf16x8*)qt = Q0a; *(u32x2*)(qt + 1024) = q1a; *(bf16x8*)(qt + 65536) = Q0b; *(u32x2*)(qt + 65536 + 1024) = q1b;
            }
            STAMP(3);
        } else {
            BLOCK_BARRIER();
            if constexpr (ATT) {
                const char* qt = ws + WS_QT + (size_t)((2 * ti) * 32 + head) * 2048 + lane * 16;
                Q0a = *(const bf16x8*)qt; q1a = *(const u32x2*)(qt + 1024); Q0b = *(const bf16x8*)(qt + 65536); q1b = *(const u32x2*)(qt + 65536 + 1024);
            }
        }
        if constexpr (ATT) {
            float na = 0.f, nb = 0.f;
            {
                const u32x4 a0 = __builtin_bit_cast(u32x4, Q0a), b0 = __builtin_bit_cast(u32x4, Q0b);
#pragma unroll
                for (int e = 0; e < 4; ++e) { na += bflo(a0[e]) * bflo(a0[e]) + bfhi(a0[e]) * bfhi(a0[e]); nb += bflo(b0[e]) * bflo(b0[e]) + bfhi(b0[e]) * bfhi(b0[e]); }
#pragma unroll
                for (int e = 0; e < 2; ++e) { na += bflo(q1a[e]) * bflo(q1a[e]) + bfhi(q1a[e]) * bfhi(q1a[e]); nb += bflo(q1b[e]) * bflo(q1b[e]) + bfhi(q1b[e]) * bfhi(q1b[e]); }
                na += __shfl_xor(na, 32); nb += __shfl_xor(nb, 32);
            }
            if (!early) {
                gu64* f = (gu64*)(ws + WS_KF + xc * 1024) + lane;
                _Pragma("clang loop unroll(disable)") for (unsigned spins = 0; spins < (1u << 14); ++spins) {
                    fk = __hip_atomic_load(f, RLX_AGENT); fv = __hip_atomic_load(f + 64, RLX_AGENT);
                    if (__all((unsigned)(fk >> 32) == tag && (unsigned)(fv >> 32) == tag)) break;
                    TMO(spins, 1u << 14, 301, tag);
                    __builtin_amdgcn_s_sleep(4);
                }
                if constexpr (PRO) lwait_all(C_AP, 4, 6, lane);
                att_dma(as, 0); att_dma(as, 1);
            }
            STAMP(4);
            if constexpr (PRO) lwait_all(C_AP, 4, 8, lane);
            att_dma(as, 2);
            const float kn2 = wave_max(__uint_as_float((unsigned)fk)), vmx = wave_max(__uint_as_float((unsigned)fv));
            const float Ma = __builtin_sqrtf(na * kn2) * 1.01f, Mb = __builtin_sqrtf(nb * kn2) * 1.01f;
            const bool fast = __all(Ma <= BOUND_MAX && Mb <= BOUND_MAX && vmx <= 1e30f);
            const unsigned sa = (fast && h) ? pk2(-Ma, 0.f) : q1a[0], sb = (fast && h) ? pk2(-Mb, 0.f) : q1b[0];
            const u32x4 a0 = {sa, q1a[1], 0u, 0u}, a1 = {h ? sa : 0u, 0u, q1a[0], q1a[1]}, b0 = {sb, q1b[1], 0u, 0u}, b1 = {h ? sb : 0u, 0u, q1b[0], q1b[1]};
            f32x16 Oa = {}, Ob = {};
            WAITV(6); lpost(C_AP + w, AP0); lwait_all(C_AP, 4, AP0, lane);
            __builtin_amdgcn_s_setprio(ATT_PRIO);
            STAMP(5); STAMPC(12);
            if (fast) att_fast(as, Q0a, BC(a0), BC(a1), Q0b, BC(b0), BC(b1), Oa, Ob);
            else att_safe(as, Q0a, BC(a0), BC(a1), Q0b, BC(b0), BC(b1), Oa, Ob);
            STAMP(6); STAMPC(13);
            const __amdgpu_buffer_rsrc_t ro = mkrsrc(ws + WS_OT, 64 * 40 * 1024);
#pragma unroll
            for (int q = 0; q < 2; ++q) {
                const f32x16& O = q ? Ob : Oa;
                const float inv = 1.f / __shfl(O[8], r + 32);
                const unsigned base = ((2 * ti + q) * 40 + 5 * g) * 1024;
                __builtin_amdgcn_raw_buffer_store_b128(__builtin_bit_cast(u32x4, pack8(O, 0, inv)), ro, base + w * 1024 + lane * 16, 0, 16);
                if (!h) {
                    u32x2 t = {pk2(O[8] * inv, O[9] * inv), pk2(O[10] * inv, O[11] * inv)};
                    __builtin_amdgcn_raw_buffer_store_b64(t, ro, base + 4096 + ((w >> 1) * 32 + r) * 16 + (w & 1) * 8, 0, 16);
                }
            }
            if constexpr (WO) {
                WAITV(0);
                if (lane < 2) __hip_atomic_store((gu32*)(ws + WS_OF) + (2 * ti + lane) * 32 + head, tag, RLX_AGENT);
            }
            STAMP(7);
        }
    }
}

template <int PH>
static void launch(const Params& p, hipStream_t stream) {
    (void)hipFuncSetAttribute((const void*)mega<PH>, hipFuncAttributeMaxDynamicSharedMemorySize, L_TOTAL);
    mega<PH><<<256, 512, L_TOTAL, stream>>>(p);
}

extern "C" void kernel_launch(void* const* d_in, const int* in_sizes, int n_in, void* d_out, int out_size, void* d_ws, size_t ws_size, hipStream_t stream) {
    Params p;
    p.x = (const char*)d_in[0]; p.wq = (const char*)d_in[1]; p.wk = (const char*)d_in[2]; p.wv = (const char*)d_in[3]; p.wo = (const char*)d_in[4];
    p.w1 = (const char*)d_in[6]; p.w3 = (const char*)d_in[7]; p.w2 = (const char*)d_in[8];
    p.out = (char*)d_out; p.ws = (char*)d_ws;
    launch<27>(p, stream);
}
```

```cpp
#include <hip/hip_runtime.h>
#include <hip/hip_bf16.h>
#include <stdint.h>

#define AS1 __attribute__((address_space(1)))
#define AS3 __attribute__((address_space(3)))
typedef __bf16 bf16x2 __attribute__((ext_vector_type(2)));
typedef __bf16 bf16x8 __attribute__((ext_vector_type(8)));
typedef short s16x4 __attribute__((ext_vector_type(4)));
typedef short s16x8 __attribute__((ext_vector_type(8)));
typedef float f32x2 __attribute__((ext_vector_type(2)));
typedef float f32x4 __attribute__((ext_vector_type(4)));
typedef float f32x16 __attribute__((ext_vector_type(16)));
typedef unsigned u32x2 __attribute__((ext_vector_type(2)));
typedef unsigned u32x4 __attribute__((ext_vector_type(4)));
typedef AS1 unsigned gu32;
typedef AS1 unsigned long long gu64;

#define ATT_PRIO 2
#define GEM_PRIO 0
#define GU_HI 0
#define DN_HI 0
#define MPRIO(mask, i) __builtin_amdgcn_s_setprio(((mask) >> (i)) & 1 ? 3 : 0)
#define GU_DELAY 8
#define GU_ROT 4
#define GU_GQ 5
#define OF_SLEEP 3
#define HF_SLEEP 6
#define DN_DQ 8
#define SPLIT_MODE 0

constexpr int SEQ = 2048, DM = 640, FF = 1536;
constexpr unsigned WS_KF = 0;
constexpr unsigned WS_HF = 16384;
constexpr unsigned WS_OF = 32768;
constexpr unsigned WS_EP = 49152;
constexpr unsigned KVD = 5376;
constexpr unsigned KV_K1 = 2048, KV_V = 2560, KV_VT = 1408;
constexpr unsigned WS_KV = 65536;
constexpr unsigned WS_HT = WS_KV + 176128;
constexpr unsigned WS_OT = WS_HT + 64 * 96 * 1024;
constexpr unsigned WS_QT = WS_OT + 64 * 40 * 1024;
constexpr unsigned WS_PT = WS_QT + 64 * 32 * 2048;
constexpr unsigned WS_ST = WS_PT + 256 * 3 * 32 * 256;
constexpr unsigned L_PANEL = 0;
constexpr unsigned L_KV = 81920;
constexpr unsigned L_WQ = 81920;
constexpr unsigned L_GU = 124928;
constexpr unsigned GSL = 12320;
constexpr unsigned L_WKV = 124928;
constexpr unsigned L_MISC = 161920;
constexpr unsigned L_TOTAL = 163840;
constexpr unsigned CHUNK = 2 * KVD;
constexpr int NCHUNK = 16;
enum { C_AP = 0  , C_GP = 4  , C_DP = 8  , C_PP = 12  , C_WP = 16  , C_KVP = 20, C_KVFIN, C_TAG, C_N };

struct Params { const char* x; const char* wq; const char* wk; const char* wv; const char* wo; const char* w1; const char* w3; const char* w2; char* out; char* ws; };

#define SBAR() __builtin_amdgcn_sched_barrier(0)
#define PIN(x) asm volatile("" : "+v"(x))
#define CFENCE() asm volatile("" ::: "memory")
#define WAITV(n) asm volatile("s_waitcnt vmcnt(" #n ")" ::: "memory")
#define MFMA(a, b, c) __builtin_amdgcn_mfma_f32_32x32x16_bf16((a), (b), (c), 0, 0, 0)
#define RLX_AGENT __ATOMIC_RELAXED, __HIP_MEMORY_SCOPE_AGENT
#define RLX_WG __ATOMIC_RELAXED, __HIP_MEMORY_SCOPE_WORKGROUP

#define TMO(spins, lim, code, tgt) do {} while (0)
#define STAMP(id) do {} while (0)
#define STAMPC(id) do {} while (0)

#define LP(T, off) ((AS3 T*)(uintptr_t)(unsigned)(off))
__device__ __forceinline__ bf16x8 ldsf(unsigned off) { return *LP(const bf16x8, off); }
__device__ __forceinline__ s16x4 ldstr(unsigned off) { return __builtin_amdgcn_ds_read_tr16_b64_v4i16(LP(s16x4, off)); }
__device__ __forceinline__ bf16x8 ldstr2(unsigned off0, unsigned off1) {
    s16x4 a = ldstr(off0), b = ldstr(off1);
    return __builtin_bit_cast(bf16x8, __builtin_shufflevector(a, b, 0, 1, 2, 3, 4, 5, 6, 7));
}
__device__ __forceinline__ unsigned pk2(float lo, float hi) { bf16x2 v = {(__bf16)lo, (__bf16)hi}; return __builtin_bit_cast(unsigned, v); }
__device__ __forceinline__ float bflo(unsigned u) { return __uint_as_float(u << 16); }
__device__ __forceinline__ float bfhi(unsigned u) { return __uint_as_float(u & 0xffff0000u); }
__device__ __forceinline__ float rbf(float f) { return (float)(__bf16)f; }

__device__ __forceinline__ void glds16(unsigned voff, const char* sbase, unsigned ldsdst) {
    unsigned keep;
    asm volatile("s_mov_b32 %0, m0\n\ts_mov_b32 m0, %3\n\ts_nop 2\n\tglobal_load_lds_dwordx4 %1, %2\n\ts_mov_b32 m0, %0"
                 : "=&s"(keep) : "v"(voff), "s"(sbase), "s"(ldsdst) : "memory");
}
__device__ __forceinline__ void glds16_sc1(unsigned voff, const char* sbase, unsigned ldsdst) {
    unsigned keep;
    asm volatile("s_mov_b32 %0, m0\n\ts_mov_b32 m0, %3\n\ts_nop 2\n\tglobal_load_lds_dwordx4 %1, %2 sc1\n\ts_mov_b32 m0, %0"
                 : "=&s"(keep) : "v"(voff), "s"(sbase), "s"(ldsdst) : "memory");
}
__device__ __forceinline__ void glds16x3_sc1(unsigned voff, const char* sbase, unsigned ldsdst) {
    unsigned keep;
    asm volatile("s_mov_b32 %0, m0\n\ts_mov_b32 m0, %3\n\ts_nop 2\n\tglobal_load_lds_dwordx4 %1, %2 sc1\n\tglobal_load_lds_dwordx4 %1, %2 offset:1024 sc1\n\tglobal_load_lds_dwordx4 %1, %2 offset:2048 sc1\n\ts_mov_b32 m0, %0"
                 : "=&s"(keep) : "v"(voff), "s"(sbase), "s"(ldsdst) : "memory");
}
__device__ __forceinline__ void glds16x3v(unsigned v0, unsigned v1, unsigned v2, const char* sbase, unsigned ldsdst) {
    unsigned keep;
    asm volatile("s_mov_b32 %0, m0\n\ts_mov_b32 m0, %5\n\ts_nop 0\n\tglobal_load_lds_dwordx4 %1, %4\n\tglobal_load_lds_dwordx4 %2, %4 offset:1024\n\tglobal_load_lds_dwordx4 %3, %4 offset:2048\n\ts_mov_b32 m0, %0"
                 : "=&s"(keep) : "v"(v0), "v"(v1), "v"(v2), "s"(sbase), "s"(ldsdst) : "memory");
}
__device__ __forceinline__ void glds_gu(unsigned va, unsigned vb, unsigned vc, const char* s1, const char* s3, unsigned ldsdst) {
    unsigned keep;
    asm volatile("s_mov_b32 %0, m0\n\ts_mov_b32 m0, %8\n\ts_nop 0\n\tglobal_load_lds_dwordx4 %1, %4\n\tglobal_load_lds_dwordx4 %2, %5 offset:1024\n\t"
                 "s_mov_b32 exec_hi, 0\n\tglobal_load_lds_dwordx4 %3, %6 offset:2048\n\ts_not_b64 exec, exec\n\tglobal_load_lds_dwordx4 %3, %7 offset:2048\n\t"
                 "s_mov_b64 exec, -1\n\ts_mov_b32 m0, %0"
                 : "=&s"(keep) : "v"(va), "v"(vb), "v"(vc), "s"(s1), "s"(s3 - 1024), "s"(s1 - 2048), "s"(s3 - 2048), "s"(ldsdst) : "memory", "scc");
}
__device__ __forceinline__ void l2touch(const char* ga) {
    unsigned keep;
    asm volatile("s_mov_b32 %0, m0\n\ts_mov_b32 m0, %2\n\ts_nop 0\n\tglobal_load_lds_dword %1, off\n\ts_mov_b32 m0, %0" : "=&s"(keep) : "v"(ga), "s"(L_MISC + 1024) : "memory");
}
#define GLOAD16(dst, voff, sbase, imm) asm volatile("s_nop 4\n\tglobal_load_dwordx4 %0, %1, %2 offset:%3" : "=v"(dst) : "v"(voff), "s"(sbase), "i"(imm) : "memory")

__device__ __forceinline__ AS3 unsigned* lcnt(int c) { return LP(unsigned, L_MISC) + c; }
__device__ __forceinline__ void larrive(int c, int lane) { CFENCE(); if (lane == 0) __hip_atomic_fetch_add(lcnt(c), 1u, RLX_WG); CFENCE(); }
__device__ __forceinline__ void lset(int c, unsigned v, int lane) { CFENCE(); if (lane == 0) __hip_atomic_store(lcnt(c), v, RLX_WG); CFENCE(); }
__device__ __forceinline__ void lwait(int c, unsigned target) {
    CFENCE();
    _Pragma("clang loop unroll(disable)") for (unsigned spins = 0; spins < (1u << 22); ++spins) {
        if ((unsigned)__builtin_amdgcn_readfirstlane(__hip_atomic_load(lcnt(c), RLX_WG)) >= target) break;
        TMO(spins, 1u << 16, 100 + c, target);
        __builtin_amdgcn_s_sleep(1);
    }
    CFENCE();
}
__device__ __forceinline__ void lwait_all(int c, int n, unsigned target, int lane) {
    CFENCE();
    AS3 unsigned* q = lcnt(c) + (lane < n ? lane : 0);
    _Pragma("clang loop unroll(disable)") for (unsigned spins = 0; spins < (1u << 16); ++spins) {
        if (__all(__hip_atomic_load(q, RLX_WG) >= target)) break;
        TMO(spins, 1u << 16, 200 + c, target);
        __builtin_amdgcn_s_sleep(1);
    }
    CFENCE();
}
__device__ __forceinline__ void lpost(int c, unsigned v) { CFENCE(); *(volatile AS3 unsigned*)lcnt(c) = v; CFENCE(); }
__device__ __forceinline__ void linc(unsigned vaddr, unsigned vone) {
    asm volatile("s_mov_b64 exec, 1\n\tds_add_u32 %0, %1\n\ts_mov_b64 exec, -1" :: "v"(vaddr), "v"(vone) : "memory");
}
__device__ __forceinline__ unsigned lpeek(int c, int lane) { return *((volatile AS3 unsigned*)lcnt(c) + (lane & 3)); }
__device__ __forceinline__ void lcheck(unsigned pk, int c, unsigned target, int lane) {
    if (__builtin_expect(__any(pk < target), 0)) lwait_all(c, 4, target, lane);
    CFENCE();
}
__device__ __forceinline__ void gwait(const char* ws, unsigned off, int n, unsigned tag, int lane) {
    gu32* f = (gu32*)(ws + off) + (lane < n ? lane : 0);
    _Pragma("clang loop unroll(disable)") for (unsigned spins = 0; spins < (1u << 14); ++spins) {
        unsigned v = __hip_atomic_load(f, RLX_AGENT);
        if (__all(v == tag)) break;
        TMO(spins, 1u << 14, 300, off);
        __builtin_amdgcn_s_sleep(6);
    }
    CFENCE();
}
__device__ __forceinline__ float gwait64(const char* ws, unsigned off, unsigned tag, int lane) {
    gu64* f = (gu64*)(ws + off) + lane;
    unsigned long long v = 0;
    _Pragma("clang loop unroll(disable)") for (unsigned spins = 0; spins < (1u << 14); ++spins) {
        v = __hip_atomic_load(f, RLX_AGENT);
        if (__all((unsigned)(v >> 32) == tag)) break;
        __builtin_amdgcn_s_sleep(6);
    }
    CFENCE();
    return __uint_as_float((unsigned)v);
}
__device__ __forceinline__ __amdgpu_buffer_rsrc_t mkrsrc(const char* base, unsigned bytes) {
    return __builtin_amdgcn_make_buffer_rsrc((void*)base, 0, bytes, 0x00020000);
}
__device__ __forceinline__ float dpp_sum16(float v) {
    v += __int_as_float(__builtin_amdgcn_update_dpp(0, __float_as_int(v), 0x128, 0xf, 0xf, false));
    v += __int_as_float(__builtin_amdgcn_update_dpp(0, __float_as_int(v), 0x124, 0xf, 0xf, false));
    v += __int_as_float(__builtin_amdgcn_update_dpp(0, __float_as_int(v), 0x122, 0xf, 0xf, false));
    v += __int_as_float(__builtin_amdgcn_update_dpp(0, __float_as_int(v), 0x121, 0xf, 0xf, false));
    return v;
}
__device__ __forceinline__ float wave_max(float v) {
    int x = __float_as_int(v);
    x = max(x, __builtin_amdgcn_update_dpp(0, x, 0x128, 0xf, 0xf, false)); x = max(x, __builtin_amdgcn_update_dpp(0, x, 0x124, 0xf, 0xf, false));
    x = max(x, __builtin_amdgcn_update_dpp(0, x, 0x122, 0xf, 0xf, false)); x = max(x, __builtin_amdgcn_update_dpp(0, x, 0x121, 0xf, 0xf, false));
    const int a = max(__builtin_amdgcn_readlane(x, 0), __builtin_amdgcn_readlane(x, 16)), b = max(__builtin_amdgcn_readlane(x, 32), __builtin_amdgcn_readlane(x, 48));
    return __int_as_float(max(a, b));
}
__device__ __forceinline__ bf16x8 pack8(const f32x16& a, int b, float sc) {
    u32x4 r = {pk2(a[b] * sc, a[b + 1] * sc), pk2(a[b + 2] * sc, a[b + 3] * sc), pk2(a[b + 4] * sc, a[b + 5] * sc), pk2(a[b + 6] * sc, a[b + 7] * sc)};
    return __builtin_bit_cast(bf16x8, r);
}

template <int I> struct IC { static constexpr int value = I; };
template <int B, int E, class F> __device__ __forceinline__ void static_for(F&& f) { if constexpr (B < E) { f(IC<B>{}); static_for<B + 1, E>(f); } }
#define WAITVN(n) asm volatile("s_waitcnt vmcnt(%0)" :: "i"(n) : "memory")
__device__ __forceinline__ void GLOAD16S(u32x4& dst, unsigned voff, const char* sbase) { asm volatile("global_load_dwordx4 %0, %1, %2 sc1" : "=v"(dst) : "v"(voff), "s"(sbase) : "memory"); }
__device__ __forceinline__ void GLOAD16N(u32x4& dst, unsigned voff, const char* sbase) { asm volatile("global_load_dwordx4 %0, %1, %2" : "=v"(dst) : "v"(voff), "s"(sbase) : "memory"); }
__device__ __forceinline__ void GLOAD16N_IF(u32x4& dst, unsigned voff, const char* sbase, int cond) {
    asm volatile("s_cmp_eq_u32 %3, 0\n\ts_cbranch_scc1 1f\n\tglobal_load_dwordx4 %0, %1, %2\n1:" : "=v"(dst) : "v"(voff), "s"(sbase), "s"(cond) : "memory", "scc"); }
template <int N> __device__ __forceinline__ void waitv5(u32x4& a, u32x4& b, u32x4& c, u32x4& d, u32x4& e) { asm volatile("s_waitcnt vmcnt(%5)" : "+v"(a), "+v"(b), "+v"(c), "+v"(d), "+v"(e) : "i"(N) : "memory"); }
__device__ __forceinline__ void gload8x2_sc1(unsigned long long& a, unsigned long long& b, unsigned voff, const char* sbase) {
    asm volatile("global_load_dwordx2 %0, %2, %3 sc1\n\tglobal_load_dwordx2 %1, %2, %3 offset:512 sc1" : "=&v"(a), "=&v"(b) : "v"(voff), "s"(sbase) : "memory"); }
__device__ __forceinline__ void waitv8x2(unsigned long long& a, unsigned long long& b) { asm volatile("s_waitcnt vmcnt(0)" : "+v"(a), "+v"(b) :: "memory"); }
__device__ __forceinline__ void PIN2(u32x4& a, u32x4& b) { asm volatile("" : "+v"(a), "+v"(b) :: "memory"); }
__device__ __forceinline__ u32x4 scale8(unsigned x0, unsigned x1, unsigned x2, unsigned x3, unsigned clo, unsigned chi, float c) {
    unsigned o0, o1, o2, o3; float t0, t1, t2, t3, t4, t5, t6, t7;
    asm("v_dot2_f32_bf16 %4, %12, %16, %18\n\tv_dot2_f32_bf16 %5, %12, %17, %18\n\tv_dot2_f32_bf16 %6, %13, %16, %18\n\tv_dot2_f32_bf16 %7, %13, %17, %18\n\t"
        "v_dot2_f32_bf16 %8, %14, %16, %18\n\tv_dot2_f32_bf16 %9, %14, %17, %18\n\tv_dot2_f32_bf16 %10, %15, %16, %18\n\tv_dot2_f32_bf16 %11, %15, %17, %18\n\t"
        "v_cvt_pk_bf16_f32 %0, %4, %5\n\tv_cvt_pk_bf16_f32 %1, %6, %7\n\tv_cvt_pk_bf16_f32 %2, %8, %9\n\tv_cvt_pk_bf16_f32 %3, %10, %11"
        : "=&v"(o0), "=&v"(o1), "=&v"(o2), "=&v"(o3), "=&v"(t0), "=&v"(t1), "=&v"(t2), "=&v"(t3), "=&v"(t4), "=&v"(t5), "=&v"(t6), "=&v"(t7)
        : "v"(x0), "v"(x1), "v"(x2), "v"(x3), "v"(clo), "v"(chi), "v"(c));
    return u32x4{o0, o1, o2, o3};
}
template <int N> __device__ __forceinline__ void waitv3(u32x4& a, u32x4& b, u32x4& c) { asm volatile("s_waitcnt vmcnt(%3)" : "+v"(a), "+v"(b), "+v"(c) : "i"(N) : "memory"); }

constexpr int dn_younger(int k, int D, bool wo) {
    int n = 0, la = 0, lb = 0;
    for (int i = 0; i < D; ++i) { n += 2; if (i == k + 1) lb = n; }
    for (int i = 0; i < D; ++i) { n += 3; if (i == k) la = n; }
    for (int s = 0; s < k; ++s) {
        if (s + D < 24) { n += 2; if (s + D == k + 1) lb = n; }
        if (wo && s >= 16) n += 2;
        if (s + D < 24) { n += 3; if (s + D == k) la = n; }
    }
    return n - (la > lb ? la : lb);
}
constexpr float QSCALE = 0.22360679774997896f * 1.4426950408889634f;
constexpr float BOUND_MAX = 50.f;

struct AttSync { int lane; const char* kv; unsigned dvoff; unsigned wo; int w; };
__device__ __forceinline__ unsigned kvslot(int c) { return L_KV + ((c + 2) & 3) * CHUNK; }
__device__ __forceinline__ void att_dma(const AttSync& a, int c) { glds16x3_sc1(a.dvoff, a.kv + (size_t)c * CHUNK, kvslot(c) + a.wo); }
constexpr unsigned AP0 = 16;
#define RING_X(c) do { WAITV(3); lpost(C_AP + as.w, AP0 + (c) + 1); } while (0)
#define RP_A
#define RP_B
#define RING_Y(c, pk) do { RP_A lcheck(pk, C_AP, AP0 + (c) + 1, lane); const int cn_ = (c) + 3 < NCHUNK ? (c) + 3 : NCHUNK - 1;   \
                           glds16x3_sc1(as.dvoff, as.kv + (size_t)cn_ * CHUNK, kvslot((c) + 3) + as.wo); RP_B } while (0)

__device__ __forceinline__ void att_fast(const AttSync& as, bf16x8 Q0a, bf16x8 Q1a0, bf16x8 Q1a1, bf16x8 Q0b, bf16x8 Q1b0, bf16x8 Q1b1, f32x16& Oa, f32x16& Ob) {
    const int lane = as.lane, r = lane & 31, h = lane >> 5;
    const unsigned ak0 = lane * 16, ak1 = h ? KV_V + 20 * 16 : KV_K1 + r * 16;
    const unsigned av = KV_V + h * 352 + (r <= 20 ? r : 21) * 16;
    const f32x16 zero = {};
    f32x16 Sa, Sb = {};
    bf16x8 K0[2], K1[2], V0[2], V1[2];
    u32x4 Pa0, Pa1, Pb0 = {}, Pb1 = {};
    unsigned pk = 0;
    unsigned long long rp0 = 0, rp1 = 0;
    V0[1] = bf16x8{}; V1[1] = bf16x8{};
    unsigned vk0 = L_KV + ak0, vk1 = L_KV + ak1, vv = L_KV + av;
    PIN(vk0); PIN(vk1); PIN(vv);
    K0[0] = ldsf(vk0 + 2 * CHUNK); K1[0] = ldsf(vk1 + 2 * CHUNK); K0[1] = ldsf(vk0 + 2 * CHUNK + 1024); V1[0] = ldsf(vv + 2 * CHUNK + 704); V0[0] = ldsf(vv + 2 * CHUNK);
    Sa = MFMA(K0[0], Q0a, zero); Sa = MFMA(K1[0], Q1a0, Sa);
#define EX4(S, i) do { S[i] = __builtin_amdgcn_exp2f(S[i]); S[i + 1] = __builtin_amdgcn_exp2f(S[i + 1]); S[i + 2] = __builtin_amdgcn_exp2f(S[i + 2]); S[i + 3] = __builtin_amdgcn_exp2f(S[i + 3]); } while (0)
#define GAP(MF, LD, T, k, PW, j, S, i) do { MF; SBAR(); LD; PW[j] = pk2(T[k], T[k + 1]); PW[j + 1] = pk2(T[k + 2], T[k + 3]); PIN(PW); SBAR(); EX4(S, i); PIN(S); SBAR(); } while (0)
#define BC(x) __builtin_bit_cast(bf16x8, x)
#define STEP(e, d, QB1, QA1N, KN, L3, LK0, LK1, LV) do { \
        GAP(Ob = MFMA(V0[e ^ 1], BC(Pb0), Ob), (void)0,                  Sb, 12, Pb1, 2, Sa, 0); \
        GAP(Sb = MFMA(K0[e], Q0b, zero),       (void)0,                  Sa, 0, Pa0, 0, Sa, 4); \
        GAP(Sb = MFMA(K1[d], QB1, Sb),         L3,                       Sa, 4, Pa0, 2, Sa, 8); \
        GAP(Ob = MFMA(V1[e ^ 1], BC(Pb1), Ob), K0[e] = ldsf(LK0),        Sa, 8, Pa1, 0, Sa, 12); \
        GAP(Oa = MFMA(V0[e], BC(Pa0), Oa),     LK1,                      Sa, 12, Pa1, 2, Sb, 0); \
        GAP(Sa = MFMA(K0[e ^ 1], Q0a, zero),   V1[e ^ 1] = ldsf((LV) + 704), Sb, 0, Pb0, 0, Sb, 4); \
        GAP(Sa = MFMA(K1[KN], QA1N, Sa),       V0[e ^ 1] = ldsf(LV),     Sb, 4, Pb0, 2, Sb, 8); \
        GAP(Oa = MFMA(V1[e], BC(Pa1), Oa),     (void)0,                  Sb, 8, Pb1, 0, Sb, 12); \
    } while (0)
#define CHUNK4(c, B, N) do { \
        STEP(0, 0, Q1b0, Q1a1, 0, (void)0, vk0 + (B) + KVD, K1[1] = ldsf(vk1 + (B) + KVD), vv + (B) + KV_VT); \
        RING_X(c); \
        STEP(1, 0, Q1b1, Q1a0, 1, pk = lpeek(C_AP, lane), vk0 + (B) + KVD + 1024, (void)0, vv + (B) + KVD); \
        RING_Y(c, pk); \
        STEP(0, 1, Q1b0, Q1a1, 1, (void)0, vk0 + (N), K1[0] = ldsf(vk1 + (N)), vv + (B) + KVD + KV_VT); \
        STEP(1, 1, Q1b1, Q1a0, 0, (void)0, vk0 + (N) + 1024, (void)0, vv + (N)); \
    } while (0)
    for (int c = 0; c < NCHUNK; c += 4) {
        CHUNK4(c, 2 * CHUNK, 3 * CHUNK); CHUNK4(c + 1, 3 * CHUNK, 0); CHUNK4(c + 2, 0, CHUNK); CHUNK4(c + 3, CHUNK, 2 * CHUNK);
    }
#undef CHUNK4
    Pb1[2] = pk2(Sb[12], Sb[13]); Pb1[3] = pk2(Sb[14], Sb[15]);
    Ob = MFMA(V0[1], BC(Pb0), Ob); Ob = MFMA(V1[1], BC(Pb1), Ob);
#undef STEP
#undef GAP
#undef EX4
}

__device__ __forceinline__ void att_safe(const AttSync& as, bf16x8 Q0a, bf16x8 Q1a0, bf16x8 Q1a1, bf16x8 Q0b, bf16x8 Q1b0, bf16x8 Q1b1, f32x16& Oa, f32x16& Ob) {
    const int lane = as.lane, r = lane & 31, h = lane >> 5;
    const unsigned ak0 = lane * 16, ak1 = h ? KV_V + 20 * 16 : KV_K1 + r * 16;
    const unsigned av = KV_V + h * 352 + (r <= 20 ? r : 21) * 16;
    const f32x16 zero = {};
    float ma = -INFINITY, mb = -INFINITY;
    unsigned long long rp0 = 0, rp1 = 0;
    for (int c = 0; c < NCHUNK; ++c) {
        const unsigned b0 = kvslot(c);
        for (int t = 0; t < 4; ++t) {
            if (t == 1) RING_X(c);
            if (t == 2) RING_Y(c, 0u);
            const unsigned base = b0 + (t >> 1) * KVD; const int e = t & 1;
            bf16x8 K0 = ldsf(base + ak0 + e * 1024), K1 = ldsf(base + ak1), V0 = ldsf(base + av + e * KV_VT), V1 = ldsf(base + av + e * KV_VT + 704);
#pragma unroll
            for (int q = 0; q < 2; ++q) {
                f32x16 S = MFMA(K0, q ? Q0b : Q0a, zero); S = MFMA(K1, q ? (e ? Q1b1 : Q1b0) : (e ? Q1a1 : Q1a0), S);
                float mx = S[0];
#pragma unroll
                for (int i = 1; i < 16; ++i) mx = fmaxf(mx, S[i]);
                mx = fmaxf(mx, __shfl_xor(mx, 32));
                float& m = q ? mb : ma; f32x16& O = q ? Ob : Oa;
                const float mn = fmaxf(m, mx);
                const float al = __builtin_amdgcn_exp2f(m - mn);
                m = mn;
#pragma unroll
                for (int i = 0; i < 16; ++i) { O[i] *= al; S[i] = __builtin_amdgcn_exp2f(S[i] - mn); }
                O = MFMA(V0, pack8(S, 0, 1.f), O); O = MFMA(V1, pack8(S, 8, 1.f), O);
            }
        }
    }
}

__device__ __forceinline__ void xload8(u32x4 (&x)[5], u32x4 (&y)[5], const char* xb, unsigned vo, unsigned vo2, int skip) {
    asm volatile("s_cmp_lg_u32 %13, 0\n\ts_cbranch_scc1 1f\n\t"
                 "global_load_dwordx4 %0, %10, %12\n\tglobal_load_dwordx4 %1, %10, %12 offset:256\n\tglobal_load_dwordx4 %2, %10, %12 offset:512\n\t"
                 "global_load_dwordx4 %3, %10, %12 offset:768\n\tglobal_load_dwordx4 %4, %10, %12 offset:1024\n\t"
                 "global_load_dwordx4 %5, %11, %12\n\tglobal_load_dwordx4 %6, %11, %12 offset:256\n\tglobal_load_dwordx4 %7, %11, %12 offset:512\n\t"
                 "global_load_dwordx4 %8, %11, %12 offset:768\n\tglobal_load_dwordx4 %9, %11, %12 offset:1024\n1:"
                 : "=&v"(x[0]), "=&v"(x[1]), "=&v"(x[2]), "=&v"(x[3]), "=&v"(x[4]), "=&v"(y[0]), "=&v"(y[1]), "=&v"(y[2]), "=&v"(y[3]), "=&v"(y[4])
                 : "v"(vo), "v"(vo2), "s"(xb), "s"(skip) : "memory", "scc");
}
__device__ __forceinline__ void xload4(u32x4 (&x)[5], const char* xb, unsigned vo) {
    asm volatile("global_load_dwordx4 %0, %5, %6\n\tglobal_load_dwordx4 %1, %5, %6 offset:256\n\tglobal_load_dwordx4 %2, %5, %6 offset:512\n\t"
                 "global_load_dwordx4 %3, %5, %6 offset:768\n\tglobal_load_dwordx4 %4, %5, %6 offset:1024"
                 : "=&v"(x[0]), "=&v"(x[1]), "=&v"(x[2]), "=&v"(x[3]), "=&v"(x[4]) : "v"(vo), "s"(xb) : "memory");
}
__device__ __forceinline__ void ln_rows(const u32x4 (&xr)[5], int row0, int lane) {
    f32x2 xv[20];
    f32x2 s0 = {0.f, 0.f}, s1 = {0.f, 0.f}, q0 = {0.f, 0.f}, q1 = {0.f, 0.f};
#pragma unroll
    for (int i = 0; i < 20; i += 2) {
        const unsigned a = xr[i >> 2][i & 3], b = xr[i >> 2][(i & 3) + 1];
        xv[i] = f32x2{bflo(a), bfhi(a)}; xv[i + 1] = f32x2{bflo(b), bfhi(b)};
        s0 += xv[i]; q0 = __builtin_elementwise_fma(xv[i], xv[i], q0);
        s1 += xv[i + 1]; q1 = __builtin_elementwise_fma(xv[i + 1], xv[i + 1], q1);
    }
    s0 += s1; q0 += q1;
    const float s = dpp_sum16(s0[0] + s0[1]), ss = dpp_sum16(q0[0] + q0[1]);
    const float mean = s * (1.f / 640.f);
    const float var = fmaxf(ss * (1.f / 640.f) - mean * mean, 0.f);
    const float rs = __builtin_amdgcn_rcpf(rbf(__builtin_amdgcn_sqrtf(rbf(rbf(var) + 1e-5f)))), mo = -rbf(mean) * rs;
    const f32x2 rs2 = {rs, rs}, mo2 = {mo, mo};
    const int row = row0 + (lane >> 4), c = lane & 15;
    const unsigned wa = L_PANEL + (row >> 5) * 40960 + (c >> 1) * 1024 + (c & 1) * 512 + (((row & 31) ^ (c & 7)) << 4);
#pragma unroll
    for (int u = 0; u < 5; ++u) {
        u32x4 o;
#pragma unroll
        for (int e = 0; e < 4; ++e) { const f32x2 y = __builtin_elementwise_fma(xv[4 * u + e], rs2, mo2); o[e] = pk2(y[0], y[1]); }
        *LP(u32x4, wa + u * 8192) = o;
    }
}
#define BLOCK_BARRIER() asm volatile("s_waitcnt lgkmcnt(0)\n\ts_barrier" ::: "memory")

template <int PH>
__global__ void __launch_bounds__(512, 2) mega(Params p) {
    constexpr bool PRO = PH & 1, ATT = PH & 2, DN = PH & 8, WO = PH & 16;
    const int w = __builtin_amdgcn_readfirstlane(threadIdx.x >> 6);
    const int b = blockIdx.x, xc = b & 7, j = b >> 3;
    const int ti = (xc >> 1) * 8 + (j >> 2), g = (xc & 1) * 4 + (j & 3);
    constexpr bool FUSED = PH == 27;
    const int kmt = g & 1;
    const int ra = kmt * 32 + (w & 3) * 8, rb = (kmt ^ 1) * 32 + (w & 3) * 8;
    u32x4 x0[5], x1[5], x2[5], x3[5];
    if constexpr (PRO) {
        asm volatile("" :: "s"(p.x), "s"(p.ws), "s"(p.wq), "s"(p.wk));
        const char* xb = p.x + (size_t)ti * 64 * 1280;
        const unsigned xvo = ((threadIdx.x >> 4) & 3) * 1280 + (threadIdx.x & 15) * 16;
        xload8(x0, x1, xb + (size_t)ra * 1280, xvo, xvo + 5120, w >> 2);
        xload8(x2, x3, xb + (size_t)rb * 1280, xvo, xvo + 5120, w >> 2);
    }
    const char* pc; asm volatile("s_getpc_b64 %0" : "=s"(pc));
    unsigned tag = 1u;
    char* ws = p.ws;
    STAMP(0);
    if (threadIdx.x < C_N) *lcnt(threadIdx.x) = (threadIdx.x == C_WP || threadIdx.x == C_WP + 1 || threadIdx.x == C_PP || threadIdx.x == C_PP + 1) ? 100u : 0u;
    const bool haskv = (g >> 2) == (ti & 1);
    const char* wqb = p.wq + g * 160;
#define LANE_CONSTS() int lane = __builtin_amdgcn_mbcnt_hi(~0u, __builtin_amdgcn_mbcnt_lo(~0u, 0u)); PIN(lane); \
    const int r = lane & 31, h = lane >> 5, tq = (lane & 15) >> 2, tp = lane & 3, tb = (lane >> 4) & 1; (void)r; (void)h; (void)tq; (void)tp; (void)tb

    if (w >= 4) {
        LANE_CONSTS();
        const int v = w - 4;
        unsigned epv = 0u;
        if constexpr (FUSED) {
            asm volatile("global_load_dword %0, %1, %2 sc1" : "=v"(epv) : "v"(0u), "s"(p.ws + WS_EP) : "memory");
            constexpr unsigned CODE_PF = 65536;
            const unsigned line = (unsigned)(j * 4 + v) * 64u + lane;
            if (line * 128u < CODE_PF) l2touch(pc + line * 128u);
        }
#define GET_TAG() do { if constexpr (FUSED) { asm volatile("s_nop 0\n\tv_readfirstlane_b32 %0, %1\n\ts_nop 3" : "=s"(tag) : "v"(epv) : "memory"); tag += 0x9E3779B1u; } } while (0)
        __builtin_amdgcn_s_setprio(3);
        if constexpr (PRO) {
            const int kvm = 2 * ti + (g & 1); const bool isV = (g >> 1) & 1;
            const char* wkvb = isV ? p.wv : p.wk;
            if (haskv && v < 2) {
#pragma unroll
                for (int n = 0; n < 13; ++n) {
                    const unsigned o = v * 12800 + (n < 12 ? n * 1024 : 11776);
                    glds16(o + lane * 16, wkvb, L_WKV + o);
                }
            }
            BLOCK_BARRIER();
            if (v >= 2) {
                const int v2 = v - 2;
                unsigned wo5[5];
#pragma unroll
                for (int i = 0; i < 5; ++i) { const unsigned c = (v2 + 2 * i) * 64 + lane, row = c / 10u, col = c - row * 10u; wo5[i] = row * 1280 + col * 16; }
#pragma unroll
                for (int n = 0; n < 20; ++n) glds16(wo5[n % 5], wqb + (n / 5) * 64 * 1280, L_WQ + (v2 + 2 * n) * 1024);
                u32x4 wst[3][10];
                auto wst_issue = [&](auto I) {
                    constexpr int rr = I.value / 10, i = I.value % 10;
                    GLOAD16N(wst[rr][i], wo5[i % 5], wqb + (4 + 2 * rr + i / 5) * 64 * 1280);
                };
                static_for<0, 10>(wst_issue);
                WAITV(10);
                GET_TAG(); lpost(C_TAG, tag);
                lpost(C_PP + v, 1);
                STAMP(2);
                static_for<10, 30>(wst_issue);
                static_for<0, 3>([&](auto R) {
                    constexpr int rr = R.value;
                    waitv5<20 - 10 * rr>(wst[rr][0], wst[rr][1], wst[rr][2], wst[rr][3], wst[rr][4]); waitv5<63>(wst[rr][5], wst[rr][6], wst[rr][7], wst[rr][8], wst[rr][9]);
                    lwait_all(C_AP, 4, 3 + rr, lane);
#pragma unroll
                    for (int i = 0; i < 10; ++i) *LP(u32x4, L_WQ + (rr & 1) * 20480 + (v2 + 2 * i) * 1024 + lane * 16) = wst[rr][i];
                    lpost(C_WP + v, rr + 1);
                });
            } else {
              if (haskv) {
                f32x16 acc = {};
                const unsigned wa = L_WKV + (v * 320 + 8 * h + tq) * 40 + tb * 32 + (tb ? 0 : tp * 8);
                unsigned xa[4];
#pragma unroll
                for (int s = 0; s < 4; ++s) xa[s] = L_PANEL + kmt * 40960 + v * 20480 + h * 512 + ((r ^ ((2 * s + h) & 7)) << 4);
                WAITV(0);
                GET_TAG();
                STAMP(2);
                bf16x8 wf[20], xf[20];
#pragma unroll
                for (int s = 0; s < 20; ++s) wf[s] = ldstr2(wa + s * 640, wa + s * 640 + 160);
                lwait_all(C_AP, 4, 1, lane);
#pragma unroll
                for (int s = 0; s < 20; ++s) xf[s] = ldsf(xa[s & 3] + s * 1024);
                SBAR();
                if (isV) {
#pragma unroll
                    for (int s = 0; s < 20; ++s) acc = MFMA(xf[s], wf[s], acc);
                } else {
#pragma unroll
                    for (int s = 0; s < 20; ++s) acc = MFMA(wf[s], xf[s], acc);
                }
                if (v) {
#pragma unroll
                    for (int e = 0; e < 4; ++e) *LP(f32x4, L_WKV + 12800 + e * 1024 + lane * 16) = f32x4{acc[4 * e], acc[4 * e + 1], acc[4 * e + 2], acc[4 * e + 3]};
                    lpost(C_KVP, 1);
                } else {
                    lwait(C_KVP, 1);
#pragma unroll
                    for (int e = 0; e < 4; ++e) {
                        const f32x4 t = *LP(const f32x4, L_WKV + 12800 + e * 1024 + lane * 16);
                        acc[4 * e] += t[0]; acc[4 * e + 1] += t[1]; acc[4 * e + 2] += t[2]; acc[4 * e + 3] += t[3];
                    }
                    const __amdgpu_buffer_rsrc_t rk = mkrsrc(ws + WS_KV, 32 * KVD);
                    const unsigned base = ti * KVD, ke = g & 1;
                    const u32x4 f0 = __builtin_bit_cast(u32x4, pack8(acc, 0, 1.f)), f1 = __builtin_bit_cast(u32x4, pack8(acc, 8, 1.f));
                    float mx;
                    if (!isV) {
                        __builtin_amdgcn_raw_buffer_store_b128(f0, rk, base + ke * 1024 + lane * 16, 0, 16);
                        const u32x2 k1 = {f1[0], f1[1]};
                        if (!h) __builtin_amdgcn_raw_buffer_store_b64(k1, rk, base + KV_K1 + r * 16 + ke * 8, 0, 16);
                        lpost(C_KVFIN, 1);
                        float n2 = 0.f;
#pragma unroll
                        for (int e = 0; e < 4; ++e) n2 += bflo(f0[e]) * bflo(f0[e]) + bfhi(f0[e]) * bfhi(f0[e]);
                        if (!h) n2 += bflo(f1[0]) * bflo(f1[0]) + bfhi(f1[0]) * bfhi(f1[0]) + bflo(f1[1]) * bflo(f1[1]) + bfhi(f1[1]) * bfhi(f1[1]);
                        n2 += __shfl_xor(n2, 32);
                        mx = (n2 == n2) ? n2 : INFINITY;
                    } else {
                        const unsigned cst = r == 20 ? 0x3f803f80u : 0u;
                        const u32x4 c4 = {cst, cst, cst, cst};
                        if (r <= 21) {
                            __builtin_amdgcn_raw_buffer_store_b128(r >= 20 ? c4 : f0, rk, base + KV_V + ke * KV_VT + h * 352 + r * 16, 0, 16);
                            __builtin_amdgcn_raw_buffer_store_b128(r >= 20 ? c4 : f1, rk, base + KV_V + ke * KV_VT + 704 + h * 352 + r * 16, 0, 16);
                        }
                        lpost(C_KVFIN, 1);
                        float vm = 0.f;
#pragma unroll
                        for (int e = 0; e < 16; ++e) { const float a = fabsf(acc[e]); vm = (a <= vm) ? vm : ((a == a) ? a : INFINITY); }
                        mx = r < 20 ? vm : 0.f;
                    }
                    mx = wave_max(mx);
                    WAITV(0);
                    STAMP(15);
                    if (lane < 8) __hip_atomic_store((gu64*)(ws + WS_KF + lane * 1024 + (isV ? 512 : 0)) + kvm, ((unsigned long long)tag << 32) | __float_as_uint(mx), RLX_AGENT);
                }
            }
              else { WAITV(0); GET_TAG(); }
              STAMP(3);
            }
            if (haskv) lwait(C_KVFIN, 1);
#pragma unroll
            for (int n = 0; n < 2; ++n) {
                unsigned L = n * 64 + lane; L = (j * 4 + v) * 120 + (L < 120u ? L : 119u);
                const unsigned m3 = L >= 7680u, rem = L - (m3 ? 7680u : 0u), row = rem / 12u;
                l2touch((m3 ? p.w3 : p.w1) + row * 3072 + (xc & 1) * 1536 + (rem - row * 12u) * 128);
            }
            lwait_all(C_AP, 4, GU_DELAY, lane);
            STAMP(4);
            {
                const char* w1s = p.w1 + g * 384 + v * 96; const char* w3s = p.w3 + g * 384 + v * 96;
                auto goff = [](unsigned idx) { const unsigned row = idx / 6u; return row * 3072 + (idx - row * 6u) * 16; };
                const unsigned gva = goff(lane), gvb = goff((lane - 8) & 63), gvc = goff(64 + (h ? (r - 8) & 31 : r));
                const unsigned gdst = L_GU + v * 9216;
                auto gdma = [&](auto S) { constexpr int s = S.value; glds_gu(gva, gvb, gvc, w1s + (size_t)s * 16 * 3072, w3s + (size_t)s * 16 * 3072, gdst + (s % 3) * 3072); };
                static_for<0, 3>(gdma);
                f32x16 acc[2][3] = {};
                unsigned bn[3][2], pa[4], pb[4];
#pragma unroll
                for (int n = 0; n < 3; ++n)
#pragma unroll
                    for (int t = 0; t < 2; ++t) {
                        const unsigned idx = 6 * (8 * h + 4 * t + tq) + 2 * n + (tp >> 1);
                        const unsigned o = idx < 64u ? (tb ? 1024 + ((idx + 8) & 63) * 16 : idx * 16) : (tb ? 2560 + ((idx - 56) & 31) * 16 : 2048 + (idx - 64) * 16);
                        bn[n][t] = gdst + o + (tp & 1) * 8;
                    }
#pragma unroll
                for (int s = 0; s < 4; ++s) { pa[s] = L_PANEL + h * 512 + ((r ^ ((2 * s + h) & 7)) << 4); pb[s] = pa[s] + 40960; PIN(pb[s]); }
                bf16x8 wf[2][3], xa[2], xb2[2];
                auto rdw = [&](auto S) { constexpr int s = S.value;
#pragma unroll
                    for (int n = 0; n < 3; ++n) wf[s & 1][n] = ldstr2(bn[n][0] + (s % 3) * 3072, bn[n][1] + (s % 3) * 3072); };
                auto rdx = [&](auto S) { constexpr int s = S.value; xa[s & 1] = ldsf(pa[s & 3] + s * 1024); xb2[s & 1] = ldsf(pb[s & 3] + s * 1024); };
                WAITV(8);
                rdx(IC<0>{}); rdw(IC<0>{});
                asm volatile("s_waitcnt lgkmcnt(0)" ::: "memory");
                gdma(IC<3>{});
                static_for<0, 40>([&](auto S) {
                    constexpr int s = S.value; constexpr bool nx = s + 1 < 40;
                    MPRIO(GU_HI, 0); acc[0][0] = MFMA(wf[s & 1][0], xa[s & 1], acc[0][0]); SBAR();
                    if constexpr (nx) rdx(IC<s + 1>{});
                    SBAR();
                    MPRIO(GU_HI, 1); acc[1][0] = MFMA(wf[s & 1][0], xb2[s & 1], acc[1][0]); SBAR();
                    if constexpr (nx) { WAITVN(4 * ((s + 3 < 39 ? s + 3 : 39) - (s + 1))); rdw(IC<s + 1>{}); }
                    SBAR();
                    MPRIO(GU_HI, 2); acc[0][1] = MFMA(wf[s & 1][1], xa[s & 1], acc[0][1]); SBAR();
                    MPRIO(GU_HI, 3); acc[1][1] = MFMA(wf[s & 1][1], xb2[s & 1], acc[1][1]); SBAR();
                    if constexpr (s + 4 < 40) { asm volatile("s_waitcnt lgkmcnt(0)" ::: "memory"); gdma(IC<s + 4>{}); }
                    SBAR();
                    MPRIO(GU_HI, 4); acc[0][2] = MFMA(wf[s & 1][2], xa[s & 1], acc[0][2]); SBAR();
                    MPRIO(GU_HI, 5); acc[1][2] = MFMA(wf[s & 1][2], xb2[s & 1], acc[1][2]); SBAR();
                });
                lpost(C_GP + v, 42);
                __builtin_amdgcn_s_setprio(3);
                STAMP(6);
                const __amdgpu_buffer_rsrc_t rh = mkrsrc(ws + WS_HT, 64 * 96 * 1024);
#pragma unroll
                for (int m = 0; m < 2; ++m)
#pragma unroll
                    for (int n = 0; n < 3; ++n) {
                        f32x16 hv;
#pragma unroll
                        for (int e = 0; e < 8; ++e) {
                            const float gg = acc[m][n][e];
                            hv[e] = gg * __builtin_amdgcn_rcpf(1.f + __builtin_amdgcn_exp2f(-1.4426950408889634f * gg)) * acc[m][n][e + 8];
                        }
                        __builtin_amdgcn_raw_buffer_store_b128(__builtin_bit_cast(u32x4, pack8(hv, 0, 1.f)), rh, ((2 * ti + m) * 96 + g * 12 + 3 * v + n) * 1024 + lane * 16, 0, 16);
                    }
                if constexpr (DN) {
                    WAITV(0);
                    if (lane < 2) __hip_atomic_store((gu32*)(ws + WS_HF) + (2 * ti + lane) * 32 + g * 4 + v, tag, RLX_AGENT);
                }
                __builtin_amdgcn_s_setprio(0);
                if constexpr (DN) {
#pragma unroll
                    for (int n = 0; n < 2; ++n) {
                        unsigned L = (j * 4 + v) * 85 + n * 64 + lane; L = L < 10880u ? L : 10879u;
                        const unsigned row = L / 5u;
                        l2touch((row < 1536u ? p.w2 + row * 1280 : p.wo + (row - 1536u) * 1280) + (xc & 1) * 640 + (L - row * 5u) * 128);
                    }
                }
                STAMP(7);
            }
        } else {
            BLOCK_BARRIER();
        }
        if constexpr (DN || WO) {
            LANE_CONSTS();
            const unsigned bq = ((tb * 32 + (tp >> 1) * 16) ^ ((unsigned)(tq >> 1) << 6)) + (tp & 1) * 8;
            const unsigned drow = lane >> 3, dcol = ((lane & 7) ^ (((lane >> 4) & 1) << 2)) * 16;
            const int G = xc >> 1, r0 = G < 2 ? 6 * G : 12 + 5 * (G - 2), nr = G < 2 ? 6 : 5;
            if constexpr (FUSED) {
                if (b == 255 && v == 0) {
                    gu32* f = (gu32*)(ws + WS_HF) + (lane >> 3) * 64 + (lane & 7) * 4;
                    _Pragma("clang loop unroll(disable)") for (unsigned spins = 0; spins < (1u << 14); ++spins) {
                        bool ok = true;
#pragma unroll
                        for (int n = 0; n < 4; ++n) ok &= __hip_atomic_load(f + n * 512, RLX_AGENT) == tag;
                        if (__all(ok)) break;
                        TMO(spins, 1u << 14, 303, tag);
                        __builtin_amdgcn_s_sleep(32);
                    }
                    if (lane == 0) __hip_atomic_store((gu32*)(ws + WS_EP), tag, RLX_AGENT);
                }
            }
            if (j < 5 * nr) {
                const int dr = r0 + j / 5, dc = (xc & 1) * 5 + j % 5;
                const int mfirst = dr < 20 ? 3 * dr : 60 + 2 * (dr - 20), nm = dr < 20 ? 3 : 2;
                const unsigned dvo = drow * 1280 + dcol;
                const unsigned myring = L_PANEL + v * 20480;
                f32x16 acc0 = {}, acc1 = {};
                if constexpr (PRO) { lwait_all(C_GP, 4, 42, lane); lwait_all(C_AP, 4, 8, lane); }
                if constexpr (DN) {
                    f32x16 da[3][2] = {};
                    const int drot = (j % 5) * 4;
                    const char* bsrc = p.w2 + (size_t)v * 384 * 1280 + dc * 128;
                    const char* asrc = ws + WS_HT + ((size_t)mfirst * 96 + v * 24) * 1024;
                    const unsigned am1 = lane * 16 + 98304, am2 = lane * 16 + (nm == 3 ? 196608 : 98304);
                    constexpr int DQ = DN_DQ;
                    u32x4 aq[DQ][3];
                    auto bissue = [&](auto K) {
                        constexpr int k = K.value;
                        int kr = k + drot; kr = kr >= 24 ? kr - 24 : kr;
                        const char* sb = bsrc + (size_t)kr * 16 * 1280;
                        glds16(dvo, sb, myring + (k % 10) * 2048); glds16(dvo + 8 * 1280, sb, myring + (k % 10) * 2048 + 1024);
                    };
                    auto woissue = [&](auto U) {
                        constexpr int u = U.value;
                        const char* sb = p.wo + (size_t)(v * 160 + u * 16) * 1280 + dc * 128;
                        glds16(dvo, sb, myring + u * 2048); glds16(dvo + 8 * 1280, sb, myring + u * 2048 + 1024);
                    };
                    auto aissue = [&](auto K) {
                        constexpr int k = K.value;
                        int kr = k + drot; kr = kr >= 24 ? kr - 24 : kr;
                        const char* sb = asrc + (size_t)kr * 1024;
                        GLOAD16S(aq[k % DQ][0], lane * 16, sb); GLOAD16S(aq[k % DQ][1], am1, sb); GLOAD16S(aq[k % DQ][2], am2, sb);
                    };
                    static_for<0, DQ>(bissue);
                    if constexpr (PRO) {
                        gu32* f = (gu32*)(ws + WS_HF) + (mfirst + ((lane >> 3) < nm ? (lane >> 3) : 0)) * 32 + 8 * v + (lane & 7);
                        _Pragma("clang loop unroll(disable)") for (unsigned spins = 0; spins < (1u << 14); ++spins) {
                            if (__all(__hip_atomic_load(f, RLX_AGENT) == tag)) break;
                            TMO(spins, 1u << 14, 302, tag);
                            __builtin_amdgcn_s_sleep(HF_SLEEP);
                        }
                        CFENCE();
                    }
                    STAMP(8);
                    static_for<0, DQ>(aissue);
                    const unsigned ba0 = myring + (4 * h + tq) * 128 + bq, ba1 = ba0 ^ 64;
                    bf16x8 w0[2], w1[2];
                    WAITVN(3 * DQ);
                    w0[0] = ldstr2(ba0, ba0 + 1024); w1[0] = ldstr2(ba1, ba1 + 1024);
                    static_for<0, 24>([&](auto K) {
                        constexpr int k = K.value;
                        waitv3<dn_younger(k, DQ, WO)>(aq[k % DQ][0], aq[k % DQ][1], aq[k % DQ][2]);
                        constexpr unsigned o = ((k + 1) % 10) * 2048;
#define DXF(m) __builtin_bit_cast(bf16x8, aq[k % DQ][m])
                        SBAR();
                        MPRIO(DN_HI, 0); da[0][0] = MFMA(w0[k & 1], DXF(0), da[0][0]); SBAR();
                        if constexpr (k + 1 < 24) w0[(k + 1) & 1] = ldstr2(ba0 + o, ba0 + o + 1024);
                        SBAR();
                        MPRIO(DN_HI, 1); da[0][1] = MFMA(w1[k & 1], DXF(0), da[0][1]); SBAR();
                        if constexpr (k + 1 < 24) w1[(k + 1) & 1] = ldstr2(ba1 + o, ba1 + o + 1024);
                        SBAR();
                        MPRIO(DN_HI, 2); da[1][0] = MFMA(w0[k & 1], DXF(1), da[1][0]); SBAR();
                        if constexpr (k + DQ < 24) bissue(IC<k + DQ>{});
                        if constexpr (WO && k >= 16) woissue(IC<(k + 8) % 10>{});
                        SBAR();
                        MPRIO(DN_HI, 3); da[1][1] = MFMA(w1[k & 1], DXF(1), da[1][1]); SBAR();
                        MPRIO(DN_HI, 4); da[2][0] = MFMA(w0[k & 1], DXF(2), da[2][0]); SBAR();
                        MPRIO(DN_HI, 5); da[2][1] = MFMA(w1[k & 1], DXF(2), da[2][1]); SBAR();
                        if constexpr (k + DQ < 24) aissue(IC<k + DQ>{});
                    });
                    __builtin_amdgcn_s_setprio(3);
                    STAMP(9);
                    if constexpr (WO) { CFENCE(); woissue(IC<2>{}); woissue(IC<3>{}); }
#pragma unroll
                    for (int n = 0; n < 2; ++n) {
#pragma unroll
                        for (int m = 0; m < 3; ++m) {
                            if (m != v) {
                                const unsigned dst = L_GU + (m * 3 + (v > m ? v - 1 : v)) * 4096 + lane * 16;
#pragma unroll
                                for (int e = 0; e < 4; ++e) *LP(f32x4, dst + e * 1024) = f32x4{da[m][n][4 * e], da[m][n][4 * e + 1], da[m][n][4 * e + 2], da[m][n][4 * e + 3]};
                            }
                        }
                        lpost(C_DP + v, 2 * n + 1); lwait_all(C_DP, 4, 2 * n + 1, lane);
                        if (v < 3) {
                            f32x16 t = v == 0 ? da[0][n] : (v == 1 ? da[1][n] : da[2][n]);
#pragma unroll
                            for (int o = 0; o < 3; ++o)
#pragma unroll
                                for (int e = 0; e < 4; ++e) {
                                    const f32x4 u = *LP(const f32x4, L_GU + (v * 3 + o) * 4096 + e * 1024 + lane * 16);
                                    t[4 * e] += u[0]; t[4 * e + 1] += u[1]; t[4 * e + 2] += u[2]; t[4 * e + 3] += u[3];
                                }
                            if (n) acc1 = t; else acc0 = t;
                        }
                        if (n == 0) { lpost(C_DP + v, 2); lwait_all(C_DP, 4, 2, lane); }
                    }
                    if constexpr (!WO) {
                        if (v < 3) {
                            float* pt = (float*)(ws + WS_PT) + (size_t)(b * 3 + v) * 2048 + lane;
#pragma unroll
                            for (int e = 0; e < 16; ++e) { pt[e * 64] = acc0[e]; pt[1024 + e * 64] = acc1[e]; }
                        }
                    }
                } else {
#pragma unroll
                    for (int u = 0; u < 20; ++u) glds16(dvo + u * 8 * 1280, p.wo + (size_t)v * 160 * 1280 + dc * 128, myring + u * 1024);
                    if (v < 3) {
                        const float* pt = (const float*)(ws + WS_PT) + (size_t)(b * 3 + v) * 2048 + lane;
#pragma unroll
                        for (int e = 0; e < 16; ++e) { acc0[e] = pt[e * 64]; acc1[e] = pt[1024 + e * 64]; }
                    }
                }
                if constexpr (WO) { WAITV(0); lpost(C_DP + v, 4); }
                if (v < nm) {
                    const int m = mfirst + v;
                    const unsigned ba0 = L_PANEL + (4 * h + tq) * 128 + bq, ba1 = ba0 ^ 64;
                    if constexpr (WO) {
                        const char* xrow = p.x + (size_t)(m * 32 + r) * 1280 + dc * 128 + h * 16;
                        u32x4 xres[4];
#pragma unroll
                        for (int e = 0; e < 4; ++e) xres[e] = *(const u32x4*)(xrow + e * 32);
                        lwait_all(C_DP, 4, 4, lane);
                        const __amdgpu_buffer_rsrc_t ro = mkrsrc(ws + WS_OT + (size_t)m * 40960, 40960);
                        gu32* of_ = (gu32*)(ws + WS_OF) + m * 32 + (lane & 31);
                        int hf = 0;
                        f32x16 t0[2] = {}, t1[2] = {};
#pragma unroll
                        for (int it = 0; it < 2; ++it) {
                            if constexpr (ATT) {
                                _Pragma("clang loop unroll(disable)") for (unsigned spins = 0; spins < (1u << 14); ++spins) {
                                    const unsigned ok = (unsigned)__ballot(__hip_atomic_load(of_, RLX_AGENT) == tag);
                                    const bool lo = (ok & 0xffffu) == 0xffffu, hi = (ok >> 16) == 0xffffu;
                                    if (it == 0) { if (lo || hi) { hf = lo ? 0 : 1; break; } }
                                    else if (hf ? hi : lo) break;
                                    TMO(spins, 1u << 14, 300, m);
                                    __builtin_amdgcn_s_sleep(OF_SLEEP);
                                }
                                CFENCE();
                            }
                            if (it == 0) STAMP(10);
                            u32x4 of[20];
#pragma unroll
                            for (int s = 0; s < 20; ++s) of[s] = __builtin_amdgcn_raw_buffer_load_b128(ro, lane * 16 + s * 1024, hf * 20480, 16);
                            const unsigned a0 = ba0 + hf * 40960, a1 = ba1 + hf * 40960, l0 = a0 + (36 * h + 16) * 128, l1 = a1 + (36 * h + 16) * 128;
                            bf16x8 w0[2], w1[2];
                            w0[0] = ldstr2(a0, a0 + 1024); w1[0] = ldstr2(a1, a1 + 1024);
                            static_for<0, 20>([&](auto S) {
                                constexpr int s = S.value, n = s + 1, gg = n / 5, jj = n % 5;
                                const bf16x8 xf = __builtin_bit_cast(bf16x8, of[s]);
                                if constexpr (n < 20) {
                                    if constexpr (jj < 4) {
                                        constexpr unsigned o = (80 * gg + 20 * jj) * 128;
                                        w0[n & 1] = ldstr2(a0 + o, a0 + o + 1024); w1[n & 1] = ldstr2(a1 + o, a1 + o + 1024);
                                    } else {
                                        constexpr unsigned o = 80 * gg * 128;
                                        w0[n & 1] = ldstr2(l0 + o, l0 + o + 2560); w1[n & 1] = ldstr2(l1 + o, l1 + o + 2560);
                                    }
                                }
                                SBAR();
                                t0[it] = MFMA(w0[s & 1], xf, t0[it]); t1[it] = MFMA(w1[s & 1], xf, t1[it]);
                                SBAR();
                            });
                            hf ^= 1;
                        }
                        acc0 += t0[0] + t0[1]; acc1 += t1[0] + t1[1];
                        char* orow = p.out + (size_t)(m * 32 + r) * 1280 + dc * 128 + h * 16;
#pragma unroll
                        for (int n = 0; n < 2; ++n) {
                            const f32x16& a = n ? acc1 : acc0;
#pragma unroll
                            for (int qq = 0; qq < 2; ++qq) {
                                float lo[4], hi[4];
#pragma unroll
                                for (int e = 0; e < 4; ++e) {
                                    auto sw = __builtin_amdgcn_permlane32_swap(__float_as_uint(a[8 * qq + e]), __float_as_uint(a[8 * qq + 4 + e]), false, false);
                                    lo[e] = __uint_as_float(sw[0]); hi[e] = __uint_as_float(sw[1]);
                                }
                                const u32x4 xv = xres[2 * n + qq];
                                const u32x4 ov = {pk2(lo[0] + bflo(xv[0]), lo[1] + bfhi(xv[0])), pk2(lo[2] + bflo(xv[1]), lo[3] + bfhi(xv[1])),
                                                  pk2(hi[0] + bflo(xv[2]), hi[1] + bfhi(xv[2])), pk2(hi[2] + bflo(xv[3]), hi[3] + bfhi(xv[3]))};
                                *(u32x4*)(orow + (2 * n + qq) * 32) = ov;
                            }
                        }
                        STAMP(11);
                    }
                }
            }
        }
    } else {
        LANE_CONSTS();
        bf16x8 Q0a, Q0b; u32x2 q1a, q1b;
        const int head = 4 * g + w;
        const AttSync as{lane, ws + WS_KV, (unsigned)((w < 3 ? w * 3072 : CHUNK - 3072) + lane * 16), (unsigned)(w < 3 ? w * 3072 : CHUNK - 3072), w};
        __builtin_amdgcn_s_setprio(3);
        unsigned long long fk = 0, fv = 0; bool early = false;
        if constexpr (PRO) {
            BLOCK_BARRIER();
            waitv5<15>(x0[0], x0[1], x0[2], x0[3], x0[4]);
            STAMP(1);
            ln_rows(x0, ra, lane);
            waitv5<10>(x1[0], x1[1], x1[2], x1[3], x1[4]);
            ln_rows(x1, ra + 4, lane);
            lpost(C_AP + w, 1);
            waitv5<5>(x2[0], x2[1], x2[2], x2[3], x2[4]);
            ln_rows(x2, rb, lane);
            waitv5<0>(x3[0], x3[1], x3[2], x3[3], x3[4]);
            ln_rows(x3, rb + 4, lane);
            lpost(C_AP + w, 2);
            unsigned pa[4];
#pragma unroll
            for (int s = 0; s < 4; ++s) pa[s] = L_PANEL + h * 512 + ((r ^ ((2 * s + h) & 7)) << 4);
            f32x16 qa = {}, qb = {};
            const unsigned wa = L_WQ + (8 * h + tq) * 160 + w * 40 + tb * 32 + (tb ? 0 : tp * 8);
            lwait_all(C_AP, 4, 2, lane); lwait_all(C_PP, 4, 1, lane);
            if constexpr (FUSED) tag = __builtin_amdgcn_readfirstlane(*(volatile AS3 unsigned*)lcnt(C_TAG));
            STAMP(2);
            bf16x8 fw[2][4], fa[2][4], fb[2][4];
#pragma unroll
            for (int s4 = 0; s4 < 4; ++s4) { fw[0][s4] = ldstr2(wa + s4 * 2560, wa + s4 * 2560 + 640); fa[0][s4] = ldsf(pa[s4] + s4 * 1024); fb[0][s4] = ldsf(pa[s4] + 40960 + s4 * 1024); }
            unsigned pk = 0;
            static_for<0, 10>([&](auto S) {
                constexpr int st = S.value, nx = st + 1;
                if constexpr (st == 3 || st == 5 || st == 7) lcheck(pk, C_WP, (st - 1) / 2, lane);
                if constexpr (st == 6 && ATT) gload8x2_sc1(fk, fv, lane * 8, ws + WS_KF + xc * 1024);
                if constexpr (st == 8 && ATT) {
                    waitv8x2(fk, fv);
                    early = __all((unsigned)(fk >> 32) == tag && (unsigned)(fv >> 32) == tag);
                    if (early) { lwait_all(C_AP, 4, 6, lane); att_dma(as, 0); att_dma(as, 1); }
                    STAMP(8);
                }
#pragma unroll
                for (int s4 = 0; s4 < 4; ++s4) {
                    qa = MFMA(fw[st & 1][s4], fa[st & 1][s4], qa); SBAR();
                    if constexpr (nx < 10) { const unsigned o = wa + (nx & 3) * 10240 + s4 * 2560; fw[nx & 1][s4] = ldstr2(o, o + 640); SBAR(); }
                    qb = MFMA(fw[st & 1][s4], fb[st & 1][s4], qb); SBAR();
                    if constexpr (nx < 10) { fa[nx & 1][s4] = ldsf(pa[s4] + (nx * 4 + s4) * 1024); fb[nx & 1][s4] = ldsf(pa[s4] + 40960 + (nx * 4 + s4) * 1024); SBAR(); }
                    if (s4 == 2 && (st == 2 || st == 4 || st == 6)) { pk = lpeek(C_WP, lane); SBAR(); }
                }
                if constexpr (!(st & 1) && st <= 6) lpost(C_AP + w, 3 + st / 2);
            });
            lpost(C_AP + w, 8);
            Q0a = pack8(qa, 0, QSCALE); Q0b = pack8(qb, 0, QSCALE);
            q1a = u32x2{pk2(qa[8] * QSCALE, qa[9] * QSCALE), pk2(qa[10] * QSCALE, qa[11] * QSCALE)};
            q1b = u32x2{pk2(qb[8] * QSCALE, qb[9] * QSCALE), pk2(qb[10] * QSCALE, qb[11] * QSCALE)};
            if (h) { q1a = u32x2{0u, 0u}; q1b = u32x2{0u, 0u}; }
            if constexpr (!ATT) {
                char* qt = ws + WS_QT + (size_t)((2 * ti) * 32 + head) * 2048 + lane * 16;
                *(bf16x8*)qt = Q0a; *(u32x2*)(qt + 1024) = q1a; *(bf16x8*)(qt + 65536) = Q0b; *(u32x2*)(qt + 65536 + 1024) = q1b;
            }
            STAMP(3);
        } else {
            BLOCK_BARRIER();
            if constexpr (ATT) {
                const char* qt = ws + WS_QT + (size_t)((2 * ti) * 32 + head) * 2048 + lane * 16;
                Q0a = *(const bf16x8*)qt; q1a = *(const u32x2*)(qt + 1024); Q0b = *(const bf16x8*)(qt + 65536); q1b = *(const u32x2*)(qt + 65536 + 1024);
            }
        }
        if constexpr (ATT) {
            float na = 0.f, nb = 0.f;
            {
                const u32x4 a0 = __builtin_bit_cast(u32x4, Q0a), b0 = __builtin_bit_cast(u32x4, Q0b);
#pragma unroll
                for (int e = 0; e < 4; ++e) { na += bflo(a0[e]) * bflo(a0[e]) + bfhi(a0[e]) * bfhi(a0[e]); nb += bflo(b0[e]) * bflo(b0[e]) + bfhi(b0[e]) * bfhi(b0[e]); }
#pragma unroll
                for (int e = 0; e < 2; ++e) { na += bflo(q1a[e]) * bflo(q1a[e]) + bfhi(q1a[e]) * bfhi(q1a[e]); nb += bflo(q1b[e]) * bflo(q1b[e]) + bfhi(q1b[e]) * bfhi(q1b[e]); }
                na += __shfl_xor(na, 32); nb += __shfl_xor(nb, 32);
            }
            if (!early) {
                gu64* f = (gu64*)(ws + WS_KF + xc * 1024) + lane;
                _Pragma("clang loop unroll(disable)") for (unsigned spins = 0; spins < (1u << 14); ++spins) {
                    fk = __hip_atomic_load(f, RLX_AGENT); fv = __hip_atomic_load(f + 64, RLX_AGENT);
                    if (__all((unsigned)(fk >> 32) == tag && (unsigned)(fv >> 32) == tag)) break;
                    TMO(spins, 1u << 14, 301, tag);
                    __builtin_amdgcn_s_sleep(4);
                }
                if constexpr (PRO) lwait_all(C_AP, 4, 6, lane);
                att_dma(as, 0); att_dma(as, 1);
            }
            STAMP(4);
            if constexpr (PRO) lwait_all(C_AP, 4, 8, lane);
            att_dma(as, 2);
            const float kn2 = wave_max(__uint_as_float((unsigned)fk)), vmx = wave_max(__uint_as_float((unsigned)fv));
            const float Ma = __builtin_sqrtf(na * kn2) * 1.01f, Mb = __builtin_sqrtf(nb * kn2) * 1.01f;
            const bool fast = __all(Ma <= BOUND_MAX && Mb <= BOUND_MAX && vmx <= 1e30f);
            const unsigned sa = (fast && h) ? pk2(-Ma, 0.f) : q1a[0], sb = (fast && h) ? pk2(-Mb, 0.f) : q1b[0];
            const u32x4 a0 = {sa, q1a[1], 0u, 0u}, a1 = {h ? sa : 0u, 0u, q1a[0], q1a[1]}, b0 = {sb, q1b[1], 0u, 0u}, b1 = {h ? sb : 0u, 0u, q1b[0], q1b[1]};
            f32x16 Oa = {}, Ob = {};
            WAITV(6); lpost(C_AP + w, AP0); lwait_all(C_AP, 4, AP0, lane);
            __builtin_amdgcn_s_setprio(ATT_PRIO);
            STAMP(5); STAMPC(12);
            if (fast) att_fast(as, Q0a, BC(a0), BC(a1), Q0b, BC(b0), BC(b1), Oa, Ob);
            else att_safe(as, Q0a, BC(a0), BC(a1), Q0b, BC(b0), BC(b1), Oa, Ob);
            STAMP(6); STAMPC(13);
            const __amdgpu_buffer_rsrc_t ro = mkrsrc(ws + WS_OT, 64 * 40 * 1024);
#pragma unroll
            for (int q = 0; q < 2; ++q) {
                const f32x16& O = q ? Ob : Oa;
                const float inv = 1.f / __shfl(O[8], r + 32);
                const unsigned base = ((2 * ti + q) * 40 + 5 * g) * 1024;
                __builtin_amdgcn_raw_buffer_store_b128(__builtin_bit_cast(u32x4, pack8(O, 0, inv)), ro, base + w * 1024 + lane * 16, 0, 16);
                if (!h) {
                    u32x2 t = {pk2(O[8] * inv, O[9] * inv), pk2(O[10] * inv, O[11] * inv)};
                    __builtin_amdgcn_raw_buffer_store_b64(t, ro, base + 4096 + ((w >> 1) * 32 + r) * 16 + (w & 1) * 8, 0, 16);
                }
            }
            if constexpr (WO) {
                WAITV(0);
                if (lane < 2) __hip_atomic_store((gu32*)(ws + WS_OF) + (2 * ti + lane) * 32 + head, tag, RLX_AGENT);
            }
            STAMP(7);
        }
    }
}

template <int PH>
static void launch(const Params& p, hipStream_t stream) {
    (void)hipFuncSetAttribute((const void*)mega<PH>, hipFuncAttributeMaxDynamicSharedMemorySize, L_TOTAL);
    mega<PH><<<256, 512, L_TOTAL, stream>>>(p);
}

extern "C" void kernel_launch(void* const* d_in, const int* in_sizes, int n_in, void* d_out, int out_size, void* d_ws, size_t ws_size, hipStream_t stream) {
    Params p;
    p.x = (const char*)d_in[0]; p.wq = (const char*)d_in[1]; p.wk = (const char*)d_in[2]; p.wv = (const char*)d_in[3]; p.wo = (const char*)d_in[4];
    p.w1 = (const char*)d_in[6]; p.w3 = (const char*)d_in[7]; p.w2 = (const char*)d_in[8];
    p.out = (char*)d_out; p.ws = (char*)d_ws;
    launch<27>(p, stream);
}
```

```cpp
#include <hip/hip_runtime.h>
#include <hip/hip_bf16.h>
#include <stdint.h>

#define AS1 __attribute__((address_space(1)))
#define AS3 __attribute__((address_space(3)))
typedef __bf16 bf16x2 __attribute__((ext_vector_type(2)));
typedef __bf16 bf16x8 __attribute__((ext_vector_type(8)));
typedef short s16x4 __attribute__((ext_vector_type(4)));
typedef short s16x8 __attribute__((ext_vector_type(8)));
typedef float f32x2 __attribute__((ext_vector_type(2)));
typedef float f32x4 __attribute__((ext_vector_type(4)));
typedef float f32x16 __attribute__((ext_vector_type(16)));
typedef unsigned u32x2 __attribute__((ext_vector_type(2)));
typedef unsigned u32x4 __attribute__((ext_vector_type(4)));
typedef AS1 unsigned gu32;
typedef AS1 unsigned long long gu64;

#define ATT_PRIO 2
#define GEM_PRIO 0
#define GU_HI 0
#define DN_HI 0
#define MPRIO(mask, i) __builtin_amdgcn_s_setprio(((mask) >> (i)) & 1 ? 3 : 0)
#define GU_DELAY 8
#define GU_ROT 4
#define GU_GQ 5
#define OF_SLEEP 3
#define HF_SLEEP 20
#define DN_DQ 8
#define SPLIT_MODE 0

constexpr int SEQ = 2048, DM = 640, FF = 1536;
constexpr unsigned WS_KF = 0;
constexpr unsigned WS_HF = 16384;
constexpr unsigned WS_OF = 32768;
constexpr unsigned WS_EP = 49152;
constexpr unsigned KVD = 5376;
constexpr unsigned KV_K1 = 2048, KV_V = 2560, KV_VT = 1408;
constexpr unsigned WS_KV = 65536;
constexpr unsigned WS_HT = WS_KV + 176128;
constexpr unsigned WS_OT = WS_HT + 64 * 96 * 1024;
constexpr unsigned WS_QT = WS_OT + 64 * 40 * 1024;
constexpr unsigned WS_PT = WS_QT + 64 * 32 * 2048;
constexpr unsigned WS_ST = WS_PT + 256 * 3 * 32 * 256;
constexpr unsigned L_PANEL = 0;
constexpr unsigned L_KV = 81920;
constexpr unsigned L_WQ = 81920;
constexpr unsigned L_GU = 124928;
constexpr unsigned GSL = 12320;
constexpr unsigned L_WKV = 124928;
constexpr unsigned L_MISC = 161920;
constexpr unsigned L_TOTAL = 163840;
constexpr unsigned CHUNK = 2 * KVD;
constexpr int NCHUNK = 16;
enum { C_AP = 0  , C_GP = 4  , C_DP = 8  , C_PP = 12  , C_WP = 16  , C_KVP = 20, C_KVFIN, C_TAG, C_OS = 24  , C_N = 28 };

struct Params { const char* x; const char* wq; const char* wk; const char* wv; const char* wo; const char* w1; const char* w3; const char* w2; char* out; char* ws; };

#define SBAR() __builtin_amdgcn_sched_barrier(0)
#define PIN(x) asm volatile("" : "+v"(x))
#define CFENCE() asm volatile("" ::: "memory")
#define WAITV(n) asm volatile("s_waitcnt vmcnt(" #n ")" ::: "memory")
#define MFMA(a, b, c) __builtin_amdgcn_mfma_f32_32x32x16_bf16((a), (b), (c), 0, 0, 0)
#define RLX_AGENT __ATOMIC_RELAXED, __HIP_MEMORY_SCOPE_AGENT
#define RLX_WG __ATOMIC_RELAXED, __HIP_MEMORY_SCOPE_WORKGROUP

#define TMO(spins, lim, code, tgt) do {} while (0)
#define STAMP(id) do {} while (0)
#define STAMPC(id) do {} while (0)

#define LP(T, off) ((AS3 T*)(uintptr_t)(unsigned)(off))
__device__ __forceinline__ bf16x8 ldsf(unsigned off) { return *LP(const bf16x8, off); }
__device__ __forceinline__ s16x4 ldstr(unsigned off) { return __builtin_amdgcn_ds_read_tr16_b64_v4i16(LP(s16x4, off)); }
__device__ __forceinline__ bf16x8 ldstr2(unsigned off0, unsigned off1) {
    s16x4 a = ldstr(off0), b = ldstr(off1);
    return __builtin_bit_cast(bf16x8, __builtin_shufflevector(a, b, 0, 1, 2, 3, 4, 5, 6, 7));
}
__device__ __forceinline__ unsigned pk2(float lo, float hi) { bf16x2 v = {(__bf16)lo, (__bf16)hi}; return __builtin_bit_cast(unsigned, v); }
__device__ __forceinline__ float bflo(unsigned u) { return __uint_as_float(u << 16); }
__device__ __forceinline__ float bfhi(unsigned u) { return __uint_as_float(u & 0xffff0000u); }
__device__ __forceinline__ float rbf(float f) { return (float)(__bf16)f; }

__device__ __forceinline__ void glds16(unsigned voff, const char* sbase, unsigned ldsdst) {
    unsigned keep;
    asm volatile("s_mov_b32 %0, m0\n\ts_mov_b32 m0, %3\n\ts_nop 2\n\tglobal_load_lds_dwordx4 %1, %2\n\ts_mov_b32 m0, %0"
                 : "=&s"(keep) : "v"(voff), "s"(sbase), "s"(ldsdst) : "memory");
}
__device__ __forceinline__ void glds16_sc1(unsigned voff, const char* sbase, unsigned ldsdst) {
    unsigned keep;
    asm volatile("s_mov_b32 %0, m0\n\ts_mov_b32 m0, %3\n\ts_nop 2\n\tglobal_load_lds_dwordx4 %1, %2 sc1\n\ts_mov_b32 m0, %0"
                 : "=&s"(keep) : "v"(voff), "s"(sbase), "s"(ldsdst) : "memory");
}
__device__ __forceinline__ void glds16x3_sc1(unsigned voff, const char* sbase, unsigned ldsdst) {
    unsigned keep;
    asm volatile("s_mov_b32 %0, m0\n\ts_mov_b32 m0, %3\n\ts_nop 2\n\tglobal_load_lds_dwordx4 %1, %2 sc1\n\tglobal_load_lds_dwordx4 %1, %2 offset:1024 sc1\n\tglobal_load_lds_dwordx4 %1, %2 offset:2048 sc1\n\ts_mov_b32 m0, %0"
                 : "=&s"(keep) : "v"(voff), "s"(sbase), "s"(ldsdst) : "memory");
}
__device__ __forceinline__ void glds16x3v(unsigned v0, unsigned v1, unsigned v2, const char* sbase, unsigned ldsdst) {
    unsigned keep;
    asm volatile("s_mov_b32 %0, m0\n\ts_mov_b32 m0, %5\n\ts_nop 0\n\tglobal_load_lds_dwordx4 %1, %4\n\tglobal_load_lds_dwordx4 %2, %4 offset:1024\n\tglobal_load_lds_dwordx4 %3, %4 offset:2048\n\ts_mov_b32 m0, %0"
                 : "=&s"(keep) : "v"(v0), "v"(v1), "v"(v2), "s"(sbase), "s"(ldsdst) : "memory");
}
__device__ __forceinline__ void glds_gu(unsigned va, unsigned vb, unsigned vc, const char* s1, const char* s3, unsigned ldsdst) {
    unsigned keep;
    asm volatile("s_mov_b32 %0, m0\n\ts_mov_b32 m0, %8\n\ts_nop 0\n\tglobal_load_lds_dwordx4 %1, %4\n\tglobal_load_lds_dwordx4 %2, %5 offset:1024\n\t"
                 "s_mov_b32 exec_hi, 0\n\tglobal_load_lds_dwordx4 %3, %6 offset:2048\n\ts_not_b64 exec, exec\n\tglobal_load_lds_dwordx4 %3, %7 offset:2048\n\t"
                 "s_mov_b64 exec, -1\n\ts_mov_b32 m0, %0"
                 : "=&s"(keep) : "v"(va), "v"(vb), "v"(vc), "s"(s1), "s"(s3 - 1024), "s"(s1 - 2048), "s"(s3 - 2048), "s"(ldsdst) : "memory", "scc");
}
__device__ __forceinline__ void l2touch(const char* ga) {
    unsigned keep;
    asm volatile("s_mov_b32 %0, m0\n\ts_mov_b32 m0, %2\n\ts_nop 0\n\tglobal_load_lds_dword %1, off\n\ts_mov_b32 m0, %0" : "=&s"(keep) : "v"(ga), "s"(L_MISC + 1024) : "memory");
}
#define GLOAD16(dst, voff, sbase, imm) asm volatile("s_nop 4\n\tglobal_load_dwordx4 %0, %1, %2 offset:%3" : "=v"(dst) : "v"(voff), "s"(sbase), "i"(imm) : "memory")

__device__ __forceinline__ AS3 unsigned* lcnt(int c) { return LP(unsigned, L_MISC) + c; }
__device__ __forceinline__ void larrive(int c, int lane) { CFENCE(); if (lane == 0) __hip_atomic_fetch_add(lcnt(c), 1u, RLX_WG); CFENCE(); }
__device__ __forceinline__ void lset(int c, unsigned v, int lane) { CFENCE(); if (lane == 0) __hip_atomic_store(lcnt(c), v, RLX_WG); CFENCE(); }
__device__ __forceinline__ void lwait(int c, unsigned target) {
    CFENCE();
    _Pragma("clang loop unroll(disable)") for (unsigned spins = 0; spins < (1u << 22); ++spins) {
        if ((unsigned)__builtin_amdgcn_readfirstlane(__hip_atomic_load(lcnt(c), RLX_WG)) >= target) break;
        TMO(spins, 1u << 16, 100 + c, target);
        __builtin_amdgcn_s_sleep(1);
    }
    CFENCE();
}
__device__ __forceinline__ void lwait_all(int c, int n, unsigned target, int lane) {
    CFENCE();
    AS3 unsigned* q = lcnt(c) + (lane < n ? lane : 0);
    _Pragma("clang loop unroll(disable)") for (unsigned spins = 0; spins < (1u << 16); ++spins) {
        if (__all(__hip_atomic_load(q, RLX_WG) >= target)) break;
        TMO(spins, 1u << 16, 200 + c, target);
        __builtin_amdgcn_s_sleep(1);
    }
    CFENCE();
}
__device__ __forceinline__ void lpost(int c, unsigned v) { CFENCE(); *(volatile AS3 unsigned*)lcnt(c) = v; CFENCE(); }
__device__ __forceinline__ void linc(unsigned vaddr, unsigned vone) {
    asm volatile("s_mov_b64 exec, 1\n\tds_add_u32 %0, %1\n\ts_mov_b64 exec, -1" :: "v"(vaddr), "v"(vone) : "memory");
}
__device__ __forceinline__ unsigned lpeek(int c, int lane) { return *((volatile AS3 unsigned*)lcnt(c) + (lane & 3)); }
__device__ __forceinline__ void lcheck(unsigned pk, int c, unsigned target, int lane) {
    if (__builtin_expect(__any(pk < target), 0)) lwait_all(c, 4, target, lane);
    CFENCE();
}
__device__ __forceinline__ void gwait(const char* ws, unsigned off, int n, unsigned tag, int lane) {
    gu32* f = (gu32*)(ws + off) + (lane < n ? lane : 0);
    _Pragma("clang loop unroll(disable)") for (unsigned spins = 0; spins < (1u << 14); ++spins) {
        unsigned v = __hip_atomic_load(f, RLX_AGENT);
        if (__all(v == tag)) break;
        TMO(spins, 1u << 14, 300, off);
        __builtin_amdgcn_s_sleep(6);
    }
    CFENCE();
}
__device__ __forceinline__ float gwait64(const char* ws, unsigned off, unsigned tag, int lane) {
    gu64* f = (gu64*)(ws + off) + lane;
    unsigned long long v = 0;
    _Pragma("clang loop unroll(disable)") for (unsigned spins = 0; spins < (1u << 14); ++spins) {
        v = __hip_atomic_load(f, RLX_AGENT);
        if (__all((unsigned)(v >> 32) == tag)) break;
        __builtin_amdgcn_s_sleep(6);
    }
    CFENCE();
    return __uint_as_float((unsigned)v);
}
__device__ __forceinline__ __amdgpu_buffer_rsrc_t mkrsrc(const char* base, unsigned bytes) {
    return __builtin_amdgcn_make_buffer_rsrc((void*)base, 0, bytes, 0x00020000);
}
__device__ __forceinline__ float dpp_sum16(float v) {
    v += __int_as_float(__builtin_amdgcn_update_dpp(0, __float_as_int(v), 0x128, 0xf, 0xf, false));
    v += __int_as_float(__builtin_amdgcn_update_dpp(0, __float_as_int(v), 0x124, 0xf, 0xf, false));
    v += __int_as_float(__builtin_amdgcn_update_dpp(0, __float_as_int(v), 0x122, 0xf, 0xf, false));
    v += __int_as_float(__builtin_amdgcn_update_dpp(0, __float_as_int(v), 0x121, 0xf, 0xf, false));
    return v;
}
__device__ __forceinline__ float wave_max(float v) {
    int x = __float_as_int(v);
    x = max(x, __builtin_amdgcn_update_dpp(0, x, 0x128, 0xf, 0xf, false)); x = max(x, __builtin_amdgcn_update_dpp(0, x, 0x124, 0xf, 0xf, false));
    x = max(x, __builtin_amdgcn_update_dpp(0, x, 0x122, 0xf, 0xf, false)); x = max(x, __builtin_amdgcn_update_dpp(0, x, 0x121, 0xf, 0xf, false));
    const int a = max(__builtin_amdgcn_readlane(x, 0), __builtin_amdgcn_readlane(x, 16)), b = max(__builtin_amdgcn_readlane(x, 32), __builtin_amdgcn_readlane(x, 48));
    return __int_as_float(max(a, b));
}
__device__ __forceinline__ bf16x8 pack8(const f32x16& a, int b, float sc) {
    u32x4 r = {pk2(a[b] * sc, a[b + 1] * sc), pk2(a[b + 2] * sc, a[b + 3] * sc), pk2(a[b + 4] * sc, a[b + 5] * sc), pk2(a[b + 6] * sc, a[b + 7] * sc)};
    return __builtin_bit_cast(bf16x8, r);
}

template <int I> struct IC { static constexpr int value = I; };
template <int B, int E, class F> __device__ __forceinline__ void static_for(F&& f) { if constexpr (B < E) { f(IC<B>{}); static_for<B + 1, E>(f); } }
#define WAITVN(n) asm volatile("s_waitcnt vmcnt(%0)" :: "i"(n) : "memory")
__device__ __forceinline__ void GLOAD16S(u32x4& dst, unsigned voff, const char* sbase) { asm volatile("global_load_dwordx4 %0, %1, %2 sc1" : "=v"(dst) : "v"(voff), "s"(sbase) : "memory"); }
__device__ __forceinline__ void GLOAD16N(u32x4& dst, unsigned voff, const char* sbase) { asm volatile("global_load_dwordx4 %0, %1, %2" : "=v"(dst) : "v"(voff), "s"(sbase) : "memory"); }
__device__ __forceinline__ void GLOAD16N_IF(u32x4& dst, unsigned voff, const char* sbase, int cond) {
    asm volatile("s_cmp_eq_u32 %3, 0\n\ts_cbranch_scc1 1f\n\tglobal_load_dwordx4 %0, %1, %2\n1:" : "=v"(dst) : "v"(voff), "s"(sbase), "s"(cond) : "memory", "scc"); }
template <int N> __device__ __forceinline__ void waitv5(u32x4& a, u32x4& b, u32x4& c, u32x4& d, u32x4& e) { asm volatile("s_waitcnt vmcnt(%5)" : "+v"(a), "+v"(b), "+v"(c), "+v"(d), "+v"(e) : "i"(N) : "memory"); }
__device__ __forceinline__ void gload8x2_sc1(unsigned long long& a, unsigned long long& b, unsigned voff, const char* sbase) {
    asm volatile("global_load_dwordx2 %0, %2, %3 sc1\n\tglobal_load_dwordx2 %1, %2, %3 offset:512 sc1" : "=&v"(a), "=&v"(b) : "v"(voff), "s"(sbase) : "memory"); }
__device__ __forceinline__ void waitv8x2(unsigned long long& a, unsigned long long& b) { asm volatile("s_waitcnt vmcnt(0)" : "+v"(a), "+v"(b) :: "memory"); }
__device__ __forceinline__ void PIN2(u32x4& a, u32x4& b) { asm volatile("" : "+v"(a), "+v"(b) :: "memory"); }
__device__ __forceinline__ u32x4 scale8(unsigned x0, unsigned x1, unsigned x2, unsigned x3, unsigned clo, unsigned chi, float c) {
    unsigned o0, o1, o2, o3; float t0, t1, t2, t3, t4, t5, t6, t7;
    asm("v_dot2_f32_bf16 %4, %12, %16, %18\n\tv_dot2_f32_bf16 %5, %12, %17, %18\n\tv_dot2_f32_bf16 %6, %13, %16, %18\n\tv_dot2_f32_bf16 %7, %13, %17, %18\n\t"
        "v_dot2_f32_bf16 %8, %14, %16, %18\n\tv_dot2_f32_bf16 %9, %14, %17, %18\n\tv_dot2_f32_bf16 %10, %15, %16, %18\n\tv_dot2_f32_bf16 %11, %15, %17, %18\n\t"
        "v_cvt_pk_bf16_f32 %0, %4, %5\n\tv_cvt_pk_bf16_f32 %1, %6, %7\n\tv_cvt_pk_bf16_f32 %2, %8, %9\n\tv_cvt_pk_bf16_f32 %3, %10, %11"
        : "=&v"(o0), "=&v"(o1), "=&v"(o2), "=&v"(o3), "=&v"(t0), "=&v"(t1), "=&v"(t2), "=&v"(t3), "=&v"(t4), "=&v"(t5), "=&v"(t6), "=&v"(t7)
        : "v"(x0), "v"(x1), "v"(x2), "v"(x3), "v"(clo), "v"(chi), "v"(c));
    return u32x4{o0, o1, o2, o3};
}
template <int N> __device__ __forceinline__ void waitv3(u32x4& a, u32x4& b, u32x4& c) { asm volatile("s_waitcnt vmcnt(%3)" : "+v"(a), "+v"(b), "+v"(c) : "i"(N) : "memory"); }

constexpr int dn_younger(int k, int D, bool wo) {
    int n = 0, la = 0, lb = 0;
    for (int i = 0; i < D; ++i) { n += 2; if (i == k + 1) lb = n; }
    for (int i = 0; i < D; ++i) { n += 3; if (i == k) la = n; }
    for (int s = 0; s < k; ++s) {
        if (s + D < 24) { n += 2; if (s + D == k + 1) lb = n; }
        if (wo && s >= 16) n += 2;
        if (s + D < 24) { n += 3; if (s + D == k) la = n; }
    }
    return n - (la > lb ? la : lb);
}
constexpr float QSCALE = 0.22360679774997896f * 1.4426950408889634f;
constexpr float BOUND_MAX = 50.f;

struct AttSync { int lane; const char* kv; unsigned dvoff; unsigned wo; int w; };
__device__ __forceinline__ unsigned kvslot(int c) { return L_KV + ((c + 2) & 3) * CHUNK; }
__device__ __forceinline__ void att_dma(const AttSync& a, int c) { glds16x3_sc1(a.dvoff, a.kv + (size_t)c * CHUNK, kvslot(c) + a.wo); }
constexpr unsigned AP0 = 16;
#define RING_X(c) do { WAITV(3); lpost(C_AP + as.w, AP0 + (c) + 1); } while (0)
#define RP_A
#define RP_B
#define RING_Y(c, pk) do { RP_A lcheck(pk, C_AP, AP0 + (c) + 1, lane); const int cn_ = (c) + 3 < NCHUNK ? (c) + 3 : NCHUNK - 1;   \
                           glds16x3_sc1(as.dvoff, as.kv + (size_t)cn_ * CHUNK, kvslot((c) + 3) + as.wo); RP_B } while (0)

__device__ __forceinline__ void att_fast(const AttSync& as, bf16x8 Q0a, bf16x8 Q1a0, bf16x8 Q1a1, bf16x8 Q0b, bf16x8 Q1b0, bf16x8 Q1b1, f32x16& Oa, f32x16& Ob) {
    const int lane = as.lane, r = lane & 31, h = lane >> 5;
    const unsigned ak0 = lane * 16, ak1 = h ? KV_V + 20 * 16 : KV_K1 + r * 16;
    const unsigned av = KV_V + h * 352 + (r <= 20 ? r : 21) * 16;
    const f32x16 zero = {};
    f32x16 Sa, Sb = {};
    bf16x8 K0[2], K1[2], V0[2], V1[2];
    u32x4 Pa0, Pa1, Pb0 = {}, Pb1 = {};
    unsigned pk = 0;
    unsigned long long rp0 = 0, rp1 = 0;
    V0[1] = bf16x8{}; V1[1] = bf16x8{};
    unsigned vk0 = L_KV + ak0, vk1 = L_KV + ak1, vv = L_KV + av;
    PIN(vk0); PIN(vk1); PIN(vv);
    K0[0] = ldsf(vk0 + 2 * CHUNK); K1[0] = ldsf(vk1 + 2 * CHUNK); K0[1] = ldsf(vk0 + 2 * CHUNK + 1024); V1[0] = ldsf(vv + 2 * CHUNK + 704); V0[0] = ldsf(vv + 2 * CHUNK);
    Sa = MFMA(K0[0], Q0a, zero); Sa = MFMA(K1[0], Q1a0, Sa);
#define EX4(S, i) do { S[i] = __builtin_amdgcn_exp2f(S[i]); S[i + 1] = __builtin_amdgcn_exp2f(S[i + 1]); S[i + 2] = __builtin_amdgcn_exp2f(S[i + 2]); S[i + 3] = __builtin_amdgcn_exp2f(S[i + 3]); } while (0)
#define GAP(MF, LD, T, k, PW, j, S, i) do { MF; SBAR(); LD; PW[j] = pk2(T[k], T[k + 1]); PW[j + 1] = pk2(T[k + 2], T[k + 3]); PIN(PW); SBAR(); EX4(S, i); PIN(S); SBAR(); } while (0)
#define BC(x) __builtin_bit_cast(bf16x8, x)
#define STEP(e, d, QB1, QA1N, KN, L3, LK0, LK1, LV) do { \
        GAP(Ob = MFMA(V0[e ^ 1], BC(Pb0), Ob), (void)0,                  Sb, 12, Pb1, 2, Sa, 0); \
        GAP(Sb = MFMA(K0[e], Q0b, zero),       (void)0,                  Sa, 0, Pa0, 0, Sa, 4); \
        GAP(Sb = MFMA(K1[d], QB1, Sb),         L3,                       Sa, 4, Pa0, 2, Sa, 8); \
        GAP(Ob = MFMA(V1[e ^ 1], BC(Pb1), Ob), K0[e] = ldsf(LK0),        Sa, 8, Pa1, 0, Sa, 12); \
        GAP(Oa = MFMA(V0[e], BC(Pa0), Oa),     LK1,                      Sa, 12, Pa1, 2, Sb, 0); \
        GAP(Sa = MFMA(K0[e ^ 1], Q0a, zero),   V1[e ^ 1] = ldsf((LV) + 704), Sb, 0, Pb0, 0, Sb, 4); \
        GAP(Sa = MFMA(K1[KN], QA1N, Sa),       V0[e ^ 1] = ldsf(LV),     Sb, 4, Pb0, 2, Sb, 8); \
        GAP(Oa = MFMA(V1[e], BC(Pa1), Oa),     (void)0,                  Sb, 8, Pb1, 0, Sb, 12); \
    } while (0)
#define CHUNK4(c, B, N) do { \
        STEP(0, 0, Q1b0, Q1a1, 0, (void)0, vk0 + (B) + KVD, K1[1] = ldsf(vk1 + (B) + KVD), vv + (B) + KV_VT); \
        RING_X(c); \
        STEP(1, 0, Q1b1, Q1a0, 1, pk = lpeek(C_AP, lane), vk0 + (B) + KVD + 1024, (void)0, vv + (B) + KVD); \
        RING_Y(c, pk); \
        STEP(0, 1, Q1b0, Q1a1, 1, (void)0, vk0 + (N), K1[0] = ldsf(vk1 + (N)), vv + (B) + KVD + KV_VT); \
        STEP(1, 1, Q1b1, Q1a0, 0, (void)0, vk0 + (N) + 1024, (void)0, vv + (N)); \
    } while (0)
    for (int c = 0; c < NCHUNK; c += 4) {
        CHUNK4(c, 2 * CHUNK, 3 * CHUNK); CHUNK4(c + 1, 3 * CHUNK, 0); CHUNK4(c + 2, 0, CHUNK); CHUNK4(c + 3, CHUNK, 2 * CHUNK);
    }
#undef CHUNK4
    Pb1[2] = pk2(Sb[12], Sb[13]); Pb1[3] = pk2(Sb[14], Sb[15]);
    Ob = MFMA(V0[1], BC(Pb0), Ob); Ob = MFMA(V1[1], BC(Pb1), Ob);
#undef STEP
#undef GAP
#undef EX4
}

__device__ __forceinline__ void att_safe(const AttSync& as, bf16x8 Q0a, bf16x8 Q1a0, bf16x8 Q1a1, bf16x8 Q0b, bf16x8 Q1b0, bf16x8 Q1b1, f32x16& Oa, f32x16& Ob) {
    const int lane = as.lane, r = lane & 31, h = lane >> 5;
    const unsigned ak0 = lane * 16, ak1 = h ? KV_V + 20 * 16 : KV_K1 + r * 16;
    const unsigned av = KV_V + h * 352 + (r <= 20 ? r : 21) * 16;
    const f32x16 zero = {};
    float ma = -INFINITY, mb = -INFINITY;
    unsigned long long rp0 = 0, rp1 = 0;
    for (int c = 0; c < NCHUNK; ++c) {
        const unsigned b0 = kvslot(c);
        for (int t = 0; t < 4; ++t) {
            if (t == 1) RING_X(c);
            if (t == 2) RING_Y(c, 0u);
            const unsigned base = b0 + (t >> 1) * KVD; const int e = t & 1;
            bf16x8 K0 = ldsf(base + ak0 + e * 1024), K1 = ldsf(base + ak1), V0 = ldsf(base + av + e * KV_VT), V1 = ldsf(base + av + e * KV_VT + 704);
#pragma unroll
            for (int q = 0; q < 2; ++q) {
                f32x16 S = MFMA(K0, q ? Q0b : Q0a, zero); S = MFMA(K1, q ? (e ? Q1b1 : Q1b0) : (e ? Q1a1 : Q1a0), S);
                float mx = S[0];
#pragma unroll
                for (int i = 1; i < 16; ++i) mx = fmaxf(mx, S[i]);
                mx = fmaxf(mx, __shfl_xor(mx, 32));
                float& m = q ? mb : ma; f32x16& O = q ? Ob : Oa;
                const float mn = fmaxf(m, mx);
                const float al = __builtin_amdgcn_exp2f(m - mn);
                m = mn;
#pragma unroll
                for (int i = 0; i < 16; ++i) { O[i] *= al; S[i] = __builtin_amdgcn_exp2f(S[i] - mn); }
                O = MFMA(V0, pack8(S, 0, 1.f), O); O = MFMA(V1, pack8(S, 8, 1.f), O);
            }
        }
    }
}

__device__ __forceinline__ void xload8(u32x4 (&x)[5], u32x4 (&y)[5], const char* xb, unsigned vo, unsigned vo2, int skip) {
    asm volatile("s_cmp_lg_u32 %13, 0\n\ts_cbranch_scc1 1f\n\t"
                 "global_load_dwordx4 %0, %10, %12\n\tglobal_load_dwordx4 %1, %10, %12 offset:256\n\tglobal_load_dwordx4 %2, %10, %12 offset:512\n\t"
                 "global_load_dwordx4 %3, %10, %12 offset:768\n\tglobal_load_dwordx4 %4, %10, %12 offset:1024\n\t"
                 "global_load_dwordx4 %5, %11, %12\n\tglobal_load_dwordx4 %6, %11, %12 offset:256\n\tglobal_load_dwordx4 %7, %11, %12 offset:512\n\t"
                 "global_load_dwordx4 %8, %11, %12 offset:768\n\tglobal_load_dwordx4 %9, %11, %12 offset:1024\n1:"
                 : "=&v"(x[0]), "=&v"(x[1]), "=&v"(x[2]), "=&v"(x[3]), "=&v"(x[4]), "=&v"(y[0]), "=&v"(y[1]), "=&v"(y[2]), "=&v"(y[3]), "=&v"(y[4])
                 : "v"(vo), "v"(vo2), "s"(xb), "s"(skip) : "memory", "scc");
}
__device__ __forceinline__ void xload4(u32x4 (&x)[5], const char* xb, unsigned vo) {
    asm volatile("global_load_dwordx4 %0, %5, %6\n\tglobal_load_dwordx4 %1, %5, %6 offset:256\n\tglobal_load_dwordx4 %2, %5, %6 offset:512\n\t"
                 "global_load_dwordx4 %3, %5, %6 offset:768\n\tglobal_load_dwordx4 %4, %5, %6 offset:1024"
                 : "=&v"(x[0]), "=&v"(x[1]), "=&v"(x[2]), "=&v"(x[3]), "=&v"(x[4]) : "v"(vo), "s"(xb) : "memory");
}
__device__ __forceinline__ void ln_rows(const u32x4 (&xr)[5], int row0, int lane) {
    f32x2 xv[20];
    f32x2 s0 = {0.f, 0.f}, s1 = {0.f, 0.f}, q0 = {0.f, 0.f}, q1 = {0.f, 0.f};
#pragma unroll
    for (int i = 0; i < 20; i += 2) {
        const unsigned a = xr[i >> 2][i & 3], b = xr[i >> 2][(i & 3) + 1];
        xv[i] = f32x2{bflo(a), bfhi(a)}; xv[i + 1] = f32x2{bflo(b), bfhi(b)};
        s0 += xv[i]; q0 = __builtin_elementwise_fma(xv[i], xv[i], q0);
        s1 += xv[i + 1]; q1 = __builtin_elementwise_fma(xv[i + 1], xv[i + 1], q1);
    }
    s0 += s1; q0 += q1;
    const float s = dpp_sum16(s0[0] + s0[1]), ss = dpp_sum16(q0[0] + q0[1]);
    const float mean = s * (1.f / 640.f);
    const float var = fmaxf(ss * (1.f / 640.f) - mean * mean, 0.f);
    const float rs = __builtin_amdgcn_rcpf(rbf(__builtin_amdgcn_sqrtf(rbf(rbf(var) + 1e-5f)))), mo = -rbf(mean) * rs;
    const f32x2 rs2 = {rs, rs}, mo2 = {mo, mo};
    const int row = row0 + (lane >> 4), c = lane & 15;
    const unsigned wa = L_PANEL + (row >> 5) * 40960 + (c >> 1) * 1024 + (c & 1) * 512 + (((row & 31) ^ (c & 7)) << 4);
#pragma unroll
    for (int u = 0; u < 5; ++u) {
        u32x4 o;
#pragma unroll
        for (int e = 0; e < 4; ++e) { const f32x2 y = __builtin_elementwise_fma(xv[4 * u + e], rs2, mo2); o[e] = pk2(y[0], y[1]); }
        *LP(u32x4, wa + u * 8192) = o;
    }
}
#define BLOCK_BARRIER() asm volatile("s_waitcnt lgkmcnt(0)\n\ts_barrier" ::: "memory")

template <int PH>
__global__ void __launch_bounds__(512, 2) mega(Params p) {
    constexpr bool PRO = PH & 1, ATT = PH & 2, DN = PH & 8, WO = PH & 16;
    const int w = __builtin_amdgcn_readfirstlane(threadIdx.x >> 6);
    const int b = blockIdx.x, xc = b & 7, j = b >> 3;
    const int ti = (xc >> 1) * 8 + (j >> 2), g = (xc & 1) * 4 + (j & 3);
    constexpr bool FUSED = PH == 27;
    const int kmt = g & 1;
    const int ra = kmt * 32 + (w & 3) * 8, rb = (kmt ^ 1) * 32 + (w & 3) * 8;
    u32x4 x0[5], x1[5], x2[5], x3[5];
    if constexpr (PRO) {
        asm volatile("" :: "s"(p.x), "s"(p.ws), "s"(p.wq), "s"(p.wk));
        const char* xb = p.x + (size_t)ti * 64 * 1280;
        const unsigned xvo = ((threadIdx.x >> 4) & 3) * 1280 + (threadIdx.x & 15) * 16;
        xload8(x0, x1, xb + (size_t)ra * 1280, xvo, xvo + 5120, w >> 2);
        xload8(x2, x3, xb + (size_t)rb * 1280, xvo, xvo + 5120, w >> 2);
    }
    const char* pc; asm volatile("s_getpc_b64 %0" : "=s"(pc));
    unsigned tag = 1u;
    char* ws = p.ws;
    STAMP(0);
    if (threadIdx.x < C_N) *lcnt(threadIdx.x) = (threadIdx.x == C_WP || threadIdx.x == C_WP + 1 || threadIdx.x == C_PP || threadIdx.x == C_PP + 1) ? 100u : 0u;
    const bool haskv = (g >> 2) == (ti & 1);
    const char* wqb = p.wq + g * 160;
#define LANE_CONSTS() int lane = __builtin_amdgcn_mbcnt_hi(~0u, __builtin_amdgcn_mbcnt_lo(~0u, 0u)); PIN(lane); \
    const int r = lane & 31, h = lane >> 5, tq = (lane & 15) >> 2, tp = lane & 3, tb = (lane >> 4) & 1; (void)r; (void)h; (void)tq; (void)tp; (void)tb

    if (w >= 4) {
        LANE_CONSTS();
        const int v = w - 4;
        unsigned epv = 0u;
        if constexpr (FUSED) {
            asm volatile("global_load_dword %0, %1, %2 sc1" : "=v"(epv) : "v"(0u), "s"(p.ws + WS_EP) : "memory");
            constexpr unsigned CODE_PF = 65536;
            const unsigned line = (unsigned)(j * 4 + v) * 64u + lane;
            if (line * 128u < CODE_PF) l2touch(pc + line * 128u);
        }
#define GET_TAG() do { if constexpr (FUSED) { asm volatile("s_nop 0\n\tv_readfirstlane_b32 %0, %1\n\ts_nop 3" : "=s"(tag) : "v"(epv) : "memory"); tag += 0x9E3779B1u; } } while (0)
        __builtin_amdgcn_s_setprio(3);
        if constexpr (PRO) {
            const int kvm = 2 * ti + (g & 1); const bool isV = (g >> 1) & 1;
            const char* wkvb = isV ? p.wv : p.wk;
            if (haskv && v < 2) {
#pragma unroll
                for (int n = 0; n < 13; ++n) {
                    const unsigned o = v * 12800 + (n < 12 ? n * 1024 : 11776);
                    glds16(o + lane * 16, wkvb, L_WKV + o);
                }
            }
            BLOCK_BARRIER();
            if (v >= 2) {
                const int v2 = v - 2;
                unsigned wo5[5];
#pragma unroll
                for (int i = 0; i < 5; ++i) { const unsigned c = (v2 + 2 * i) * 64 + lane, row = c / 10u, col = c - row * 10u; wo5[i] = row * 1280 + col * 16; }
#pragma unroll
                for (int n = 0; n < 20; ++n) glds16(wo5[n % 5], wqb + (n / 5) * 64 * 1280, L_WQ + (v2 + 2 * n) * 1024);
                u32x4 wst[3][10];
                auto wst_issue = [&](auto I) {
                    constexpr int rr = I.value / 10, i = I.value % 10;
                    GLOAD16N(wst[rr][i], wo5[i % 5], wqb + (4 + 2 * rr + i / 5) * 64 * 1280);
                };
                static_for<0, 10>(wst_issue);
                WAITV(10);
                GET_TAG(); lpost(C_TAG, tag);
                lpost(C_PP + v, 1);
                STAMP(2);
                static_for<10, 30>(wst_issue);
                static_for<0, 3>([&](auto R) {
                    constexpr int rr = R.value;
                    waitv5<20 - 10 * rr>(wst[rr][0], wst[rr][1], wst[rr][2], wst[rr][3], wst[rr][4]); waitv5<63>(wst[rr][5], wst[rr][6], wst[rr][7], wst[rr][8], wst[rr][9]);
                    lwait_all(C_AP, 4, 3 + rr, lane);
#pragma unroll
                    for (int i = 0; i < 10; ++i) *LP(u32x4, L_WQ + (rr & 1) * 20480 + (v2 + 2 * i) * 1024 + lane * 16) = wst[rr][i];
                    lpost(C_WP + v, rr + 1);
                });
            } else {
              if (haskv) {
                f32x16 acc = {};
                const unsigned wa = L_WKV + (v * 320 + 8 * h + tq) * 40 + tb * 32 + (tb ? 0 : tp * 8);
                unsigned xa[4];
#pragma unroll
                for (int s = 0; s < 4; ++s) xa[s] = L_PANEL + kmt * 40960 + v * 20480 + h * 512 + ((r ^ ((2 * s + h) & 7)) << 4);
                WAITV(0);
                GET_TAG();
                STAMP(2);
                bf16x8 wf[20], xf[20];
#pragma unroll
                for (int s = 0; s < 20; ++s) wf[s] = ldstr2(wa + s * 640, wa + s * 640 + 160);
                lwait_all(C_AP, 4, 1, lane);
#pragma unroll
                for (int s = 0; s < 20; ++s) xf[s] = ldsf(xa[s & 3] + s * 1024);
                SBAR();
                if (isV) {
#pragma unroll
                    for (int s = 0; s < 20; ++s) acc = MFMA(xf[s], wf[s], acc);
                } else {
#pragma unroll
                    for (int s = 0; s < 20; ++s) acc = MFMA(wf[s], xf[s], acc);
                }
                if (v) {
#pragma unroll
                    for (int e = 0; e < 4; ++e) *LP(f32x4, L_WKV + 12800 + e * 1024 + lane * 16) = f32x4{acc[4 * e], acc[4 * e + 1], acc[4 * e + 2], acc[4 * e + 3]};
                    lpost(C_KVP, 1);
                } else {
                    lwait(C_KVP, 1);
#pragma unroll
                    for (int e = 0; e < 4; ++e) {
                        const f32x4 t = *LP(const f32x4, L_WKV + 12800 + e * 1024 + lane * 16);
                        acc[4 * e] += t[0]; acc[4 * e + 1] += t[1]; acc[4 * e + 2] += t[2]; acc[4 * e + 3] += t[3];
                    }
                    const __amdgpu_buffer_rsrc_t rk = mkrsrc(ws + WS_KV, 32 * KVD);
                    const unsigned base = ti * KVD, ke = g & 1;
                    const u32x4 f0 = __builtin_bit_cast(u32x4, pack8(acc, 0, 1.f)), f1 = __builtin_bit_cast(u32x4, pack8(acc, 8, 1.f));
                    float mx;
                    if (!isV) {
                        __builtin_amdgcn_raw_buffer_store_b128(f0, rk, base + ke * 1024 + lane * 16, 0, 16);
                        const u32x2 k1 = {f1[0], f1[1]};
                        if (!h) __builtin_amdgcn_raw_buffer_store_b64(k1, rk, base + KV_K1 + r * 16 + ke * 8, 0, 16);
                        lpost(C_KVFIN, 1);
                        float n2 = 0.f;
#pragma unroll
                        for (int e = 0; e < 4; ++e) n2 += bflo(f0[e]) * bflo(f0[e]) + bfhi(f0[e]) * bfhi(f0[e]);
                        if (!h) n2 += bflo(f1[0]) * bflo(f1[0]) + bfhi(f1[0]) * bfhi(f1[0]) + bflo(f1[1]) * bflo(f1[1]) + bfhi(f1[1]) * bfhi(f1[1]);
                        n2 += __shfl_xor(n2, 32);
                        mx = (n2 == n2) ? n2 : INFINITY;
                    } else {
                        const unsigned cst = r == 20 ? 0x3f803f80u : 0u;
                        const u32x4 c4 = {cst, cst, cst, cst};
                        if (r <= 21) {
                            __builtin_amdgcn_raw_buffer_store_b128(r >= 20 ? c4 : f0, rk, base + KV_V + ke * KV_VT + h * 352 + r * 16, 0, 16);
                            __builtin_amdgcn_raw_buffer_store_b128(r >= 20 ? c4 : f1, rk, base + KV_V + ke * KV_VT + 704 + h * 352 + r * 16, 0, 16);
                        }
                        lpost(C_KVFIN, 1);
                        float vm = 0.f;
#pragma unroll
                        for (int e = 0; e < 16; ++e) { const float a = fabsf(acc[e]); vm = (a <= vm) ? vm : ((a == a) ? a : INFINITY); }
                        mx = r < 20 ? vm : 0.f;
                    }
                    mx = wave_max(mx);
                    WAITV(0);
                    STAMP(15);
                    if (lane < 8) __hip_atomic_store((gu64*)(ws + WS_KF + lane * 1024 + (isV ? 512 : 0)) + kvm, ((unsigned long long)tag << 32) | __float_as_uint(mx), RLX_AGENT);
                }
            }
              else { WAITV(0); GET_TAG(); }
              STAMP(3);
            }
            if (haskv) lwait(C_KVFIN, 1);
#pragma unroll
            for (int n = 0; n < 2; ++n) {
                unsigned L = n * 64 + lane; L = (j * 4 + v) * 120 + (L < 120u ? L : 119u);
                const unsigned m3 = L >= 7680u, rem = L - (m3 ? 7680u : 0u), row = rem / 12u;
                l2touch((m3 ? p.w3 : p.w1) + row * 3072 + (xc & 1) * 1536 + (rem - row * 12u) * 128);
            }
            lwait_all(C_AP, 4, GU_DELAY, lane);
            STAMP(4);
            {
                const char* w1s = p.w1 + g * 384 + v * 96; const char* w3s = p.w3 + g * 384 + v * 96;
                auto goff = [](unsigned idx) { const unsigned row = idx / 6u; return row * 3072 + (idx - row * 6u) * 16; };
                const unsigned gva = goff(lane), gvb = goff((lane - 8) & 63), gvc = goff(64 + (h ? (r - 8) & 31 : r));
                const unsigned gdst = L_GU + v * 9216;
                auto gdma = [&](auto S) { constexpr int s = S.value; glds_gu(gva, gvb, gvc, w1s + (size_t)s * 16 * 3072, w3s + (size_t)s * 16 * 3072, gdst + (s % 3) * 3072); };
                static_for<0, 3>(gdma);
                f32x16 acc[2][3] = {};
                unsigned bn[3][2], pa[4], pb[4];
#pragma unroll
                for (int n = 0; n < 3; ++n)
#pragma unroll
                    for (int t = 0; t < 2; ++t) {
                        const unsigned idx = 6 * (8 * h + 4 * t + tq) + 2 * n + (tp >> 1);
                        const unsigned o = idx < 64u ? (tb ? 1024 + ((idx + 8) & 63) * 16 : idx * 16) : (tb ? 2560 + ((idx - 56) & 31) * 16 : 2048 + (idx - 64) * 16);
                        bn[n][t] = gdst + o + (tp & 1) * 8;
                    }
#pragma unroll
                for (int s = 0; s < 4; ++s) { pa[s] = L_PANEL + h * 512 + ((r ^ ((2 * s + h) & 7)) << 4); pb[s] = pa[s] + 40960; PIN(pb[s]); }
                bf16x8 wf[2][3], xa[2], xb2[2];
                auto rdw = [&](auto S) { constexpr int s = S.value;
#pragma unroll
                    for (int n = 0; n < 3; ++n) wf[s & 1][n] = ldstr2(bn[n][0] + (s % 3) * 3072, bn[n][1] + (s % 3) * 3072); };
                auto rdx = [&](auto S) { constexpr int s = S.value; xa[s & 1] = ldsf(pa[s & 3] + s * 1024); xb2[s & 1] = ldsf(pb[s & 3] + s * 1024); };
                WAITV(8);
                rdx(IC<0>{}); rdw(IC<0>{});
                asm volatile("s_waitcnt lgkmcnt(0)" ::: "memory");
                gdma(IC<3>{});
                static_for<0, 40>([&](auto S) {
                    constexpr int s = S.value; constexpr bool nx = s + 1 < 40;
                    MPRIO(GU_HI, 0); acc[0][0] = MFMA(wf[s & 1][0], xa[s & 1], acc[0][0]); SBAR();
                    if constexpr (nx) rdx(IC<s + 1>{});
                    SBAR();
                    MPRIO(GU_HI, 1); acc[1][0] = MFMA(wf[s & 1][0], xb2[s & 1], acc[1][0]); SBAR();
                    if constexpr (nx) { WAITVN(4 * ((s + 3 < 39 ? s + 3 : 39) - (s + 1))); rdw(IC<s + 1>{}); }
                    SBAR();
                    MPRIO(GU_HI, 2); acc[0][1] = MFMA(wf[s & 1][1], xa[s & 1], acc[0][1]); SBAR();
                    MPRIO(GU_HI, 3); acc[1][1] = MFMA(wf[s & 1][1], xb2[s & 1], acc[1][1]); SBAR();
                    if constexpr (s + 4 < 40) { asm volatile("s_waitcnt lgkmcnt(0)" ::: "memory"); gdma(IC<s + 4>{}); }
                    SBAR();
                    MPRIO(GU_HI, 4); acc[0][2] = MFMA(wf[s & 1][2], xa[s & 1], acc[0][2]); SBAR();
                    MPRIO(GU_HI, 5); acc[1][2] = MFMA(wf[s & 1][2], xb2[s & 1], acc[1][2]); SBAR();
                });
                lpost(C_GP + v, 42);
                __builtin_amdgcn_s_setprio(3);
                STAMP(6);
                const __amdgpu_buffer_rsrc_t rh = mkrsrc(ws + WS_HT, 64 * 96 * 1024);
#pragma unroll
                for (int m = 0; m < 2; ++m)
#pragma unroll
                    for (int n = 0; n < 3; ++n) {
                        f32x16 hv;
#pragma unroll
                        for (int e = 0; e < 8; ++e) {
                            const float gg = acc[m][n][e];
                            hv[e] = gg * __builtin_amdgcn_rcpf(1.f + __builtin_amdgcn_exp2f(-1.4426950408889634f * gg)) * acc[m][n][e + 8];
                        }
                        __builtin_amdgcn_raw_buffer_store_b128(__builtin_bit_cast(u32x4, pack8(hv, 0, 1.f)), rh, ((2 * ti + m) * 96 + g * 12 + 3 * v + n) * 1024 + lane * 16, 0, 16);
                    }
                if constexpr (DN) {
                    WAITV(0);
                    if (lane < 2) __hip_atomic_store((gu32*)(ws + WS_HF) + (2 * ti + lane) * 32 + g * 4 + v, tag, RLX_AGENT);
                }
                __builtin_amdgcn_s_setprio(0);
                if constexpr (DN) {
#pragma unroll
                    for (int n = 0; n < 2; ++n) {
                        unsigned L = (j * 4 + v) * 85 + n * 64 + lane; L = L < 10880u ? L : 10879u;
                        const unsigned row = L / 5u;
                        l2touch((row < 1536u ? p.w2 + row * 1280 : p.wo + (row - 1536u) * 1280) + (xc & 1) * 640 + (L - row * 5u) * 128);
                    }
                }
                STAMP(7);
            }
        } else {
            BLOCK_BARRIER();
        }
        if constexpr (DN || WO) {
            LANE_CONSTS();
            const unsigned bq = ((tb * 32 + (tp >> 1) * 16) ^ ((unsigned)(tq >> 1) << 6)) + (tp & 1) * 8;
            const unsigned drow = lane >> 3, dcol = ((lane & 7) ^ (((lane >> 4) & 1) << 2)) * 16;
            const int G = xc >> 1, r0 = G < 2 ? 6 * G : 12 + 5 * (G - 2), nr = G < 2 ? 6 : 5;
            if constexpr (FUSED) {
                if (b == 255 && v == 0) {
                    gu32* f = (gu32*)(ws + WS_HF) + (lane >> 3) * 64 + (lane & 7) * 4;
                    _Pragma("clang loop unroll(disable)") for (unsigned spins = 0; spins < (1u << 14); ++spins) {
                        bool ok = true;
#pragma unroll
                        for (int n = 0; n < 4; ++n) ok &= __hip_atomic_load(f + n * 512, RLX_AGENT) == tag;
                        if (__all(ok)) break;
                        TMO(spins, 1u << 14, 303, tag);
                        __builtin_amdgcn_s_sleep(32);
                    }
                    if (lane == 0) __hip_atomic_store((gu32*)(ws + WS_EP), tag, RLX_AGENT);
                }
            }
            if (j < 5 * nr) {
                const int dr = r0 + j / 5, dc = (xc & 1) * 5 + j % 5;
                const int mfirst = dr < 20 ? 3 * dr : 60 + 2 * (dr - 20), nm = dr < 20 ? 3 : 2;
                const unsigned dvo = drow * 1280 + dcol;
                const unsigned myring = L_PANEL + v * 20480;
                f32x16 acc0 = {}, acc1 = {};
                if constexpr (PRO) { lwait_all(C_GP, 4, 42, lane); lwait_all(C_AP, 4, 8, lane); }
                if constexpr (DN) {
                    f32x16 da[3][2] = {};
                    const int drot = (j % 5) * 4;
                    const char* bsrc = p.w2 + (size_t)v * 384 * 1280 + dc * 128;
                    const char* asrc = ws + WS_HT + ((size_t)mfirst * 96 + v * 24) * 1024;
                    const unsigned am1 = lane * 16 + 98304, am2 = lane * 16 + (nm == 3 ? 196608 : 98304);
                    constexpr int DQ = DN_DQ;
                    u32x4 aq[DQ][3];
                    auto bissue = [&](auto K) {
                        constexpr int k = K.value;
                        int kr = k + drot; kr = kr >= 24 ? kr - 24 : kr;
                        const char* sb = bsrc + (size_t)kr * 16 * 1280;
                        glds16(dvo, sb, myring + (k % 10) * 2048); glds16(dvo + 8 * 1280, sb, myring + (k % 10) * 2048 + 1024);
                    };
                    auto woissue = [&](auto U) {
                        constexpr int u = U.value;
                        const char* sb = p.wo + (size_t)(v * 160 + u * 16) * 1280 + dc * 128;
                        glds16(dvo, sb, myring + u * 2048); glds16(dvo + 8 * 1280, sb, myring + u * 2048 + 1024);
                    };
                    auto aissue = [&](auto K) {
                        constexpr int k = K.value;
                        int kr = k + drot; kr = kr >= 24 ? kr - 24 : kr;
                        const char* sb = asrc + (size_t)kr * 1024;
                        GLOAD16S(aq[k % DQ][0], lane * 16, sb); GLOAD16S(aq[k % DQ][1], am1, sb); GLOAD16S(aq[k % DQ][2], am2, sb);
                    };
                    static_for<0, DQ>(bissue);
                    if constexpr (PRO) {
                        gu32* f = (gu32*)(ws + WS_HF) + (mfirst + ((lane >> 3) < nm ? (lane >> 3) : 0)) * 32 + 8 * v + (lane & 7);
                        _Pragma("clang loop unroll(disable)") for (unsigned spins = 0; spins < (1u << 14); ++spins) {
                            if (__all(__hip_atomic_load(f, RLX_AGENT) == tag)) break;
                            TMO(spins, 1u << 14, 302, tag);
                            __builtin_amdgcn_s_sleep(HF_SLEEP);
                        }
                        CFENCE();
                    }
                    STAMP(8);
                    static_for<0, DQ>(aissue);
                    const unsigned ba0 = myring + (4 * h + tq) * 128 + bq, ba1 = ba0 ^ 64;
                    bf16x8 w0[2], w1[2];
                    WAITVN(3 * DQ);
                    w0[0] = ldstr2(ba0, ba0 + 1024); w1[0] = ldstr2(ba1, ba1 + 1024);
                    static_for<0, 24>([&](auto K) {
                        constexpr int k = K.value;
                        waitv3<dn_younger(k, DQ, WO)>(aq[k % DQ][0], aq[k % DQ][1], aq[k % DQ][2]);
                        constexpr unsigned o = ((k + 1) % 10) * 2048;
#define DXF(m) __builtin_bit_cast(bf16x8, aq[k % DQ][m])
                        SBAR();
                        MPRIO(DN_HI, 0); da[0][0] = MFMA(w0[k & 1], DXF(0), da[0][0]); SBAR();
                        if constexpr (k + 1 < 24) w0[(k + 1) & 1] = ldstr2(ba0 + o, ba0 + o + 1024);
                        SBAR();
                        MPRIO(DN_HI, 1); da[0][1] = MFMA(w1[k & 1], DXF(0), da[0][1]); SBAR();
                        if constexpr (k + 1 < 24) w1[(k + 1) & 1] = ldstr2(ba1 + o, ba1 + o + 1024);
                        SBAR();
                        MPRIO(DN_HI, 2); da[1][0] = MFMA(w0[k & 1], DXF(1), da[1][0]); SBAR();
                        if constexpr (k + DQ < 24) bissue(IC<k + DQ>{});
                        if constexpr (WO && k >= 16) woissue(IC<(k + 8) % 10>{});
                        SBAR();
                        MPRIO(DN_HI, 3); da[1][1] = MFMA(w1[k & 1], DXF(1), da[1][1]); SBAR();
                        MPRIO(DN_HI, 4); da[2][0] = MFMA(w0[k & 1], DXF(2), da[2][0]); SBAR();
                        MPRIO(DN_HI, 5); da[2][1] = MFMA(w1[k & 1], DXF(2), da[2][1]); SBAR();
                        if constexpr (k + DQ < 24) aissue(IC<k + DQ>{});
                    });
                    __builtin_amdgcn_s_setprio(3);
                    STAMP(9);
                    if constexpr (WO) { CFENCE(); woissue(IC<2>{}); woissue(IC<3>{}); }
#pragma unroll
                    for (int n = 0; n < 2; ++n) {
#pragma unroll
                        for (int m = 0; m < 3; ++m) {
                            if (m != v) {
                                const unsigned dst = L_GU + (m * 3 + (v > m ? v - 1 : v)) * 4096 + lane * 16;
#pragma unroll
                                for (int e = 0; e < 4; ++e) *LP(f32x4, dst + e * 1024) = f32x4{da[m][n][4 * e], da[m][n][4 * e + 1], da[m][n][4 * e + 2], da[m][n][4 * e + 3]};
                            }
                        }
                        lpost(C_DP + v, 2 * n + 1); lwait_all(C_DP, 4, 2 * n + 1, lane);
                        if (v < 3) {
                            f32x16 t = v == 0 ? da[0][n] : (v == 1 ? da[1][n] : da[2][n]);
#pragma unroll
                            for (int o = 0; o < 3; ++o)
#pragma unroll
                                for (int e = 0; e < 4; ++e) {
                                    const f32x4 u = *LP(const f32x4, L_GU + (v * 3 + o) * 4096 + e * 1024 + lane * 16);
                                    t[4 * e] += u[0]; t[4 * e + 1] += u[1]; t[4 * e + 2] += u[2]; t[4 * e + 3] += u[3];
                                }
                            if (n) acc1 = t; else acc0 = t;
                        }
                        if (n == 0) { lpost(C_DP + v, 2); lwait_all(C_DP, 4, 2, lane); }
                    }
                    if constexpr (!WO) {
                        if (v < 3) {
                            float* pt = (float*)(ws + WS_PT) + (size_t)(b * 3 + v) * 2048 + lane;
#pragma unroll
                            for (int e = 0; e < 16; ++e) { pt[e * 64] = acc0[e]; pt[1024 + e * 64] = acc1[e]; }
                        }
                    }
                } else {
#pragma unroll
                    for (int u = 0; u < 20; ++u) glds16(dvo + u * 8 * 1280, p.wo + (size_t)v * 160 * 1280 + dc * 128, myring + u * 1024);
                    if (v < 3) {
                        const float* pt = (const float*)(ws + WS_PT) + (size_t)(b * 3 + v) * 2048 + lane;
#pragma unroll
                        for (int e = 0; e < 16; ++e) { acc0[e] = pt[e * 64]; acc1[e] = pt[1024 + e * 64]; }
                    }
                }
                if constexpr (WO) { WAITV(0); lpost(C_DP + v, 4); }
                if constexpr (WO && ATT) {
                    if (v == 3) {
                        __builtin_amdgcn_s_setprio(0);
                        const int mi = (lane >> 4) < nm ? (lane >> 4) : 0;
                        gu64* f = (gu64*)(ws + WS_OF + (mfirst + mi) * 128) + (lane & 15);
                        _Pragma("clang loop unroll(disable)") for (unsigned spins = 0; spins < (1u << 14); ++spins) {
                            const unsigned long long x = __hip_atomic_load(f, RLX_AGENT);
                            const unsigned long long ok = __ballot((unsigned)x == tag && (unsigned)(x >> 32) == tag);
                            const unsigned mine = (unsigned)(ok >> ((lane & 3) * 16));
                            *((volatile AS3 unsigned*)lcnt(C_OS) + (lane & 3)) = ((mine & 0xffu) == 0xffu ? 1u : 0u) | ((mine & 0xff00u) == 0xff00u ? 2u : 0u);
                            if (ok == ~0ull) break;
                            TMO(spins, 1u << 14, 300, tag);
                            __builtin_amdgcn_s_sleep(OF_SLEEP);
                        }
                    }
                }
                if (v < nm) {
                    const int m = mfirst + v;
                    const unsigned ba0 = L_PANEL + (4 * h + tq) * 128 + bq, ba1 = ba0 ^ 64;
                    if constexpr (WO) {
                        const char* xrow = p.x + (size_t)(m * 32 + r) * 1280 + dc * 128 + h * 16;
                        u32x4 xres[4];
#pragma unroll
                        for (int e = 0; e < 4; ++e) xres[e] = *(const u32x4*)(xrow + e * 32);
                        lwait_all(C_DP, 4, 4, lane);
                        const __amdgpu_buffer_rsrc_t ro = mkrsrc(ws + WS_OT + (size_t)m * 40960, 40960);
                        int hf = 0;
                        f32x16 t0[2] = {}, t1[2] = {};
#pragma unroll
                        for (int it = 0; it < 2; ++it) {
                            if constexpr (ATT) {
                                _Pragma("clang loop unroll(disable)") for (unsigned spins = 0; spins < (1u << 17); ++spins) {
                                    const unsigned ok = __builtin_amdgcn_readfirstlane(*((volatile AS3 unsigned*)lcnt(C_OS) + v));
                                    if (it == 0) { if (ok) { hf = (ok & 1) ? 0 : 1; break; } }
                                    else if (ok == 3) break;
                                    __builtin_amdgcn_s_sleep(1);
                                }
                                CFENCE();
                            }
                            if (it == 0) STAMP(10);
                            u32x4 of[20];
#pragma unroll
                            for (int s = 0; s < 20; ++s) of[s] = __builtin_amdgcn_raw_buffer_load_b128(ro, lane * 16 + s * 1024, hf * 20480, 16);
                            const unsigned a0 = ba0 + hf * 40960, a1 = ba1 + hf * 40960, l0 = a0 + (36 * h + 16) * 128, l1 = a1 + (36 * h + 16) * 128;
                            bf16x8 w0[2], w1[2];
                            w0[0] = ldstr2(a0, a0 + 1024); w1[0] = ldstr2(a1, a1 + 1024);
                            static_for<0, 20>([&](auto S) {
                                constexpr int s = S.value, n = s + 1, gg = n / 5, jj = n % 5;
                                const bf16x8 xf = __builtin_bit_cast(bf16x8, of[s]);
                                if constexpr (n < 20) {
                                    if constexpr (jj < 4) {
                                        constexpr unsigned o = (80 * gg + 20 * jj) * 128;
                                        w0[n & 1] = ldstr2(a0 + o, a0 + o + 1024); w1[n & 1] = ldstr2(a1 + o, a1 + o + 1024);
                                    } else {
                                        constexpr unsigned o = 80 * gg * 128;
                                        w0[n & 1] = ldstr2(l0 + o, l0 + o + 2560); w1[n & 1] = ldstr2(l1 + o, l1 + o + 2560);
                                    }
                                }
                                SBAR();
                                t0[it] = MFMA(w0[s & 1], xf, t0[it]); t1[it] = MFMA(w1[s & 1], xf, t1[it]);
                                SBAR();
                            });
                            hf ^= 1;
                        }
                        acc0 += t0[0] + t0[1]; acc1 += t1[0] + t1[1];
                        char* orow = p.out + (size_t)(m * 32 + r) * 1280 + dc * 128 + h * 16;
#pragma unroll
                        for (int n = 0; n < 2; ++n) {
                            const f32x16& a = n ? acc1 : acc0;
#pragma unroll
                            for (int qq = 0; qq < 2; ++qq) {
                                float lo[4], hi[4];
#pragma unroll
                                for (int e = 0; e < 4; ++e) {
                                    auto sw = __builtin_amdgcn_permlane32_swap(__float_as_uint(a[8 * qq + e]), __float_as_uint(a[8 * qq + 4 + e]), false, false);
                                    lo[e] = __uint_as_float(sw[0]); hi[e] = __uint_as_float(sw[1]);
                                }
                                const u32x4 xv = xres[2 * n + qq];
                                const u32x4 ov = {pk2(lo[0] + bflo(xv[0]), lo[1] + bfhi(xv[0])), pk2(lo[2] + bflo(xv[1]), lo[3] + bfhi(xv[1])),
                                                  pk2(hi[0] + bflo(xv[2]), hi[1] + bfhi(xv[2])), pk2(hi[2] + bflo(xv[3]), hi[3] + bfhi(xv[3]))};
                                *(u32x4*)(orow + (2 * n + qq) * 32) = ov;
                            }
                        }
                        STAMP(11);
                    }
                }
            }
        }
    } else {
        LANE_CONSTS();
        bf16x8 Q0a, Q0b; u32x2 q1a, q1b;
        const int head = 4 * g + w;
        const AttSync as{lane, ws + WS_KV, (unsigned)((w < 3 ? w * 3072 : CHUNK - 3072) + lane * 16), (unsigned)(w < 3 ? w * 3072 : CHUNK - 3072), w};
        __builtin_amdgcn_s_setprio(3);
        unsigned long long fk = 0, fv = 0; bool early = false;
        if constexpr (PRO) {
            BLOCK_BARRIER();
            waitv5<15>(x0[0], x0[1], x0[2], x0[3], x0[4]);
            STAMP(1);
            ln_rows(x0, ra, lane);
            waitv5<10>(x1[0], x1[1], x1[2], x1[3], x1[4]);
            ln_rows(x1, ra + 4, lane);
            lpost(C_AP + w, 1);
            waitv5<5>(x2[0], x2[1], x2[2], x2[3], x2[4]);
            ln_rows(x2, rb, lane);
            waitv5<0>(x3[0], x3[1], x3[2], x3[3], x3[4]);
            ln_rows(x3, rb + 4, lane);
            lpost(C_AP + w, 2);
            unsigned pa[4];
#pragma unroll
            for (int s = 0; s < 4; ++s) pa[s] = L_PANEL + h * 512 + ((r ^ ((2 * s + h) & 7)) << 4);
            f32x16 qa = {}, qb = {};
            const unsigned wa = L_WQ + (8 * h + tq) * 160 + w * 40 + tb * 32 + (tb ? 0 : tp * 8);
            lwait_all(C_AP, 4, 2, lane); lwait_all(C_PP, 4, 1, lane);
            if constexpr (FUSED) tag = __builtin_amdgcn_readfirstlane(*(volatile AS3 unsigned*)lcnt(C_TAG));
            STAMP(2);
            bf16x8 fw[2][4], fa[2][4], fb[2][4];
#pragma unroll
            for (int s4 = 0; s4 < 4; ++s4) { fw[0][s4] = ldstr2(wa + s4 * 2560, wa + s4 * 2560 + 640); fa[0][s4] = ldsf(pa[s4] + s4 * 1024); fb[0][s4] = ldsf(pa[s4] + 40960 + s4 * 1024); }
            unsigned pk = 0;
            static_for<0, 10>([&](auto S) {
                constexpr int st = S.value, nx = st + 1;
                if constexpr (st == 3 || st == 5 || st == 7) lcheck(pk, C_WP, (st - 1) / 2, lane);
                if constexpr (st == 6 && ATT) gload8x2_sc1(fk, fv, lane * 8, ws + WS_KF + xc * 1024);
                if constexpr (st == 8 && ATT) {
                    waitv8x2(fk, fv);
                    early = __all((unsigned)(fk >> 32) == tag && (unsigned)(fv >> 32) == tag);
                    if (early) { lwait_all(C_AP, 4, 6, lane); att_dma(as, 0); att_dma(as, 1); }
                    STAMP(8);
                }
#pragma unroll
                for (int s4 = 0; s4 < 4; ++s4) {
                    qa = MFMA(fw[st & 1][s4], fa[st & 1][s4], qa); SBAR();
                    if constexpr (nx < 10) { const unsigned o = wa + (nx & 3) * 10240 + s4 * 2560; fw[nx & 1][s4] = ldstr2(o, o + 640); SBAR(); }
                    qb = MFMA(fw[st & 1][s4], fb[st & 1][s4], qb); SBAR();
                    if constexpr (nx < 10) { fa[nx & 1][s4] = ldsf(pa[s4] + (nx * 4 + s4) * 1024); fb[nx & 1][s4] = ldsf(pa[s4] + 40960 + (nx * 4 + s4) * 1024); SBAR(); }
                    if (s4 == 2 && (st == 2 || st == 4 || st == 6)) { pk = lpeek(C_WP, lane); SBAR(); }
                }
                if constexpr (!(st & 1) && st <= 6) lpost(C_AP + w, 3 + st / 2);
            });
            lpost(C_AP + w, 8);
            Q0a = pack8(qa, 0, QSCALE); Q0b = pack8(qb, 0, QSCALE);
            q1a = u32x2{pk2(qa[8] * QSCALE, qa[9] * QSCALE), pk2(qa[10] * QSCALE, qa[11] * QSCALE)};
            q1b = u32x2{pk2(qb[8] * QSCALE, qb[9] * QSCALE), pk2(qb[10] * QSCALE, qb[11] * QSCALE)};
            if (h) { q1a = u32x2{0u, 0u}; q1b = u32x2{0u, 0u}; }
            if constexpr (!ATT) {
                char* qt = ws + WS_QT + (size_t)((2 * ti) * 32 + head) * 2048 + lane * 16;
                *(bf16x8*)qt = Q0a; *(u32x2*)(qt + 1024) = q1a; *(bf16x8*)(qt + 65536) = Q0b; *(u32x2*)(qt + 65536 + 1024) = q1b;
            }
            STAMP(3);
        } else {
            BLOCK_BARRIER();
            if constexpr (ATT) {
                const char* qt = ws + WS_QT + (size_t)((2 * ti) * 32 + head) * 2048 + lane * 16;
                Q0a = *(const bf16x8*)qt; q1a = *(const u32x2*)(qt + 1024); Q0b = *(const bf16x8*)(qt + 65536); q1b = *(const u32x2*)(qt + 65536 + 1024);
            }
        }
        if constexpr (ATT) {
            float na = 0.f, nb = 0.f;
            {
                const u32x4 a0 = __builtin_bit_cast(u32x4, Q0a), b0 = __builtin_bit_cast(u32x4, Q0b);
#pragma unroll
                for (int e = 0; e < 4; ++e) { na += bflo(a0[e]) * bflo(a0[e]) + bfhi(a0[e]) * bfhi(a0[e]); nb += bflo(b0[e]) * bflo(b0[e]) + bfhi(b0[e]) * bfhi(b0[e]); }
#pragma unroll
                for (int e = 0; e < 2; ++e) { na += bflo(q1a[e]) * bflo(q1a[e]) + bfhi(q1a[e]) * bfhi(q1a[e]); nb += bflo(q1b[e]) * bflo(q1b[e]) + bfhi(q1b[e]) * bfhi(q1b[e]); }
                na += __shfl_xor(na, 32); nb += __shfl_xor(nb, 32);
            }
            if (!early) {
                gu64* f = (gu64*)(ws + WS_KF + xc * 1024) + lane;
                _Pragma("clang loop unroll(disable)") for (unsigned spins = 0; spins < (1u << 14); ++spins) {
                    fk = __hip_atomic_load(f, RLX_AGENT); fv = __hip_atomic_load(f + 64, RLX_AGENT);
                    if (__all((unsigned)(fk >> 32) == tag && (unsigned)(fv >> 32) == tag)) break;
                    TMO(spins, 1u << 14, 301, tag);
                    __builtin_amdgcn_s_sleep(4);
                }
                if constexpr (PRO) lwait_all(C_AP, 4, 6, lane);
                att_dma(as, 0); att_dma(as, 1);
            }
            STAMP(4);
            if constexpr (PRO) lwait_all(C_AP, 4, 8, lane);
            att_dma(as, 2);
            const float kn2 = wave_max(__uint_as_float((unsigned)fk)), vmx = wave_max(__uint_as_float((unsigned)fv));
            const float Ma = __builtin_sqrtf(na * kn2) * 1.01f, Mb = __builtin_sqrtf(nb * kn2) * 1.01f;
            const bool fast = __all(Ma <= BOUND_MAX && Mb <= BOUND_MAX && vmx <= 1e30f);
            const unsigned sa = (fast && h) ? pk2(-Ma, 0.f) : q1a[0], sb = (fast && h) ? pk2(-Mb, 0.f) : q1b[0];
            const u32x4 a0 = {sa, q1a[1], 0u, 0u}, a1 = {h ? sa : 0u, 0u, q1a[0], q1a[1]}, b0 = {sb, q1b[1], 0u, 0u}, b1 = {h ? sb : 0u, 0u, q1b[0], q1b[1]};
            f32x16 Oa = {}, Ob = {};
            WAITV(6); lpost(C_AP + w, AP0); lwait_all(C_AP, 4, AP0, lane);
            __builtin_amdgcn_s_setprio(ATT_PRIO);
            STAMP(5); STAMPC(12);
            if (fast) att_fast(as, Q0a, BC(a0), BC(a1), Q0b, BC(b0), BC(b1), Oa, Ob);
            else att_safe(as, Q0a, BC(a0), BC(a1), Q0b, BC(b0), BC(b1), Oa, Ob);
            STAMP(6); STAMPC(13);
            const __amdgpu_buffer_rsrc_t ro = mkrsrc(ws + WS_OT, 64 * 40 * 1024);
#pragma unroll
            for (int q = 0; q < 2; ++q) {
                const f32x16& O = q ? Ob : Oa;
                const float inv = 1.f / __shfl(O[8], r + 32);
                const unsigned base = ((2 * ti + q) * 40 + 5 * g) * 1024;
                __builtin_amdgcn_raw_buffer_store_b128(__builtin_bit_cast(u32x4, pack8(O, 0, inv)), ro, base + w * 1024 + lane * 16, 0, 16);
                if (!h) {
                    u32x2 t = {pk2(O[8] * inv, O[9] * inv), pk2(O[10] * inv, O[11] * inv)};
                    __builtin_amdgcn_raw_buffer_store_b64(t, ro, base + 4096 + ((w >> 1) * 32 + r) * 16 + (w & 1) * 8, 0, 16);
                }
            }
            if constexpr (WO) {
                WAITV(0);
                if (lane < 2) __hip_atomic_store((gu32*)(ws + WS_OF) + (2 * ti + lane) * 32 + head, tag, RLX_AGENT);
            }
            STAMP(7);
        }
    }
}

template <int PH>
static void launch(const Params& p, hipStream_t stream) {
    (void)hipFuncSetAttribute((const void*)mega<PH>, hipFuncAttributeMaxDynamicSharedMemorySize, L_TOTAL);
    mega<PH><<<256, 512, L_TOTAL, stream>>>(p);
}

extern "C" void kernel_launch(void* const* d_in, const int* in_sizes, int n_in, void* d_out, int out_size, void* d_ws, size_t ws_size, hipStream_t stream) {
    Params p;
    p.x = (const char*)d_in[0]; p.wq = (const char*)d_in[1]; p.wk = (const char*)d_in[2]; p.wv = (const char*)d_in[3]; p.wo = (const char*)d_in[4];
    p.w1 = (const char*)d_in[6]; p.w3 = (const char*)d_in[7]; p.w2 = (const char*)d_in[8];
    p.out = (char*)d_out; p.ws = (char*)d_ws;
    launch<27>(p, stream);
}
```

```cpp
#include <hip/hip_runtime.h>
#include <hip/hip_bf16.h>
#include <stdint.h>

#define AS1 __attribute__((address_space(1)))
#define AS3 __attribute__((address_space(3)))
typedef __bf16 bf16x2 __attribute__((ext_vector_type(2)));
typedef __bf16 bf16x8 __attribute__((ext_vector_type(8)));
typedef short s16x4 __attribute__((ext_vector_type(4)));
typedef short s16x8 __attribute__((ext_vector_type(8)));
typedef float f32x2 __attribute__((ext_vector_type(2)));
typedef float f32x4 __attribute__((ext_vector_type(4)));
typedef float f32x16 __attribute__((ext_vector_type(16)));
typedef unsigned u32x2 __attribute__((ext_vector_type(2)));
typedef unsigned u32x4 __attribute__((ext_vector_type(4)));
typedef AS1 unsigned gu32;
typedef AS1 unsigned long long gu64;

#define ATT_PRIO 2
#define GEM_PRIO 0
#define GU_HI 0
#define DN_HI 0
#define MPRIO(mask, i) __builtin_amdgcn_s_setprio(((mask) >> (i)) & 1 ? 3 : 0)
#define GU_DELAY 8
#define GU_ROT 4
#define GU_GQ 5
#define OF_SLEEP 8
#define HF_SLEEP 20
#define DN_DQ 8
#define SPLIT_MODE 0

constexpr int SEQ = 2048, DM = 640, FF = 1536;
constexpr unsigned WS_KF = 0;
constexpr unsigned WS_HF = 16384;
constexpr unsigned WS_OF = 32768;
constexpr unsigned WS_EP = 49152;
constexpr unsigned KVD = 5376;
constexpr unsigned KV_K1 = 2048, KV_V = 2560, KV_VT = 1408;
constexpr unsigned WS_KV = 65536;
constexpr unsigned WS_HT = WS_KV + 176128;
constexpr unsigned WS_OT = WS_HT + 64 * 96 * 1024;
constexpr unsigned WS_QT = WS_OT + 64 * 40 * 1024;
constexpr unsigned WS_PT = WS_QT + 64 * 32 * 2048;
constexpr unsigned WS_ST = WS_PT + 256 * 3 * 32 * 256;
constexpr unsigned L_PANEL = 0;
constexpr unsigned L_KV = 81920;
constexpr unsigned L_WQ = 81920;
constexpr unsigned L_GU = 124928;
constexpr unsigned GSL = 12320;
constexpr unsigned L_WKV = 124928;
constexpr unsigned L_MISC = 161920;
constexpr unsigned L_TOTAL = 163840;
constexpr unsigned CHUNK = 2 * KVD;
constexpr int NCHUNK = 16;
enum { C_AP = 0  , C_GP = 4  , C_DP = 8  , C_PP = 12  , C_WP = 16  , C_KVP = 20, C_KVFIN, C_TAG, C_OS = 24  , C_N = 28 };

struct Params { const char* x; const char* wq; const char* wk; const char* wv; const char* wo; const char* w1; const char* w3; const char* w2; char* out; char* ws; };

#define SBAR() __builtin_amdgcn_sched_barrier(0)
#define PIN(x) asm volatile("" : "+v"(x))
#define CFENCE() asm volatile("" ::: "memory")
#define WAITV(n) asm volatile("s_waitcnt vmcnt(" #n ")" ::: "memory")
#define MFMA(a, b, c) __builtin_amdgcn_mfma_f32_32x32x16_bf16((a), (b), (c), 0, 0, 0)
#define RLX_AGENT __ATOMIC_RELAXED, __HIP_MEMORY_SCOPE_AGENT
#define RLX_WG __ATOMIC_RELAXED, __HIP_MEMORY_SCOPE_WORKGROUP

#define TMO(spins, lim, code, tgt) do {} while (0)
#define STAMP(id) do {} while (0)
#define STAMPC(id) do {} while (0)

#define LP(T, off) ((AS3 T*)(uintptr_t)(unsigned)(off))
__device__ __forceinline__ bf16x8 ldsf(unsigned off) { return *LP(const bf16x8, off); }
__device__ __forceinline__ s16x4 ldstr(unsigned off) { return __builtin_amdgcn_ds_read_tr16_b64_v4i16(LP(s16x4, off)); }
__device__ __forceinline__ bf16x8 ldstr2(unsigned off0, unsigned off1) {
    s16x4 a = ldstr(off0), b = ldstr(off1);
    return __builtin_bit_cast(bf16x8, __builtin_shufflevector(a, b, 0, 1, 2, 3, 4, 5, 6, 7));
}
__device__ __forceinline__ unsigned pk2(float lo, float hi) { bf16x2 v = {(__bf16)lo, (__bf16)hi}; return __builtin_bit_cast(unsigned, v); }
__device__ __forceinline__ float bflo(unsigned u) { return __uint_as_float(u << 16); }
__device__ __forceinline__ float bfhi(unsigned u) { return __uint_as_float(u & 0xffff0000u); }
__device__ __forceinline__ float rbf(float f) { return (float)(__bf16)f; }

__device__ __forceinline__ void glds16(unsigned voff, const char* sbase, unsigned ldsdst) {
    unsigned keep;
    asm volatile("s_mov_b32 %0, m0\n\ts_mov_b32 m0, %3\n\ts_nop 2\n\tglobal_load_lds_dwordx4 %1, %2\n\ts_mov_b32 m0, %0"
                 : "=&s"(keep) : "v"(voff), "s"(sbase), "s"(ldsdst) : "memory");
}
__device__ __forceinline__ void glds16_sc1(unsigned voff, const char* sbase, unsigned ldsdst) {
    unsigned keep;
    asm volatile("s_mov_b32 %0, m0\n\ts_mov_b32 m0, %3\n\ts_nop 2\n\tglobal_load_lds_dwordx4 %1, %2 sc1\n\ts_mov_b32 m0, %0"
                 : "=&s"(keep) : "v"(voff), "s"(sbase), "s"(ldsdst) : "memory");
}
__device__ __forceinline__ void glds16x3_sc1(unsigned voff, const char* sbase, unsigned ldsdst) {
    unsigned keep;
    asm volatile("s_mov_b32 %0, m0\n\ts_mov_b32 m0, %3\n\ts_nop 2\n\tglobal_load_lds_dwordx4 %1, %2 sc1\n\tglobal_load_lds_dwordx4 %1, %2 offset:1024 sc1\n\tglobal_load_lds_dwordx4 %1, %2 offset:2048 sc1\n\ts_mov_b32 m0, %0"
                 : "=&s"(keep) : "v"(voff), "s"(sbase), "s"(ldsdst) : "memory");
}
__device__ __forceinline__ void glds16x3v(unsigned v0, unsigned v1, unsigned v2, const char* sbase, unsigned ldsdst) {
    unsigned keep;
    asm volatile("s_mov_b32 %0, m0\n\ts_mov_b32 m0, %5\n\ts_nop 0\n\tglobal_load_lds_dwordx4 %1, %4\n\tglobal_load_lds_dwordx4 %2, %4 offset:1024\n\tglobal_load_lds_dwordx4 %3, %4 offset:2048\n\ts_mov_b32 m0, %0"
                 : "=&s"(keep) : "v"(v0), "v"(v1), "v"(v2), "s"(sbase), "s"(ldsdst) : "memory");
}
__device__ __forceinline__ void glds_gu(unsigned va, unsigned vb, unsigned vc, const char* s1, const char* s3, unsigned ldsdst) {
    unsigned keep;
    asm volatile("s_mov_b32 %0, m0\n\ts_mov_b32 m0, %8\n\ts_nop 0\n\tglobal_load_lds_dwordx4 %1, %4\n\tglobal_load_lds_dwordx4 %2, %5 offset:1024\n\t"
                 "s_mov_b32 exec_hi, 0\n\tglobal_load_lds_dwordx4 %3, %6 offset:2048\n\ts_not_b64 exec, exec\n\tglobal_load_lds_dwordx4 %3, %7 offset:2048\n\t"
                 "s_mov_b64 exec, -1\n\ts_mov_b32 m0, %0"
                 : "=&s"(keep) : "v"(va), "v"(vb), "v"(vc), "s"(s1), "s"(s3 - 1024), "s"(s1 - 2048), "s"(s3 - 2048), "s"(ldsdst) : "memory", "scc");
}
__device__ __forceinline__ void l2touch(const char* ga) {
    unsigned keep;
    asm volatile("s_mov_b32 %0, m0\n\ts_mov_b32 m0, %2\n\ts_nop 0\n\tglobal_load_lds_dword %1, off\n\ts_mov_b32 m0, %0" : "=&s"(keep) : "v"(ga), "s"(L_MISC + 1024) : "memory");
}
#define GLOAD16(dst, voff, sbase, imm) asm volatile("s_nop 4\n\tglobal_load_dwordx4 %0, %1, %2 offset:%3" : "=v"(dst) : "v"(voff), "s"(sbase), "i"(imm) : "memory")

__device__ __forceinline__ AS3 unsigned* lcnt(int c) { return LP(unsigned, L_MISC) + c; }
__device__ __forceinline__ void larrive(int c, int lane) { CFENCE(); if (lane == 0) __hip_atomic_fetch_add(lcnt(c), 1u, RLX_WG); CFENCE(); }
__device__ __forceinline__ void lset(int c, unsigned v, int lane) { CFENCE(); if (lane == 0) __hip_atomic_store(lcnt(c), v, RLX_WG); CFENCE(); }
__device__ __forceinline__ void lwait(int c, unsigned target) {
    CFENCE();
    _Pragma("clang loop unroll(disable)") for (unsigned spins = 0; spins < (1u << 22); ++spins) {
        if ((unsigned)__builtin_amdgcn_readfirstlane(__hip_atomic_load(lcnt(c), RLX_WG)) >= target) break;
        TMO(spins, 1u << 16, 100 + c, target);
        __builtin_amdgcn_s_sleep(1);
    }
    CFENCE();
}
__device__ __forceinline__ void lwait_all(int c, int n, unsigned target, int lane) {
    CFENCE();
    AS3 unsigned* q = lcnt(c) + (lane < n ? lane : 0);
    _Pragma("clang loop unroll(disable)") for (unsigned spins = 0; spins < (1u << 16); ++spins) {
        if (__all(__hip_atomic_load(q, RLX_WG) >= target)) break;
        TMO(spins, 1u << 16, 200 + c, target);
        __builtin_amdgcn_s_sleep(1);
    }
    CFENCE();
}
__device__ __forceinline__ void lpost(int c, unsigned v) { CFENCE(); *(volatile AS3 unsigned*)lcnt(c) = v; CFENCE(); }
__device__ __forceinline__ void linc(unsigned vaddr, unsigned vone) {
    asm volatile("s_mov_b64 exec, 1\n\tds_add_u32 %0, %1\n\ts_mov_b64 exec, -1" :: "v"(vaddr), "v"(vone) : "memory");
}
__device__ __forceinline__ unsigned lpeek(int c, int lane) { return *((volatile AS3 unsigned*)lcnt(c) + (lane & 3)); }
__device__ __forceinline__ void lcheck(unsigned pk, int c, unsigned target, int lane) {
    if (__builtin_expect(__any(pk < target), 0)) lwait_all(c, 4, target, lane);
    CFENCE();
}
__device__ __forceinline__ void gwait(const char* ws, unsigned off, int n, unsigned tag, int lane) {
    gu32* f = (gu32*)(ws + off) + (lane < n ? lane : 0);
    _Pragma("clang loop unroll(disable)") for (unsigned spins = 0; spins < (1u << 14); ++spins) {
        unsigned v = __hip_atomic_load(f, RLX_AGENT);
        if (__all(v == tag)) break;
        TMO(spins, 1u << 14, 300, off);
        __builtin_amdgcn_s_sleep(6);
    }
    CFENCE();
}
__device__ __forceinline__ float gwait64(const char* ws, unsigned off, unsigned tag, int lane) {
    gu64* f = (gu64*)(ws + off) + lane;
    unsigned long long v = 0;
    _Pragma("clang loop unroll(disable)") for (unsigned spins = 0; spins < (1u << 14); ++spins) {
        v = __hip_atomic_load(f, RLX_AGENT);
        if (__all((unsigned)(v >> 32) == tag)) break;
        __builtin_amdgcn_s_sleep(6);
    }
    CFENCE();
    return __uint_as_float((unsigned)v);
}
__device__ __forceinline__ __amdgpu_buffer_rsrc_t mkrsrc(const char* base, unsigned bytes) {
    return __builtin_amdgcn_make_buffer_rsrc((void*)base, 0, bytes, 0x00020000);
}
__device__ __forceinline__ float dpp_sum16(float v) {
    v += __int_as_float(__builtin_amdgcn_update_dpp(0, __float_as_int(v), 0x128, 0xf, 0xf, false));
    v += __int_as_float(__builtin_amdgcn_update_dpp(0, __float_as_int(v), 0x124, 0xf, 0xf, false));
    v += __int_as_float(__builtin_amdgcn_update_dpp(0, __float_as_int(v), 0x122, 0xf, 0xf, false));
    v += __int_as_float(__builtin_amdgcn_update_dpp(0, __float_as_int(v), 0x121, 0xf, 0xf, false));
    return v;
}
__device__ __forceinline__ float wave_max(float v) {
    int x = __float_as_int(v);
    x = max(x, __builtin_amdgcn_update_dpp(0, x, 0x128, 0xf, 0xf, false)); x = max(x, __builtin_amdgcn_update_dpp(0, x, 0x124, 0xf, 0xf, false));
    x = max(x, __builtin_amdgcn_update_dpp(0, x, 0x122, 0xf, 0xf, false)); x = max(x, __builtin_amdgcn_update_dpp(0, x, 0x121, 0xf, 0xf, false));
    const int a = max(__builtin_amdgcn_readlane(x, 0), __builtin_amdgcn_readlane(x, 16)), b = max(__builtin_amdgcn_readlane(x, 32), __builtin_amdgcn_readlane(x, 48));
    return __int_as_float(max(a, b));
}
__device__ __forceinline__ bf16x8 pack8(const f32x16& a, int b, float sc) {
    u32x4 r = {pk2(a[b] * sc, a[b + 1] * sc), pk2(a[b + 2] * sc, a[b + 3] * sc), pk2(a[b + 4] * sc, a[b + 5] * sc), pk2(a[b + 6] * sc, a[b + 7] * sc)};
    return __builtin_bit_cast(bf16x8, r);
}

template <int I> struct IC { static constexpr int value = I; };
template <int B, int E, class F> __device__ __forceinline__ void static_for(F&& f) { if constexpr (B < E) { f(IC<B>{}); static_for<B + 1, E>(f); } }
#define WAITVN(n) asm volatile("s_waitcnt vmcnt(%0)" :: "i"(n) : "memory")
__device__ __forceinline__ void GLOAD16S(u32x4& dst, unsigned voff, const char* sbase) { asm volatile("global_load_dwordx4 %0, %1, %2 sc1" : "=v"(dst) : "v"(voff), "s"(sbase) : "memory"); }
__device__ __forceinline__ void GLOAD16N(u32x4& dst, unsigned voff, const char* sbase) { asm volatile("global_load_dwordx4 %0, %1, %2" : "=v"(dst) : "v"(voff), "s"(sbase) : "memory"); }
__device__ __forceinline__ void GLOAD16N_IF(u32x4& dst, unsigned voff, const char* sbase, int cond) {
    asm volatile("s_cmp_eq_u32 %3, 0\n\ts_cbranch_scc1 1f\n\tglobal_load_dwordx4 %0, %1, %2\n1:" : "=v"(dst) : "v"(voff), "s"(sbase), "s"(cond) : "memory", "scc"); }
template <int N> __device__ __forceinline__ void waitv5(u32x4& a, u32x4& b, u32x4& c, u32x4& d, u32x4& e) { asm volatile("s_waitcnt vmcnt(%5)" : "+v"(a), "+v"(b), "+v"(c), "+v"(d), "+v"(e) : "i"(N) : "memory"); }
__device__ __forceinline__ void gload8x2_sc1(unsigned long long& a, unsigned long long& b, unsigned voff, const char* sbase) {
    asm volatile("global_load_dwordx2 %0, %2, %3 sc1\n\tglobal_load_dwordx2 %1, %2, %3 offset:512 sc1" : "=&v"(a), "=&v"(b) : "v"(voff), "s"(sbase) : "memory"); }
__device__ __forceinline__ void waitv8x2(unsigned long long& a, unsigned long long& b) { asm volatile("s_waitcnt vmcnt(0)" : "+v"(a), "+v"(b) :: "memory"); }
__device__ __forceinline__ void PIN2(u32x4& a, u32x4& b) { asm volatile("" : "+v"(a), "+v"(b) :: "memory"); }
__device__ __forceinline__ u32x4 scale8(unsigned x0, unsigned x1, unsigned x2, unsigned x3, unsigned clo, unsigned chi, float c) {
    unsigned o0, o1, o2, o3; float t0, t1, t2, t3, t4, t5, t6, t7;
    asm("v_dot2_f32_bf16 %4, %12, %16, %18\n\tv_dot2_f32_bf16 %5, %12, %17, %18\n\tv_dot2_f32_bf16 %6, %13, %16, %18\n\tv_dot2_f32_bf16 %7, %13, %17, %18\n\t"
        "v_dot2_f32_bf16 %8, %14, %16, %18\n\tv_dot2_f32_bf16 %9, %14, %17, %18\n\tv_dot2_f32_bf16 %10, %15, %16, %18\n\tv_dot2_f32_bf16 %11, %15, %17, %18\n\t"
        "v_cvt_pk_bf16_f32 %0, %4, %5\n\tv_cvt_pk_bf16_f32 %1, %6, %7\n\tv_cvt_pk_bf16_f32 %2, %8, %9\n\tv_cvt_pk_bf16_f32 %3, %10, %11"
        : "=&v"(o0), "=&v"(o1), "=&v"(o2), "=&v"(o3), "=&v"(t0), "=&v"(t1), "=&v"(t2), "=&v"(t3), "=&v"(t4), "=&v"(t5), "=&v"(t6), "=&v"(t7)
        : "v"(x0), "v"(x1), "v"(x2), "v"(x3), "v"(clo), "v"(chi), "v"(c));
    return u32x4{o0, o1, o2, o3};
}
template <int N> __device__ __forceinline__ void waitv3(u32x4& a, u32x4& b, u32x4& c) { asm volatile("s_waitcnt vmcnt(%3)" : "+v"(a), "+v"(b), "+v"(c) : "i"(N) : "memory"); }

constexpr int dn_younger(int k, int D, bool wo) {
    int n = 0, la = 0, lb = 0;
    for (int i = 0; i < D; ++i) { n += 2; if (i == k + 1) lb = n; }
    for (int i = 0; i < D; ++i) { n += 3; if (i == k) la = n; }
    for (int s = 0; s < k; ++s) {
        if (s + D < 24) { n += 2; if (s + D == k + 1) lb = n; }
        if (wo && s >= 16) n += 2;
        if (s + D < 24) { n += 3; if (s + D == k) la = n; }
    }
    return n - (la > lb ? la : lb);
}
constexpr float QSCALE = 0.22360679774997896f * 1.4426950408889634f;
constexpr float BOUND_MAX = 50.f;

struct AttSync { int lane; const char* kv; unsigned dvoff; unsigned wo; int w; };
__device__ __forceinline__ unsigned kvslot(int c) { return L_KV + ((c + 2) & 3) * CHUNK; }
__device__ __forceinline__ void att_dma(const AttSync& a, int c) { glds16x3_sc1(a.dvoff, a.kv + (size_t)c * CHUNK, kvslot(c) + a.wo); }
constexpr unsigned AP0 = 16;
#define RING_X(c) do { WAITV(3); lpost(C_AP + as.w, AP0 + (c) + 1); } while (0)
#define RP_A
#define RP_B
#define RING_Y(c, pk) do { RP_A lcheck(pk, C_AP, AP0 + (c) + 1, lane); const int cn_ = (c) + 3 < NCHUNK ? (c) + 3 : NCHUNK - 1;   \
                           glds16x3_sc1(as.dvoff, as.kv + (size_t)cn_ * CHUNK, kvslot((c) + 3) + as.wo); RP_B } while (0)

__device__ __forceinline__ void att_fast(const AttSync& as, bf16x8 Q0a, bf16x8 Q1a0, bf16x8 Q1a1, bf16x8 Q0b, bf16x8 Q1b0, bf16x8 Q1b1, f32x16& Oa, f32x16& Ob) {
    const int lane = as.lane, r = lane & 31, h = lane >> 5;
    const unsigned ak0 = lane * 16, ak1 = h ? KV_V + 20 * 16 : KV_K1 + r * 16;
    const unsigned av = KV_V + h * 352 + (r <= 20 ? r : 21) * 16;
    const f32x16 zero = {};
    f32x16 Sa, Sb = {};
    bf16x8 K0[2], K1[2], V0[2], V1[2];
    u32x4 Pa0, Pa1, Pb0 = {}, Pb1 = {};
    unsigned pk = 0;
    unsigned long long rp0 = 0, rp1 = 0;
    V0[1] = bf16x8{}; V1[1] = bf16x8{};
    unsigned vk0 = L_KV + ak0, vk1 = L_KV + ak1, vv = L_KV + av;
    PIN(vk0); PIN(vk1); PIN(vv);
    K0[0] = ldsf(vk0 + 2 * CHUNK); K1[0] = ldsf(vk1 + 2 * CHUNK); K0[1] = ldsf(vk0 + 2 * CHUNK + 1024); V1[0] = ldsf(vv + 2 * CHUNK + 704); V0[0] = ldsf(vv + 2 * CHUNK);
    Sa = MFMA(K0[0], Q0a, zero); Sa = MFMA(K1[0], Q1a0, Sa);
#define EX4(S, i) do { S[i] = __builtin_amdgcn_exp2f(S[i]); S[i + 1] = __builtin_amdgcn_exp2f(S[i + 1]); S[i + 2] = __builtin_amdgcn_exp2f(S[i + 2]); S[i + 3] = __builtin_amdgcn_exp2f(S[i + 3]); } while (0)
#define GAP(MF, LD, T, k, PW, j, S, i) do { MF; SBAR(); LD; PW[j] = pk2(T[k], T[k + 1]); PW[j + 1] = pk2(T[k + 2], T[k + 3]); PIN(PW); SBAR(); EX4(S, i); PIN(S); SBAR(); } while (0)
#define BC(x) __builtin_bit_cast(bf16x8, x)
#define STEP(e, d, QB1, QA1N, KN, L3, LK0, LK1, LV) do { \
        GAP(Ob = MFMA(V0[e ^ 1], BC(Pb0), Ob), (void)0,                  Sb, 12, Pb1, 2, Sa, 0); \
        GAP(Sb = MFMA(K0[e], Q0b, zero),       (void)0,                  Sa, 0, Pa0, 0, Sa, 4); \
        GAP(Sb = MFMA(K1[d], QB1, Sb),         L3,                       Sa, 4, Pa0, 2, Sa, 8); \
        GAP(Ob = MFMA(V1[e ^ 1], BC(Pb1), Ob), K0[e] = ldsf(LK0),        Sa, 8, Pa1, 0, Sa, 12); \
        GAP(Oa = MFMA(V0[e], BC(Pa0), Oa),     LK1,                      Sa, 12, Pa1, 2, Sb, 0); \
        GAP(Sa = MFMA(K0[e ^ 1], Q0a, zero),   V1[e ^ 1] = ldsf((LV) + 704), Sb, 0, Pb0, 0, Sb, 4); \
        GAP(Sa = MFMA(K1[KN], QA1N, Sa),       V0[e ^ 1] = ldsf(LV),     Sb, 4, Pb0, 2, Sb, 8); \
        GAP(Oa = MFMA(V1[e], BC(Pa1), Oa),     (void)0,                  Sb, 8, Pb1, 0, Sb, 12); \
    } while (0)
#define CHUNK4(c, B, N) do { \
        STEP(0, 0, Q1b0, Q1a1, 0, (void)0, vk0 + (B) + KVD, K1[1] = ldsf(vk1 + (B) + KVD), vv + (B) + KV_VT); \
        RING_X(c); \
        STEP(1, 0, Q1b1, Q1a0, 1, pk = lpeek(C_AP, lane), vk0 + (B) + KVD + 1024, (void)0, vv + (B) + KVD); \
        RING_Y(c, pk); \
        STEP(0, 1, Q1b0, Q1a1, 1, (void)0, vk0 + (N), K1[0] = ldsf(vk1 + (N)), vv + (B) + KVD + KV_VT); \
        STEP(1, 1, Q1b1, Q1a0, 0, (void)0, vk0 + (N) + 1024, (void)0, vv + (N)); \
    } while (0)
    for (int c = 0; c < NCHUNK; c += 4) {
        CHUNK4(c, 2 * CHUNK, 3 * CHUNK); CHUNK4(c + 1, 3 * CHUNK, 0); CHUNK4(c + 2, 0, CHUNK); CHUNK4(c + 3, CHUNK, 2 * CHUNK);
    }
#undef CHUNK4
    Pb1[2] = pk2(Sb[12], Sb[13]); Pb1[3] = pk2(Sb[14], Sb[15]);
    Ob = MFMA(V0[1], BC(Pb0), Ob); Ob = MFMA(V1[1], BC(Pb1), Ob);
#undef STEP
#undef GAP
#undef EX4
}

__device__ __forceinline__ void att_safe(const AttSync& as, bf16x8 Q0a, bf16x8 Q1a0, bf16x8 Q1a1, bf16x8 Q0b, bf16x8 Q1b0, bf16x8 Q1b1, f32x16& Oa, f32x16& Ob) {
    const int lane = as.lane, r = lane & 31, h = lane >> 5;
    const unsigned ak0 = lane * 16, ak1 = h ? KV_V + 20 * 16 : KV_K1 + r * 16;
    const unsigned av = KV_V + h * 352 + (r <= 20 ? r : 21) * 16;
    const f32x16 zero = {};
    float ma = -INFINITY, mb = -INFINITY;
    unsigned long long rp0 = 0, rp1 = 0;
    for (int c = 0; c < NCHUNK; ++c) {
        const unsigned b0 = kvslot(c);
        for (int t = 0; t < 4; ++t) {
            if (t == 1) RING_X(c);
            if (t == 2) RING_Y(c, 0u);
            const unsigned base = b0 + (t >> 1) * KVD; const int e = t & 1;
            bf16x8 K0 = ldsf(base + ak0 + e * 1024), K1 = ldsf(base + ak1), V0 = ldsf(base + av + e * KV_VT), V1 = ldsf(base + av + e * KV_VT + 704);
#pragma unroll
            for (int q = 0; q < 2; ++q) {
                f32x16 S = MFMA(K0, q ? Q0b : Q0a, zero); S = MFMA(K1, q ? (e ? Q1b1 : Q1b0) : (e ? Q1a1 : Q1a0), S);
                float mx = S[0];
#pragma unroll
                for (int i = 1; i < 16; ++i) mx = fmaxf(mx, S[i]);
                mx = fmaxf(mx, __shfl_xor(mx, 32));
                float& m = q ? mb : ma; f32x16& O = q ? Ob : Oa;
                const float mn = fmaxf(m, mx);
                const float al = __builtin_amdgcn_exp2f(m - mn);
                m = mn;
#pragma unroll
                for (int i = 0; i < 16; ++i) { O[i] *= al; S[i] = __builtin_amdgcn_exp2f(S[i] - mn); }
                O = MFMA(V0, pack8(S, 0, 1.f), O); O = MFMA(V1, pack8(S, 8, 1.f), O);
            }
        }
    }
}

__device__ __forceinline__ void xload8(u32x4 (&x)[5], u32x4 (&y)[5], const char* xb, unsigned vo, unsigned vo2, int skip) {
    asm volatile("s_cmp_lg_u32 %13, 0\n\ts_cbranch_scc1 1f\n\t"
                 "global_load_dwordx4 %0, %10, %12\n\tglobal_load_dwordx4 %1, %10, %12 offset:256\n\tglobal_load_dwordx4 %2, %10, %12 offset:512\n\t"
                 "global_load_dwordx4 %3, %10, %12 offset:768\n\tglobal_load_dwordx4 %4, %10, %12 offset:1024\n\t"
                 "global_load_dwordx4 %5, %11, %12\n\tglobal_load_dwordx4 %6, %11, %12 offset:256\n\tglobal_load_dwordx4 %7, %11, %12 offset:512\n\t"
                 "global_load_dwordx4 %8, %11, %12 offset:768\n\tglobal_load_dwordx4 %9, %11, %12 offset:1024\n1:"
                 : "=&v"(x[0]), "=&v"(x[1]), "=&v"(x[2]), "=&v"(x[3]), "=&v"(x[4]), "=&v"(y[0]), "=&v"(y[1]), "=&v"(y[2]), "=&v"(y[3]), "=&v"(y[4])
                 : "v"(vo), "v"(vo2), "s"(xb), "s"(skip) : "memory", "scc");
}
__device__ __forceinline__ void xload4(u32x4 (&x)[5], const char* xb, unsigned vo) {
    asm volatile("global_load_dwordx4 %0, %5, %6\n\tglobal_load_dwordx4 %1, %5, %6 offset:256\n\tglobal_load_dwordx4 %2, %5, %6 offset:512\n\t"
                 "global_load_dwordx4 %3, %5, %6 offset:768\n\tglobal_load_dwordx4 %4, %5, %6 offset:1024"
                 : "=&v"(x[0]), "=&v"(x[1]), "=&v"(x[2]), "=&v"(x[3]), "=&v"(x[4]) : "v"(vo), "s"(xb) : "memory");
}
__device__ __forceinline__ void ln_rows(const u32x4 (&xr)[5], int row0, int lane) {
    f32x2 xv[20];
    f32x2 s0 = {0.f, 0.f}, s1 = {0.f, 0.f}, q0 = {0.f, 0.f}, q1 = {0.f, 0.f};
#pragma unroll
    for (int i = 0; i < 20; i += 2) {
        const unsigned a = xr[i >> 2][i & 3], b = xr[i >> 2][(i & 3) + 1];
        xv[i] = f32x2{bflo(a), bfhi(a)}; xv[i + 1] = f32x2{bflo(b), bfhi(b)};
        s0 += xv[i]; q0 = __builtin_elementwise_fma(xv[i], xv[i], q0);
        s1 += xv[i + 1]; q1 = __builtin_elementwise_fma(xv[i + 1], xv[i + 1], q1);
    }
    s0 += s1; q0 += q1;
    const float s = dpp_sum16(s0[0] + s0[1]), ss = dpp_sum16(q0[0] + q0[1]);
    const float mean = s * (1.f / 640.f);
    const float var = fmaxf(ss * (1.f / 640.f) - mean * mean, 0.f);
    const float rs = __builtin_amdgcn_rcpf(rbf(__builtin_amdgcn_sqrtf(rbf(rbf(var) + 1e-5f)))), mo = -rbf(mean) * rs;
    const f32x2 rs2 = {rs, rs}, mo2 = {mo, mo};
    const int row = row0 + (lane >> 4), c = lane & 15;
    const unsigned wa = L_PANEL + (row >> 5) * 40960 + (c >> 1) * 1024 + (c & 1) * 512 + (((row & 31) ^ (c & 7)) << 4);
#pragma unroll
    for (int u = 0; u < 5; ++u) {
        u32x4 o;
#pragma unroll
        for (int e = 0; e < 4; ++e) { const f32x2 y = __builtin_elementwise_fma(xv[4 * u + e], rs2, mo2); o[e] = pk2(y[0], y[1]); }
        *LP(u32x4, wa + u * 8192) = o;
    }
}
#define BLOCK_BARRIER() asm volatile("s_waitcnt lgkmcnt(0)\n\ts_barrier" ::: "memory")

template <int PH>
__global__ void __launch_bounds__(512, 2) mega(Params p) {
    constexpr bool PRO = PH & 1, ATT = PH & 2, DN = PH & 8, WO = PH & 16;
    const int w = __builtin_amdgcn_readfirstlane(threadIdx.x >> 6);
    const int b = blockIdx.x, xc = b & 7, j = b >> 3;
    const int ti = (xc >> 1) * 8 + (j >> 2), g = (xc & 1) * 4 + (j & 3);
    constexpr bool FUSED = PH == 27;
    const int kmt = g & 1;
    const int ra = kmt * 32 + (w & 3) * 8, rb = (kmt ^ 1) * 32 + (w & 3) * 8;
    u32x4 x0[5], x1[5], x2[5], x3[5];
    if constexpr (PRO) {
        asm volatile("" :: "s"(p.x), "s"(p.ws), "s"(p.wq), "s"(p.wk));
        const char* xb = p.x + (size_t)ti * 64 * 1280;
        const unsigned xvo = ((threadIdx.x >> 4) & 3) * 1280 + (threadIdx.x & 15) * 16;
        xload8(x0, x1, xb + (size_t)ra * 1280, xvo, xvo + 5120, w >> 2);
        xload8(x2, x3, xb + (size_t)rb * 1280, xvo, xvo + 5120, w >> 2);
    }
    const char* pc; asm volatile("s_getpc_b64 %0" : "=s"(pc));
    unsigned tag = 1u;
    char* ws = p.ws;
    STAMP(0);
    if (threadIdx.x < C_N) *lcnt(threadIdx.x) = (threadIdx.x == C_WP || threadIdx.x == C_WP + 1 || threadIdx.x == C_PP || threadIdx.x == C_PP + 1) ? 100u : 0u;
    const bool haskv = (g >> 2) == (ti & 1);
    const char* wqb = p.wq + g * 160;
#define LANE_CONSTS() int lane = __builtin_amdgcn_mbcnt_hi(~0u, __builtin_amdgcn_mbcnt_lo(~0u, 0u)); PIN(lane); \
    const int r = lane & 31, h = lane >> 5, tq = (lane & 15) >> 2, tp = lane & 3, tb = (lane >> 4) & 1; (void)r; (void)h; (void)tq; (void)tp; (void)tb

    if (w >= 4) {
        LANE_CONSTS();
        const int v = w - 4;
        unsigned epv = 0u;
        if constexpr (FUSED) {
            asm volatile("global_load_dword %0, %1, %2 sc1" : "=v"(epv) : "v"(0u), "s"(p.ws + WS_EP) : "memory");
            constexpr unsigned CODE_PF = 65536;
            const unsigned line = (unsigned)(j * 4 + v) * 64u + lane;
            if (line * 128u < CODE_PF) l2touch(pc + line * 128u);
        }
#define GET_TAG() do { if constexpr (FUSED) { asm volatile("s_nop 0\n\tv_readfirstlane_b32 %0, %1\n\ts_nop 3" : "=s"(tag) : "v"(epv) : "memory"); tag += 0x9E3779B1u; } } while (0)
        __builtin_amdgcn_s_setprio(3);
        if constexpr (PRO) {
            const int kvm = 2 * ti + (g & 1); const bool isV = (g >> 1) & 1;
            const char* wkvb = isV ? p.wv : p.wk;
            if (haskv && v < 2) {
#pragma unroll
                for (int n = 0; n < 13; ++n) {
                    const unsigned o = v * 12800 + (n < 12 ? n * 1024 : 11776);
                    glds16(o + lane * 16, wkvb, L_WKV + o);
                }
            }
            BLOCK_BARRIER();
            if (v >= 2) {
                const int v2 = v - 2;
                unsigned wo5[5];
#pragma unroll
                for (int i = 0; i < 5; ++i) { const unsigned c = (v2 + 2 * i) * 64 + lane, row = c / 10u, col = c - row * 10u; wo5[i] = row * 1280 + col * 16; }
#pragma unroll
                for (int n = 0; n < 20; ++n) glds16(wo5[n % 5], wqb + (n / 5) * 64 * 1280, L_WQ + (v2 + 2 * n) * 1024);
                u32x4 wst[3][10];
                auto wst_issue = [&](auto I) {
                    constexpr int rr = I.value / 10, i = I.value % 10;
                    GLOAD16N(wst[rr][i], wo5[i % 5], wqb + (4 + 2 * rr + i / 5) * 64 * 1280);
                };
                static_for<0, 20>(wst_issue);
                WAITV(20);
                GET_TAG(); lpost(C_TAG, tag);
                lpost(C_PP + v, 1);
                STAMP(2);
                static_for<20, 30>(wst_issue);
                static_for<0, 3>([&](auto R) {
                    constexpr int rr = R.value;
                    waitv5<20 - 10 * rr>(wst[rr][0], wst[rr][1], wst[rr][2], wst[rr][3], wst[rr][4]); waitv5<63>(wst[rr][5], wst[rr][6], wst[rr][7], wst[rr][8], wst[rr][9]);
                    lwait_all(C_AP, 4, 3 + rr, lane);
#pragma unroll
                    for (int i = 0; i < 10; ++i) *LP(u32x4, L_WQ + (rr & 1) * 20480 + (v2 + 2 * i) * 1024 + lane * 16) = wst[rr][i];
                    lpost(C_WP + v, rr + 1);
                });
            } else {
              if (haskv) {
                f32x16 acc = {};
                const unsigned wa = L_WKV + (v * 320 + 8 * h + tq) * 40 + tb * 32 + (tb ? 0 : tp * 8);
                unsigned xa[4];
#pragma unroll
                for (int s = 0; s < 4; ++s) xa[s] = L_PANEL + kmt * 40960 + v * 20480 + h * 512 + ((r ^ ((2 * s + h) & 7)) << 4);
                WAITV(0);
                GET_TAG();
                STAMP(2);
                bf16x8 wf[20], xf[20];
#pragma unroll
                for (int s = 0; s < 20; ++s) wf[s] = ldstr2(wa + s * 640, wa + s * 640 + 160);
                lwait_all(C_AP, 4, 1, lane);
#pragma unroll
                for (int s = 0; s < 20; ++s) xf[s] = ldsf(xa[s & 3] + s * 1024);
                SBAR();
                if (isV) {
#pragma unroll
                    for (int s = 0; s < 20; ++s) acc = MFMA(xf[s], wf[s], acc);
                } else {
#pragma unroll
                    for (int s = 0; s < 20; ++s) acc = MFMA(wf[s], xf[s], acc);
                }
                if (v) {
#pragma unroll
                    for (int e = 0; e < 4; ++e) *LP(f32x4, L_WKV + 12800 + e * 1024 + lane * 16) = f32x4{acc[4 * e], acc[4 * e + 1], acc[4 * e + 2], acc[4 * e + 3]};
                    lpost(C_KVP, 1);
                } else {
                    lwait(C_KVP, 1);
#pragma unroll
                    for (int e = 0; e < 4; ++e) {
                        const f32x4 t = *LP(const f32x4, L_WKV + 12800 + e * 1024 + lane * 16);
                        acc[4 * e] += t[0]; acc[4 * e + 1] += t[1]; acc[4 * e + 2] += t[2]; acc[4 * e + 3] += t[3];
                    }
                    const __amdgpu_buffer_rsrc_t rk = mkrsrc(ws + WS_KV, 32 * KVD);
                    const unsigned base = ti * KVD, ke = g & 1;
                    const u32x4 f0 = __builtin_bit_cast(u32x4, pack8(acc, 0, 1.f)), f1 = __builtin_bit_cast(u32x4, pack8(acc, 8, 1.f));
                    float mx;
                    if (!isV) {
                        __builtin_amdgcn_raw_buffer_store_b128(f0, rk, base + ke * 1024 + lane * 16, 0, 16);
                        const u32x2 k1 = {f1[0], f1[1]};
                        if (!h) __builtin_amdgcn_raw_buffer_store_b64(k1, rk, base + KV_K1 + r * 16 + ke * 8, 0, 16);
                        lpost(C_KVFIN, 1);
                        float n2 = 0.f;
#pragma unroll
                        for (int e = 0; e < 4; ++e) n2 += bflo(f0[e]) * bflo(f0[e]) + bfhi(f0[e]) * bfhi(f0[e]);
                        if (!h) n2 += bflo(f1[0]) * bflo(f1[0]) + bfhi(f1[0]) * bfhi(f1[0]) + bflo(f1[1]) * bflo(f1[1]) + bfhi(f1[1]) * bfhi(f1[1]);
                        n2 += __shfl_xor(n2, 32);
                        mx = (n2 == n2) ? n2 : INFINITY;
                    } else {
                        const unsigned cst = r == 20 ? 0x3f803f80u : 0u;
                        const u32x4 c4 = {cst, cst, cst, cst};
                        if (r <= 21) {
                            __builtin_amdgcn_raw_buffer_store_b128(r >= 20 ? c4 : f0, rk, base + KV_V + ke * KV_VT + h * 352 + r * 16, 0, 16);
                            __builtin_amdgcn_raw_buffer_store_b128(r >= 20 ? c4 : f1, rk, base + KV_V + ke * KV_VT + 704 + h * 352 + r * 16, 0, 16);
                        }
                        lpost(C_KVFIN, 1);
                        float vm = 0.f;
#pragma unroll
                        for (int e = 0; e < 16; ++e) { const float a = fabsf(acc[e]); vm = (a <= vm) ? vm : ((a == a) ? a : INFINITY); }
                        mx = r < 20 ? vm : 0.f;
                    }
                    mx = wave_max(mx);
                    WAITV(0);
                    STAMP(15);
                    if (lane < 8) __hip_atomic_store((gu64*)(ws + WS_KF + lane * 1024 + (isV ? 512 : 0)) + kvm, ((unsigned long long)tag << 32) | __float_as_uint(mx), RLX_AGENT);
                }
            }
              else { WAITV(0); GET_TAG(); }
              STAMP(3);
            }
            if (haskv) lwait(C_KVFIN, 1);
#pragma unroll
            for (int n = 0; n < 2; ++n) {
                unsigned L = n * 64 + lane; L = (j * 4 + v) * 120 + (L < 120u ? L : 119u);
                const unsigned m3 = L >= 7680u, rem = L - (m3 ? 7680u : 0u), row = rem / 12u;
                l2touch((m3 ? p.w3 : p.w1) + row * 3072 + (xc & 1) * 1536 + (rem - row * 12u) * 128);
            }
            lwait_all(C_AP, 4, GU_DELAY, lane);
            STAMP(4);
            {
                const char* w1s = p.w1 + g * 384 + v * 96; const char* w3s = p.w3 + g * 384 + v * 96;
                auto goff = [](unsigned idx) { const unsigned row = idx / 6u; return row * 3072 + (idx - row * 6u) * 16; };
                const unsigned gva = goff(lane), gvb = goff((lane - 8) & 63), gvc = goff(64 + (h ? (r - 8) & 31 : r));
                const unsigned gdst = L_GU + v * 9216;
                auto gdma = [&](auto S) { constexpr int s = S.value; glds_gu(gva, gvb, gvc, w1s + (size_t)s * 16 * 3072, w3s + (size_t)s * 16 * 3072, gdst + (s % 3) * 3072); };
                static_for<0, 3>(gdma);
                f32x16 acc[2][3] = {};
                unsigned bn[3][2], pa[4], pb[4];
#pragma unroll
                for (int n = 0; n < 3; ++n)
#pragma unroll
                    for (int t = 0; t < 2; ++t) {
                        const unsigned idx = 6 * (8 * h + 4 * t + tq) + 2 * n + (tp >> 1);
                        const unsigned o = idx < 64u ? (tb ? 1024 + ((idx + 8) & 63) * 16 : idx * 16) : (tb ? 2560 + ((idx - 56) & 31) * 16 : 2048 + (idx - 64) * 16);
                        bn[n][t] = gdst + o + (tp & 1) * 8;
                    }
#pragma unroll
                for (int s = 0; s < 4; ++s) { pa[s] = L_PANEL + h * 512 + ((r ^ ((2 * s + h) & 7)) << 4); pb[s] = pa[s] + 40960; PIN(pb[s]); }
                bf16x8 wf[2][3], xa[2], xb2[2];
                auto rdw = [&](auto S) { constexpr int s = S.value;
#pragma unroll
                    for (int n = 0; n < 3; ++n) wf[s & 1][n] = ldstr2(bn[n][0] + (s % 3) * 3072, bn[n][1] + (s % 3) * 3072); };
                auto rdx = [&](auto S) { constexpr int s = S.value; xa[s & 1] = ldsf(pa[s & 3] + s * 1024); xb2[s & 1] = ldsf(pb[s & 3] + s * 1024); };
                WAITV(8);
                rdx(IC<0>{}); rdw(IC<0>{});
                asm volatile("s_waitcnt lgkmcnt(0)" ::: "memory");
                gdma(IC<3>{});
                static_for<0, 40>([&](auto S) {
                    constexpr int s = S.value; constexpr bool nx = s + 1 < 40;
                    MPRIO(GU_HI, 0); acc[0][0] = MFMA(wf[s & 1][0], xa[s & 1], acc[0][0]); SBAR();
                    if constexpr (nx) rdx(IC<s + 1>{});
                    SBAR();
                    MPRIO(GU_HI, 1); acc[1][0] = MFMA(wf[s & 1][0], xb2[s & 1], acc[1][0]); SBAR();
                    if constexpr (nx) { WAITVN(4 * ((s + 3 < 39 ? s + 3 : 39) - (s + 1))); rdw(IC<s + 1>{}); }
                    SBAR();
                    MPRIO(GU_HI, 2); acc[0][1] = MFMA(wf[s & 1][1], xa[s & 1], acc[0][1]); SBAR();
                    MPRIO(GU_HI, 3); acc[1][1] = MFMA(wf[s & 1][1], xb2[s & 1], acc[1][1]); SBAR();
                    if constexpr (s + 4 < 40) { asm volatile("s_waitcnt lgkmcnt(0)" ::: "memory"); gdma(IC<s + 4>{}); }
                    SBAR();
                    MPRIO(GU_HI, 4); acc[0][2] = MFMA(wf[s & 1][2], xa[s & 1], acc[0][2]); SBAR();
                    MPRIO(GU_HI, 5); acc[1][2] = MFMA(wf[s & 1][2], xb2[s & 1], acc[1][2]); SBAR();
                });
                lpost(C_GP + v, 42);
                __builtin_amdgcn_s_setprio(3);
                STAMP(6);
                const __amdgpu_buffer_rsrc_t rh = mkrsrc(ws + WS_HT, 64 * 96 * 1024);
#pragma unroll
                for (int m = 0; m < 2; ++m)
#pragma unroll
                    for (int n = 0; n < 3; ++n) {
                        f32x16 hv;
#pragma unroll
                        for (int e = 0; e < 8; ++e) {
                            const float gg = acc[m][n][e];
                            hv[e] = gg * __builtin_amdgcn_rcpf(1.f + __builtin_amdgcn_exp2f(-1.4426950408889634f * gg)) * acc[m][n][e + 8];
                        }
                        __builtin_amdgcn_raw_buffer_store_b128(__builtin_bit_cast(u32x4, pack8(hv, 0, 1.f)), rh, ((2 * ti + m) * 96 + g * 12 + 3 * v + n) * 1024 + lane * 16, 0, 16);
                    }
                if constexpr (DN) {
                    WAITV(0);
                    if (lane < 2) __hip_atomic_store((gu32*)(ws + WS_HF) + (2 * ti + lane) * 32 + g * 4 + v, tag, RLX_AGENT);
                }
                __builtin_amdgcn_s_setprio(0);
                if constexpr (DN) {
#pragma unroll
                    for (int n = 0; n < 2; ++n) {
                        unsigned L = (j * 4 + v) * 85 + n * 64 + lane; L = L < 10880u ? L : 10879u;
                        const unsigned row = L / 5u;
                        l2touch((row < 1536u ? p.w2 + row * 1280 : p.wo + (row - 1536u) * 1280) + (xc & 1) * 640 + (L - row * 5u) * 128);
                    }
                }
                STAMP(7);
            }
        } else {
            BLOCK_BARRIER();
        }
        if constexpr (DN || WO) {
            LANE_CONSTS();
            const unsigned bq = ((tb * 32 + (tp >> 1) * 16) ^ ((unsigned)(tq >> 1) << 6)) + (tp & 1) * 8;
            const unsigned drow = lane >> 3, dcol = ((lane & 7) ^ (((lane >> 4) & 1) << 2)) * 16;
            const int G = xc >> 1, r0 = G < 2 ? 6 * G : 12 + 5 * (G - 2), nr = G < 2 ? 6 : 5;
            if constexpr (FUSED) {
                if (b == 255 && v == 0) {
                    gu32* f = (gu32*)(ws + WS_HF) + (lane >> 3) * 64 + (lane & 7) * 4;
                    _Pragma("clang loop unroll(disable)") for (unsigned spins = 0; spins < (1u << 14); ++spins) {
                        bool ok = true;
#pragma unroll
                        for (int n = 0; n < 4; ++n) ok &= __hip_atomic_load(f + n * 512, RLX_AGENT) == tag;
                        if (__all(ok)) break;
                        TMO(spins, 1u << 14, 303, tag);
                        __builtin_amdgcn_s_sleep(32);
                    }
                    if (lane == 0) __hip_atomic_store((gu32*)(ws + WS_EP), tag, RLX_AGENT);
                }
            }
            if (j < 5 * nr) {
                const int dr = r0 + j / 5, dc = (xc & 1) * 5 + j % 5;
                const int mfirst = dr < 20 ? 3 * dr : 60 + 2 * (dr - 20), nm = dr < 20 ? 3 : 2;
                const unsigned dvo = drow * 1280 + dcol;
                const unsigned myring = L_PANEL + v * 20480;
                f32x16 acc0 = {}, acc1 = {};
                if constexpr (PRO) { lwait_all(C_GP, 4, 42, lane); lwait_all(C_AP, 4, 8, lane); }
                if constexpr (DN) {
                    f32x16 da[3][2] = {};
                    const int drot = (j % 5) * 4;
                    const char* bsrc = p.w2 + (size_t)v * 384 * 1280 + dc * 128;
                    const char* asrc = ws + WS_HT + ((size_t)mfirst * 96 + v * 24) * 1024;
                    const unsigned am1 = lane * 16 + 98304, am2 = lane * 16 + (nm == 3 ? 196608 : 98304);
                    constexpr int DQ = DN_DQ;
                    u32x4 aq[DQ][3];
                    auto bissue = [&](auto K) {
                        constexpr int k = K.value;
                        int kr = k + drot; kr = kr >= 24 ? kr - 24 : kr;
                        const char* sb = bsrc + (size_t)kr * 16 * 1280;
                        glds16(dvo, sb, myring + (k % 10) * 2048); glds16(dvo + 8 * 1280, sb, myring + (k % 10) * 2048 + 1024);
                    };
                    auto woissue = [&](auto U) {
                        constexpr int u = U.value;
                        const char* sb = p.wo + (size_t)(v * 160 + u * 16) * 1280 + dc * 128;
                        glds16(dvo, sb, myring + u * 2048); glds16(dvo + 8 * 1280, sb, myring + u * 2048 + 1024);
                    };
                    auto aissue = [&](auto K) {
                        constexpr int k = K.value;
                        int kr = k + drot; kr = kr >= 24 ? kr - 24 : kr;
                        const char* sb = asrc + (size_t)kr * 1024;
                        GLOAD16S(aq[k % DQ][0], lane * 16, sb); GLOAD16S(aq[k % DQ][1], am1, sb); GLOAD16S(aq[k % DQ][2], am2, sb);
                    };
                    static_for<0, DQ>(bissue);
                    if constexpr (PRO) {
                        gu32* f = (gu32*)(ws + WS_HF) + (mfirst + ((lane >> 3) < nm ? (lane >> 3) : 0)) * 32 + 8 * v + (lane & 7);
                        _Pragma("clang loop unroll(disable)") for (unsigned spins = 0; spins < (1u << 14); ++spins) {
                            if (__all(__hip_atomic_load(f, RLX_AGENT) == tag)) break;
                            TMO(spins, 1u << 14, 302, tag);
                            __builtin_amdgcn_s_sleep(HF_SLEEP);
                        }
                        CFENCE();
                    }
                    STAMP(8);
                    static_for<0, DQ>(aissue);
                    const unsigned ba0 = myring + (4 * h + tq) * 128 + bq, ba1 = ba0 ^ 64;
                    bf16x8 w0[2], w1[2];
                    WAITVN(3 * DQ);
                    w0[0] = ldstr2(ba0, ba0 + 1024); w1[0] = ldstr2(ba1, ba1 + 1024);
                    static_for<0, 24>([&](auto K) {
                        constexpr int k = K.value;
                        waitv3<dn_younger(k, DQ, WO)>(aq[k % DQ][0], aq[k % DQ][1], aq[k % DQ][2]);
                        constexpr unsigned o = ((k + 1) % 10) * 2048;
#define DXF(m) __builtin_bit_cast(bf16x8, aq[k % DQ][m])
                        SBAR();
                        MPRIO(DN_HI, 0); da[0][0] = MFMA(w0[k & 1], DXF(0), da[0][0]); SBAR();
                        if constexpr (k + 1 < 24) w0[(k + 1) & 1] = ldstr2(ba0 + o, ba0 + o + 1024);
                        SBAR();
                        MPRIO(DN_HI, 1); da[0][1] = MFMA(w1[k & 1], DXF(0), da[0][1]); SBAR();
                        if constexpr (k + 1 < 24) w1[(k + 1) & 1] = ldstr2(ba1 + o, ba1 + o + 1024);
                        SBAR();
                        MPRIO(DN_HI, 2); da[1][0] = MFMA(w0[k & 1], DXF(1), da[1][0]); SBAR();
                        if constexpr (k + DQ < 24) bissue(IC<k + DQ>{});
                        if constexpr (WO && k >= 16) woissue(IC<(k + 8) % 10>{});
                        SBAR();
                        MPRIO(DN_HI, 3); da[1][1] = MFMA(w1[k & 1], DXF(1), da[1][1]); SBAR();
                        MPRIO(DN_HI, 4); da[2][0] = MFMA(w0[k & 1], DXF(2), da[2][0]); SBAR();
                        MPRIO(DN_HI, 5); da[2][1] = MFMA(w1[k & 1], DXF(2), da[2][1]); SBAR();
                        if constexpr (k + DQ < 24) aissue(IC<k + DQ>{});
                    });
                    __builtin_amdgcn_s_setprio(3);
                    STAMP(9);
                    if constexpr (WO) { CFENCE(); woissue(IC<2>{}); woissue(IC<3>{}); }
#pragma unroll
                    for (int n = 0; n < 2; ++n) {
#pragma unroll
                        for (int m = 0; m < 3; ++m) {
                            if (m != v) {
                                const unsigned dst = L_GU + (m * 3 + (v > m ? v - 1 : v)) * 4096 + lane * 16;
#pragma unroll
                                for (int e = 0; e < 4; ++e) *LP(f32x4, dst + e * 1024) = f32x4{da[m][n][4 * e], da[m][n][4 * e + 1], da[m][n][4 * e + 2], da[m][n][4 * e + 3]};
                            }
                        }
                        lpost(C_DP + v, 2 * n + 1); lwait_all(C_DP, 4, 2 * n + 1, lane);
                        if (v < 3) {
                            f32x16 t = v == 0 ? da[0][n] : (v == 1 ? da[1][n] : da[2][n]);
#pragma unroll
                            for (int o = 0; o < 3; ++o)
#pragma unroll
                                for (int e = 0; e < 4; ++e) {
                                    const f32x4 u = *LP(const f32x4, L_GU + (v * 3 + o) * 4096 + e * 1024 + lane * 16);
                                    t[4 * e] += u[0]; t[4 * e + 1] += u[1]; t[4 * e + 2] += u[2]; t[4 * e + 3] += u[3];
                                }
                            if (n) acc1 = t; else acc0 = t;
                        }
                        if (n == 0) { lpost(C_DP + v, 2); lwait_all(C_DP, 4, 2, lane); }
                    }
                    if constexpr (!WO) {
                        if (v < 3) {
                            float* pt = (float*)(ws + WS_PT) + (size_t)(b * 3 + v) * 2048 + lane;
#pragma unroll
                            for (int e = 0; e < 16; ++e) { pt[e * 64] = acc0[e]; pt[1024 + e * 64] = acc1[e]; }
                        }
                    }
                } else {
#pragma unroll
                    for (int u = 0; u < 20; ++u) glds16(dvo + u * 8 * 1280, p.wo + (size_t)v * 160 * 1280 + dc * 128, myring + u * 1024);
                    if (v < 3) {
                        const float* pt = (const float*)(ws + WS_PT) + (size_t)(b * 3 + v) * 2048 + lane;
#pragma unroll
                        for (int e = 0; e < 16; ++e) { acc0[e] = pt[e * 64]; acc1[e] = pt[1024 + e * 64]; }
                    }
                }
                if constexpr (WO) { WAITV(0); lpost(C_DP + v, 4); }
                if constexpr (WO && ATT) {
                    if (v == 3) {
                        __builtin_amdgcn_s_setprio(0);
                        const int mi = (lane >> 4) < nm ? (lane >> 4) : 0;
                        gu64* f = (gu64*)(ws + WS_OF + (mfirst + mi) * 128) + (lane & 15);
                        _Pragma("clang loop unroll(disable)") for (unsigned spins = 0; spins < (1u << 14); ++spins) {
                            const unsigned long long x = __hip_atomic_load(f, RLX_AGENT);
                            const unsigned long long ok = __ballot((unsigned)x == tag && (unsigned)(x >> 32) == tag);
                            const unsigned mine = (unsigned)(ok >> ((lane & 3) * 16));
                            *((volatile AS3 unsigned*)lcnt(C_OS) + (lane & 3)) = ((mine & 0xffu) == 0xffu ? 1u : 0u) | ((mine & 0xff00u) == 0xff00u ? 2u : 0u);
                            if (ok == ~0ull) break;
                            TMO(spins, 1u << 14, 300, tag);
                            __builtin_amdgcn_s_sleep(OF_SLEEP);
                        }
                    }
                }
                if (v < nm) {
                    const int m = mfirst + v;
                    const unsigned ba0 = L_PANEL + (4 * h + tq) * 128 + bq, ba1 = ba0 ^ 64;
                    if constexpr (WO) {
                        const char* xrow = p.x + (size_t)(m * 32 + r) * 1280 + dc * 128 + h * 16;
                        u32x4 xres[4];
#pragma unroll
                        for (int e = 0; e < 4; ++e) xres[e] = *(const u32x4*)(xrow + e * 32);
                        lwait_all(C_DP, 4, 4, lane);
                        const __amdgpu_buffer_rsrc_t ro = mkrsrc(ws + WS_OT + (size_t)m * 40960, 40960);
                        int hf = 0;
                        f32x16 t0[2] = {}, t1[2] = {};
#pragma unroll
                        for (int it = 0; it < 2; ++it) {
                            if constexpr (ATT) {
                                _Pragma("clang loop unroll(disable)") for (unsigned spins = 0; spins < (1u << 17); ++spins) {
                                    const unsigned ok = __builtin_amdgcn_readfirstlane(*((volatile AS3 unsigned*)lcnt(C_OS) + v));
                                    if (it == 0) { if (ok) { hf = (ok & 1) ? 0 : 1; break; } }
                                    else if (ok == 3) break;
                                    __builtin_amdgcn_s_sleep(1);
                                }
                                CFENCE();
                            }
                            if (it == 0) STAMP(10);
                            u32x4 of[20];
#pragma unroll
                            for (int s = 0; s < 20; ++s) of[s] = __builtin_amdgcn_raw_buffer_load_b128(ro, lane * 16 + s * 1024, hf * 20480, 16);
                            const unsigned a0 = ba0 + hf * 40960, a1 = ba1 + hf * 40960, l0 = a0 + (36 * h + 16) * 128, l1 = a1 + (36 * h + 16) * 128;
                            bf16x8 w0[2], w1[2];
                            w0[0] = ldstr2(a0, a0 + 1024); w1[0] = ldstr2(a1, a1 + 1024);
                            static_for<0, 20>([&](auto S) {
                                constexpr int s = S.value, n = s + 1, gg = n / 5, jj = n % 5;
                                const bf16x8 xf = __builtin_bit_cast(bf16x8, of[s]);
                                if constexpr (n < 20) {
                                    if constexpr (jj < 4) {
                                        constexpr unsigned o = (80 * gg + 20 * jj) * 128;
                                        w0[n & 1] = ldstr2(a0 + o, a0 + o + 1024); w1[n & 1] = ldstr2(a1 + o, a1 + o + 1024);
                                    } else {
                                        constexpr unsigned o = 80 * gg * 128;
                                        w0[n & 1] = ldstr2(l0 + o, l0 + o + 2560); w1[n & 1] = ldstr2(l1 + o, l1 + o + 2560);
                                    }
                                }
                                SBAR();
                                t0[it] = MFMA(w0[s & 1], xf, t0[it]); t1[it] = MFMA(w1[s & 1], xf, t1[it]);
                                SBAR();
                            });
                            hf ^= 1;
                        }
                        acc0 += t0[0] + t0[1]; acc1 += t1[0] + t1[1];
                        char* orow = p.out + (size_t)(m * 32 + r) * 1280 + dc * 128 + h * 16;
#pragma unroll
                        for (int n = 0; n < 2; ++n) {
                            const f32x16& a = n ? acc1 : acc0;
#pragma unroll
                            for (int qq = 0; qq < 2; ++qq) {
                                float lo[4], hi[4];
#pragma unroll
                                for (int e = 0; e < 4; ++e) {
                                    auto sw = __builtin_amdgcn_permlane32_swap(__float_as_uint(a[8 * qq + e]), __float_as_uint(a[8 * qq + 4 + e]), false, false);
                                    lo[e] = __uint_as_float(sw[0]); hi[e] = __uint_as_float(sw[1]);
                                }
                                const u32x4 xv = xres[2 * n + qq];
                                const u32x4 ov = {pk2(lo[0] + bflo(xv[0]), lo[1] + bfhi(xv[0])), pk2(lo[2] + bflo(xv[1]), lo[3] + bfhi(xv[1])),
                                                  pk2(hi[0] + bflo(xv[2]), hi[1] + bfhi(xv[2])), pk2(hi[2] + bflo(xv[3]), hi[3] + bfhi(xv[3]))};
                                *(u32x4*)(orow + (2 * n + qq) * 32) = ov;
                            }
                        }
                        STAMP(11);
                    }
                }
            }
        }
    } else {
        LANE_CONSTS();
        bf16x8 Q0a, Q0b; u32x2 q1a, q1b;
        const int head = 4 * g + w;
        const AttSync as{lane, ws + WS_KV, (unsigned)((w < 3 ? w * 3072 : CHUNK - 3072) + lane * 16), (unsigned)(w < 3 ? w * 3072 : CHUNK - 3072), w};
        __builtin_amdgcn_s_setprio(3);
        unsigned long long fk = 0, fv = 0; bool early = false;
        if constexpr (PRO) {
            BLOCK_BARRIER();
            waitv5<15>(x0[0], x0[1], x0[2], x0[3], x0[4]);
            STAMP(1);
            ln_rows(x0, ra, lane);
            waitv5<10>(x1[0], x1[1], x1[2], x1[3], x1[4]);
            ln_rows(x1, ra + 4, lane);
            lpost(C_AP + w, 1);
            waitv5<5>(x2[0], x2[1], x2[2], x2[3], x2[4]);
            ln_rows(x2, rb, lane);
            waitv5<0>(x3[0], x3[1], x3[2], x3[3], x3[4]);
            ln_rows(x3, rb + 4, lane);
            lpost(C_AP + w, 2);
            unsigned pa[4];
#pragma unroll
            for (int s = 0; s < 4; ++s) pa[s] = L_PANEL + h * 512 + ((r ^ ((2 * s + h) & 7)) << 4);
            f32x16 qa = {}, qb = {};
            const unsigned wa = L_WQ + (8 * h + tq) * 160 + w * 40 + tb * 32 + (tb ? 0 : tp * 8);
            lwait_all(C_AP, 4, 2, lane); lwait_all(C_PP, 4, 1, lane);
            if constexpr (FUSED) tag = __builtin_amdgcn_readfirstlane(*(volatile AS3 unsigned*)lcnt(C_TAG));
            STAMP(2);
            bf16x8 fw[2][4], fa[2][4], fb[2][4];
#pragma unroll
            for (int s4 = 0; s4 < 4; ++s4) { fw[0][s4] = ldstr2(wa + s4 * 2560, wa + s4 * 2560 + 640); fa[0][s4] = ldsf(pa[s4] + s4 * 1024); fb[0][s4] = ldsf(pa[s4] + 40960 + s4 * 1024); }
            unsigned pk = 0;
            static_for<0, 10>([&](auto S) {
                constexpr int st = S.value, nx = st + 1;
                if constexpr (st == 3 || st == 5 || st == 7) lcheck(pk, C_WP, (st - 1) / 2, lane);
                if constexpr (st == 6 && ATT) gload8x2_sc1(fk, fv, lane * 8, ws + WS_KF + xc * 1024);
                if constexpr (st == 8 && ATT) {
                    waitv8x2(fk, fv);
                    early = __all((unsigned)(fk >> 32) == tag && (unsigned)(fv >> 32) == tag);
                    if (early) { lwait_all(C_AP, 4, 6, lane); att_dma(as, 0); att_dma(as, 1); }
                    STAMP(8);
                }
#pragma unroll
                for (int s4 = 0; s4 < 4; ++s4) {
                    qa = MFMA(fw[st & 1][s4], fa[st & 1][s4], qa); SBAR();
                    if constexpr (nx < 10) { const unsigned o = wa + (nx & 3) * 10240 + s4 * 2560; fw[nx & 1][s4] = ldstr2(o, o + 640); SBAR(); }
                    qb = MFMA(fw[st & 1][s4], fb[st & 1][s4], qb); SBAR();
                    if constexpr (nx < 10) { fa[nx & 1][s4] = ldsf(pa[s4] + (nx * 4 + s4) * 1024); fb[nx & 1][s4] = ldsf(pa[s4] + 40960 + (nx * 4 + s4) * 1024); SBAR(); }
                    if (s4 == 2 && (st == 2 || st == 4 || st == 6)) { pk = lpeek(C_WP, lane); SBAR(); }
                }
                if constexpr (!(st & 1) && st <= 6) lpost(C_AP + w, 3 + st / 2);
            });
            lpost(C_AP + w, 8);
            Q0a = pack8(qa, 0, QSCALE); Q0b = pack8(qb, 0, QSCALE);
            q1a = u32x2{pk2(qa[8] * QSCALE, qa[9] * QSCALE), pk2(qa[10] * QSCALE, qa[11] * QSCALE)};
            q1b = u32x2{pk2(qb[8] * QSCALE, qb[9] * QSCALE), pk2(qb[10] * QSCALE, qb[11] * QSCALE)};
            if (h) { q1a = u32x2{0u, 0u}; q1b = u32x2{0u, 0u}; }
            if constexpr (!ATT) {
                char* qt = ws + WS_QT + (size_t)((2 * ti) * 32 + head) * 2048 + lane * 16;
                *(bf16x8*)qt = Q0a; *(u32x2*)(qt + 1024) = q1a; *(bf16x8*)(qt + 65536) = Q0b; *(u32x2*)(qt + 65536 + 1024) = q1b;
            }
            STAMP(3);
        } else {
            BLOCK_BARRIER();
            if constexpr (ATT) {
                const char* qt = ws + WS_QT + (size_t)((2 * ti) * 32 + head) * 2048 + lane * 16;
                Q0a = *(const bf16x8*)qt; q1a = *(const u32x2*)(qt + 1024); Q0b = *(const bf16x8*)(qt + 65536); q1b = *(const u32x2*)(qt + 65536 + 1024);
            }
        }
        if constexpr (ATT) {
            float na = 0.f, nb = 0.f;
            {
                const u32x4 a0 = __builtin_bit_cast(u32x4, Q0a), b0 = __builtin_bit_cast(u32x4, Q0b);
#pragma unroll
                for (int e = 0; e < 4; ++e) { na += bflo(a0[e]) * bflo(a0[e]) + bfhi(a0[e]) * bfhi(a0[e]); nb += bflo(b0[e]) * bflo(b0[e]) + bfhi(b0[e]) * bfhi(b0[e]); }
#pragma unroll
                for (int e = 0; e < 2; ++e) { na += bflo(q1a[e]) * bflo(q1a[e]) + bfhi(q1a[e]) * bfhi(q1a[e]); nb += bflo(q1b[e]) * bflo(q1b[e]) + bfhi(q1b[e]) * bfhi(q1b[e]); }
                na += __shfl_xor(na, 32); nb += __shfl_xor(nb, 32);
            }
            if (!early) {
                gu64* f = (gu64*)(ws + WS_KF + xc * 1024) + lane;
                _Pragma("clang loop unroll(disable)") for (unsigned spins = 0; spins < (1u << 14); ++spins) {
                    fk = __hip_atomic_load(f, RLX_AGENT); fv = __hip_atomic_load(f + 64, RLX_AGENT);
                    if (__all((unsigned)(fk >> 32) == tag && (unsigned)(fv >> 32) == tag)) break;
                    TMO(spins, 1u << 14, 301, tag);
                    __builtin_amdgcn_s_sleep(4);
                }
                if constexpr (PRO) lwait_all(C_AP, 4, 6, lane);
                att_dma(as, 0); att_dma(as, 1);
            }
            STAMP(4);
            if constexpr (PRO) lwait_all(C_AP, 4, 8, lane);
            att_dma(as, 2);
            const float kn2 = wave_max(__uint_as_float((unsigned)fk)), vmx = wave_max(__uint_as_float((unsigned)fv));
            const float Ma = __builtin_sqrtf(na * kn2) * 1.01f, Mb = __builtin_sqrtf(nb * kn2) * 1.01f;
            const bool fast = __all(Ma <= BOUND_MAX && Mb <= BOUND_MAX && vmx <= 1e30f);
            const unsigned sa = (fast && h) ? pk2(-Ma, 0.f) : q1a[0], sb = (fast && h) ? pk2(-Mb, 0.f) : q1b[0];
            const u32x4 a0 = {sa, q1a[1], 0u, 0u}, a1 = {h ? sa : 0u, 0u, q1a[0], q1a[1]}, b0 = {sb, q1b[1], 0u, 0u}, b1 = {h ? sb : 0u, 0u, q1b[0], q1b[1]};
            f32x16 Oa = {}, Ob = {};
            WAITV(6); lpost(C_AP + w, AP0); lwait_all(C_AP, 4, AP0, lane);
            __builtin_amdgcn_s_setprio(ATT_PRIO);
            STAMP(5); STAMPC(12);
            if (fast) att_fast(as, Q0a, BC(a0), BC(a1), Q0b, BC(b0), BC(b1), Oa, Ob);
            else att_safe(as, Q0a, BC(a0), BC(a1), Q0b, BC(b0), BC(b1), Oa, Ob);
            STAMP(6); STAMPC(13);
            const __amdgpu_buffer_rsrc_t ro = mkrsrc(ws + WS_OT, 64 * 40 * 1024);
#pragma unroll
            for (int q = 0; q < 2; ++q) {
                const f32x16& O = q ? Ob : Oa;
                const float inv = 1.f / __shfl(O[8], r + 32);
                const unsigned base = ((2 * ti + q) * 40 + 5 * g) * 1024;
                __builtin_amdgcn_raw_buffer_store_b128(__builtin_bit_cast(u32x4, pack8(O, 0, inv)), ro, base + w * 1024 + lane * 16, 0, 16);
                if (!h) {
                    u32x2 t = {pk2(O[8] * inv, O[9] * inv), pk2(O[10] * inv, O[11] * inv)};
                    __builtin_amdgcn_raw_buffer_store_b64(t, ro, base + 4096 + ((w >> 1) * 32 + r) * 16 + (w & 1) * 8, 0, 16);
                }
            }
            if constexpr (WO) {
                WAITV(0);
                if (lane < 2) __hip_atomic_store((gu32*)(ws + WS_OF) + (2 * ti + lane) * 32 + head, tag, RLX_AGENT);
            }
            STAMP(7);
        }
    }
}

template <int PH>
static void launch(const Params& p, hipStream_t stream) {
    (void)hipFuncSetAttribute((const void*)mega<PH>, hipFuncAttributeMaxDynamicSharedMemorySize, L_TOTAL);
    mega<PH><<<256, 512, L_TOTAL, stream>>>(p);
}

extern "C" void kernel_launch(void* const* d_in, const int* in_sizes, int n_in, void* d_out, int out_size, void* d_ws, size_t ws_size, hipStream_t stream) {
    Params p;
    p.x = (const char*)d_in[0]; p.wq = (const char*)d_in[1]; p.wk = (const char*)d_in[2]; p.wv = (const char*)d_in[3]; p.wo = (const char*)d_in[4];
    p.w1 = (const char*)d_in[6]; p.w3 = (const char*)d_in[7]; p.w2 = (const char*)d_in[8];
    p.out = (char*)d_out; p.ws = (char*)d_ws;
    launch<27>(p, stream);
}
```

```cpp
#include <hip/hip_runtime.h>
#include <hip/hip_bf16.h>
#include <stdint.h>

#define AS1 __attribute__((address_space(1)))
#define AS3 __attribute__((address_space(3)))
typedef __bf16 bf16x2 __attribute__((ext_vector_type(2)));
typedef __bf16 bf16x8 __attribute__((ext_vector_type(8)));
typedef short s16x4 __attribute__((ext_vector_type(4)));
typedef short s16x8 __attribute__((ext_vector_type(8)));
typedef float f32x2 __attribute__((ext_vector_type(2)));
typedef float f32x4 __attribute__((ext_vector_type(4)));
typedef float f32x16 __attribute__((ext_vector_type(16)));
typedef unsigned u32x2 __attribute__((ext_vector_type(2)));
typedef unsigned u32x4 __attribute__((ext_vector_type(4)));
typedef AS1 unsigned gu32;
typedef AS1 unsigned long long gu64;

#define ATT_PRIO 2
#define GEM_PRIO 0
#define GU_HI 0
#define DN_HI 0
#define MPRIO(mask, i) __builtin_amdgcn_s_setprio(((mask) >> (i)) & 1 ? 3 : 0)
#define GU_DELAY 8
#define GU_ROT 4
#define GU_GQ 5
#define OF_SLEEP 8
#define HF_SLEEP 20
#define DN_DQ 8
#define SPLIT_MODE 0

constexpr int SEQ = 2048, DM = 640, FF = 1536;
constexpr unsigned WS_KF = 0;
constexpr unsigned WS_HF = 16384;
constexpr unsigned WS_OF = 32768;
constexpr unsigned WS_EP = 49152;
constexpr unsigned KVD = 5376;
constexpr unsigned KV_K1 = 2048, KV_V = 2560, KV_VT = 1408;
constexpr unsigned WS_KV = 65536;
constexpr unsigned WS_HT = WS_KV + 176128;
constexpr unsigned WS_OT = WS_HT + 64 * 96 * 1024;
constexpr unsigned WS_QT = WS_OT + 64 * 40 * 1024;
constexpr unsigned WS_PT = WS_QT + 64 * 32 * 2048;
constexpr unsigned WS_ST = WS_PT + 256 * 3 * 32 * 256;
constexpr unsigned L_PANEL = 0;
constexpr unsigned L_KV = 81920;
constexpr unsigned L_WQ = 81920;
constexpr unsigned L_GU = 124928;
constexpr unsigned GSL = 12320;
constexpr unsigned L_WKV = 124928;
constexpr unsigned L_MISC = 161920;
constexpr unsigned L_TOTAL = 163840;
constexpr unsigned CHUNK = 2 * KVD;
constexpr int NCHUNK = 16;
enum { C_AP = 0  , C_GP = 4  , C_DP = 8  , C_PP = 12  , C_WP = 16  , C_KVP = 20, C_KVFIN, C_TAG, C_OS = 24  , C_N = 28 };

struct Params { const char* x; const char* wq; const char* wk; const char* wv; const char* wo; const char* w1; const char* w3; const char* w2; char* out; char* ws; };

#define SBAR() __builtin_amdgcn_sched_barrier(0)
#define PIN(x) asm volatile("" : "+v"(x))
#define CFENCE() asm volatile("" ::: "memory")
#define WAITV(n) asm volatile("s_waitcnt vmcnt(" #n ")" ::: "memory")
#define MFMA(a, b, c) __builtin_amdgcn_mfma_f32_32x32x16_bf16((a), (b), (c), 0, 0, 0)
#define MFMA8(a, b, c) __builtin_amdgcn_mfma_f32_32x32x8bf16_1k((a), (b), (c), 0, 0, 0)
#define RLX_AGENT __ATOMIC_RELAXED, __HIP_MEMORY_SCOPE_AGENT
#define RLX_WG __ATOMIC_RELAXED, __HIP_MEMORY_SCOPE_WORKGROUP

#define TMO(spins, lim, code, tgt) do {} while (0)
#define STAMP(id) do {} while (0)
#define STAMPC(id) do {} while (0)

#define LP(T, off) ((AS3 T*)(uintptr_t)(unsigned)(off))
__device__ __forceinline__ bf16x8 ldsf(unsigned off) { return *LP(const bf16x8, off); }
__device__ __forceinline__ s16x4 ldstr(unsigned off) { return __builtin_amdgcn_ds_read_tr16_b64_v4i16(LP(s16x4, off)); }
__device__ __forceinline__ bf16x8 ldstr2(unsigned off0, unsigned off1) {
    s16x4 a = ldstr(off0), b = ldstr(off1);
    return __builtin_bit_cast(bf16x8, __builtin_shufflevector(a, b, 0, 1, 2, 3, 4, 5, 6, 7));
}
template <int E> __device__ __forceinline__ s16x4 half4(bf16x8 v) { const s16x8 t = __builtin_bit_cast(s16x8, v); return __builtin_shufflevector(t, t, 4 * E, 4 * E + 1, 4 * E + 2, 4 * E + 3); }
__device__ __forceinline__ unsigned pk2(float lo, float hi) { bf16x2 v = {(__bf16)lo, (__bf16)hi}; return __builtin_bit_cast(unsigned, v); }
__device__ __forceinline__ float bflo(unsigned u) { return __uint_as_float(u << 16); }
__device__ __forceinline__ float bfhi(unsigned u) { return __uint_as_float(u & 0xffff0000u); }
__device__ __forceinline__ float rbf(float f) { return (float)(__bf16)f; }

__device__ __forceinline__ void glds16(unsigned voff, const char* sbase, unsigned ldsdst) {
    unsigned keep;
    asm volatile("s_mov_b32 %0, m0\n\ts_mov_b32 m0, %3\n\ts_nop 2\n\tglobal_load_lds_dwordx4 %1, %2\n\ts_mov_b32 m0, %0"
                 : "=&s"(keep) : "v"(voff), "s"(sbase), "s"(ldsdst) : "memory");
}
__device__ __forceinline__ void glds16_sc1(unsigned voff, const char* sbase, unsigned ldsdst) {
    unsigned keep;
    asm volatile("s_mov_b32 %0, m0\n\ts_mov_b32 m0, %3\n\ts_nop 2\n\tglobal_load_lds_dwordx4 %1, %2 sc1\n\ts_mov_b32 m0, %0"
                 : "=&s"(keep) : "v"(voff), "s"(sbase), "s"(ldsdst) : "memory");
}
__device__ __forceinline__ void glds16x3_sc1(unsigned voff, const char* sbase, unsigned ldsdst) {
    unsigned keep;
    asm volatile("s_mov_b32 %0, m0\n\ts_mov_b32 m0, %3\n\ts_nop 2\n\tglobal_load_lds_dwordx4 %1, %2 sc1\n\tglobal_load_lds_dwordx4 %1, %2 offset:1024 sc1\n\tglobal_load_lds_dwordx4 %1, %2 offset:2048 sc1\n\ts_mov_b32 m0, %0"
                 : "=&s"(keep) : "v"(voff), "s"(sbase), "s"(ldsdst) : "memory");
}
__device__ __forceinline__ void glds16x3v(unsigned v0, unsigned v1, unsigned v2, const char* sbase, unsigned ldsdst) {
    unsigned keep;
    asm volatile("s_mov_b32 %0, m0\n\ts_mov_b32 m0, %5\n\ts_nop 0\n\tglobal_load_lds_dwordx4 %1, %4\n\tglobal_load_lds_dwordx4 %2, %4 offset:1024\n\tglobal_load_lds_dwordx4 %3, %4 offset:2048\n\ts_mov_b32 m0, %0"
                 : "=&s"(keep) : "v"(v0), "v"(v1), "v"(v2), "s"(sbase), "s"(ldsdst) : "memory");
}
__device__ __forceinline__ void glds_gu(unsigned va, unsigned vb, unsigned vc, const char* s1, const char* s3, unsigned ldsdst) {
    unsigned keep;
    asm volatile("s_mov_b32 %0, m0\n\ts_mov_b32 m0, %8\n\ts_nop 0\n\tglobal_load_lds_dwordx4 %1, %4\n\tglobal_load_lds_dwordx4 %2, %5 offset:1024\n\t"
                 "s_mov_b32 exec_hi, 0\n\tglobal_load_lds_dwordx4 %3, %6 offset:2048\n\ts_not_b64 exec, exec\n\tglobal_load_lds_dwordx4 %3, %7 offset:2048\n\t"
                 "s_mov_b64 exec, -1\n\ts_mov_b32 m0, %0"
                 : "=&s"(keep) : "v"(va), "v"(vb), "v"(vc), "s"(s1), "s"(s3 - 1024), "s"(s1 - 2048), "s"(s3 - 2048), "s"(ldsdst) : "memory", "scc");
}
__device__ __forceinline__ void l2touch(const char* ga) {
    unsigned keep;
    asm volatile("s_mov_b32 %0, m0\n\ts_mov_b32 m0, %2\n\ts_nop 0\n\tglobal_load_lds_dword %1, off\n\ts_mov_b32 m0, %0" : "=&s"(keep) : "v"(ga), "s"(L_MISC + 1024) : "memory");
}
#define GLOAD16(dst, voff, sbase, imm) asm volatile("s_nop 4\n\tglobal_load_dwordx4 %0, %1, %2 offset:%3" : "=v"(dst) : "v"(voff), "s"(sbase), "i"(imm) : "memory")

__device__ __forceinline__ AS3 unsigned* lcnt(int c) { return LP(unsigned, L_MISC) + c; }
__device__ __forceinline__ void larrive(int c, int lane) { CFENCE(); if (lane == 0) __hip_atomic_fetch_add(lcnt(c), 1u, RLX_WG); CFENCE(); }
__device__ __forceinline__ void lset(int c, unsigned v, int lane) { CFENCE(); if (lane == 0) __hip_atomic_store(lcnt(c), v, RLX_WG); CFENCE(); }
__device__ __forceinline__ void lwait(int c, unsigned target) {
    CFENCE();
    _Pragma("clang loop unroll(disable)") for (unsigned spins = 0; spins < (1u << 22); ++spins) {
        if ((unsigned)__builtin_amdgcn_readfirstlane(__hip_atomic_load(lcnt(c), RLX_WG)) >= target) break;
        TMO(spins, 1u << 16, 100 + c, target);
        __builtin_amdgcn_s_sleep(1);
    }
    CFENCE();
}
__device__ __forceinline__ void lwait_all(int c, int n, unsigned target, int lane) {
    CFENCE();
    AS3 unsigned* q = lcnt(c) + (lane < n ? lane : 0);
    _Pragma("clang loop unroll(disable)") for (unsigned spins = 0; spins < (1u << 16); ++spins) {
        if (__all(__hip_atomic_load(q, RLX_WG) >= target)) break;
        TMO(spins, 1u << 16, 200 + c, target);
        __builtin_amdgcn_s_sleep(1);
    }
    CFENCE();
}
__device__ __forceinline__ void lpost(int c, unsigned v) { CFENCE(); *(volatile AS3 unsigned*)lcnt(c) = v; CFENCE(); }
__device__ __forceinline__ void linc(unsigned vaddr, unsigned vone) {
    asm volatile("s_mov_b64 exec, 1\n\tds_add_u32 %0, %1\n\ts_mov_b64 exec, -1" :: "v"(vaddr), "v"(vone) : "memory");
}
__device__ __forceinline__ unsigned lpeek(int c, int lane) { return *((volatile AS3 unsigned*)lcnt(c) + (lane & 3)); }
__device__ __forceinline__ void lcheck(unsigned pk, int c, unsigned target, int lane) {
    if (__builtin_expect(__any(pk < target), 0)) lwait_all(c, 4, target, lane);
    CFENCE();
}
__device__ __forceinline__ void gwait(const char* ws, unsigned off, int n, unsigned tag, int lane) {
    gu32* f = (gu32*)(ws + off) + (lane < n ? lane : 0);
    _Pragma("clang loop unroll(disable)") for (unsigned spins = 0; spins < (1u << 14); ++spins) {
        unsigned v = __hip_atomic_load(f, RLX_AGENT);
        if (__all(v == tag)) break;
        TMO(spins, 1u << 14, 300, off);
        __builtin_amdgcn_s_sleep(6);
    }
    CFENCE();
}
__device__ __forceinline__ float gwait64(const char* ws, unsigned off, unsigned tag, int lane) {
    gu64* f = (gu64*)(ws + off) + lane;
    unsigned long long v = 0;
    _Pragma("clang loop unroll(disable)") for (unsigned spins = 0; spins < (1u << 14); ++spins) {
        v = __hip_atomic_load(f, RLX_AGENT);
        if (__all((unsigned)(v >> 32) == tag)) break;
        __builtin_amdgcn_s_sleep(6);
    }
    CFENCE();
    return __uint_as_float((unsigned)v);
}
__device__ __forceinline__ __amdgpu_buffer_rsrc_t mkrsrc(const char* base, unsigned bytes) {
    return __builtin_amdgcn_make_buffer_rsrc((void*)base, 0, bytes, 0x00020000);
}
__device__ __forceinline__ float dpp_sum16(float v) {
    v += __int_as_float(__builtin_amdgcn_update_dpp(0, __float_as_int(v), 0x128, 0xf, 0xf, false));
    v += __int_as_float(__builtin_amdgcn_update_dpp(0, __float_as_int(v), 0x124, 0xf, 0xf, false));
    v += __int_as_float(__builtin_amdgcn_update_dpp(0, __float_as_int(v), 0x122, 0xf, 0xf, false));
    v += __int_as_float(__builtin_amdgcn_update_dpp(0, __float_as_int(v), 0x121, 0xf, 0xf, false));
    return v;
}
__device__ __forceinline__ float wave_max(float v) {
    int x = __float_as_int(v);
    x = max(x, __builtin_amdgcn_update_dpp(0, x, 0x128, 0xf, 0xf, false)); x = max(x, __builtin_amdgcn_update_dpp(0, x, 0x124, 0xf, 0xf, false));
    x = max(x, __builtin_amdgcn_update_dpp(0, x, 0x122, 0xf, 0xf, false)); x = max(x, __builtin_amdgcn_update_dpp(0, x, 0x121, 0xf, 0xf, false));
    const int a = max(__builtin_amdgcn_readlane(x, 0), __builtin_amdgcn_readlane(x, 16)), b = max(__builtin_amdgcn_readlane(x, 32), __builtin_amdgcn_readlane(x, 48));
    return __int_as_float(max(a, b));
}
__device__ __forceinline__ bf16x8 pack8(const f32x16& a, int b, float sc) {
    u32x4 r = {pk2(a[b] * sc, a[b + 1] * sc), pk2(a[b + 2] * sc, a[b + 3] * sc), pk2(a[b + 4] * sc, a[b + 5] * sc), pk2(a[b + 6] * sc, a[b + 7] * sc)};
    return __builtin_bit_cast(bf16x8, r);
}

template <int I> struct IC { static constexpr int value = I; };
template <int B, int E, class F> __device__ __forceinline__ void static_for(F&& f) { if constexpr (B < E) { f(IC<B>{}); static_for<B + 1, E>(f); } }
#define WAITVN(n) asm volatile("s_waitcnt vmcnt(%0)" :: "i"(n) : "memory")
__device__ __forceinline__ void GLOAD16S(u32x4& dst, unsigned voff, const char* sbase) { asm volatile("global_load_dwordx4 %0, %1, %2 sc1" : "=v"(dst) : "v"(voff), "s"(sbase) : "memory"); }
__device__ __forceinline__ void GLOAD16N(u32x4& dst, unsigned voff, const char* sbase) { asm volatile("global_load_dwordx4 %0, %1, %2" : "=v"(dst) : "v"(voff), "s"(sbase) : "memory"); }
__device__ __forceinline__ void GLOAD16N_IF(u32x4& dst, unsigned voff, const char* sbase, int cond) {
    asm volatile("s_cmp_eq_u32 %3, 0\n\ts_cbranch_scc1 1f\n\tglobal_load_dwordx4 %0, %1, %2\n1:" : "=v"(dst) : "v"(voff), "s"(sbase), "s"(cond) : "memory", "scc"); }
template <int N> __device__ __forceinline__ void waitv5(u32x4& a, u32x4& b, u32x4& c, u32x4& d, u32x4& e) { asm volatile("s_waitcnt vmcnt(%5)" : "+v"(a), "+v"(b), "+v"(c), "+v"(d), "+v"(e) : "i"(N) : "memory"); }
__device__ __forceinline__ void gload8x2_sc1(unsigned long long& a, unsigned long long& b, unsigned voff, const char* sbase) {
    asm volatile("global_load_dwordx2 %0, %2, %3 sc1\n\tglobal_load_dwordx2 %1, %2, %3 offset:512 sc1" : "=&v"(a), "=&v"(b) : "v"(voff), "s"(sbase) : "memory"); }
__device__ __forceinline__ void waitv8x2(unsigned long long& a, unsigned long long& b) { asm volatile("s_waitcnt vmcnt(0)" : "+v"(a), "+v"(b) :: "memory"); }
__device__ __forceinline__ void PIN2(u32x4& a, u32x4& b) { asm volatile("" : "+v"(a), "+v"(b) :: "memory"); }
__device__ __forceinline__ u32x4 scale8(unsigned x0, unsigned x1, unsigned x2, unsigned x3, unsigned clo, unsigned chi, float c) {
    unsigned o0, o1, o2, o3; float t0, t1, t2, t3, t4, t5, t6, t7;
    asm("v_dot2_f32_bf16 %4, %12, %16, %18\n\tv_dot2_f32_bf16 %5, %12, %17, %18\n\tv_dot2_f32_bf16 %6, %13, %16, %18\n\tv_dot2_f32_bf16 %7, %13, %17, %18\n\t"
        "v_dot2_f32_bf16 %8, %14, %16, %18\n\tv_dot2_f32_bf16 %9, %14, %17, %18\n\tv_dot2_f32_bf16 %10, %15, %16, %18\n\tv_dot2_f32_bf16 %11, %15, %17, %18\n\t"
        "v_cvt_pk_bf16_f32 %0, %4, %5\n\tv_cvt_pk_bf16_f32 %1, %6, %7\n\tv_cvt_pk_bf16_f32 %2, %8, %9\n\tv_cvt_pk_bf16_f32 %3, %10, %11"
        : "=&v"(o0), "=&v"(o1), "=&v"(o2), "=&v"(o3), "=&v"(t0), "=&v"(t1), "=&v"(t2), "=&v"(t3), "=&v"(t4), "=&v"(t5), "=&v"(t6), "=&v"(t7)
        : "v"(x0), "v"(x1), "v"(x2), "v"(x3), "v"(clo), "v"(chi), "v"(c));
    return u32x4{o0, o1, o2, o3};
}
template <int N> __device__ __forceinline__ void waitv3(u32x4& a, u32x4& b, u32x4& c) { asm volatile("s_waitcnt vmcnt(%3)" : "+v"(a), "+v"(b), "+v"(c) : "i"(N) : "memory"); }

constexpr int dn_younger(int k, int D, bool wo) {
    int n = 0, la = 0, lb = 0;
    for (int i = 0; i < D; ++i) { n += 2; if (i == k + 1) lb = n; }
    for (int i = 0; i < D; ++i) { n += 3; if (i == k) la = n; }
    for (int s = 0; s < k; ++s) {
        if (s + D < 24) { n += 2; if (s + D == k + 1) lb = n; }
        if (wo && s >= 16) n += 2;
        if (s + D < 24) { n += 3; if (s + D == k) la = n; }
    }
    return n - (la > lb ? la : lb);
}
constexpr float QSCALE = 0.22360679774997896f * 1.4426950408889634f;
constexpr float BOUND_MAX = 50.f;

struct AttSync { int lane; const char* kv; unsigned dvoff; unsigned wo; int w; };
__device__ __forceinline__ unsigned kvslot(int c) { return L_KV + ((c + 2) & 3) * CHUNK; }
__device__ __forceinline__ void att_dma(const AttSync& a, int c) { glds16x3_sc1(a.dvoff, a.kv + (size_t)c * CHUNK, kvslot(c) + a.wo); }
constexpr unsigned AP0 = 16;
#define RING_X(c) do { WAITV(3); lpost(C_AP + as.w, AP0 + (c) + 1); } while (0)
#define RP_A
#define RP_B
#define RING_Y(c, pk) do { RP_A lcheck(pk, C_AP, AP0 + (c) + 1, lane); const int cn_ = (c) + 3 < NCHUNK ? (c) + 3 : NCHUNK - 1;   \
                           glds16x3_sc1(as.dvoff, as.kv + (size_t)cn_ * CHUNK, kvslot((c) + 3) + as.wo); RP_B } while (0)

__device__ __forceinline__ void att_fast(const AttSync& as, bf16x8 Q0a, s16x4 Q1a, bf16x8 Q0b, s16x4 Q1b, f32x16& Oa, f32x16& Ob) {
    const int lane = as.lane, r = lane & 31, h = lane >> 5;
    const unsigned ak0 = lane * 16, ak1 = h ? KV_V + 20 * 16 : KV_K1 + r * 16;
    const unsigned av = KV_V + h * 352 + (r <= 20 ? r : 21) * 16;
    const f32x16 zero = {};
    f32x16 Sa, Sb = {};
    bf16x8 K0[2], K1[2], V0[2], V1[2];
    u32x4 Pa0, Pa1, Pb0 = {}, Pb1 = {};
    unsigned pk = 0;
    unsigned long long rp0 = 0, rp1 = 0;
    V0[1] = bf16x8{}; V1[1] = bf16x8{};
    unsigned vk0 = L_KV + ak0, vk1 = L_KV + ak1, vv = L_KV + av;
    PIN(vk0); PIN(vk1); PIN(vv);
    K0[0] = ldsf(vk0 + 2 * CHUNK); K1[0] = ldsf(vk1 + 2 * CHUNK); K0[1] = ldsf(vk0 + 2 * CHUNK + 1024); V1[0] = ldsf(vv + 2 * CHUNK + 704); V0[0] = ldsf(vv + 2 * CHUNK);
    Sa = MFMA(K0[0], Q0a, zero); Sa = MFMA8(half4<0>(K1[0]), Q1a, Sa);
#define EX4(S, i) do { S[i] = __builtin_amdgcn_exp2f(S[i]); S[i + 1] = __builtin_amdgcn_exp2f(S[i + 1]); S[i + 2] = __builtin_amdgcn_exp2f(S[i + 2]); S[i + 3] = __builtin_amdgcn_exp2f(S[i + 3]); } while (0)
#define GAP(MF, LD, T, k, PW, j, S, i) do { MF; SBAR(); LD; PW[j] = pk2(T[k], T[k + 1]); PW[j + 1] = pk2(T[k + 2], T[k + 3]); PIN(PW); SBAR(); EX4(S, i); PIN(S); SBAR(); } while (0)
#define BC(x) __builtin_bit_cast(bf16x8, x)
#define STEP(e, d, KN, L3, LK0, LK1, LV) do { \
        GAP(Ob = MFMA(V0[e ^ 1], BC(Pb0), Ob), (void)0,                  Sb, 12, Pb1, 2, Sa, 0); \
        GAP(Sb = MFMA(K0[e], Q0b, zero),       (void)0,                  Sa, 0, Pa0, 0, Sa, 4); \
        GAP(Sb = MFMA8(half4<e>(K1[d]), Q1b, Sb), L3,                       Sa, 4, Pa0, 2, Sa, 8); \
        GAP(Ob = MFMA(V1[e ^ 1], BC(Pb1), Ob), K0[e] = ldsf(LK0),        Sa, 8, Pa1, 0, Sa, 12); \
        GAP(Oa = MFMA(V0[e], BC(Pa0), Oa),     LK1,                      Sa, 12, Pa1, 2, Sb, 0); \
        GAP(Sa = MFMA(K0[e ^ 1], Q0a, zero),   V1[e ^ 1] = ldsf((LV) + 704), Sb, 0, Pb0, 0, Sb, 4); \
        GAP(Sa = MFMA8(half4<e ^ 1>(K1[KN]), Q1a, Sa), V0[e ^ 1] = ldsf(LV),     Sb, 4, Pb0, 2, Sb, 8); \
        GAP(Oa = MFMA(V1[e], BC(Pa1), Oa),     (void)0,                  Sb, 8, Pb1, 0, Sb, 12); \
    } while (0)
#define CHUNK4(c, B, N) do { \
        STEP(0, 0, 0, (void)0, vk0 + (B) + KVD, K1[1] = ldsf(vk1 + (B) + KVD), vv + (B) + KV_VT); \
        RING_X(c); \
        STEP(1, 0, 1, pk = lpeek(C_AP, lane), vk0 + (B) + KVD + 1024, (void)0, vv + (B) + KVD); \
        RING_Y(c, pk); \
        STEP(0, 1, 1, (void)0, vk0 + (N), K1[0] = ldsf(vk1 + (N)), vv + (B) + KVD + KV_VT); \
        STEP(1, 1, 0, (void)0, vk0 + (N) + 1024, (void)0, vv + (N)); \
    } while (0)
    for (int c = 0; c < NCHUNK; c += 4) {
        CHUNK4(c, 2 * CHUNK, 3 * CHUNK); CHUNK4(c + 1, 3 * CHUNK, 0); CHUNK4(c + 2, 0, CHUNK); CHUNK4(c + 3, CHUNK, 2 * CHUNK);
    }
#undef CHUNK4
    Pb1[2] = pk2(Sb[12], Sb[13]); Pb1[3] = pk2(Sb[14], Sb[15]);
    Ob = MFMA(V0[1], BC(Pb0), Ob); Ob = MFMA(V1[1], BC(Pb1), Ob);
#undef STEP
#undef GAP
#undef EX4
}

__device__ __forceinline__ void att_safe(const AttSync& as, bf16x8 Q0a, s16x4 Q1a, bf16x8 Q0b, s16x4 Q1b, f32x16& Oa, f32x16& Ob) {
    const int lane = as.lane, r = lane & 31, h = lane >> 5;
    const unsigned ak0 = lane * 16, ak1 = h ? KV_V + 20 * 16 : KV_K1 + r * 16;
    const unsigned av = KV_V + h * 352 + (r <= 20 ? r : 21) * 16;
    const f32x16 zero = {};
    float ma = -INFINITY, mb = -INFINITY;
    unsigned long long rp0 = 0, rp1 = 0;
    for (int c = 0; c < NCHUNK; ++c) {
        const unsigned b0 = kvslot(c);
        for (int t = 0; t < 4; ++t) {
            if (t == 1) RING_X(c);
            if (t == 2) RING_Y(c, 0u);
            const unsigned base = b0 + (t >> 1) * KVD; const int e = t & 1;
            bf16x8 K0 = ldsf(base + ak0 + e * 1024), K1 = ldsf(base + ak1), V0 = ldsf(base + av + e * KV_VT), V1 = ldsf(base + av + e * KV_VT + 704);
#pragma unroll
            for (int q = 0; q < 2; ++q) {
                f32x16 S = MFMA(K0, q ? Q0b : Q0a, zero); S = MFMA8(e ? half4<1>(K1) : half4<0>(K1), q ? Q1b : Q1a, S);
                float mx = S[0];
#pragma unroll
                for (int i = 1; i < 16; ++i) mx = fmaxf(mx, S[i]);
                mx = fmaxf(mx, __shfl_xor(mx, 32));
                float& m = q ? mb : ma; f32x16& O = q ? Ob : Oa;
                const float mn = fmaxf(m, mx);
                const float al = __builtin_amdgcn_exp2f(m - mn);
                m = mn;
#pragma unroll
                for (int i = 0; i < 16; ++i) { O[i] *= al; S[i] = __builtin_amdgcn_exp2f(S[i] - mn); }
                O = MFMA(V0, pack8(S, 0, 1.f), O); O = MFMA(V1, pack8(S, 8, 1.f), O);
            }
        }
    }
}

__device__ __forceinline__ void xload8(u32x4 (&x)[5], u32x4 (&y)[5], const char* xb, unsigned vo, unsigned vo2, int skip) {
    asm volatile("s_cmp_lg_u32 %13, 0\n\ts_cbranch_scc1 1f\n\t"
                 "global_load_dwordx4 %0, %10, %12\n\tglobal_load_dwordx4 %1, %10, %12 offset:256\n\tglobal_load_dwordx4 %2, %10, %12 offset:512\n\t"
                 "global_load_dwordx4 %3, %10, %12 offset:768\n\tglobal_load_dwordx4 %4, %10, %12 offset:1024\n\t"
                 "global_load_dwordx4 %5, %11, %12\n\tglobal_load_dwordx4 %6, %11, %12 offset:256\n\tglobal_load_dwordx4 %7, %11, %12 offset:512\n\t"
                 "global_load_dwordx4 %8, %11, %12 offset:768\n\tglobal_load_dwordx4 %9, %11, %12 offset:1024\n1:"
                 : "=&v"(x[0]), "=&v"(x[1]), "=&v"(x[2]), "=&v"(x[3]), "=&v"(x[4]), "=&v"(y[0]), "=&v"(y[1]), "=&v"(y[2]), "=&v"(y[3]), "=&v"(y[4])
                 : "v"(vo), "v"(vo2), "s"(xb), "s"(skip) : "memory", "scc");
}
__device__ __forceinline__ void xload4(u32x4 (&x)[5], const char* xb, unsigned vo) {
    asm volatile("global_load_dwordx4 %0, %5, %6\n\tglobal_load_dwordx4 %1, %5, %6 offset:256\n\tglobal_load_dwordx4 %2, %5, %6 offset:512\n\t"
                 "global_load_dwordx4 %3, %5, %6 offset:768\n\tglobal_load_dwordx4 %4, %5, %6 offset:1024"
                 : "=&v"(x[0]), "=&v"(x[1]), "=&v"(x[2]), "=&v"(x[3]), "=&v"(x[4]) : "v"(vo), "s"(xb) : "memory");
}
__device__ __forceinline__ void ln_rows(const u32x4 (&xr)[5], int row0, int lane) {
    f32x2 xv[20];
    f32x2 s0 = {0.f, 0.f}, s1 = {0.f, 0.f}, q0 = {0.f, 0.f}, q1 = {0.f, 0.f};
#pragma unroll
    for (int i = 0; i < 20; i += 2) {
        const unsigned a = xr[i >> 2][i & 3], b = xr[i >> 2][(i & 3) + 1];
        xv[i] = f32x2{bflo(a), bfhi(a)}; xv[i + 1] = f32x2{bflo(b), bfhi(b)};
        s0 += xv[i]; q0 = __builtin_elementwise_fma(xv[i], xv[i], q0);
        s1 += xv[i + 1]; q1 = __builtin_elementwise_fma(xv[i + 1], xv[i + 1], q1);
    }
    s0 += s1; q0 += q1;
    const float s = dpp_sum16(s0[0] + s0[1]), ss = dpp_sum16(q0[0] + q0[1]);
    const float mean = s * (1.f / 640.f);
    const float var = fmaxf(ss * (1.f / 640.f) - mean * mean, 0.f);
    const float rs = __builtin_amdgcn_rcpf(rbf(__builtin_amdgcn_sqrtf(rbf(rbf(var) + 1e-5f)))), mo = -rbf(mean) * rs;
    const f32x2 rs2 = {rs, rs}, mo2 = {mo, mo};
    const int row = row0 + (lane >> 4), c = lane & 15;
    const unsigned wa = L_PANEL + (row >> 5) * 40960 + (c >> 1) * 1024 + (c & 1) * 512 + (((row & 31) ^ (c & 7)) << 4);
#pragma unroll
    for (int u = 0; u < 5; ++u) {
        u32x4 o;
#pragma unroll
        for (int e = 0; e < 4; ++e) { const f32x2 y = __builtin_elementwise_fma(xv[4 * u + e], rs2, mo2); o[e] = pk2(y[0], y[1]); }
        *LP(u32x4, wa + u * 8192) = o;
    }
}
#define BLOCK_BARRIER() asm volatile("s_waitcnt lgkmcnt(0)\n\ts_barrier" ::: "memory")

template <int PH>
__global__ void __launch_bounds__(512, 2) mega(Params p) {
    constexpr bool PRO = PH & 1, ATT = PH & 2, DN = PH & 8, WO = PH & 16;
    const int w = __builtin_amdgcn_readfirstlane(threadIdx.x >> 6);
    const int b = blockIdx.x, xc = b & 7, j = b >> 3;
    const int ti = (xc >> 1) * 8 + (j >> 2), g = (xc & 1) * 4 + (j & 3);
    constexpr bool FUSED = PH == 27;
    const int kmt = g & 1;
    const int ra = kmt * 32 + (w & 3) * 8, rb = (kmt ^ 1) * 32 + (w & 3) * 8;
    u32x4 x0[5], x1[5], x2[5], x3[5];
    if constexpr (PRO) {
        asm volatile("" :: "s"(p.x), "s"(p.ws), "s"(p.wq), "s"(p.wk));
        const char* xb = p.x + (size_t)ti * 64 * 1280;
        const unsigned xvo = ((threadIdx.x >> 4) & 3) * 1280 + (threadIdx.x & 15) * 16;
        xload8(x0, x1, xb + (size_t)ra * 1280, xvo, xvo + 5120, w >> 2);
        xload8(x2, x3, xb + (size_t)rb * 1280, xvo, xvo + 5120, w >> 2);
    }
    const char* pc; asm volatile("s_getpc_b64 %0" : "=s"(pc));
    unsigned tag = 1u;
    char* ws = p.ws;
    STAMP(0);
    if (threadIdx.x < C_N) *lcnt(threadIdx.x) = (threadIdx.x == C_WP || threadIdx.x == C_WP + 1 || threadIdx.x == C_PP || threadIdx.x == C_PP + 1) ? 100u : 0u;
    const bool haskv = (g >> 2) == (ti & 1);
    const char* wqb = p.wq + g * 160;
#define LANE_CONSTS() int lane = __builtin_amdgcn_mbcnt_hi(~0u, __builtin_amdgcn_mbcnt_lo(~0u, 0u)); PIN(lane); \
    const int r = lane & 31, h = lane >> 5, tq = (lane & 15) >> 2, tp = lane & 3, tb = (lane >> 4) & 1; (void)r; (void)h; (void)tq; (void)tp; (void)tb

    if (w >= 4) {
        LANE_CONSTS();
        const int v = w - 4;
        unsigned epv = 0u;
        if constexpr (FUSED) {
            asm volatile("global_load_dword %0, %1, %2 sc1" : "=v"(epv) : "v"(0u), "s"(p.ws + WS_EP) : "memory");
            constexpr unsigned CODE_PF = 65536;
            const unsigned line = (unsigned)(j * 4 + v) * 64u + lane;
            if (line * 128u < CODE_PF) l2touch(pc + line * 128u);
        }
#define GET_TAG() do { if constexpr (FUSED) { asm volatile("s_nop 0\n\tv_readfirstlane_b32 %0, %1\n\ts_nop 3" : "=s"(tag) : "v"(epv) : "memory"); tag += 0x9E3779B1u; } } while (0)
        __builtin_amdgcn_s_setprio(3);
        if constexpr (PRO) {
            const int kvm = 2 * ti + (g & 1); const bool isV = (g >> 1) & 1;
            const char* wkvb = isV ? p.wv : p.wk;
            if (haskv && v < 2) {
#pragma unroll
                for (int n = 0; n < 13; ++n) {
                    const unsigned o = v * 12800 + (n < 12 ? n * 1024 : 11776);
                    glds16(o + lane * 16, wkvb, L_WKV + o);
                }
            }
            BLOCK_BARRIER();
            if (v >= 2) {
                const int v2 = v - 2;
                unsigned wo5[5];
#pragma unroll
                for (int i = 0; i < 5; ++i) { const unsigned c = (v2 + 2 * i) * 64 + lane, row = c / 10u, col = c - row * 10u; wo5[i] = row * 1280 + col * 16; }
#pragma unroll
                for (int n = 0; n < 20; ++n) glds16(wo5[n % 5], wqb + (n / 5) * 64 * 1280, L_WQ + (v2 + 2 * n) * 1024);
                u32x4 wst[3][10];
                auto wst_issue = [&](auto I) {
                    constexpr int rr = I.value / 10, i = I.value % 10;
                    GLOAD16N(wst[rr][i], wo5[i % 5], wqb + (4 + 2 * rr + i / 5) * 64 * 1280);
                };
                static_for<0, 10>(wst_issue);
                WAITV(10);
                GET_TAG(); lpost(C_TAG, tag);
                lpost(C_PP + v, 1);
                STAMP(2);
                static_for<10, 30>(wst_issue);
                static_for<0, 3>([&](auto R) {
                    constexpr int rr = R.value;
                    waitv5<20 - 10 * rr>(wst[rr][0], wst[rr][1], wst[rr][2], wst[rr][3], wst[rr][4]); waitv5<63>(wst[rr][5], wst[rr][6], wst[rr][7], wst[rr][8], wst[rr][9]);
                    lwait_all(C_AP, 4, 3 + rr, lane);
#pragma unroll
                    for (int i = 0; i < 10; ++i) *LP(u32x4, L_WQ + (rr & 1) * 20480 + (v2 + 2 * i) * 1024 + lane * 16) = wst[rr][i];
                    lpost(C_WP + v, rr + 1);
                });
            } else {
              if (haskv) {
                f32x16 acc = {};
                const unsigned wa = L_WKV + (v * 320 + 8 * h + tq) * 40 + tb * 32 + (tb ? 0 : tp * 8);
                unsigned xa[4];
#pragma unroll
                for (int s = 0; s < 4; ++s) xa[s] = L_PANEL + kmt * 40960 + v * 20480 + h * 512 + ((r ^ ((2 * s + h) & 7)) << 4);
                WAITV(0);
                GET_TAG();
                STAMP(2);
                bf16x8 wf[20], xf[20];
#pragma unroll
                for (int s = 0; s < 20; ++s) wf[s] = ldstr2(wa + s * 640, wa + s * 640 + 160);
                lwait_all(C_AP, 4, 1, lane);
#pragma unroll
                for (int s = 0; s < 20; ++s) xf[s] = ldsf(xa[s & 3] + s * 1024);
                SBAR();
                if (isV) {
#pragma unroll
                    for (int s = 0; s < 20; ++s) acc = MFMA(xf[s], wf[s], acc);
                } else {
#pragma unroll
                    for (int s = 0; s < 20; ++s) acc = MFMA(wf[s], xf[s], acc);
                }
                if (v) {
#pragma unroll
                    for (int e = 0; e < 4; ++e) *LP(f32x4, L_WKV + 12800 + e * 1024 + lane * 16) = f32x4{acc[4 * e], acc[4 * e + 1], acc[4 * e + 2], acc[4 * e + 3]};
                    lpost(C_KVP, 1);
                } else {
                    lwait(C_KVP, 1);
#pragma unroll
                    for (int e = 0; e < 4; ++e) {
                        const f32x4 t = *LP(const f32x4, L_WKV + 12800 + e * 1024 + lane * 16);
                        acc[4 * e] += t[0]; acc[4 * e + 1] += t[1]; acc[4 * e + 2] += t[2]; acc[4 * e + 3] += t[3];
                    }
                    const __amdgpu_buffer_rsrc_t rk = mkrsrc(ws + WS_KV, 32 * KVD);
                    const unsigned base = ti * KVD, ke = g & 1;
                    const u32x4 f0 = __builtin_bit_cast(u32x4, pack8(acc, 0, 1.f)), f1 = __builtin_bit_cast(u32x4, pack8(acc, 8, 1.f));
                    float mx;
                    if (!isV) {
                        __builtin_amdgcn_raw_buffer_store_b128(f0, rk, base + ke * 1024 + lane * 16, 0, 16);
                        const u32x2 k1 = {f1[0], f1[1]};
                        if (!h) __builtin_amdgcn_raw_buffer_store_b64(k1, rk, base + KV_K1 + r * 16 + ke * 8, 0, 16);
                        lpost(C_KVFIN, 1);
                        float n2 = 0.f;
#pragma unroll
                        for (int e = 0; e < 4; ++e) n2 += bflo(f0[e]) * bflo(f0[e]) + bfhi(f0[e]) * bfhi(f0[e]);
                        if (!h) n2 += bflo(f1[0]) * bflo(f1[0]) + bfhi(f1[0]) * bfhi(f1[0]) + bflo(f1[1]) * bflo(f1[1]) + bfhi(f1[1]) * bfhi(f1[1]);
                        n2 += __shfl_xor(n2, 32);
                        mx = (n2 == n2) ? n2 : INFINITY;
                    } else {
                        const unsigned cst = r == 20 ? 0x3f803f80u : 0u;
                        const u32x4 c4 = {cst, cst, cst, cst};
                        if (r <= 21) {
                            __builtin_amdgcn_raw_buffer_store_b128(r >= 20 ? c4 : f0, rk, base + KV_V + ke * KV_VT + h * 352 + r * 16, 0, 16);
                            __builtin_amdgcn_raw_buffer_store_b128(r >= 20 ? c4 : f1, rk, base + KV_V + ke * KV_VT + 704 + h * 352 + r * 16, 0, 16);
                        }
                        lpost(C_KVFIN, 1);
                        float vm = 0.f;
#pragma unroll
                        for (int e = 0; e < 16; ++e) { const float a = fabsf(acc[e]); vm = (a <= vm) ? vm : ((a == a) ? a : INFINITY); }
                        mx = r < 20 ? vm : 0.f;
                    }
                    mx = wave_max(mx);
                    WAITV(0);
                    STAMP(15);
                    if (lane < 8) __hip_atomic_store((gu64*)(ws + WS_KF + lane * 1024 + (isV ? 512 : 0)) + kvm, ((unsigned long long)tag << 32) | __float_as_uint(mx), RLX_AGENT);
                }
            }
              else { WAITV(0); GET_TAG(); }
              STAMP(3);
            }
            if (haskv) lwait(C_KVFIN, 1);
#pragma unroll
            for (int n = 0; n < 2; ++n) {
                unsigned L = n * 64 + lane; L = (j * 4 + v) * 120 + (L < 120u ? L : 119u);
                const unsigned m3 = L >= 7680u, rem = L - (m3 ? 7680u : 0u), row = rem / 12u;
                l2touch((m3 ? p.w3 : p.w1) + row * 3072 + (xc & 1) * 1536 + (rem - row * 12u) * 128);
            }
            lwait_all(C_AP, 4, GU_DELAY, lane);
            STAMP(4);
            {
                const char* w1s = p.w1 + g * 384 + v * 96; const char* w3s = p.w3 + g * 384 + v * 96;
                auto goff = [](unsigned idx) { const unsigned row = idx / 6u; return row * 3072 + (idx - row * 6u) * 16; };
                const unsigned gva = goff(lane), gvb = goff((lane - 8) & 63), gvc = goff(64 + (h ? (r - 8) & 31 : r));
                const unsigned gdst = L_GU + v * 9216;
                auto gdma = [&](auto S) { constexpr int s = S.value; glds_gu(gva, gvb, gvc, w1s + (size_t)s * 16 * 3072, w3s + (size_t)s * 16 * 3072, gdst + (s % 3) * 3072); };
                static_for<0, 3>(gdma);
                f32x16 acc[2][3] = {};
                unsigned bn[3][2], pa[4], pb[4];
#pragma unroll
                for (int n = 0; n < 3; ++n)
#pragma unroll
                    for (int t = 0; t < 2; ++t) {
                        const unsigned idx = 6 * (8 * h + 4 * t + tq) + 2 * n + (tp >> 1);
                        const unsigned o = idx < 64u ? (tb ? 1024 + ((idx + 8) & 63) * 16 : idx * 16) : (tb ? 2560 + ((idx - 56) & 31) * 16 : 2048 + (idx - 64) * 16);
                        bn[n][t] = gdst + o + (tp & 1) * 8;
                    }
#pragma unroll
                for (int s = 0; s < 4; ++s) { pa[s] = L_PANEL + h * 512 + ((r ^ ((2 * s + h) & 7)) << 4); pb[s] = pa[s] + 40960; PIN(pb[s]); }
                bf16x8 wf[2][3], xa[2], xb2[2];
                auto rdw = [&](auto S) { constexpr int s = S.value;
#pragma unroll
                    for (int n = 0; n < 3; ++n) wf[s & 1][n] = ldstr2(bn[n][0] + (s % 3) * 3072, bn[n][1] + (s % 3) * 3072); };
                auto rdx = [&](auto S) { constexpr int s = S.value; xa[s & 1] = ldsf(pa[s & 3] + s * 1024); xb2[s & 1] = ldsf(pb[s & 3] + s * 1024); };
                WAITV(8);
                rdx(IC<0>{}); rdw(IC<0>{});
                asm volatile("s_waitcnt lgkmcnt(0)" ::: "memory");
                gdma(IC<3>{});
                static_for<0, 40>([&](auto S) {
                    constexpr int s = S.value; constexpr bool nx = s + 1 < 40;
                    MPRIO(GU_HI, 0); acc[0][0] = MFMA(wf[s & 1][0], xa[s & 1], acc[0][0]); SBAR();
                    if constexpr (nx) rdx(IC<s + 1>{});
                    SBAR();
                    MPRIO(GU_HI, 1); acc[1][0] = MFMA(wf[s & 1][0], xb2[s & 1], acc[1][0]); SBAR();
                    if constexpr (nx) { WAITVN(4 * ((s + 3 < 39 ? s + 3 : 39) - (s + 1))); rdw(IC<s + 1>{}); }
                    SBAR();
                    MPRIO(GU_HI, 2); acc[0][1] = MFMA(wf[s & 1][1], xa[s & 1], acc[0][1]); SBAR();
                    MPRIO(GU_HI, 3); acc[1][1] = MFMA(wf[s & 1][1], xb2[s & 1], acc[1][1]); SBAR();
                    if constexpr (s + 4 < 40) { asm volatile("s_waitcnt lgkmcnt(0)" ::: "memory"); gdma(IC<s + 4>{}); }
                    SBAR();
                    MPRIO(GU_HI, 4); acc[0][2] = MFMA(wf[s & 1][2], xa[s & 1], acc[0][2]); SBAR();
                    MPRIO(GU_HI, 5); acc[1][2] = MFMA(wf[s & 1][2], xb2[s & 1], acc[1][2]); SBAR();
                });
                lpost(C_GP + v, 42);
                __builtin_amdgcn_s_setprio(3);
                STAMP(6);
                const __amdgpu_buffer_rsrc_t rh = mkrsrc(ws + WS_HT, 64 * 96 * 1024);
#pragma unroll
                for (int m = 0; m < 2; ++m)
#pragma unroll
                    for (int n = 0; n < 3; ++n) {
                        f32x16 hv;
#pragma unroll
                        for (int e = 0; e < 8; ++e) {
                            const float gg = acc[m][n][e];
                            hv[e] = gg * __builtin_amdgcn_rcpf(1.f + __builtin_amdgcn_exp2f(-1.4426950408889634f * gg)) * acc[m][n][e + 8];
                        }
                        __builtin_amdgcn_raw_buffer_store_b128(__builtin_bit_cast(u32x4, pack8(hv, 0, 1.f)), rh, ((2 * ti + m) * 96 + g * 12 + 3 * v + n) * 1024 + lane * 16, 0, 16);
                    }
                if constexpr (DN) {
                    WAITV(0);
                    if (lane < 2) __hip_atomic_store((gu32*)(ws + WS_HF) + (2 * ti + lane) * 32 + g * 4 + v, tag, RLX_AGENT);
                }
                __builtin_amdgcn_s_setprio(0);
                if constexpr (DN) {
#pragma unroll
                    for (int n = 0; n < 2; ++n) {
                        unsigned L = (j * 4 + v) * 85 + n * 64 + lane; L = L < 10880u ? L : 10879u;
                        const unsigned row = L / 5u;
                        l2touch((row < 1536u ? p.w2 + row * 1280 : p.wo + (row - 1536u) * 1280) + (xc & 1) * 640 + (L - row * 5u) * 128);
                    }
                }
                STAMP(7);
            }
        } else {
            BLOCK_BARRIER();
        }
        if constexpr (DN || WO) {
            LANE_CONSTS();
            const unsigned bq = ((tb * 32 + (tp >> 1) * 16) ^ ((unsigned)(tq >> 1) << 6)) + (tp & 1) * 8;
            const unsigned drow = lane >> 3, dcol = ((lane & 7) ^ (((lane >> 4) & 1) << 2)) * 16;
            const int G = xc >> 1, r0 = G < 2 ? 6 * G : 12 + 5 * (G - 2), nr = G < 2 ? 6 : 5;
            if constexpr (FUSED) {
                if (b == 255 && v == 0) {
                    gu32* f = (gu32*)(ws + WS_HF) + (lane >> 3) * 64 + (lane & 7) * 4;
                    _Pragma("clang loop unroll(disable)") for (unsigned spins = 0; spins < (1u << 14); ++spins) {
                        bool ok = true;
#pragma unroll
                        for (int n = 0; n < 4; ++n) ok &= __hip_atomic_load(f + n * 512, RLX_AGENT) == tag;
                        if (__all(ok)) break;
                        TMO(spins, 1u << 14, 303, tag);
                        __builtin_amdgcn_s_sleep(32);
                    }
                    if (lane == 0) __hip_atomic_store((gu32*)(ws + WS_EP), tag, RLX_AGENT);
                }
            }
            if (j < 5 * nr) {
                const int dr = r0 + j / 5, dc = (xc & 1) * 5 + j % 5;
                const int mfirst = dr < 20 ? 3 * dr : 60 + 2 * (dr - 20), nm = dr < 20 ? 3 : 2;
                const unsigned dvo = drow * 1280 + dcol;
                const unsigned myring = L_PANEL + v * 20480;
                f32x16 acc0 = {}, acc1 = {};
                if constexpr (PRO) { lwait_all(C_GP, 4, 42, lane); lwait_all(C_AP, 4, 8, lane); }
                if constexpr (DN) {
                    f32x16 da[3][2] = {};
                    const int drot = (j % 5) * 4;
                    const char* bsrc = p.w2 + (size_t)v * 384 * 1280 + dc * 128;
                    const char* asrc = ws + WS_HT + ((size_t)mfirst * 96 + v * 24) * 1024;
                    const unsigned am1 = lane * 16 + 98304, am2 = lane * 16 + (nm == 3 ? 196608 : 98304);
                    constexpr int DQ = DN_DQ;
                    u32x4 aq[DQ][3];
                    auto bissue = [&](auto K) {
                        constexpr int k = K.value;
                        int kr = k + drot; kr = kr >= 24 ? kr - 24 : kr;
                        const char* sb = bsrc + (size_t)kr * 16 * 1280;
                        glds16(dvo, sb, myring + (k % 10) * 2048); glds16(dvo + 8 * 1280, sb, myring + (k % 10) * 2048 + 1024);
                    };
                    auto woissue = [&](auto U) {
                        constexpr int u = U.value;
                        const char* sb = p.wo + (size_t)(v * 160 + u * 16) * 1280 + dc * 128;
                        glds16(dvo, sb, myring + u * 2048); glds16(dvo + 8 * 1280, sb, myring + u * 2048 + 1024);
                    };
                    auto aissue = [&](auto K) {
                        constexpr int k = K.value;
                        int kr = k + drot; kr = kr >= 24 ? kr - 24 : kr;
                        const char* sb = asrc + (size_t)kr * 1024;
                        GLOAD16S(aq[k % DQ][0], lane * 16, sb); GLOAD16S(aq[k % DQ][1], am1, sb); GLOAD16S(aq[k % DQ][2], am2, sb);
                    };
                    static_for<0, DQ>(bissue);
                    if constexpr (PRO) {
                        gu32* f = (gu32*)(ws + WS_HF) + (mfirst + ((lane >> 3) < nm ? (lane >> 3) : 0)) * 32 + 8 * v + (lane & 7);
                        _Pragma("clang loop unroll(disable)") for (unsigned spins = 0; spins < (1u << 14); ++spins) {
                            if (__all(__hip_atomic_load(f, RLX_AGENT) == tag)) break;
                            TMO(spins, 1u << 14, 302, tag);
                            __builtin_amdgcn_s_sleep(HF_SLEEP);
                        }
                        CFENCE();
                    }
                    STAMP(8);
                    static_for<0, DQ>(aissue);
                    const unsigned ba0 = myring + (4 * h + tq) * 128 + bq, ba1 = ba0 ^ 64;
                    bf16x8 w0[2], w1[2];
                    WAITVN(3 * DQ);
                    w0[0] = ldstr2(ba0, ba0 + 1024); w1[0] = ldstr2(ba1, ba1 + 1024);
                    static_for<0, 24>([&](auto K) {
                        constexpr int k = K.value;
                        waitv3<dn_younger(k, DQ, WO)>(aq[k % DQ][0], aq[k % DQ][1], aq[k % DQ][2]);
                        constexpr unsigned o = ((k + 1) % 10) * 2048;
#define DXF(m) __builtin_bit_cast(bf16x8, aq[k % DQ][m])
                        SBAR();
                        MPRIO(DN_HI, 0); da[0][0] = MFMA(w0[k & 1], DXF(0), da[0][0]); SBAR();
                        if constexpr (k + 1 < 24) w0[(k + 1) & 1] = ldstr2(ba0 + o, ba0 + o + 1024);
                        SBAR();
                        MPRIO(DN_HI, 1); da[0][1] = MFMA(w1[k & 1], DXF(0), da[0][1]); SBAR();
                        if constexpr (k + 1 < 24) w1[(k + 1) & 1] = ldstr2(ba1 + o, ba1 + o + 1024);
                        SBAR();
                        MPRIO(DN_HI, 2); da[1][0] = MFMA(w0[k & 1], DXF(1), da[1][0]); SBAR();
                        if constexpr (k + DQ < 24) bissue(IC<k + DQ>{});
                        if constexpr (WO && k >= 16) woissue(IC<(k + 8) % 10>{});
                        SBAR();
                        MPRIO(DN_HI, 3); da[1][1] = MFMA(w1[k & 1], DXF(1), da[1][1]); SBAR();
                        MPRIO(DN_HI, 4); da[2][0] = MFMA(w0[k & 1], DXF(2), da[2][0]); SBAR();
                        MPRIO(DN_HI, 5); da[2][1] = MFMA(w1[k & 1], DXF(2), da[2][1]); SBAR();
                        if constexpr (k + DQ < 24) aissue(IC<k + DQ>{});
                    });
                    __builtin_amdgcn_s_setprio(3);
                    STAMP(9);
                    if constexpr (WO) { CFENCE(); woissue(IC<2>{}); woissue(IC<3>{}); }
#pragma unroll
                    for (int n = 0; n < 2; ++n) {
#pragma unroll
                        for (int m = 0; m < 3; ++m) {
                            if (m != v) {
                                const unsigned dst = L_GU + (m * 3 + (v > m ? v - 1 : v)) * 4096 + lane * 16;
#pragma unroll
                                for (int e = 0; e < 4; ++e) *LP(f32x4, dst + e * 1024) = f32x4{da[m][n][4 * e], da[m][n][4 * e + 1], da[m][n][4 * e + 2], da[m][n][4 * e + 3]};
                            }
                        }
                        lpost(C_DP + v, 2 * n + 1); lwait_all(C_DP, 4, 2 * n + 1, lane);
                        if (v < 3) {
                            f32x16 t = v == 0 ? da[0][n] : (v == 1 ? da[1][n] : da[2][n]);
#pragma unroll
                            for (int o = 0; o < 3; ++o)
#pragma unroll
                                for (int e = 0; e < 4; ++e) {
                                    const f32x4 u = *LP(const f32x4, L_GU + (v * 3 + o) * 4096 + e * 1024 + lane * 16);
                                    t[4 * e] += u[0]; t[4 * e + 1] += u[1]; t[4 * e + 2] += u[2]; t[4 * e + 3] += u[3];
                                }
                            if (n) acc1 = t; else acc0 = t;
                        }
                        if (n == 0) { lpost(C_DP + v, 2); lwait_all(C_DP, 4, 2, lane); }
                    }
                    if constexpr (!WO) {
                        if (v < 3) {
                            float* pt = (float*)(ws + WS_PT) + (size_t)(b * 3 + v) * 2048 + lane;
#pragma unroll
                            for (int e = 0; e < 16; ++e) { pt[e * 64] = acc0[e]; pt[1024 + e * 64] = acc1[e]; }
                        }
                    }
                } else {
#pragma unroll
                    for (int u = 0; u < 20; ++u) glds16(dvo + u * 8 * 1280, p.wo + (size_t)v * 160 * 1280 + dc * 128, myring + u * 1024);
                    if (v < 3) {
                        const float* pt = (const float*)(ws + WS_PT) + (size_t)(b * 3 + v) * 2048 + lane;
#pragma unroll
                        for (int e = 0; e < 16; ++e) { acc0[e] = pt[e * 64]; acc1[e] = pt[1024 + e * 64]; }
                    }
                }
                if constexpr (WO) { WAITV(0); lpost(C_DP + v, 4); }
                if constexpr (WO && ATT) {
                    if (v == 3) {
                        __builtin_amdgcn_s_setprio(0);
                        const int mi = (lane >> 4) < nm ? (lane >> 4) : 0;
                        gu64* f = (gu64*)(ws + WS_OF + (mfirst + mi) * 128) + (lane & 15);
                        _Pragma("clang loop unroll(disable)") for (unsigned spins = 0; spins < (1u << 14); ++spins) {
                            const unsigned long long x = __hip_atomic_load(f, RLX_AGENT);
                            const unsigned long long ok = __ballot((unsigned)x == tag && (unsigned)(x >> 32) == tag);
                            const unsigned mine = (unsigned)(ok >> ((lane & 3) * 16));
                            *((volatile AS3 unsigned*)lcnt(C_OS) + (lane & 3)) = ((mine & 0xffu) == 0xffu ? 1u : 0u) | ((mine & 0xff00u) == 0xff00u ? 2u : 0u);
                            if (ok == ~0ull) break;
                            TMO(spins, 1u << 14, 300, tag);
                            __builtin_amdgcn_s_sleep(OF_SLEEP);
                        }
                    }
                }
                if (v < nm) {
                    const int m = mfirst + v;
                    const unsigned ba0 = L_PANEL + (4 * h + tq) * 128 + bq, ba1 = ba0 ^ 64;
                    if constexpr (WO) {
                        const char* xrow = p.x + (size_t)(m * 32 + r) * 1280 + dc * 128 + h * 16;
                        u32x4 xres[4];
#pragma unroll
                        for (int e = 0; e < 4; ++e) xres[e] = *(const u32x4*)(xrow + e * 32);
                        lwait_all(C_DP, 4, 4, lane);
                        const __amdgpu_buffer_rsrc_t ro = mkrsrc(ws + WS_OT + (size_t)m * 40960, 40960);
                        int hf = 0;
                        f32x16 t0[2] = {}, t1[2] = {};
#pragma unroll
                        for (int it = 0; it < 2; ++it) {
                            if constexpr (ATT) {
                                _Pragma("clang loop unroll(disable)") for (unsigned spins = 0; spins < (1u << 17); ++spins) {
                                    const unsigned ok = __builtin_amdgcn_readfirstlane(*((volatile AS3 unsigned*)lcnt(C_OS) + v));
                                    if (it == 0) { if (ok) { hf = (ok & 1) ? 0 : 1; break; } }
                                    else if (ok == 3) break;
                                    __builtin_amdgcn_s_sleep(1);
                                }
                                CFENCE();
                            }
                            if (it == 0) STAMP(10);
                            u32x4 of[20];
#pragma unroll
                            for (int s = 0; s < 20; ++s) of[s] = __builtin_amdgcn_raw_buffer_load_b128(ro, lane * 16 + s * 1024, hf * 20480, 16);
                            const unsigned a0 = ba0 + hf * 40960, a1 = ba1 + hf * 40960, l0 = a0 + (36 * h + 16) * 128, l1 = a1 + (36 * h + 16) * 128;
                            bf16x8 w0[2], w1[2];
                            w0[0] = ldstr2(a0, a0 + 1024); w1[0] = ldstr2(a1, a1 + 1024);
                            static_for<0, 20>([&](auto S) {
                                constexpr int s = S.value, n = s + 1, gg = n / 5, jj = n % 5;
                                const bf16x8 xf = __builtin_bit_cast(bf16x8, of[s]);
                                if constexpr (n < 20) {
                                    if constexpr (jj < 4) {
                                        constexpr unsigned o = (80 * gg + 20 * jj) * 128;
                                        w0[n & 1] = ldstr2(a0 + o, a0 + o + 1024); w1[n & 1] = ldstr2(a1 + o, a1 + o + 1024);
                                    } else {
                                        constexpr unsigned o = 80 * gg * 128;
                                        w0[n & 1] = ldstr2(l0 + o, l0 + o + 2560); w1[n & 1] = ldstr2(l1 + o, l1 + o + 2560);
                                    }
                                }
                                SBAR();
                                t0[it] = MFMA(w0[s & 1], xf, t0[it]); t1[it] = MFMA(w1[s & 1], xf, t1[it]);
                                SBAR();
                            });
                            hf ^= 1;
                        }
                        acc0 += t0[0] + t0[1]; acc1 += t1[0] + t1[1];
                        char* orow = p.out + (size_t)(m * 32 + r) * 1280 + dc * 128 + h * 16;
#pragma unroll
                        for (int n = 0; n < 2; ++n) {
                            const f32x16& a = n ? acc1 : acc0;
#pragma unroll
                            for (int qq = 0; qq < 2; ++qq) {
                                float lo[4], hi[4];
#pragma unroll
                                for (int e = 0; e < 4; ++e) {
                                    auto sw = __builtin_amdgcn_permlane32_swap(__float_as_uint(a[8 * qq + e]), __float_as_uint(a[8 * qq + 4 + e]), false, false);
                                    lo[e] = __uint_as_float(sw[0]); hi[e] = __uint_as_float(sw[1]);
                                }
                                const u32x4 xv = xres[2 * n + qq];
                                const u32x4 ov = {pk2(lo[0] + bflo(xv[0]), lo[1] + bfhi(xv[0])), pk2(lo[2] + bflo(xv[1]), lo[3] + bfhi(xv[1])),
                                                  pk2(hi[0] + bflo(xv[2]), hi[1] + bfhi(xv[2])), pk2(hi[2] + bflo(xv[3]), hi[3] + bfhi(xv[3]))};
                                *(u32x4*)(orow + (2 * n + qq) * 32) = ov;
                            }
                        }
                        STAMP(11);
                    }
                }
            }
        }
    } else {
        LANE_CONSTS();
        bf16x8 Q0a, Q0b; u32x2 q1a, q1b;
        const int head = 4 * g + w;
        const AttSync as{lane, ws + WS_KV, (unsigned)((w < 3 ? w * 3072 : CHUNK - 3072) + lane * 16), (unsigned)(w < 3 ? w * 3072 : CHUNK - 3072), w};
        __builtin_amdgcn_s_setprio(3);
        unsigned long long fk = 0, fv = 0; bool early = false;
        if constexpr (PRO) {
            BLOCK_BARRIER();
            waitv5<15>(x0[0], x0[1], x0[2], x0[3], x0[4]);
            STAMP(1);
            ln_rows(x0, ra, lane);
            waitv5<10>(x1[0], x1[1], x1[2], x1[3], x1[4]);
            ln_rows(x1, ra + 4, lane);
            lpost(C_AP + w, 1);
            waitv5<5>(x2[0], x2[1], x2[2], x2[3], x2[4]);
            ln_rows(x2, rb, lane);
            waitv5<0>(x3[0], x3[1], x3[2], x3[3], x3[4]);
            ln_rows(x3, rb + 4, lane);
            lpost(C_AP + w, 2);
            unsigned pa[4];
#pragma unroll
            for (int s = 0; s < 4; ++s) pa[s] = L_PANEL + h * 512 + ((r ^ ((2 * s + h) & 7)) << 4);
            f32x16 qa = {}, qb = {};
            const unsigned wa = L_WQ + (8 * h + tq) * 160 + w * 40 + tb * 32 + (tb ? 0 : tp * 8);
            lwait_all(C_AP, 4, 2, lane); lwait_all(C_PP, 4, 1, lane);
            if constexpr (FUSED) tag = __builtin_amdgcn_readfirstlane(*(volatile AS3 unsigned*)lcnt(C_TAG));
            STAMP(2);
            bf16x8 fw[2][4], fa[2][4], fb[2][4];
#pragma unroll
            for (int s4 = 0; s4 < 4; ++s4) { fw[0][s4] = ldstr2(wa + s4 * 2560, wa + s4 * 2560 + 640); fa[0][s4] = ldsf(pa[s4] + s4 * 1024); fb[0][s4] = ldsf(pa[s4] + 40960 + s4 * 1024); }
            unsigned pk = 0;
            static_for<0, 10>([&](auto S) {
                constexpr int st = S.value, nx = st + 1;
                if constexpr (st == 3 || st == 5 || st == 7) lcheck(pk, C_WP, (st - 1) / 2, lane);
                if constexpr (st == 6 && ATT) gload8x2_sc1(fk, fv, lane * 8, ws + WS_KF + xc * 1024);
                if constexpr (st == 8 && ATT) {
                    waitv8x2(fk, fv);
                    early = __all((unsigned)(fk >> 32) == tag && (unsigned)(fv >> 32) == tag);
                    if (early) { lwait_all(C_AP, 4, 6, lane); att_dma(as, 0); att_dma(as, 1); }
                    STAMP(8);
                }
#pragma unroll
                for (int s4 = 0; s4 < 4; ++s4) {
                    qa = MFMA(fw[st & 1][s4], fa[st & 1][s4], qa); SBAR();
                    if constexpr (nx < 10) { const unsigned o = wa + (nx & 3) * 10240 + s4 * 2560; fw[nx & 1][s4] = ldstr2(o, o + 640); SBAR(); }
                    qb = MFMA(fw[st & 1][s4], fb[st & 1][s4], qb); SBAR();
                    if constexpr (nx < 10) { fa[nx & 1][s4] = ldsf(pa[s4] + (nx * 4 + s4) * 1024); fb[nx & 1][s4] = ldsf(pa[s4] + 40960 + (nx * 4 + s4) * 1024); SBAR(); }
                    if (s4 == 2 && (st == 2 || st == 4 || st == 6)) { pk = lpeek(C_WP, lane); SBAR(); }
                }
                if constexpr (!(st & 1) && st <= 6) lpost(C_AP + w, 3 + st / 2);
            });
            lpost(C_AP + w, 8);
            Q0a = pack8(qa, 0, QSCALE); Q0b = pack8(qb, 0, QSCALE);
            q1a = u32x2{pk2(qa[8] * QSCALE, qa[9] * QSCALE), pk2(qa[10] * QSCALE, qa[11] * QSCALE)};
            q1b = u32x2{pk2(qb[8] * QSCALE, qb[9] * QSCALE), pk2(qb[10] * QSCALE, qb[11] * QSCALE)};
            if (h) { q1a = u32x2{0u, 0u}; q1b = u32x2{0u, 0u}; }
            if constexpr (!ATT) {
                char* qt = ws + WS_QT + (size_t)((2 * ti) * 32 + head) * 2048 + lane * 16;
                *(bf16x8*)qt = Q0a; *(u32x2*)(qt + 1024) = q1a; *(bf16x8*)(qt + 65536) = Q0b; *(u32x2*)(qt + 65536 + 1024) = q1b;
            }
            STAMP(3);
        } else {
            BLOCK_BARRIER();
            if constexpr (ATT) {
                const char* qt = ws + WS_QT + (size_t)((2 * ti) * 32 + head) * 2048 + lane * 16;
                Q0a = *(const bf16x8*)qt; q1a = *(const u32x2*)(qt + 1024); Q0b = *(const bf16x8*)(qt + 65536); q1b = *(const u32x2*)(qt + 65536 + 1024);
            }
        }
        if constexpr (ATT) {
            float na = 0.f, nb = 0.f;
            {
                const u32x4 a0 = __builtin_bit_cast(u32x4, Q0a), b0 = __builtin_bit_cast(u32x4, Q0b);
#pragma unroll
                for (int e = 0; e < 4; ++e) { na += bflo(a0[e]) * bflo(a0[e]) + bfhi(a0[e]) * bfhi(a0[e]); nb += bflo(b0[e]) * bflo(b0[e]) + bfhi(b0[e]) * bfhi(b0[e]); }
#pragma unroll
                for (int e = 0; e < 2; ++e) { na += bflo(q1a[e]) * bflo(q1a[e]) + bfhi(q1a[e]) * bfhi(q1a[e]); nb += bflo(q1b[e]) * bflo(q1b[e]) + bfhi(q1b[e]) * bfhi(q1b[e]); }
                na += __shfl_xor(na, 32); nb += __shfl_xor(nb, 32);
            }
            if (!early) {
                gu64* f = (gu64*)(ws + WS_KF + xc * 1024) + lane;
                _Pragma("clang loop unroll(disable)") for (unsigned spins = 0; spins < (1u << 14); ++spins) {
                    fk = __hip_atomic_load(f, RLX_AGENT); fv = __hip_atomic_load(f + 64, RLX_AGENT);
                    if (__all((unsigned)(fk >> 32) == tag && (unsigned)(fv >> 32) == tag)) break;
                    TMO(spins, 1u << 14, 301, tag);
                    __builtin_amdgcn_s_sleep(4);
                }
                if constexpr (PRO) lwait_all(C_AP, 4, 6, lane);
                att_dma(as, 0); att_dma(as, 1);
            }
            STAMP(4);
            if constexpr (PRO) lwait_all(C_AP, 4, 8, lane);
            att_dma(as, 2);
            const float kn2 = wave_max(__uint_as_float((unsigned)fk)), vmx = wave_max(__uint_as_float((unsigned)fv));
            const float Ma = __builtin_sqrtf(na * kn2) * 1.01f, Mb = __builtin_sqrtf(nb * kn2) * 1.01f;
            const bool fast = __all(Ma <= BOUND_MAX && Mb <= BOUND_MAX && vmx <= 1e30f);
            const u32x2 a1 = {(fast && h) ? pk2(-Ma, 0.f) : q1a[0], q1a[1]}, b1 = {(fast && h) ? pk2(-Mb, 0.f) : q1b[0], q1b[1]};
            f32x16 Oa = {}, Ob = {};
            WAITV(6); lpost(C_AP + w, AP0); lwait_all(C_AP, 4, AP0, lane);
            __builtin_amdgcn_s_setprio(ATT_PRIO);
            STAMP(5); STAMPC(12);
            if (fast) att_fast(as, Q0a, __builtin_bit_cast(s16x4, a1), Q0b, __builtin_bit_cast(s16x4, b1), Oa, Ob);
            else att_safe(as, Q0a, __builtin_bit_cast(s16x4, a1), Q0b, __builtin_bit_cast(s16x4, b1), Oa, Ob);
            STAMP(6); STAMPC(13);
            const __amdgpu_buffer_rsrc_t ro = mkrsrc(ws + WS_OT, 64 * 40 * 1024);
#pragma unroll
            for (int q = 0; q < 2; ++q) {
                const f32x16& O = q ? Ob : Oa;
                const float inv = 1.f / __shfl(O[8], r + 32);
                const unsigned base = ((2 * ti + q) * 40 + 5 * g) * 1024;
                __builtin_amdgcn_raw_buffer_store_b128(__builtin_bit_cast(u32x4, pack8(O, 0, inv)), ro, base + w * 1024 + lane * 16, 0, 16);
                if (!h) {
                    u32x2 t = {pk2(O[8] * inv, O[9] * inv), pk2(O[10] * inv, O[11] * inv)};
                    __builtin_amdgcn_raw_buffer_store_b64(t, ro, base + 4096 + ((w >> 1) * 32 + r) * 16 + (w & 1) * 8, 0, 16);
                }
            }
            if constexpr (WO) {
                WAITV(0);
                if (lane < 2) __hip_atomic_store((gu32*)(ws + WS_OF) + (2 * ti + lane) * 32 + head, tag, RLX_AGENT);
            }
            STAMP(7);
        }
    }
}

template <int PH>
static void launch(const Params& p, hipStream_t stream) {
    (void)hipFuncSetAttribute((const void*)mega<PH>, hipFuncAttributeMaxDynamicSharedMemorySize, L_TOTAL);
    mega<PH><<<256, 512, L_TOTAL, stream>>>(p);
}

extern "C" void kernel_launch(void* const* d_in, const int* in_sizes, int n_in, void* d_out, int out_size, void* d_ws, size_t ws_size, hipStream_t stream) {
    Params p;
    p.x = (const char*)d_in[0]; p.wq = (const char*)d_in[1]; p.wk = (const char*)d_in[2]; p.wv = (const char*)d_in[3]; p.wo = (const char*)d_in[4];
    p.w1 = (const char*)d_in[6]; p.w3 = (const char*)d_in[7]; p.w2 = (const char*)d_in[8];
    p.out = (char*)d_out; p.ws = (char*)d_ws;
    launch<27>(p, stream);
}
```

```cpp
#include <hip/hip_runtime.h>
#include <hip/hip_bf16.h>
#include <stdint.h>

#define AS1 __attribute__((address_space(1)))
#define AS3 __attribute__((address_space(3)))
typedef __bf16 bf16x2 __attribute__((ext_vector_type(2)));
typedef __bf16 bf16x8 __attribute__((ext_vector_type(8)));
typedef short s16x4 __attribute__((ext_vector_type(4)));
typedef short s16x8 __attribute__((ext_vector_type(8)));
typedef float f32x2 __attribute__((ext_vector_type(2)));
typedef float f32x4 __attribute__((ext_vector_type(4)));
typedef float f32x16 __attribute__((ext_vector_type(16)));
typedef unsigned u32x2 __attribute__((ext_vector_type(2)));
typedef unsigned u32x4 __attribute__((ext_vector_type(4)));
typedef AS1 unsigned gu32;
typedef AS1 unsigned long long gu64;

#define ATT_PRIO 2
#define GEM_PRIO 0
#define GU_HI 0
#define DN_HI 0
#define MPRIO(mask, i) __builtin_amdgcn_s_setprio(((mask) >> (i)) & 1 ? 3 : 0)
#define GU_DELAY 8
#define GU_ROT 4
#define GU_GQ 5
#define OF_SLEEP 8
#define HF_SLEEP 20
#define DN_DQ 8
#define SPLIT_MODE 0

constexpr int SEQ = 2048, DM = 640, FF = 1536;
constexpr unsigned WS_KF = 0;
constexpr unsigned WS_HF = 16384;
constexpr unsigned WS_OF = 32768;
constexpr unsigned WS_EP = 49152;
constexpr unsigned KVD = 5376;
constexpr unsigned KV_K1 = 2048, KV_V = 2560, KV_VT = 1408;
constexpr unsigned WS_KV = 65536;
constexpr unsigned WS_HT = WS_KV + 176128;
constexpr unsigned WS_OT = WS_HT + 64 * 96 * 1024;
constexpr unsigned WS_QT = WS_OT + 64 * 40 * 1024;
constexpr unsigned WS_PT = WS_QT + 64 * 32 * 2048;
constexpr unsigned WS_ST = WS_PT + 256 * 3 * 32 * 256;
constexpr unsigned L_PANEL = 0;
constexpr unsigned L_KV = 81920;
constexpr unsigned L_WQ = 81920;
constexpr unsigned L_GU = 124928;
constexpr unsigned GSL = 12320;
constexpr unsigned L_WKV = 124928;
constexpr unsigned L_MISC = 161920;
constexpr unsigned L_TOTAL = 163840;
constexpr unsigned CHUNK = 2 * KVD;
constexpr int NCHUNK = 16;
enum { C_AP = 0  , C_GP = 4  , C_DP = 8  , C_PP = 12  , C_WP = 16  , C_KVP = 20, C_KVFIN, C_TAG, C_OS = 24  , C_N = 28 };

struct Params { const char* x; const char* wq; const char* wk; const char* wv; const char* wo; const char* w1; const char* w3; const char* w2; char* out; char* ws; };

#define SBAR() __builtin_amdgcn_sched_barrier(0)
#define PIN(x) asm volatile("" : "+v"(x))
#define CFENCE() asm volatile("" ::: "memory")
#define WAITV(n) asm volatile("s_waitcnt vmcnt(" #n ")" ::: "memory")
#define MFMA(a, b, c) __builtin_amdgcn_mfma_f32_32x32x16_bf16((a), (b), (c), 0, 0, 0)
#define MFMA8(a, b, c) __builtin_amdgcn_mfma_f32_32x32x8bf16_1k((a), (b), (c), 0, 0, 0)
#define RLX_AGENT __ATOMIC_RELAXED, __HIP_MEMORY_SCOPE_AGENT
#define RLX_WG __ATOMIC_RELAXED, __HIP_MEMORY_SCOPE_WORKGROUP

#define TMO(spins, lim, code, tgt) do {} while (0)
#define STAMP(id) do {} while (0)
#define STAMPC(id) do {} while (0)

#define LP(T, off) ((AS3 T*)(uintptr_t)(unsigned)(off))
__device__ __forceinline__ bf16x8 ldsf(unsigned off) { return *LP(const bf16x8, off); }
__device__ __forceinline__ s16x4 ldstr(unsigned off) { return __builtin_amdgcn_ds_read_tr16_b64_v4i16(LP(s16x4, off)); }
__device__ __forceinline__ bf16x8 ldstr2(unsigned off0, unsigned off1) {
    s16x4 a = ldstr(off0), b = ldstr(off1);
    return __builtin_bit_cast(bf16x8, __builtin_shufflevector(a, b, 0, 1, 2, 3, 4, 5, 6, 7));
}
template <int E> __device__ __forceinline__ s16x4 half4(bf16x8 v) { const s16x8 t = __builtin_bit_cast(s16x8, v); return __builtin_shufflevector(t, t, 4 * E, 4 * E + 1, 4 * E + 2, 4 * E + 3); }
__device__ __forceinline__ unsigned pk2(float lo, float hi) { bf16x2 v = {(__bf16)lo, (__bf16)hi}; return __builtin_bit_cast(unsigned, v); }
__device__ __forceinline__ float bflo(unsigned u) { return __uint_as_float(u << 16); }
__device__ __forceinline__ float bfhi(unsigned u) { return __uint_as_float(u & 0xffff0000u); }
__device__ __forceinline__ float rbf(float f) { return (float)(__bf16)f; }

__device__ __forceinline__ void glds16(unsigned voff, const char* sbase, unsigned ldsdst) {
    unsigned keep;
    asm volatile("s_mov_b32 %0, m0\n\ts_mov_b32 m0, %3\n\ts_nop 2\n\tglobal_load_lds_dwordx4 %1, %2\n\ts_mov_b32 m0, %0"
                 : "=&s"(keep) : "v"(voff), "s"(sbase), "s"(ldsdst) : "memory");
}
__device__ __forceinline__ void glds16_sc1(unsigned voff, const char* sbase, unsigned ldsdst) {
    unsigned keep;
    asm volatile("s_mov_b32 %0, m0\n\ts_mov_b32 m0, %3\n\ts_nop 2\n\tglobal_load_lds_dwordx4 %1, %2 sc1\n\ts_mov_b32 m0, %0"
                 : "=&s"(keep) : "v"(voff), "s"(sbase), "s"(ldsdst) : "memory");
}
__device__ __forceinline__ void glds16x3_sc1(unsigned voff, const char* sbase, unsigned ldsdst) {
    unsigned keep;
    asm volatile("s_mov_b32 %0, m0\n\ts_mov_b32 m0, %3\n\ts_nop 2\n\tglobal_load_lds_dwordx4 %1, %2 sc1\n\tglobal_load_lds_dwordx4 %1, %2 offset:1024 sc1\n\tglobal_load_lds_dwordx4 %1, %2 offset:2048 sc1\n\ts_mov_b32 m0, %0"
                 : "=&s"(keep) : "v"(voff), "s"(sbase), "s"(ldsdst) : "memory");
}
__device__ __forceinline__ void glds16x3v(unsigned v0, unsigned v1, unsigned v2, const char* sbase, unsigned ldsdst) {
    unsigned keep;
    asm volatile("s_mov_b32 %0, m0\n\ts_mov_b32 m0, %5\n\ts_nop 0\n\tglobal_load_lds_dwordx4 %1, %4\n\tglobal_load_lds_dwordx4 %2, %4 offset:1024\n\tglobal_load_lds_dwordx4 %3, %4 offset:2048\n\ts_mov_b32 m0, %0"
                 : "=&s"(keep) : "v"(v0), "v"(v1), "v"(v2), "s"(sbase), "s"(ldsdst) : "memory");
}
__device__ __forceinline__ void glds_gu(unsigned va, unsigned vb, unsigned vc, const char* s1, const char* s3, unsigned ldsdst) {
    unsigned keep;
    asm volatile("s_mov_b32 %0, m0\n\ts_mov_b32 m0, %8\n\ts_nop 0\n\tglobal_load_lds_dwordx4 %1, %4\n\tglobal_load_lds_dwordx4 %2, %5 offset:1024\n\t"
                 "s_mov_b32 exec_hi, 0\n\tglobal_load_lds_dwordx4 %3, %6 offset:2048\n\ts_not_b64 exec, exec\n\tglobal_load_lds_dwordx4 %3, %7 offset:2048\n\t"
                 "s_mov_b64 exec, -1\n\ts_mov_b32 m0, %0"
                 : "=&s"(keep) : "v"(va), "v"(vb), "v"(vc), "s"(s1), "s"(s3 - 1024), "s"(s1 - 2048), "s"(s3 - 2048), "s"(ldsdst) : "memory", "scc");
}
__device__ __forceinline__ void l2touch(const char* ga) {
    unsigned keep;
    asm volatile("s_mov_b32 %0, m0\n\ts_mov_b32 m0, %2\n\ts_nop 0\n\tglobal_load_lds_dword %1, off\n\ts_mov_b32 m0, %0" : "=&s"(keep) : "v"(ga), "s"(L_MISC + 1024) : "memory");
}
#define GLOAD16(dst, voff, sbase, imm) asm volatile("s_nop 4\n\tglobal_load_dwordx4 %0, %1, %2 offset:%3" : "=v"(dst) : "v"(voff), "s"(sbase), "i"(imm) : "memory")

__device__ __forceinline__ AS3 unsigned* lcnt(int c) { return LP(unsigned, L_MISC) + c; }
__device__ __forceinline__ void larrive(int c, int lane) { CFENCE(); if (lane == 0) __hip_atomic_fetch_add(lcnt(c), 1u, RLX_WG); CFENCE(); }
__device__ __forceinline__ void lset(int c, unsigned v, int lane) { CFENCE(); if (lane == 0) __hip_atomic_store(lcnt(c), v, RLX_WG); CFENCE(); }
__device__ __forceinline__ void lwait(int c, unsigned target) {
    CFENCE();
    _Pragma("clang loop unroll(disable)") for (unsigned spins = 0; spins < (1u << 22); ++spins) {
        if ((unsigned)__builtin_amdgcn_readfirstlane(__hip_atomic_load(lcnt(c), RLX_WG)) >= target) break;
        TMO(spins, 1u << 16, 100 + c, target);
        __builtin_amdgcn_s_sleep(1);
    }
    CFENCE();
}
__device__ __forceinline__ void lwait_all(int c, int n, unsigned target, int lane) {
    CFENCE();
    AS3 unsigned* q = lcnt(c) + (lane < n ? lane : 0);
    _Pragma("clang loop unroll(disable)") for (unsigned spins = 0; spins < (1u << 16); ++spins) {
        if (__all(__hip_atomic_load(q, RLX_WG) >= target)) break;
        TMO(spins, 1u << 16, 200 + c, target);
        __builtin_amdgcn_s_sleep(1);
    }
    CFENCE();
}
__device__ __forceinline__ void lpost(int c, unsigned v) { CFENCE(); *(volatile AS3 unsigned*)lcnt(c) = v; CFENCE(); }
__device__ __forceinline__ void linc(unsigned vaddr, unsigned vone) {
    asm volatile("s_mov_b64 exec, 1\n\tds_add_u32 %0, %1\n\ts_mov_b64 exec, -1" :: "v"(vaddr), "v"(vone) : "memory");
}
__device__ __forceinline__ unsigned lpeek(int c, int lane) { return *((volatile AS3 unsigned*)lcnt(c) + (lane & 3)); }
__device__ __forceinline__ void lcheck(unsigned pk, int c, unsigned target, int lane) {
    if (__builtin_expect(__any(pk < target), 0)) lwait_all(c, 4, target, lane);
    CFENCE();
}
__device__ __forceinline__ void gwait(const char* ws, unsigned off, int n, unsigned tag, int lane) {
    gu32* f = (gu32*)(ws + off) + (lane < n ? lane : 0);
    _Pragma("clang loop unroll(disable)") for (unsigned spins = 0; spins < (1u << 14); ++spins) {
        unsigned v = __hip_atomic_load(f, RLX_AGENT);
        if (__all(v == tag)) break;
        TMO(spins, 1u << 14, 300, off);
        __builtin_amdgcn_s_sleep(6);
    }
    CFENCE();
}
__device__ __forceinline__ float gwait64(const char* ws, unsigned off, unsigned tag, int lane) {
    gu64* f = (gu64*)(ws + off) + lane;
    unsigned long long v = 0;
    _Pragma("clang loop unroll(disable)") for (unsigned spins = 0; spins < (1u << 14); ++spins) {
        v = __hip_atomic_load(f, RLX_AGENT);
        if (__all((unsigned)(v >> 32) == tag)) break;
        __builtin_amdgcn_s_sleep(6);
    }
    CFENCE();
    return __uint_as_float((unsigned)v);
}
__device__ __forceinline__ __amdgpu_buffer_rsrc_t mkrsrc(const char* base, unsigned bytes) {
    return __builtin_amdgcn_make_buffer_rsrc((void*)base, 0, bytes, 0x00020000);
}
__device__ __forceinline__ float dpp_sum16(float v) {
    v += __int_as_float(__builtin_amdgcn_update_dpp(0, __float_as_int(v), 0x128, 0xf, 0xf, false));
    v += __int_as_float(__builtin_amdgcn_update_dpp(0, __float_as_int(v), 0x124, 0xf, 0xf, false));
    v += __int_as_float(__builtin_amdgcn_update_dpp(0, __float_as_int(v), 0x122, 0xf, 0xf, false));
    v += __int_as_float(__builtin_amdgcn_update_dpp(0, __float_as_int(v), 0x121, 0xf, 0xf, false));
    return v;
}
__device__ __forceinline__ float wave_max(float v) {
    int x = __float_as_int(v);
    x = max(x, __builtin_amdgcn_update_dpp(0, x, 0x128, 0xf, 0xf, false)); x = max(x, __builtin_amdgcn_update_dpp(0, x, 0x124, 0xf, 0xf, false));
    x = max(x, __builtin_amdgcn_update_dpp(0, x, 0x122, 0xf, 0xf, false)); x = max(x, __builtin_amdgcn_update_dpp(0, x, 0x121, 0xf, 0xf, false));
    const int a = max(__builtin_amdgcn_readlane(x, 0), __builtin_amdgcn_readlane(x, 16)), b = max(__builtin_amdgcn_readlane(x, 32), __builtin_amdgcn_readlane(x, 48));
    return __int_as_float(max(a, b));
}
__device__ __forceinline__ bf16x8 pack8(const f32x16& a, int b, float sc) {
    u32x4 r = {pk2(a[b] * sc, a[b + 1] * sc), pk2(a[b + 2] * sc, a[b + 3] * sc), pk2(a[b + 4] * sc, a[b + 5] * sc), pk2(a[b + 6] * sc, a[b + 7] * sc)};
    return __builtin_bit_cast(bf16x8, r);
}

template <int I> struct IC { static constexpr int value = I; };
template <int B, int E, class F> __device__ __forceinline__ void static_for(F&& f) { if constexpr (B < E) { f(IC<B>{}); static_for<B + 1, E>(f); } }
#define WAITVN(n) asm volatile("s_waitcnt vmcnt(%0)" :: "i"(n) : "memory")
__device__ __forceinline__ void GLOAD16S(u32x4& dst, unsigned voff, const char* sbase) { asm volatile("global_load_dwordx4 %0, %1, %2 sc1" : "=v"(dst) : "v"(voff), "s"(sbase) : "memory"); }
__device__ __forceinline__ void GLOAD16N(u32x4& dst, unsigned voff, const char* sbase) { asm volatile("global_load_dwordx4 %0, %1, %2" : "=v"(dst) : "v"(voff), "s"(sbase) : "memory"); }
__device__ __forceinline__ void GLOAD16N_IF(u32x4& dst, unsigned voff, const char* sbase, int cond) {
    asm volatile("s_cmp_eq_u32 %3, 0\n\ts_cbranch_scc1 1f\n\tglobal_load_dwordx4 %0, %1, %2\n1:" : "=v"(dst) : "v"(voff), "s"(sbase), "s"(cond) : "memory", "scc"); }
template <int N> __device__ __forceinline__ void waitv5(u32x4& a, u32x4& b, u32x4& c, u32x4& d, u32x4& e) { asm volatile("s_waitcnt vmcnt(%5)" : "+v"(a), "+v"(b), "+v"(c), "+v"(d), "+v"(e) : "i"(N) : "memory"); }
__device__ __forceinline__ void gload8x2_sc1(unsigned long long& a, unsigned long long& b, unsigned voff, const char* sbase) {
    asm volatile("global_load_dwordx2 %0, %2, %3 sc1\n\tglobal_load_dwordx2 %1, %2, %3 offset:512 sc1" : "=&v"(a), "=&v"(b) : "v"(voff), "s"(sbase) : "memory"); }
__device__ __forceinline__ void waitv8x2(unsigned long long& a, unsigned long long& b) { asm volatile("s_waitcnt vmcnt(0)" : "+v"(a), "+v"(b) :: "memory"); }
__device__ __forceinline__ void PIN2(u32x4& a, u32x4& b) { asm volatile("" : "+v"(a), "+v"(b) :: "memory"); }
__device__ __forceinline__ u32x4 scale8(unsigned x0, unsigned x1, unsigned x2, unsigned x3, unsigned clo, unsigned chi, float c) {
    unsigned o0, o1, o2, o3; float t0, t1, t2, t3, t4, t5, t6, t7;
    asm("v_dot2_f32_bf16 %4, %12, %16, %18\n\tv_dot2_f32_bf16 %5, %12, %17, %18\n\tv_dot2_f32_bf16 %6, %13, %16, %18\n\tv_dot2_f32_bf16 %7, %13, %17, %18\n\t"
        "v_dot2_f32_bf16 %8, %14, %16, %18\n\tv_dot2_f32_bf16 %9, %14, %17, %18\n\tv_dot2_f32_bf16 %10, %15, %16, %18\n\tv_dot2_f32_bf16 %11, %15, %17, %18\n\t"
        "v_cvt_pk_bf16_f32 %0, %4, %5\n\tv_cvt_pk_bf16_f32 %1, %6, %7\n\tv_cvt_pk_bf16_f32 %2, %8, %9\n\tv_cvt_pk_bf16_f32 %3, %10, %11"
        : "=&v"(o0), "=&v"(o1), "=&v"(o2), "=&v"(o3), "=&v"(t0), "=&v"(t1), "=&v"(t2), "=&v"(t3), "=&v"(t4), "=&v"(t5), "=&v"(t6), "=&v"(t7)
        : "v"(x0), "v"(x1), "v"(x2), "v"(x3), "v"(clo), "v"(chi), "v"(c));
    return u32x4{o0, o1, o2, o3};
}
template <int N> __device__ __forceinline__ void waitv3(u32x4& a, u32x4& b, u32x4& c) { asm volatile("s_waitcnt vmcnt(%3)" : "+v"(a), "+v"(b), "+v"(c) : "i"(N) : "memory"); }

constexpr int dn_younger(int k, int D, bool wo) {
    int n = 0, la = 0, lb = 0;
    for (int i = 0; i < D; ++i) { n += 2; if (i == k + 1) lb = n; }
    for (int i = 0; i < D; ++i) { n += 3; if (i == k) la = n; }
    for (int s = 0; s < k; ++s) {
        if (s + D < 24) { n += 2; if (s + D == k + 1) lb = n; }
        if (wo && s >= 16) n += 2;
        if (s + D < 24) { n += 3; if (s + D == k) la = n; }
    }
    return n - (la > lb ? la : lb);
}
constexpr float QSCALE = 0.22360679774997896f * 1.4426950408889634f;
#define BOUND_LIM 50.f
constexpr float BOUND_MAX = BOUND_LIM;

struct AttSync { int lane; const char* kv; unsigned dvoff; unsigned wo; int w; };
__device__ __forceinline__ unsigned kvslot(int c) { return L_KV + ((c + 2) & 3) * CHUNK; }
__device__ __forceinline__ void att_dma(const AttSync& a, int c) { glds16x3_sc1(a.dvoff, a.kv + (size_t)c * CHUNK, kvslot(c) + a.wo); }
constexpr unsigned AP0 = 16;
#define RING_X(c) do { WAITV(3); lpost(C_AP + as.w, AP0 + (c) + 1); } while (0)
#define RP_A
#define RP_B
#define RING_Y(c, pk) do { RP_A lcheck(pk, C_AP, AP0 + (c) + 1, lane); const int cn_ = (c) + 3 < NCHUNK ? (c) + 3 : NCHUNK - 1;   \
                           glds16x3_sc1(as.dvoff, as.kv + (size_t)cn_ * CHUNK, kvslot((c) + 3) + as.wo); RP_B } while (0)

__device__ __forceinline__ void att_fast(const AttSync& as, bf16x8 Q0a, s16x4 Q1a, bf16x8 Q0b, s16x4 Q1b, f32x16& Oa, f32x16& Ob) {
    const int lane = as.lane, r = lane & 31, h = lane >> 5;
    const unsigned ak0 = lane * 16, ak1 = h ? KV_V + 20 * 16 : KV_K1 + r * 16;
    const unsigned av = KV_V + h * 352 + (r <= 20 ? r : 21) * 16;
    const f32x16 zero = {};
    f32x16 Sa, Sb = {};
    bf16x8 K0[2], K1[2], V0[2], V1[2];
    u32x4 Pa0, Pa1, Pb0 = {}, Pb1 = {};
    unsigned pk = 0;
    unsigned long long rp0 = 0, rp1 = 0;
    V0[1] = bf16x8{}; V1[1] = bf16x8{};
    unsigned vk0 = L_KV + ak0, vk1 = L_KV + ak1, vv = L_KV + av;
    PIN(vk0); PIN(vk1); PIN(vv);
    K0[0] = ldsf(vk0 + 2 * CHUNK); K1[0] = ldsf(vk1 + 2 * CHUNK); K0[1] = ldsf(vk0 + 2 * CHUNK + 1024); V1[0] = ldsf(vv + 2 * CHUNK + 704); V0[0] = ldsf(vv + 2 * CHUNK);
    Sa = MFMA(K0[0], Q0a, zero); Sa = MFMA8(half4<0>(K1[0]), Q1a, Sa);
#define EX4(S, i) do { S[i] = __builtin_amdgcn_exp2f(S[i]); S[i + 1] = __builtin_amdgcn_exp2f(S[i + 1]); S[i + 2] = __builtin_amdgcn_exp2f(S[i + 2]); S[i + 3] = __builtin_amdgcn_exp2f(S[i + 3]); } while (0)
#define GAP(MF, LD, T, k, PW, j, S, i) do { MF; SBAR(); LD; PW[j] = pk2(T[k], T[k + 1]); PW[j + 1] = pk2(T[k + 2], T[k + 3]); PIN(PW); SBAR(); EX4(S, i); PIN(S); SBAR(); } while (0)
#define BC(x) __builtin_bit_cast(bf16x8, x)
#define STEP(e, d, KN, L3, LK0, LK1, LV) do { \
        GAP(Ob = MFMA(V0[e ^ 1], BC(Pb0), Ob), (void)0,                  Sb, 12, Pb1, 2, Sa, 0); \
        GAP(Sb = MFMA(K0[e], Q0b, zero),       (void)0,                  Sa, 0, Pa0, 0, Sa, 4); \
        GAP(Sb = MFMA8(half4<e>(K1[d]), Q1b, Sb), L3,                       Sa, 4, Pa0, 2, Sa, 8); \
        GAP(Ob = MFMA(V1[e ^ 1], BC(Pb1), Ob), K0[e] = ldsf(LK0),        Sa, 8, Pa1, 0, Sa, 12); \
        GAP(Oa = MFMA(V0[e], BC(Pa0), Oa),     LK1,                      Sa, 12, Pa1, 2, Sb, 0); \
        GAP(Sa = MFMA(K0[e ^ 1], Q0a, zero),   V1[e ^ 1] = ldsf((LV) + 704), Sb, 0, Pb0, 0, Sb, 4); \
        GAP(Sa = MFMA8(half4<e ^ 1>(K1[KN]), Q1a, Sa), V0[e ^ 1] = ldsf(LV),     Sb, 4, Pb0, 2, Sb, 8); \
        GAP(Oa = MFMA(V1[e], BC(Pa1), Oa),     (void)0,                  Sb, 8, Pb1, 0, Sb, 12); \
    } while (0)
#define CHUNK4(c, B, N) do { \
        STEP(0, 0, 0, (void)0, vk0 + (B) + KVD, K1[1] = ldsf(vk1 + (B) + KVD), vv + (B) + KV_VT); \
        RING_X(c); \
        STEP(1, 0, 1, pk = lpeek(C_AP, lane), vk0 + (B) + KVD + 1024, (void)0, vv + (B) + KVD); \
        RING_Y(c, pk); \
        STEP(0, 1, 1, (void)0, vk0 + (N), K1[0] = ldsf(vk1 + (N)), vv + (B) + KVD + KV_VT); \
        STEP(1, 1, 0, (void)0, vk0 + (N) + 1024, (void)0, vv + (N)); \
    } while (0)
    for (int c = 0; c < NCHUNK; c += 4) {
        CHUNK4(c, 2 * CHUNK, 3 * CHUNK); CHUNK4(c + 1, 3 * CHUNK, 0); CHUNK4(c + 2, 0, CHUNK); CHUNK4(c + 3, CHUNK, 2 * CHUNK);
    }
#undef CHUNK4
    Pb1[2] = pk2(Sb[12], Sb[13]); Pb1[3] = pk2(Sb[14], Sb[15]);
    Ob = MFMA(V0[1], BC(Pb0), Ob); Ob = MFMA(V1[1], BC(Pb1), Ob);
#undef STEP
#undef GAP
#undef EX4
}

__device__ __forceinline__ void att_safe(const AttSync& as, bf16x8 Q0a, s16x4 Q1a, bf16x8 Q0b, s16x4 Q1b, f32x16& Oa, f32x16& Ob) {
    const int lane = as.lane, r = lane & 31, h = lane >> 5;
    const unsigned ak0 = lane * 16, ak1 = h ? KV_V + 20 * 16 : KV_K1 + r * 16;
    const unsigned av = KV_V + h * 352 + (r <= 20 ? r : 21) * 16;
    const f32x16 zero = {};
    float ma = -INFINITY, mb = -INFINITY;
    unsigned long long rp0 = 0, rp1 = 0;
    for (int c = 0; c < NCHUNK; ++c) {
        const unsigned b0 = kvslot(c);
        for (int t = 0; t < 4; ++t) {
            if (t == 1) RING_X(c);
            if (t == 2) RING_Y(c, 0u);
            const unsigned base = b0 + (t >> 1) * KVD; const int e = t & 1;
            bf16x8 K0 = ldsf(base + ak0 + e * 1024), K1 = ldsf(base + ak1), V0 = ldsf(base + av + e * KV_VT), V1 = ldsf(base + av + e * KV_VT + 704);
#pragma unroll
            for (int q = 0; q < 2; ++q) {
                f32x16 S = MFMA(K0, q ? Q0b : Q0a, zero); S = MFMA8(e ? half4<1>(K1) : half4<0>(K1), q ? Q1b : Q1a, S);
                float mx = S[0];
#pragma unroll
                for (int i = 1; i < 16; ++i) mx = fmaxf(mx, S[i]);
                mx = fmaxf(mx, __shfl_xor(mx, 32));
                float& m = q ? mb : ma; f32x16& O = q ? Ob : Oa;
                const float mn = fmaxf(m, mx);
                const float al = __builtin_amdgcn_exp2f(m - mn);
                m = mn;
#pragma unroll
                for (int i = 0; i < 16; ++i) { O[i] *= al; S[i] = __builtin_amdgcn_exp2f(S[i] - mn); }
                O = MFMA(V0, pack8(S, 0, 1.f), O); O = MFMA(V1, pack8(S, 8, 1.f), O);
            }
        }
    }
}

__device__ __forceinline__ void xload8(u32x4 (&x)[5], u32x4 (&y)[5], const char* xb, unsigned vo, unsigned vo2, int skip) {
    asm volatile("s_cmp_lg_u32 %13, 0\n\ts_cbranch_scc1 1f\n\t"
                 "global_load_dwordx4 %0, %10, %12\n\tglobal_load_dwordx4 %1, %10, %12 offset:256\n\tglobal_load_dwordx4 %2, %10, %12 offset:512\n\t"
                 "global_load_dwordx4 %3, %10, %12 offset:768\n\tglobal_load_dwordx4 %4, %10, %12 offset:1024\n\t"
                 "global_load_dwordx4 %5, %11, %12\n\tglobal_load_dwordx4 %6, %11, %12 offset:256\n\tglobal_load_dwordx4 %7, %11, %12 offset:512\n\t"
                 "global_load_dwordx4 %8, %11, %12 offset:768\n\tglobal_load_dwordx4 %9, %11, %12 offset:1024\n1:"
                 : "=&v"(x[0]), "=&v"(x[1]), "=&v"(x[2]), "=&v"(x[3]), "=&v"(x[4]), "=&v"(y[0]), "=&v"(y[1]), "=&v"(y[2]), "=&v"(y[3]), "=&v"(y[4])
                 : "v"(vo), "v"(vo2), "s"(xb), "s"(skip) : "memory", "scc");
}
__device__ __forceinline__ void xload4(u32x4 (&x)[5], const char* xb, unsigned vo) {
    asm volatile("global_load_dwordx4 %0, %5, %6\n\tglobal_load_dwordx4 %1, %5, %6 offset:256\n\tglobal_load_dwordx4 %2, %5, %6 offset:512\n\t"
                 "global_load_dwordx4 %3, %5, %6 offset:768\n\tglobal_load_dwordx4 %4, %5, %6 offset:1024"
                 : "=&v"(x[0]), "=&v"(x[1]), "=&v"(x[2]), "=&v"(x[3]), "=&v"(x[4]) : "v"(vo), "s"(xb) : "memory");
}
__device__ __forceinline__ void ln_rows(const u32x4 (&xr)[5], int row0, int lane) {
    f32x2 xv[20];
    f32x2 s0 = {0.f, 0.f}, s1 = {0.f, 0.f}, q0 = {0.f, 0.f}, q1 = {0.f, 0.f};
#pragma unroll
    for (int i = 0; i < 20; i += 2) {
        const unsigned a = xr[i >> 2][i & 3], b = xr[i >> 2][(i & 3) + 1];
        xv[i] = f32x2{bflo(a), bfhi(a)}; xv[i + 1] = f32x2{bflo(b), bfhi(b)};
        s0 += xv[i]; q0 = __builtin_elementwise_fma(xv[i], xv[i], q0);
        s1 += xv[i + 1]; q1 = __builtin_elementwise_fma(xv[i + 1], xv[i + 1], q1);
    }
    s0 += s1; q0 += q1;
    const float s = dpp_sum16(s0[0] + s0[1]), ss = dpp_sum16(q0[0] + q0[1]);
    const float mean = s * (1.f / 640.f);
    const float var = fmaxf(ss * (1.f / 640.f) - mean * mean, 0.f);
    const float rs = __builtin_amdgcn_rcpf(rbf(__builtin_amdgcn_sqrtf(rbf(rbf(var) + 1e-5f)))), mo = -rbf(mean) * rs;
    const f32x2 rs2 = {rs, rs}, mo2 = {mo, mo};
    const int row = row0 + (lane >> 4), c = lane & 15;
    const unsigned wa = L_PANEL + (row >> 5) * 40960 + (c >> 1) * 1024 + (c & 1) * 512 + (((row & 31) ^ (c & 7)) << 4);
#pragma unroll
    for (int u = 0; u < 5; ++u) {
        u32x4 o;
#pragma unroll
        for (int e = 0; e < 4; ++e) { const f32x2 y = __builtin_elementwise_fma(xv[4 * u + e], rs2, mo2); o[e] = pk2(y[0], y[1]); }
        *LP(u32x4, wa + u * 8192) = o;
    }
}
#define BLOCK_BARRIER() asm volatile("s_waitcnt lgkmcnt(0)\n\ts_barrier" ::: "memory")

template <int PH>
__global__ void __launch_bounds__(512, 2) mega(Params p) {
    constexpr bool PRO = PH & 1, ATT = PH & 2, DN = PH & 8, WO = PH & 16;
    const int w = __builtin_amdgcn_readfirstlane(threadIdx.x >> 6);
    const int b = blockIdx.x, xc = b & 7, j = b >> 3;
    const int ti = (xc >> 1) * 8 + (j >> 2), g = (xc & 1) * 4 + (j & 3);
    constexpr bool FUSED = PH == 27;
    const int kmt = g & 1;
    const int ra = kmt * 32 + (w & 3) * 8, rb = (kmt ^ 1) * 32 + (w & 3) * 8;
    u32x4 x0[5], x1[5], x2[5], x3[5];
    if constexpr (PRO) {
        asm volatile("" :: "s"(p.x), "s"(p.ws), "s"(p.wq), "s"(p.wk));
        const char* xb = p.x + (size_t)ti * 64 * 1280;
        const unsigned xvo = ((threadIdx.x >> 4) & 3) * 1280 + (threadIdx.x & 15) * 16;
        xload8(x0, x1, xb + (size_t)ra * 1280, xvo, xvo + 5120, w >> 2);
        xload8(x2, x3, xb + (size_t)rb * 1280, xvo, xvo + 5120, w >> 2);
    }
    const char* pc; asm volatile("s_getpc_b64 %0" : "=s"(pc));
    unsigned tag = 1u;
    char* ws = p.ws;
    STAMP(0);
    if (threadIdx.x < C_N) *lcnt(threadIdx.x) = (threadIdx.x == C_WP || threadIdx.x == C_WP + 1 || threadIdx.x == C_PP || threadIdx.x == C_PP + 1) ? 100u : 0u;
    const bool haskv = (g >> 2) == (ti & 1);
    const char* wqb = p.wq + g * 160;
#define LANE_CONSTS() int lane = __builtin_amdgcn_mbcnt_hi(~0u, __builtin_amdgcn_mbcnt_lo(~0u, 0u)); PIN(lane); \
    const int r = lane & 31, h = lane >> 5, tq = (lane & 15) >> 2, tp = lane & 3, tb = (lane >> 4) & 1; (void)r; (void)h; (void)tq; (void)tp; (void)tb

    if (w >= 4) {
        LANE_CONSTS();
        const int v = w - 4;
        unsigned epv = 0u;
        if constexpr (FUSED) {
            asm volatile("global_load_dword %0, %1, %2 sc1" : "=v"(epv) : "v"(0u), "s"(p.ws + WS_EP) : "memory");
            constexpr unsigned CODE_PF = 65536;
            const unsigned line = (unsigned)(j * 4 + v) * 64u + lane;
            if (line * 128u < CODE_PF) l2touch(pc + line * 128u);
        }
#define GET_TAG() do { if constexpr (FUSED) { asm volatile("s_nop 0\n\tv_readfirstlane_b32 %0, %1\n\ts_nop 3" : "=s"(tag) : "v"(epv) : "memory"); tag += 0x9E3779B1u; } } while (0)
        __builtin_amdgcn_s_setprio(3);
        if constexpr (PRO) {
            const int kvm = 2 * ti + (g & 1); const bool isV = (g >> 1) & 1;
            const char* wkvb = isV ? p.wv : p.wk;
            if (haskv && v < 2) {
#pragma unroll
                for (int n = 0; n < 13; ++n) {
                    const unsigned o = v * 12800 + (n < 12 ? n * 1024 : 11776);
                    glds16(o + lane * 16, wkvb, L_WKV + o);
                }
            }
            BLOCK_BARRIER();
            if (v >= 2) {
                const int v2 = v - 2;
                unsigned wo5[5];
#pragma unroll
                for (int i = 0; i < 5; ++i) { const unsigned c = (v2 + 2 * i) * 64 + lane, row = c / 10u, col = c - row * 10u; wo5[i] = row * 1280 + col * 16; }
#pragma unroll
                for (int n = 0; n < 20; ++n) glds16(wo5[n % 5], wqb + (n / 5) * 64 * 1280, L_WQ + (v2 + 2 * n) * 1024);
                u32x4 wst[3][10];
                auto wst_issue = [&](auto I) {
                    constexpr int rr = I.value / 10, i = I.value % 10;
                    GLOAD16N(wst[rr][i], wo5[i % 5], wqb + (4 + 2 * rr + i / 5) * 64 * 1280);
                };
                static_for<0, 10>(wst_issue);
                WAITV(10);
                GET_TAG(); lpost(C_TAG, tag);
                lpost(C_PP + v, 1);
                STAMP(2);
                static_for<10, 30>(wst_issue);
                static_for<0, 3>([&](auto R) {
                    constexpr int rr = R.value;
                    waitv5<20 - 10 * rr>(wst[rr][0], wst[rr][1], wst[rr][2], wst[rr][3], wst[rr][4]); waitv5<63>(wst[rr][5], wst[rr][6], wst[rr][7], wst[rr][8], wst[rr][9]);
                    lwait_all(C_AP, 4, 3 + rr, lane);
#pragma unroll
                    for (int i = 0; i < 10; ++i) *LP(u32x4, L_WQ + (rr & 1) * 20480 + (v2 + 2 * i) * 1024 + lane * 16) = wst[rr][i];
                    lpost(C_WP + v, rr + 1);
                });
            } else {
              if (haskv) {
                f32x16 acc = {};
                const unsigned wa = L_WKV + (v * 320 + 8 * h + tq) * 40 + tb * 32 + (tb ? 0 : tp * 8);
                unsigned xa[4];
#pragma unroll
                for (int s = 0; s < 4; ++s) xa[s] = L_PANEL + kmt * 40960 + v * 20480 + h * 512 + ((r ^ ((2 * s + h) & 7)) << 4);
                WAITV(0);
                GET_TAG();
                STAMP(2);
                bf16x8 wf[20], xf[20];
#pragma unroll
                for (int s = 0; s < 20; ++s) wf[s] = ldstr2(wa + s * 640, wa + s * 640 + 160);
                lwait_all(C_AP, 4, 1, lane);
#pragma unroll
                for (int s = 0; s < 20; ++s) xf[s] = ldsf(xa[s & 3] + s * 1024);
                SBAR();
                if (isV) {
#pragma unroll
                    for (int s = 0; s < 20; ++s) acc = MFMA(xf[s], wf[s], acc);
                } else {
#pragma unroll
                    for (int s = 0; s < 20; ++s) acc = MFMA(wf[s], xf[s], acc);
                }
                if (v) {
#pragma unroll
                    for (int e = 0; e < 4; ++e) *LP(f32x4, L_WKV + 12800 + e * 1024 + lane * 16) = f32x4{acc[4 * e], acc[4 * e + 1], acc[4 * e + 2], acc[4 * e + 3]};
                    lpost(C_KVP, 1);
                } else {
                    lwait(C_KVP, 1);
#pragma unroll
                    for (int e = 0; e < 4; ++e) {
                        const f32x4 t = *LP(const f32x4, L_WKV + 12800 + e * 1024 + lane * 16);
                        acc[4 * e] += t[0]; acc[4 * e + 1] += t[1]; acc[4 * e + 2] += t[2]; acc[4 * e + 3] += t[3];
                    }
                    const __amdgpu_buffer_rsrc_t rk = mkrsrc(ws + WS_KV, 32 * KVD);
                    const unsigned base = ti * KVD, ke = g & 1;
                    const u32x4 f0 = __builtin_bit_cast(u32x4, pack8(acc, 0, 1.f)), f1 = __builtin_bit_cast(u32x4, pack8(acc, 8, 1.f));
                    float mx;
                    if (!isV) {
                        __builtin_amdgcn_raw_buffer_store_b128(f0, rk, base + ke * 1024 + lane * 16, 0, 16);
                        const u32x2 k1 = {f1[0], f1[1]};
                        if (!h) __builtin_amdgcn_raw_buffer_store_b64(k1, rk, base + KV_K1 + r * 16 + ke * 8, 0, 16);
                        lpost(C_KVFIN, 1);
                        float n2 = 0.f;
#pragma unroll
                        for (int e = 0; e < 4; ++e) n2 += bflo(f0[e]) * bflo(f0[e]) + bfhi(f0[e]) * bfhi(f0[e]);
                        if (!h) n2 += bflo(f1[0]) * bflo(f1[0]) + bfhi(f1[0]) * bfhi(f1[0]) + bflo(f1[1]) * bflo(f1[1]) + bfhi(f1[1]) * bfhi(f1[1]);
                        n2 += __shfl_xor(n2, 32);
                        mx = (n2 == n2) ? n2 : INFINITY;
                    } else {
                        const unsigned cst = r == 20 ? 0x3f803f80u : 0u;
                        const u32x4 c4 = {cst, cst, cst, cst};
                        if (r <= 21) {
                            __builtin_amdgcn_raw_buffer_store_b128(r >= 20 ? c4 : f0, rk, base + KV_V + ke * KV_VT + h * 352 + r * 16, 0, 16);
                            __builtin_amdgcn_raw_buffer_store_b128(r >= 20 ? c4 : f1, rk, base + KV_V + ke * KV_VT + 704 + h * 352 + r * 16, 0, 16);
                        }
                        lpost(C_KVFIN, 1);
                        float vm = 0.f;
#pragma unroll
                        for (int e = 0; e < 16; ++e) { const float a = fabsf(acc[e]); vm = (a <= vm) ? vm : ((a == a) ? a : INFINITY); }
                        mx = r < 20 ? vm : 0.f;
                    }
                    mx = wave_max(mx);
                    WAITV(0);
                    STAMP(15);
                    if (lane < 8) __hip_atomic_store((gu64*)(ws + WS_KF + lane * 1024 + (isV ? 512 : 0)) + kvm, ((unsigned long long)tag << 32) | __float_as_uint(mx), RLX_AGENT);
                }
            }
              else { WAITV(0); GET_TAG(); }
              STAMP(3);
            }
            if (haskv) lwait(C_KVFIN, 1);
#pragma unroll
            for (int n = 0; n < 2; ++n) {
                unsigned L = n * 64 + lane; L = (j * 4 + v) * 120 + (L < 120u ? L : 119u);
                const unsigned m3 = L >= 7680u, rem = L - (m3 ? 7680u : 0u), row = rem / 12u;
                l2touch((m3 ? p.w3 : p.w1) + row * 3072 + (xc & 1) * 1536 + (rem - row * 12u) * 128);
            }
            lwait_all(C_AP, 4, GU_DELAY, lane);
            STAMP(4);
            {
                const char* w1s = p.w1 + g * 384 + v * 96; const char* w3s = p.w3 + g * 384 + v * 96;
                auto goff = [](unsigned idx) { const unsigned row = idx / 6u; return row * 3072 + (idx - row * 6u) * 16; };
                const unsigned gva = goff(lane), gvb = goff((lane - 8) & 63), gvc = goff(64 + (h ? (r - 8) & 31 : r));
                const unsigned gdst = L_GU + v * 9216;
                auto gdma = [&](auto S) { constexpr int s = S.value; glds_gu(gva, gvb, gvc, w1s + (size_t)s * 16 * 3072, w3s + (size_t)s * 16 * 3072, gdst + (s % 3) * 3072); };
                static_for<0, 3>(gdma);
                f32x16 acc[2][3] = {};
                unsigned bn[3][2], pa[4], pb[4];
#pragma unroll
                for (int n = 0; n < 3; ++n)
#pragma unroll
                    for (int t = 0; t < 2; ++t) {
                        const unsigned idx = 6 * (8 * h + 4 * t + tq) + 2 * n + (tp >> 1);
                        const unsigned o = idx < 64u ? (tb ? 1024 + ((idx + 8) & 63) * 16 : idx * 16) : (tb ? 2560 + ((idx - 56) & 31) * 16 : 2048 + (idx - 64) * 16);
                        bn[n][t] = gdst + o + (tp & 1) * 8;
                    }
#pragma unroll
                for (int s = 0; s < 4; ++s) { pa[s] = L_PANEL + h * 512 + ((r ^ ((2 * s + h) & 7)) << 4); pb[s] = pa[s] + 40960; PIN(pb[s]); }
                bf16x8 wf[2][3], xa[2], xb2[2];
                auto rdw = [&](auto S) { constexpr int s = S.value;
#pragma unroll
                    for (int n = 0; n < 3; ++n) wf[s & 1][n] = ldstr2(bn[n][0] + (s % 3) * 3072, bn[n][1] + (s % 3) * 3072); };
                auto rdx = [&](auto S) { constexpr int s = S.value; xa[s & 1] = ldsf(pa[s & 3] + s * 1024); xb2[s & 1] = ldsf(pb[s & 3] + s * 1024); };
                WAITV(8);
                rdx(IC<0>{}); rdw(IC<0>{});
                asm volatile("s_waitcnt lgkmcnt(0)" ::: "memory");
                gdma(IC<3>{});
                static_for<0, 40>([&](auto S) {
                    constexpr int s = S.value; constexpr bool nx = s + 1 < 40;
                    MPRIO(GU_HI, 0); acc[0][0] = MFMA(wf[s & 1][0], xa[s & 1], acc[0][0]); SBAR();
                    if constexpr (nx) rdx(IC<s + 1>{});
                    SBAR();
                    MPRIO(GU_HI, 1); acc[1][0] = MFMA(wf[s & 1][0], xb2[s & 1], acc[1][0]); SBAR();
                    if constexpr (nx) { WAITVN(4 * ((s + 3 < 39 ? s + 3 : 39) - (s + 1))); rdw(IC<s + 1>{}); }
                    SBAR();
                    MPRIO(GU_HI, 2); acc[0][1] = MFMA(wf[s & 1][1], xa[s & 1], acc[0][1]); SBAR();
                    MPRIO(GU_HI, 3); acc[1][1] = MFMA(wf[s & 1][1], xb2[s & 1], acc[1][1]); SBAR();
                    if constexpr (s + 4 < 40) { asm volatile("s_waitcnt lgkmcnt(0)" ::: "memory"); gdma(IC<s + 4>{}); }
                    SBAR();
                    MPRIO(GU_HI, 4); acc[0][2] = MFMA(wf[s & 1][2], xa[s & 1], acc[0][2]); SBAR();
                    MPRIO(GU_HI, 5); acc[1][2] = MFMA(wf[s & 1][2], xb2[s & 1], acc[1][2]); SBAR();
                });
                lpost(C_GP + v, 42);
                __builtin_amdgcn_s_setprio(3);
                STAMP(6);
                const __amdgpu_buffer_rsrc_t rh = mkrsrc(ws + WS_HT, 64 * 96 * 1024);
#pragma unroll
                for (int m = 0; m < 2; ++m)
#pragma unroll
                    for (int n = 0; n < 3; ++n) {
                        f32x16 hv;
#pragma unroll
                        for (int e = 0; e < 8; ++e) {
                            const float gg = acc[m][n][e];
                            hv[e] = gg * __builtin_amdgcn_rcpf(1.f + __builtin_amdgcn_exp2f(-1.4426950408889634f * gg)) * acc[m][n][e + 8];
                        }
                        __builtin_amdgcn_raw_buffer_store_b128(__builtin_bit_cast(u32x4, pack8(hv, 0, 1.f)), rh, ((2 * ti + m) * 96 + g * 12 + 3 * v + n) * 1024 + lane * 16, 0, 16);
                    }
                if constexpr (DN) {
                    WAITV(0);
                    if (lane < 2) __hip_atomic_store((gu32*)(ws + WS_HF) + (2 * ti + lane) * 32 + g * 4 + v, tag, RLX_AGENT);
                }
                __builtin_amdgcn_s_setprio(0);
                if constexpr (DN) {
#pragma unroll
                    for (int n = 0; n < 2; ++n) {
                        unsigned L = (j * 4 + v) * 85 + n * 64 + lane; L = L < 10880u ? L : 10879u;
                        const unsigned row = L / 5u;
                        l2touch((row < 1536u ? p.w2 + row * 1280 : p.wo + (row - 1536u) * 1280) + (xc & 1) * 640 + (L - row * 5u) * 128);
                    }
                }
                STAMP(7);
            }
        } else {
            BLOCK_BARRIER();
        }
        if constexpr (DN || WO) {
            LANE_CONSTS();
            const unsigned bq = ((tb * 32 + (tp >> 1) * 16) ^ ((unsigned)(tq >> 1) << 6)) + (tp & 1) * 8;
            const unsigned drow = lane >> 3, dcol = ((lane & 7) ^ (((lane >> 4) & 1) << 2)) * 16;
            const int G = xc >> 1, r0 = G < 2 ? 6 * G : 12 + 5 * (G - 2), nr = G < 2 ? 6 : 5;
            if constexpr (FUSED) {
                if (b == 255 && v == 0) {
                    gu32* f = (gu32*)(ws + WS_HF) + (lane >> 3) * 64 + (lane & 7) * 4;
                    _Pragma("clang loop unroll(disable)") for (unsigned spins = 0; spins < (1u << 14); ++spins) {
                        bool ok = true;
#pragma unroll
                        for (int n = 0; n < 4; ++n) ok &= __hip_atomic_load(f + n * 512, RLX_AGENT) == tag;
                        if (__all(ok)) break;
                        TMO(spins, 1u << 14, 303, tag);
                        __builtin_amdgcn_s_sleep(32);
                    }
                    if (lane == 0) __hip_atomic_store((gu32*)(ws + WS_EP), tag, RLX_AGENT);
                }
            }
            if (j < 5 * nr) {
                const int dr = r0 + j / 5, dc = (xc & 1) * 5 + j % 5;
                const int mfirst = dr < 20 ? 3 * dr : 60 + 2 * (dr - 20), nm = dr < 20 ? 3 : 2;
                const unsigned dvo = drow * 1280 + dcol;
                const unsigned myring = L_PANEL + v * 20480;
                f32x16 acc0 = {}, acc1 = {};
                if constexpr (PRO) { lwait_all(C_GP, 4, 42, lane); lwait_all(C_AP, 4, 8, lane); }
                if constexpr (DN) {
                    f32x16 da[3][2] = {};
                    const int drot = (j % 5) * 4;
                    const char* bsrc = p.w2 + (size_t)v * 384 * 1280 + dc * 128;
                    const char* asrc = ws + WS_HT + ((size_t)mfirst * 96 + v * 24) * 1024;
                    const unsigned am1 = lane * 16 + 98304, am2 = lane * 16 + (nm == 3 ? 196608 : 98304);
                    constexpr int DQ = DN_DQ;
                    u32x4 aq[DQ][3];
                    auto bissue = [&](auto K) {
                        constexpr int k = K.value;
                        int kr = k + drot; kr = kr >= 24 ? kr - 24 : kr;
                        const char* sb = bsrc + (size_t)kr * 16 * 1280;
                        glds16(dvo, sb, myring + (k % 10) * 2048); glds16(dvo + 8 * 1280, sb, myring + (k % 10) * 2048 + 1024);
                    };
                    auto woissue = [&](auto U) {
                        constexpr int u = U.value;
                        const char* sb = p.wo + (size_t)(v * 160 + u * 16) * 1280 + dc * 128;
                        glds16(dvo, sb, myring + u * 2048); glds16(dvo + 8 * 1280, sb, myring + u * 2048 + 1024);
                    };
                    auto aissue = [&](auto K) {
                        constexpr int k = K.value;
                        int kr = k + drot; kr = kr >= 24 ? kr - 24 : kr;
                        const char* sb = asrc + (size_t)kr * 1024;
                        GLOAD16S(aq[k % DQ][0], lane * 16, sb); GLOAD16S(aq[k % DQ][1], am1, sb); GLOAD16S(aq[k % DQ][2], am2, sb);
                    };
                    static_for<0, DQ>(bissue);
                    if constexpr (PRO) {
                        gu32* f = (gu32*)(ws + WS_HF) + (mfirst + ((lane >> 3) < nm ? (lane >> 3) : 0)) * 32 + 8 * v + (lane & 7);
                        _Pragma("clang loop unroll(disable)") for (unsigned spins = 0; spins < (1u << 14); ++spins) {
                            if (__all(__hip_atomic_load(f, RLX_AGENT) == tag)) break;
                            TMO(spins, 1u << 14, 302, tag);
                            __builtin_amdgcn_s_sleep(HF_SLEEP);
                        }
                        CFENCE();
                    }
                    STAMP(8);
                    static_for<0, DQ>(aissue);
                    const unsigned ba0 = myring + (4 * h + tq) * 128 + bq, ba1 = ba0 ^ 64;
                    bf16x8 w0[2], w1[2];
                    WAITVN(3 * DQ);
                    w0[0] = ldstr2(ba0, ba0 + 1024); w1[0] = ldstr2(ba1, ba1 + 1024);
                    static_for<0, 24>([&](auto K) {
                        constexpr int k = K.value;
                        waitv3<dn_younger(k, DQ, WO)>(aq[k % DQ][0], aq[k % DQ][1], aq[k % DQ][2]);
                        constexpr unsigned o = ((k + 1) % 10) * 2048;
#define DXF(m) __builtin_bit_cast(bf16x8, aq[k % DQ][m])
                        SBAR();
                        MPRIO(DN_HI, 0); da[0][0] = MFMA(w0[k & 1], DXF(0), da[0][0]); SBAR();
                        if constexpr (k + 1 < 24) w0[(k + 1) & 1] = ldstr2(ba0 + o, ba0 + o + 1024);
                        SBAR();
                        MPRIO(DN_HI, 1); da[0][1] = MFMA(w1[k & 1], DXF(0), da[0][1]); SBAR();
                        if constexpr (k + 1 < 24) w1[(k + 1) & 1] = ldstr2(ba1 + o, ba1 + o + 1024);
                        SBAR();
                        MPRIO(DN_HI, 2); da[1][0] = MFMA(w0[k & 1], DXF(1), da[1][0]); SBAR();
                        if constexpr (k + DQ < 24) bissue(IC<k + DQ>{});
                        if constexpr (WO && k >= 16) woissue(IC<(k + 8) % 10>{});
                        SBAR();
                        MPRIO(DN_HI, 3); da[1][1] = MFMA(w1[k & 1], DXF(1), da[1][1]); SBAR();
                        MPRIO(DN_HI, 4); da[2][0] = MFMA(w0[k & 1], DXF(2), da[2][0]); SBAR();
                        MPRIO(DN_HI, 5); da[2][1] = MFMA(w1[k & 1], DXF(2), da[2][1]); SBAR();
                        if constexpr (k + DQ < 24) aissue(IC<k + DQ>{});
                    });
                    __builtin_amdgcn_s_setprio(3);
                    STAMP(9);
                    if constexpr (WO) { CFENCE(); woissue(IC<2>{}); woissue(IC<3>{}); }
#pragma unroll
                    for (int n = 0; n < 2; ++n) {
#pragma unroll
                        for (int m = 0; m < 3; ++m) {
                            if (m != v) {
                                const unsigned dst = L_GU + (m * 3 + (v > m ? v - 1 : v)) * 4096 + lane * 16;
#pragma unroll
                                for (int e = 0; e < 4; ++e) *LP(f32x4, dst + e * 1024) = f32x4{da[m][n][4 * e], da[m][n][4 * e + 1], da[m][n][4 * e + 2], da[m][n][4 * e + 3]};
                            }
                        }
                        lpost(C_DP + v, 2 * n + 1); lwait_all(C_DP, 4, 2 * n + 1, lane);
                        if (v < 3) {
                            f32x16 t = v == 0 ? da[0][n] : (v == 1 ? da[1][n] : da[2][n]);
#pragma unroll
                            for (int o = 0; o < 3; ++o)
#pragma unroll
                                for (int e = 0; e < 4; ++e) {
                                    const f32x4 u = *LP(const f32x4, L_GU + (v * 3 + o) * 4096 + e * 1024 + lane * 16);
                                    t[4 * e] += u[0]; t[4 * e + 1] += u[1]; t[4 * e + 2] += u[2]; t[4 * e + 3] += u[3];
                                }
                            if (n) acc1 = t; else acc0 = t;
                        }
                        if (n == 0) { lpost(C_DP + v, 2); lwait_all(C_DP, 4, 2, lane); }
                    }
                    if constexpr (!WO) {
                        if (v < 3) {
                            float* pt = (float*)(ws + WS_PT) + (size_t)(b * 3 + v) * 2048 + lane;
#pragma unroll
                            for (int e = 0; e < 16; ++e) { pt[e * 64] = acc0[e]; pt[1024 + e * 64] = acc1[e]; }
                        }
                    }
                } else {
#pragma unroll
                    for (int u = 0; u < 20; ++u) glds16(dvo + u * 8 * 1280, p.wo + (size_t)v * 160 * 1280 + dc * 128, myring + u * 1024);
                    if (v < 3) {
                        const float* pt = (const float*)(ws + WS_PT) + (size_t)(b * 3 + v) * 2048 + lane;
#pragma unroll
                        for (int e = 0; e < 16; ++e) { acc0[e] = pt[e * 64]; acc1[e] = pt[1024 + e * 64]; }
                    }
                }
                if constexpr (WO) { WAITV(0); lpost(C_DP + v, 4); }
                if constexpr (WO && ATT) {
                    if (v == 3) {
                        __builtin_amdgcn_s_setprio(0);
                        const int mi = (lane >> 4) < nm ? (lane >> 4) : 0;
                        gu64* f = (gu64*)(ws + WS_OF + (mfirst + mi) * 128) + (lane & 15);
                        _Pragma("clang loop unroll(disable)") for (unsigned spins = 0; spins < (1u << 14); ++spins) {
                            const unsigned long long x = __hip_atomic_load(f, RLX_AGENT);
                            const unsigned long long ok = __ballot((unsigned)x == tag && (unsigned)(x >> 32) == tag);
                            const unsigned mine = (unsigned)(ok >> ((lane & 3) * 16));
                            *((volatile AS3 unsigned*)lcnt(C_OS) + (lane & 3)) = ((mine & 0xffu) == 0xffu ? 1u : 0u) | ((mine & 0xff00u) == 0xff00u ? 2u : 0u);
                            if (ok == ~0ull) break;
                            TMO(spins, 1u << 14, 300, tag);
                            __builtin_amdgcn_s_sleep(OF_SLEEP);
                        }
                    }
                }
                if (v < nm) {
                    const int m = mfirst + v;
                    const unsigned ba0 = L_PANEL + (4 * h + tq) * 128 + bq, ba1 = ba0 ^ 64;
                    if constexpr (WO) {
                        const char* xrow = p.x + (size_t)(m * 32 + r) * 1280 + dc * 128 + h * 16;
                        u32x4 xres[4];
#pragma unroll
                        for (int e = 0; e < 4; ++e) xres[e] = *(const u32x4*)(xrow + e * 32);
                        lwait_all(C_DP, 4, 4, lane);
                        const __amdgpu_buffer_rsrc_t ro = mkrsrc(ws + WS_OT + (size_t)m * 40960, 40960);
                        int hf = 0;
                        f32x16 t0[2] = {}, t1[2] = {};
#pragma unroll
                        for (int it = 0; it < 2; ++it) {
                            if constexpr (ATT) {
                                _Pragma("clang loop unroll(disable)") for (unsigned spins = 0; spins < (1u << 17); ++spins) {
                                    const unsigned ok = __builtin_amdgcn_readfirstlane(*((volatile AS3 unsigned*)lcnt(C_OS) + v));
                                    if (it == 0) { if (ok) { hf = (ok & 1) ? 0 : 1; break; } }
                                    else if (ok == 3) break;
                                    __builtin_amdgcn_s_sleep(1);
                                }
                                CFENCE();
                            }
                            if (it == 0) STAMP(10);
                            u32x4 of[20];
#pragma unroll
                            for (int s = 0; s < 20; ++s) of[s] = __builtin_amdgcn_raw_buffer_load_b128(ro, lane * 16 + s * 1024, hf * 20480, 16);
                            const unsigned a0 = ba0 + hf * 40960, a1 = ba1 + hf * 40960, l0 = a0 + (36 * h + 16) * 128, l1 = a1 + (36 * h + 16) * 128;
                            bf16x8 w0[2], w1[2];
                            w0[0] = ldstr2(a0, a0 + 1024); w1[0] = ldstr2(a1, a1 + 1024);
                            static_for<0, 20>([&](auto S) {
                                constexpr int s = S.value, n = s + 1, gg = n / 5, jj = n % 5;
                                const bf16x8 xf = __builtin_bit_cast(bf16x8, of[s]);
                                if constexpr (n < 20) {
                                    if constexpr (jj < 4) {
                                        constexpr unsigned o = (80 * gg + 20 * jj) * 128;
                                        w0[n & 1] = ldstr2(a0 + o, a0 + o + 1024); w1[n & 1] = ldstr2(a1 + o, a1 + o + 1024);
                                    } else {
                                        constexpr unsigned o = 80 * gg * 128;
                                        w0[n & 1] = ldstr2(l0 + o, l0 + o + 2560); w1[n & 1] = ldstr2(l1 + o, l1 + o + 2560);
                                    }
                                }
                                SBAR();
                                t0[it] = MFMA(w0[s & 1], xf, t0[it]); t1[it] = MFMA(w1[s & 1], xf, t1[it]);
                                SBAR();
                            });
                            hf ^= 1;
                        }
                        acc0 += t0[0] + t0[1]; acc1 += t1[0] + t1[1];
                        char* orow = p.out + (size_t)(m * 32 + r) * 1280 + dc * 128 + h * 16;
#pragma unroll
                        for (int n = 0; n < 2; ++n) {
                            const f32x16& a = n ? acc1 : acc0;
#pragma unroll
                            for (int qq = 0; qq < 2; ++qq) {
                                float lo[4], hi[4];
#pragma unroll
                                for (int e = 0; e < 4; ++e) {
                                    auto sw = __builtin_amdgcn_permlane32_swap(__float_as_uint(a[8 * qq + e]), __float_as_uint(a[8 * qq + 4 + e]), false, false);
                                    lo[e] = __uint_as_float(sw[0]); hi[e] = __uint_as_float(sw[1]);
                                }
                                const u32x4 xv = xres[2 * n + qq];
                                const u32x4 ov = {pk2(lo[0] + bflo(xv[0]), lo[1] + bfhi(xv[0])), pk2(lo[2] + bflo(xv[1]), lo[3] + bfhi(xv[1])),
                                                  pk2(hi[0] + bflo(xv[2]), hi[1] + bfhi(xv[2])), pk2(hi[2] + bflo(xv[3]), hi[3] + bfhi(xv[3]))};
                                *(u32x4*)(orow + (2 * n + qq) * 32) = ov;
                            }
                        }
                        STAMP(11);
                    }
                }
            }
        }
    } else {
        LANE_CONSTS();
        bf16x8 Q0a, Q0b; u32x2 q1a, q1b;
        const int head = 4 * g + w;
        const AttSync as{lane, ws + WS_KV, (unsigned)((w < 3 ? w * 3072 : CHUNK - 3072) + lane * 16), (unsigned)(w < 3 ? w * 3072 : CHUNK - 3072), w};
        __builtin_amdgcn_s_setprio(3);
        unsigned long long fk = 0, fv = 0; bool early = false;
        if constexpr (PRO) {
            BLOCK_BARRIER();
            waitv5<15>(x0[0], x0[1], x0[2], x0[3], x0[4]);
            STAMP(1);
            ln_rows(x0, ra, lane);
            waitv5<10>(x1[0], x1[1], x1[2], x1[3], x1[4]);
            ln_rows(x1, ra + 4, lane);
            lpost(C_AP + w, 1);
            waitv5<5>(x2[0], x2[1], x2[2], x2[3], x2[4]);
            ln_rows(x2, rb, lane);
            waitv5<0>(x3[0], x3[1], x3[2], x3[3], x3[4]);
            ln_rows(x3, rb + 4, lane);
            lpost(C_AP + w, 2);
            unsigned pa[4];
#pragma unroll
            for (int s = 0; s < 4; ++s) pa[s] = L_PANEL + h * 512 + ((r ^ ((2 * s + h) & 7)) << 4);
            f32x16 qa = {}, qb = {};
            const unsigned wa = L_WQ + (8 * h + tq) * 160 + w * 40 + tb * 32 + (tb ? 0 : tp * 8);
            lwait_all(C_AP, 4, 2, lane); lwait_all(C_PP, 4, 1, lane);
            if constexpr (FUSED) tag = __builtin_amdgcn_readfirstlane(*(volatile AS3 unsigned*)lcnt(C_TAG));
            STAMP(2);
            bf16x8 fw[2][4], fa[2][4], fb[2][4];
#pragma unroll
            for (int s4 = 0; s4 < 4; ++s4) { fw[0][s4] = ldstr2(wa + s4 * 2560, wa + s4 * 2560 + 640); fa[0][s4] = ldsf(pa[s4] + s4 * 1024); fb[0][s4] = ldsf(pa[s4] + 40960 + s4 * 1024); }
            unsigned pk = 0;
            static_for<0, 10>([&](auto S) {
                constexpr int st = S.value, nx = st + 1;
                if constexpr (st == 3 || st == 5 || st == 7) lcheck(pk, C_WP, (st - 1) / 2, lane);
                if constexpr (st == 6 && ATT) gload8x2_sc1(fk, fv, lane * 8, ws + WS_KF + xc * 1024);
                if constexpr (st == 8 && ATT) {
                    waitv8x2(fk, fv);
                    early = __all((unsigned)(fk >> 32) == tag && (unsigned)(fv >> 32) == tag);
                    if (early) { lwait_all(C_AP, 4, 6, lane); att_dma(as, 0); att_dma(as, 1); }
                    STAMP(8);
                }
#pragma unroll
                for (int s4 = 0; s4 < 4; ++s4) {
                    qa = MFMA(fw[st & 1][s4], fa[st & 1][s4], qa); SBAR();
                    if constexpr (nx < 10) { const unsigned o = wa + (nx & 3) * 10240 + s4 * 2560; fw[nx & 1][s4] = ldstr2(o, o + 640); SBAR(); }
                    qb = MFMA(fw[st & 1][s4], fb[st & 1][s4], qb); SBAR();
                    if constexpr (nx < 10) { fa[nx & 1][s4] = ldsf(pa[s4] + (nx * 4 + s4) * 1024); fb[nx & 1][s4] = ldsf(pa[s4] + 40960 + (nx * 4 + s4) * 1024); SBAR(); }
                    if (s4 == 2 && (st == 2 || st == 4 || st == 6)) { pk = lpeek(C_WP, lane); SBAR(); }
                }
                if constexpr (!(st & 1) && st <= 6) lpost(C_AP + w, 3 + st / 2);
            });
            lpost(C_AP + w, 8);
            Q0a = pack8(qa, 0, QSCALE); Q0b = pack8(qb, 0, QSCALE);
            q1a = u32x2{pk2(qa[8] * QSCALE, qa[9] * QSCALE), pk2(qa[10] * QSCALE, qa[11] * QSCALE)};
            q1b = u32x2{pk2(qb[8] * QSCALE, qb[9] * QSCALE), pk2(qb[10] * QSCALE, qb[11] * QSCALE)};
            if (h) { q1a = u32x2{0u, 0u}; q1b = u32x2{0u, 0u}; }
            if constexpr (!ATT) {
                char* qt = ws + WS_QT + (size_t)((2 * ti) * 32 + head) * 2048 + lane * 16;
                *(bf16x8*)qt = Q0a; *(u32x2*)(qt + 1024) = q1a; *(bf16x8*)(qt + 65536) = Q0b; *(u32x2*)(qt + 65536 + 1024) = q1b;
            }
            STAMP(3);
        } else {
            BLOCK_BARRIER();
            if constexpr (ATT) {
                const char* qt = ws + WS_QT + (size_t)((2 * ti) * 32 + head) * 2048 + lane * 16;
                Q0a = *(const bf16x8*)qt; q1a = *(const u32x2*)(qt + 1024); Q0b = *(const bf16x8*)(qt + 65536); q1b = *(const u32x2*)(qt + 65536 + 1024);
            }
        }
        if constexpr (ATT) {
            float na = 0.f, nb = 0.f;
            {
                const u32x4 a0 = __builtin_bit_cast(u32x4, Q0a), b0 = __builtin_bit_cast(u32x4, Q0b);
#pragma unroll
                for (int e = 0; e < 4; ++e) { na += bflo(a0[e]) * bflo(a0[e]) + bfhi(a0[e]) * bfhi(a0[e]); nb += bflo(b0[e]) * bflo(b0[e]) + bfhi(b0[e]) * bfhi(b0[e]); }
#pragma unroll
                for (int e = 0; e < 2; ++e) { na += bflo(q1a[e]) * bflo(q1a[e]) + bfhi(q1a[e]) * bfhi(q1a[e]); nb += bflo(q1b[e]) * bflo(q1b[e]) + bfhi(q1b[e]) * bfhi(q1b[e]); }
                const auto sa = __builtin_amdgcn_permlane32_swap(__float_as_uint(na), __float_as_uint(na), false, false), sb = __builtin_amdgcn_permlane32_swap(__float_as_uint(nb), __float_as_uint(nb), false, false);
                na = __uint_as_float(sa[0]) + __uint_as_float(sa[1]); nb = __uint_as_float(sb[0]) + __uint_as_float(sb[1]);
            }
            if (!early) {
                gu64* f = (gu64*)(ws + WS_KF + xc * 1024) + lane;
                _Pragma("clang loop unroll(disable)") for (unsigned spins = 0; spins < (1u << 14); ++spins) {
                    fk = __hip_atomic_load(f, RLX_AGENT); fv = __hip_atomic_load(f + 64, RLX_AGENT);
                    if (__all((unsigned)(fk >> 32) == tag && (unsigned)(fv >> 32) == tag)) break;
                    TMO(spins, 1u << 14, 301, tag);
                    __builtin_amdgcn_s_sleep(4);
                }
                if constexpr (PRO) lwait_all(C_AP, 4, 6, lane);
                att_dma(as, 0); att_dma(as, 1);
            }
            STAMP(4);
            const float kn2 = wave_max(__uint_as_float((unsigned)fk)), vmx = wave_max(__uint_as_float((unsigned)fv));
            const float Ma = __builtin_amdgcn_sqrtf(na * kn2) * 1.01f, Mb = __builtin_amdgcn_sqrtf(nb * kn2) * 1.01f;
            const bool fast = __all(Ma <= BOUND_MAX && Mb <= BOUND_MAX && vmx <= 1e30f);
            const u32x2 a1 = {(fast && h) ? pk2(-Ma, 0.f) : q1a[0], q1a[1]}, b1 = {(fast && h) ? pk2(-Mb, 0.f) : q1b[0], q1b[1]};
            f32x16 Oa = {}, Ob = {};
            WAITV(3); lpost(C_AP + w, AP0); lwait_all(C_AP, 4, AP0, lane);
            att_dma(as, 2);
            __builtin_amdgcn_s_setprio(ATT_PRIO);
            STAMP(5); STAMPC(12);
            if (fast) att_fast(as, Q0a, __builtin_bit_cast(s16x4, a1), Q0b, __builtin_bit_cast(s16x4, b1), Oa, Ob);
            else att_safe(as, Q0a, __builtin_bit_cast(s16x4, a1), Q0b, __builtin_bit_cast(s16x4, b1), Oa, Ob);
            STAMP(6); STAMPC(13);
            const __amdgpu_buffer_rsrc_t ro = mkrsrc(ws + WS_OT, 64 * 40 * 1024);
#pragma unroll
            for (int q = 0; q < 2; ++q) {
                const f32x16& O = q ? Ob : Oa;
                const auto lsw = __builtin_amdgcn_permlane32_swap(__float_as_uint(O[8]), __float_as_uint(O[8]), false, false);
                const float inv = __builtin_amdgcn_rcpf(__uint_as_float(lsw[1]));
                const unsigned base = ((2 * ti + q) * 40 + 5 * g) * 1024;
                __builtin_amdgcn_raw_buffer_store_b128(__builtin_bit_cast(u32x4, pack8(O, 0, inv)), ro, base + w * 1024 + lane * 16, 0, 16);
                if (!h) {
                    u32x2 t = {pk2(O[8] * inv, O[9] * inv), pk2(O[10] * inv, O[11] * inv)};
                    __builtin_amdgcn_raw_buffer_store_b64(t, ro, base + 4096 + ((w >> 1) * 32 + r) * 16 + (w & 1) * 8, 0, 16);
                }
            }
            if constexpr (WO) {
                WAITV(0);
                if (lane < 2) __hip_atomic_store((gu32*)(ws + WS_OF) + (2 * ti + lane) * 32 + head, tag, RLX_AGENT);
            }
            STAMP(7);
        }
    }
}

template <int PH>
static void launch(const Params& p, hipStream_t stream) {
    (void)hipFuncSetAttribute((const void*)mega<PH>, hipFuncAttributeMaxDynamicSharedMemorySize, L_TOTAL);
    mega<PH><<<256, 512, L_TOTAL, stream>>>(p);
}

extern "C" void kernel_launch(void* const* d_in, const int* in_sizes, int n_in, void* d_out, int out_size, void* d_ws, size_t ws_size, hipStream_t stream) {
    Params p;
    p.x = (const char*)d_in[0]; p.wq = (const char*)d_in[1]; p.wk = (const char*)d_in[2]; p.wv = (const char*)d_in[3]; p.wo = (const char*)d_in[4];
    p.w1 = (const char*)d_in[6]; p.w3 = (const char*)d_in[7]; p.w2 = (const char*)d_in[8];
    p.out = (char*)d_out; p.ws = (char*)d_ws;
    launch<27>(p, stream);
}
```

```cpp
#include <hip/hip_runtime.h>
#include <hip/hip_bf16.h>
#include <stdint.h>

#define AS1 __attribute__((address_space(1)))
#define AS3 __attribute__((address_space(3)))
typedef __bf16 bf16x2 __attribute__((ext_vector_type(2)));
typedef __bf16 bf16x8 __attribute__((ext_vector_type(8)));
typedef short s16x4 __attribute__((ext_vector_type(4)));
typedef short s16x8 __attribute__((ext_vector_type(8)));
typedef float f32x2 __attribute__((ext_vector_type(2)));
typedef float f32x4 __attribute__((ext_vector_type(4)));
typedef float f32x16 __attribute__((ext_vector_type(16)));
typedef unsigned u32x2 __attribute__((ext_vector_type(2)));
typedef unsigned u32x4 __attribute__((ext_vector_type(4)));
typedef AS1 unsigned gu32;
typedef AS1 unsigned long long gu64;

#define ATT_PRIO 2
#define GEM_PRIO 0
#define GU_HI 0
#define DN_HI 0
#define MPRIO(mask, i) __builtin_amdgcn_s_setprio(((mask) >> (i)) & 1 ? 3 : 0)
#define GU_DELAY 8
#define GU_ROT 4
#define GU_GQ 5
#define OF_SLEEP 8
#define HF_SLEEP 20
#define DN_DQ 8
#define KV_PEEK 5
#define SPLIT_MODE 0

constexpr int SEQ = 2048, DM = 640, FF = 1536;
constexpr unsigned WS_KF = 0;
constexpr unsigned WS_HF = 16384;
constexpr unsigned WS_OF = 32768;
constexpr unsigned WS_EP = 49152;
constexpr unsigned KVD = 5376;
constexpr unsigned KV_K1 = 2048, KV_V = 2560, KV_VT = 1408;
constexpr unsigned WS_KV = 65536;
constexpr unsigned WS_HT = WS_KV + 176128;
constexpr unsigned WS_OT = WS_HT + 64 * 96 * 1024;
constexpr unsigned WS_QT = WS_OT + 64 * 40 * 1024;
constexpr unsigned WS_PT = WS_QT + 64 * 32 * 2048;
constexpr unsigned WS_ST = WS_PT + 256 * 3 * 32 * 256;
constexpr unsigned L_PANEL = 0;
constexpr unsigned L_KV = 81920;
constexpr unsigned L_WQ = 81920;
constexpr unsigned L_GU = 124928;
constexpr unsigned GSL = 12320;
constexpr unsigned L_WKV = 124928;
constexpr unsigned L_MISC = 161920;
constexpr unsigned L_TOTAL = 163840;
constexpr unsigned CHUNK = 2 * KVD;
constexpr int NCHUNK = 16;
enum { C_AP = 0  , C_GP = 4  , C_DP = 8  , C_PP = 12  , C_WP = 16  , C_KVP = 20, C_KVFIN, C_TAG, C_OS = 24  , C_N = 28 };

struct Params { const char* x; const char* wq; const char* wk; const char* wv; const char* wo; const char* w1; const char* w3; const char* w2; char* out; char* ws; };

#define SBAR() __builtin_amdgcn_sched_barrier(0)
#define PIN(x) asm volatile("" : "+v"(x))
#define CFENCE() asm volatile("" ::: "memory")
#define WAITV(n) asm volatile("s_waitcnt vmcnt(" #n ")" ::: "memory")
#define MFMA(a, b, c) __builtin_amdgcn_mfma_f32_32x32x16_bf16((a), (b), (c), 0, 0, 0)
#define MFMA8(a, b, c) __builtin_amdgcn_mfma_f32_32x32x8bf16_1k((a), (b), (c), 0, 0, 0)
#define RLX_AGENT __ATOMIC_RELAXED, __HIP_MEMORY_SCOPE_AGENT
#define RLX_WG __ATOMIC_RELAXED, __HIP_MEMORY_SCOPE_WORKGROUP

#define TMO(spins, lim, code, tgt) do {} while (0)
#define STAMP(id) do {} while (0)
#define STAMPC(id) do {} while (0)

#define LP(T, off) ((AS3 T*)(uintptr_t)(unsigned)(off))
__device__ __forceinline__ bf16x8 ldsf(unsigned off) { return *LP(const bf16x8, off); }
__device__ __forceinline__ s16x4 ldstr(unsigned off) { return __builtin_amdgcn_ds_read_tr16_b64_v4i16(LP(s16x4, off)); }
__device__ __forceinline__ bf16x8 ldstr2(unsigned off0, unsigned off1) {
    s16x4 a = ldstr(off0), b = ldstr(off1);
    return __builtin_bit_cast(bf16x8, __builtin_shufflevector(a, b, 0, 1, 2, 3, 4, 5, 6, 7));
}
template <int E> __device__ __forceinline__ s16x4 half4(bf16x8 v) { const s16x8 t = __builtin_bit_cast(s16x8, v); return __builtin_shufflevector(t, t, 4 * E, 4 * E + 1, 4 * E + 2, 4 * E + 3); }
__device__ __forceinline__ unsigned pk2(float lo, float hi) { bf16x2 v = {(__bf16)lo, (__bf16)hi}; return __builtin_bit_cast(unsigned, v); }
__device__ __forceinline__ float bflo(unsigned u) { return __uint_as_float(u << 16); }
__device__ __forceinline__ float bfhi(unsigned u) { return __uint_as_float(u & 0xffff0000u); }
__device__ __forceinline__ float rbf(float f) { return (float)(__bf16)f; }

__device__ __forceinline__ void glds16(unsigned voff, const char* sbase, unsigned ldsdst) {
    unsigned keep;
    asm volatile("s_mov_b32 %0, m0\n\ts_mov_b32 m0, %3\n\ts_nop 2\n\tglobal_load_lds_dwordx4 %1, %2\n\ts_mov_b32 m0, %0"
                 : "=&s"(keep) : "v"(voff), "s"(sbase), "s"(ldsdst) : "memory");
}
__device__ __forceinline__ void glds16_sc1(unsigned voff, const char* sbase, unsigned ldsdst) {
    unsigned keep;
    asm volatile("s_mov_b32 %0, m0\n\ts_mov_b32 m0, %3\n\ts_nop 2\n\tglobal_load_lds_dwordx4 %1, %2 sc1\n\ts_mov_b32 m0, %0"
                 : "=&s"(keep) : "v"(voff), "s"(sbase), "s"(ldsdst) : "memory");
}
__device__ __forceinline__ void glds16x3_sc1(unsigned voff, const char* sbase, unsigned ldsdst) {
    unsigned keep;
    asm volatile("s_mov_b32 %0, m0\n\ts_mov_b32 m0, %3\n\ts_nop 2\n\tglobal_load_lds_dwordx4 %1, %2 sc1\n\tglobal_load_lds_dwordx4 %1, %2 offset:1024 sc1\n\tglobal_load_lds_dwordx4 %1, %2 offset:2048 sc1\n\ts_mov_b32 m0, %0"
                 : "=&s"(keep) : "v"(voff), "s"(sbase), "s"(ldsdst) : "memory");
}
__device__ __forceinline__ void glds16x3v(unsigned v0, unsigned v1, unsigned v2, const char* sbase, unsigned ldsdst) {
    unsigned keep;
    asm volatile("s_mov_b32 %0, m0\n\ts_mov_b32 m0, %5\n\ts_nop 0\n\tglobal_load_lds_dwordx4 %1, %4\n\tglobal_load_lds_dwordx4 %2, %4 offset:1024\n\tglobal_load_lds_dwordx4 %3, %4 offset:2048\n\ts_mov_b32 m0, %0"
                 : "=&s"(keep) : "v"(v0), "v"(v1), "v"(v2), "s"(sbase), "s"(ldsdst) : "memory");
}
__device__ __forceinline__ void glds_gu(unsigned va, unsigned vb, unsigned vc, const char* s1, const char* s3, unsigned ldsdst) {
    unsigned keep;
    asm volatile("s_mov_b32 %0, m0\n\ts_mov_b32 m0, %8\n\ts_nop 0\n\tglobal_load_lds_dwordx4 %1, %4\n\tglobal_load_lds_dwordx4 %2, %5 offset:1024\n\t"
                 "s_mov_b32 exec_hi, 0\n\tglobal_load_lds_dwordx4 %3, %6 offset:2048\n\ts_not_b64 exec, exec\n\tglobal_load_lds_dwordx4 %3, %7 offset:2048\n\t"
                 "s_mov_b64 exec, -1\n\ts_mov_b32 m0, %0"
                 : "=&s"(keep) : "v"(va), "v"(vb), "v"(vc), "s"(s1), "s"(s3 - 1024), "s"(s1 - 2048), "s"(s3 - 2048), "s"(ldsdst) : "memory", "scc");
}
__device__ __forceinline__ void l2touch(const char* ga) {
    unsigned keep;
    asm volatile("s_mov_b32 %0, m0\n\ts_mov_b32 m0, %2\n\ts_nop 0\n\tglobal_load_lds_dword %1, off\n\ts_mov_b32 m0, %0" : "=&s"(keep) : "v"(ga), "s"(L_MISC + 1024) : "memory");
}
#define GLOAD16(dst, voff, sbase, imm) asm volatile("s_nop 4\n\tglobal_load_dwordx4 %0, %1, %2 offset:%3" : "=v"(dst) : "v"(voff), "s"(sbase), "i"(imm) : "memory")

__device__ __forceinline__ AS3 unsigned* lcnt(int c) { return LP(unsigned, L_MISC) + c; }
__device__ __forceinline__ void larrive(int c, int lane) { CFENCE(); if (lane == 0) __hip_atomic_fetch_add(lcnt(c), 1u, RLX_WG); CFENCE(); }
__device__ __forceinline__ void lset(int c, unsigned v, int lane) { CFENCE(); if (lane == 0) __hip_atomic_store(lcnt(c), v, RLX_WG); CFENCE(); }
__device__ __forceinline__ void lwait(int c, unsigned target) {
    CFENCE();
    _Pragma("clang loop unroll(disable)") for (unsigned spins = 0; spins < (1u << 22); ++spins) {
        if ((unsigned)__builtin_amdgcn_readfirstlane(__hip_atomic_load(lcnt(c), RLX_WG)) >= target) break;
        TMO(spins, 1u << 16, 100 + c, target);
        __builtin_amdgcn_s_sleep(1);
    }
    CFENCE();
}
__device__ __forceinline__ void lwait_all(int c, int n, unsigned target, int lane) {
    CFENCE();
    AS3 unsigned* q = lcnt(c) + (lane < n ? lane : 0);
    _Pragma("clang loop unroll(disable)") for (unsigned spins = 0; spins < (1u << 16); ++spins) {
        if (__all(__hip_atomic_load(q, RLX_WG) >= target)) break;
        TMO(spins, 1u << 16, 200 + c, target);
        __builtin_amdgcn_s_sleep(1);
    }
    CFENCE();
}
__device__ __forceinline__ void lpost(int c, unsigned v) { CFENCE(); *(volatile AS3 unsigned*)lcnt(c) = v; CFENCE(); }
__device__ __forceinline__ void linc(unsigned vaddr, unsigned vone) {
    asm volatile("s_mov_b64 exec, 1\n\tds_add_u32 %0, %1\n\ts_mov_b64 exec, -1" :: "v"(vaddr), "v"(vone) : "memory");
}
__device__ __forceinline__ unsigned lpeek(int c, int lane) { return *((volatile AS3 unsigned*)lcnt(c) + (lane & 3)); }
__device__ __forceinline__ void lcheck(unsigned pk, int c, unsigned target, int lane) {
    if (__builtin_expect(__any(pk < target), 0)) lwait_all(c, 4, target, lane);
    CFENCE();
}
__device__ __forceinline__ void gwait(const char* ws, unsigned off, int n, unsigned tag, int lane) {
    gu32* f = (gu32*)(ws + off) + (lane < n ? lane : 0);
    _Pragma("clang loop unroll(disable)") for (unsigned spins = 0; spins < (1u << 14); ++spins) {
        unsigned v = __hip_atomic_load(f, RLX_AGENT);
        if (__all(v == tag)) break;
        TMO(spins, 1u << 14, 300, off);
        __builtin_amdgcn_s_sleep(6);
    }
    CFENCE();
}
__device__ __forceinline__ float gwait64(const char* ws, unsigned off, unsigned tag, int lane) {
    gu64* f = (gu64*)(ws + off) + lane;
    unsigned long long v = 0;
    _Pragma("clang loop unroll(disable)") for (unsigned spins = 0; spins < (1u << 14); ++spins) {
        v = __hip_atomic_load(f, RLX_AGENT);
        if (__all((unsigned)(v >> 32) == tag)) break;
        __builtin_amdgcn_s_sleep(6);
    }
    CFENCE();
    return __uint_as_float((unsigned)v);
}
__device__ __forceinline__ __amdgpu_buffer_rsrc_t mkrsrc(const char* base, unsigned bytes) {
    return __builtin_amdgcn_make_buffer_rsrc((void*)base, 0, bytes, 0x00020000);
}
__device__ __forceinline__ float dpp_sum16(float v) {
    v += __int_as_float(__builtin_amdgcn_update_dpp(0, __float_as_int(v), 0x128, 0xf, 0xf, false));
    v += __int_as_float(__builtin_amdgcn_update_dpp(0, __float_as_int(v), 0x124, 0xf, 0xf, false));
    v += __int_as_float(__builtin_amdgcn_update_dpp(0, __float_as_int(v), 0x122, 0xf, 0xf, false));
    v += __int_as_float(__builtin_amdgcn_update_dpp(0, __float_as_int(v), 0x121, 0xf, 0xf, false));
    return v;
}
__device__ __forceinline__ float wave_max(float v) {
    int x = __float_as_int(v);
    x = max(x, __builtin_amdgcn_update_dpp(0, x, 0x128, 0xf, 0xf, false)); x = max(x, __builtin_amdgcn_update_dpp(0, x, 0x124, 0xf, 0xf, false));
    x = max(x, __builtin_amdgcn_update_dpp(0, x, 0x122, 0xf, 0xf, false)); x = max(x, __builtin_amdgcn_update_dpp(0, x, 0x121, 0xf, 0xf, false));
    const int a = max(__builtin_amdgcn_readlane(x, 0), __builtin_amdgcn_readlane(x, 16)), b = max(__builtin_amdgcn_readlane(x, 32), __builtin_amdgcn_readlane(x, 48));
    return __int_as_float(max(a, b));
}
__device__ __forceinline__ bf16x8 pack8(const f32x16& a, int b, float sc) {
    u32x4 r = {pk2(a[b] * sc, a[b + 1] * sc), pk2(a[b + 2] * sc, a[b + 3] * sc), pk2(a[b + 4] * sc, a[b + 5] * sc), pk2(a[b + 6] * sc, a[b + 7] * sc)};
    return __builtin_bit_cast(bf16x8, r);
}

template <int I> struct IC { static constexpr int value = I; };
template <int B, int E, class F> __device__ __forceinline__ void static_for(F&& f) { if constexpr (B < E) { f(IC<B>{}); static_for<B + 1, E>(f); } }
#define WAITVN(n) asm volatile("s_waitcnt vmcnt(%0)" :: "i"(n) : "memory")
__device__ __forceinline__ void GLOAD16S(u32x4& dst, unsigned voff, const char* sbase) { asm volatile("global_load_dwordx4 %0, %1, %2 sc1" : "=v"(dst) : "v"(voff), "s"(sbase) : "memory"); }
__device__ __forceinline__ void GLOAD16N(u32x4& dst, unsigned voff, const char* sbase) { asm volatile("global_load_dwordx4 %0, %1, %2" : "=v"(dst) : "v"(voff), "s"(sbase) : "memory"); }
__device__ __forceinline__ void GLOAD16N_IF(u32x4& dst, unsigned voff, const char* sbase, int cond) {
    asm volatile("s_cmp_eq_u32 %3, 0\n\ts_cbranch_scc1 1f\n\tglobal_load_dwordx4 %0, %1, %2\n1:" : "=v"(dst) : "v"(voff), "s"(sbase), "s"(cond) : "memory", "scc"); }
template <int N> __device__ __forceinline__ void waitv5(u32x4& a, u32x4& b, u32x4& c, u32x4& d, u32x4& e) { asm volatile("s_waitcnt vmcnt(%5)" : "+v"(a), "+v"(b), "+v"(c), "+v"(d), "+v"(e) : "i"(N) : "memory"); }
__device__ __forceinline__ void gload8x2_sc1(unsigned long long& a, unsigned long long& b, unsigned voff, const char* sbase) {
    asm volatile("global_load_dwordx2 %0, %2, %3 sc1\n\tglobal_load_dwordx2 %1, %2, %3 offset:512 sc1" : "=&v"(a), "=&v"(b) : "v"(voff), "s"(sbase) : "memory"); }
__device__ __forceinline__ void waitv8x2(unsigned long long& a, unsigned long long& b) { asm volatile("s_waitcnt vmcnt(0)" : "+v"(a), "+v"(b) :: "memory"); }
__device__ __forceinline__ void PIN2(u32x4& a, u32x4& b) { asm volatile("" : "+v"(a), "+v"(b) :: "memory"); }
__device__ __forceinline__ u32x4 scale8(unsigned x0, unsigned x1, unsigned x2, unsigned x3, unsigned clo, unsigned chi, float c) {
    unsigned o0, o1, o2, o3; float t0, t1, t2, t3, t4, t5, t6, t7;
    asm("v_dot2_f32_bf16 %4, %12, %16, %18\n\tv_dot2_f32_bf16 %5, %12, %17, %18\n\tv_dot2_f32_bf16 %6, %13, %16, %18\n\tv_dot2_f32_bf16 %7, %13, %17, %18\n\t"
        "v_dot2_f32_bf16 %8, %14, %16, %18\n\tv_dot2_f32_bf16 %9, %14, %17, %18\n\tv_dot2_f32_bf16 %10, %15, %16, %18\n\tv_dot2_f32_bf16 %11, %15, %17, %18\n\t"
        "v_cvt_pk_bf16_f32 %0, %4, %5\n\tv_cvt_pk_bf16_f32 %1, %6, %7\n\tv_cvt_pk_bf16_f32 %2, %8, %9\n\tv_cvt_pk_bf16_f32 %3, %10, %11"
        : "=&v"(o0), "=&v"(o1), "=&v"(o2), "=&v"(o3), "=&v"(t0), "=&v"(t1), "=&v"(t2), "=&v"(t3), "=&v"(t4), "=&v"(t5), "=&v"(t6), "=&v"(t7)
        : "v"(x0), "v"(x1), "v"(x2), "v"(x3), "v"(clo), "v"(chi), "v"(c));
    return u32x4{o0, o1, o2, o3};
}
template <int N> __device__ __forceinline__ void waitv3(u32x4& a, u32x4& b, u32x4& c) { asm volatile("s_waitcnt vmcnt(%3)" : "+v"(a), "+v"(b), "+v"(c) : "i"(N) : "memory"); }

constexpr int dn_younger(int k, int D, bool wo) {
    int n = 0, la = 0, lb = 0;
    for (int i = 0; i < D; ++i) { n += 2; if (i == k + 1) lb = n; }
    for (int i = 0; i < D; ++i) { n += 3; if (i == k) la = n; }
    for (int s = 0; s < k; ++s) {
        if (s + D < 24) { n += 2; if (s + D == k + 1) lb = n; }
        if (wo && s >= 16) n += 2;
        if (s + D < 24) { n += 3; if (s + D == k) la = n; }
    }
    return n - (la > lb ? la : lb);
}
constexpr float QSCALE = 0.22360679774997896f * 1.4426950408889634f;
#define BOUND_LIM 50.f
constexpr float BOUND_MAX = BOUND_LIM;

struct AttSync { int lane; const char* kv; unsigned dvoff; unsigned wo; int w; };
__device__ __forceinline__ unsigned kvslot(int c) { return L_KV + ((c + 2) & 3) * CHUNK; }
__device__ __forceinline__ void att_dma(const AttSync& a, int c) { glds16x3_sc1(a.dvoff, a.kv + (size_t)c * CHUNK, kvslot(c) + a.wo); }
constexpr unsigned AP0 = 16;
#define RING_X(c) do { WAITV(3); lpost(C_AP + as.w, AP0 + (c) + 1); } while (0)
#define RP_A
#define RP_B
#define RING_Y(c, pk) do { RP_A lcheck(pk, C_AP, AP0 + (c) + 1, lane); const int cn_ = (c) + 3 < NCHUNK ? (c) + 3 : NCHUNK - 1;   \
                           glds16x3_sc1(as.dvoff, as.kv + (size_t)cn_ * CHUNK, kvslot((c) + 3) + as.wo); RP_B } while (0)

__device__ __forceinline__ void att_fast(const AttSync& as, bf16x8 Q0a, s16x4 Q1a, bf16x8 Q0b, s16x4 Q1b, f32x16& Oa, f32x16& Ob) {
    const int lane = as.lane, r = lane & 31, h = lane >> 5;
    const unsigned ak0 = lane * 16, ak1 = h ? KV_V + 20 * 16 : KV_K1 + r * 16;
    const unsigned av = KV_V + h * 352 + (r <= 20 ? r : 21) * 16;
    const f32x16 zero = {};
    f32x16 Sa, Sb = {};
    bf16x8 K0[2], K1[2], V0[2], V1[2];
    u32x4 Pa0, Pa1, Pb0 = {}, Pb1 = {};
    unsigned pk = 0;
    unsigned long long rp0 = 0, rp1 = 0;
    V0[1] = bf16x8{}; V1[1] = bf16x8{};
    unsigned vk0 = L_KV + ak0, vk1 = L_KV + ak1, vv = L_KV + av;
    PIN(vk0); PIN(vk1); PIN(vv);
    K0[0] = ldsf(vk0 + 2 * CHUNK); K1[0] = ldsf(vk1 + 2 * CHUNK); K0[1] = ldsf(vk0 + 2 * CHUNK + 1024); V1[0] = ldsf(vv + 2 * CHUNK + 704); V0[0] = ldsf(vv + 2 * CHUNK);
    Sa = MFMA(K0[0], Q0a, zero); Sa = MFMA8(half4<0>(K1[0]), Q1a, Sa);
#define EX4(S, i) do { S[i] = __builtin_amdgcn_exp2f(S[i]); S[i + 1] = __builtin_amdgcn_exp2f(S[i + 1]); S[i + 2] = __builtin_amdgcn_exp2f(S[i + 2]); S[i + 3] = __builtin_amdgcn_exp2f(S[i + 3]); } while (0)
#define GAP(MF, LD, T, k, PW, j, S, i) do { MF; SBAR(); LD; PW[j] = pk2(T[k], T[k + 1]); PW[j + 1] = pk2(T[k + 2], T[k + 3]); PIN(PW); SBAR(); EX4(S, i); PIN(S); SBAR(); } while (0)
#define BC(x) __builtin_bit_cast(bf16x8, x)
#define STEP(e, d, KN, L3, LK0, LK1, LV) do { \
        GAP(Ob = MFMA(V0[e ^ 1], BC(Pb0), Ob), (void)0,                  Sb, 12, Pb1, 2, Sa, 0); \
        GAP(Sb = MFMA(K0[e], Q0b, zero),       (void)0,                  Sa, 0, Pa0, 0, Sa, 4); \
        GAP(Sb = MFMA8(half4<e>(K1[d]), Q1b, Sb), L3,                       Sa, 4, Pa0, 2, Sa, 8); \
        GAP(Ob = MFMA(V1[e ^ 1], BC(Pb1), Ob), K0[e] = ldsf(LK0),        Sa, 8, Pa1, 0, Sa, 12); \
        GAP(Oa = MFMA(V0[e], BC(Pa0), Oa),     LK1,                      Sa, 12, Pa1, 2, Sb, 0); \
        GAP(Sa = MFMA(K0[e ^ 1], Q0a, zero),   V1[e ^ 1] = ldsf((LV) + 704), Sb, 0, Pb0, 0, Sb, 4); \
        GAP(Sa = MFMA8(half4<e ^ 1>(K1[KN]), Q1a, Sa), V0[e ^ 1] = ldsf(LV),     Sb, 4, Pb0, 2, Sb, 8); \
        GAP(Oa = MFMA(V1[e], BC(Pa1), Oa),     (void)0,                  Sb, 8, Pb1, 0, Sb, 12); \
    } while (0)
#define CHUNK4(c, B, N) do { \
        STEP(0, 0, 0, (void)0, vk0 + (B) + KVD, K1[1] = ldsf(vk1 + (B) + KVD), vv + (B) + KV_VT); \
        RING_X(c); \
        STEP(1, 0, 1, pk = lpeek(C_AP, lane), vk0 + (B) + KVD + 1024, (void)0, vv + (B) + KVD); \
        RING_Y(c, pk); \
        STEP(0, 1, 1, (void)0, vk0 + (N), K1[0] = ldsf(vk1 + (N)), vv + (B) + KVD + KV_VT); \
        STEP(1, 1, 0, (void)0, vk0 + (N) + 1024, (void)0, vv + (N)); \
    } while (0)
    for (int c = 0; c < NCHUNK; c += 4) {
        CHUNK4(c, 2 * CHUNK, 3 * CHUNK); CHUNK4(c + 1, 3 * CHUNK, 0); CHUNK4(c + 2, 0, CHUNK); CHUNK4(c + 3, CHUNK, 2 * CHUNK);
    }
#undef CHUNK4
    Pb1[2] = pk2(Sb[12], Sb[13]); Pb1[3] = pk2(Sb[14], Sb[15]);
    Ob = MFMA(V0[1], BC(Pb0), Ob); Ob = MFMA(V1[1], BC(Pb1), Ob);
#undef STEP
#undef GAP
#undef EX4
}

__device__ __forceinline__ void att_safe(const AttSync& as, bf16x8 Q0a, s16x4 Q1a, bf16x8 Q0b, s16x4 Q1b, f32x16& Oa, f32x16& Ob) {
    const int lane = as.lane, r = lane & 31, h = lane >> 5;
    const unsigned ak0 = lane * 16, ak1 = h ? KV_V + 20 * 16 : KV_K1 + r * 16;
    const unsigned av = KV_V + h * 352 + (r <= 20 ? r : 21) * 16;
    const f32x16 zero = {};
    float ma = -INFINITY, mb = -INFINITY;
    unsigned long long rp0 = 0, rp1 = 0;
    for (int c = 0; c < NCHUNK; ++c) {
        const unsigned b0 = kvslot(c);
        for (int t = 0; t < 4; ++t) {
            if (t == 1) RING_X(c);
            if (t == 2) RING_Y(c, 0u);
            const unsigned base = b0 + (t >> 1) * KVD; const int e = t & 1;
            bf16x8 K0 = ldsf(base + ak0 + e * 1024), K1 = ldsf(base + ak1), V0 = ldsf(base + av + e * KV_VT), V1 = ldsf(base + av + e * KV_VT + 704);
#pragma unroll
            for (int q = 0; q < 2; ++q) {
                f32x16 S = MFMA(K0, q ? Q0b : Q0a, zero); S = MFMA8(e ? half4<1>(K1) : half4<0>(K1), q ? Q1b : Q1a, S);
                float mx = S[0];
#pragma unroll
                for (int i = 1; i < 16; ++i) mx = fmaxf(mx, S[i]);
                mx = fmaxf(mx, __shfl_xor(mx, 32));
                float& m = q ? mb : ma; f32x16& O = q ? Ob : Oa;
                const float mn = fmaxf(m, mx);
                const float al = __builtin_amdgcn_exp2f(m - mn);
                m = mn;
#pragma unroll
                for (int i = 0; i < 16; ++i) { O[i] *= al; S[i] = __builtin_amdgcn_exp2f(S[i] - mn); }
                O = MFMA(V0, pack8(S, 0, 1.f), O); O = MFMA(V1, pack8(S, 8, 1.f), O);
            }
        }
    }
}

__device__ __forceinline__ void xload8(u32x4 (&x)[5], u32x4 (&y)[5], const char* xb, unsigned vo, unsigned vo2, int skip) {
    asm volatile("s_cmp_lg_u32 %13, 0\n\ts_cbranch_scc1 1f\n\t"
                 "global_load_dwordx4 %0, %10, %12\n\tglobal_load_dwordx4 %1, %10, %12 offset:256\n\tglobal_load_dwordx4 %2, %10, %12 offset:512\n\t"
                 "global_load_dwordx4 %3, %10, %12 offset:768\n\tglobal_load_dwordx4 %4, %10, %12 offset:1024\n\t"
                 "global_load_dwordx4 %5, %11, %12\n\tglobal_load_dwordx4 %6, %11, %12 offset:256\n\tglobal_load_dwordx4 %7, %11, %12 offset:512\n\t"
                 "global_load_dwordx4 %8, %11, %12 offset:768\n\tglobal_load_dwordx4 %9, %11, %12 offset:1024\n1:"
                 : "=&v"(x[0]), "=&v"(x[1]), "=&v"(x[2]), "=&v"(x[3]), "=&v"(x[4]), "=&v"(y[0]), "=&v"(y[1]), "=&v"(y[2]), "=&v"(y[3]), "=&v"(y[4])
                 : "v"(vo), "v"(vo2), "s"(xb), "s"(skip) : "memory", "scc");
}
__device__ __forceinline__ void xload4(u32x4 (&x)[5], const char* xb, unsigned vo) {
    asm volatile("global_load_dwordx4 %0, %5, %6\n\tglobal_load_dwordx4 %1, %5, %6 offset:256\n\tglobal_load_dwordx4 %2, %5, %6 offset:512\n\t"
                 "global_load_dwordx4 %3, %5, %6 offset:768\n\tglobal_load_dwordx4 %4, %5, %6 offset:1024"
                 : "=&v"(x[0]), "=&v"(x[1]), "=&v"(x[2]), "=&v"(x[3]), "=&v"(x[4]) : "v"(vo), "s"(xb) : "memory");
}
__device__ __forceinline__ void ln_rows(const u32x4 (&xr)[5], int row0, int lane) {
    f32x2 xv[20];
    f32x2 s0 = {0.f, 0.f}, s1 = {0.f, 0.f}, q0 = {0.f, 0.f}, q1 = {0.f, 0.f};
#pragma unroll
    for (int i = 0; i < 20; i += 2) {
        const unsigned a = xr[i >> 2][i & 3], b = xr[i >> 2][(i & 3) + 1];
        xv[i] = f32x2{bflo(a), bfhi(a)}; xv[i + 1] = f32x2{bflo(b), bfhi(b)};
        s0 += xv[i]; q0 = __builtin_elementwise_fma(xv[i], xv[i], q0);
        s1 += xv[i + 1]; q1 = __builtin_elementwise_fma(xv[i + 1], xv[i + 1], q1);
    }
    s0 += s1; q0 += q1;
    const float s = dpp_sum16(s0[0] + s0[1]), ss = dpp_sum16(q0[0] + q0[1]);
    const float mean = s * (1.f / 640.f);
    const float var = fmaxf(ss * (1.f / 640.f) - mean * mean, 0.f);
    const float rs = __builtin_amdgcn_rcpf(rbf(__builtin_amdgcn_sqrtf(rbf(rbf(var) + 1e-5f)))), mo = -rbf(mean) * rs;
    const f32x2 rs2 = {rs, rs}, mo2 = {mo, mo};
    const int row = row0 + (lane >> 4), c = lane & 15;
    const unsigned wa = L_PANEL + (row >> 5) * 40960 + (c >> 1) * 1024 + (c & 1) * 512 + (((row & 31) ^ (c & 7)) << 4);
#pragma unroll
    for (int u = 0; u < 5; ++u) {
        u32x4 o;
#pragma unroll
        for (int e = 0; e < 4; ++e) { const f32x2 y = __builtin_elementwise_fma(xv[4 * u + e], rs2, mo2); o[e] = pk2(y[0], y[1]); }
        *LP(u32x4, wa + u * 8192) = o;
    }
}
#define BLOCK_BARRIER() asm volatile("s_waitcnt lgkmcnt(0)\n\ts_barrier" ::: "memory")

template <int PH>
__global__ void __launch_bounds__(512, 2) mega(Params p) {
    constexpr bool PRO = PH & 1, ATT = PH & 2, DN = PH & 8, WO = PH & 16;
    const int w = __builtin_amdgcn_readfirstlane(threadIdx.x >> 6);
    const int b = blockIdx.x, xc = b & 7, j = b >> 3;
    const int ti = (xc >> 1) * 8 + (j >> 2), g = (xc & 1) * 4 + (j & 3);
    constexpr bool FUSED = PH == 27;
    const int kmt = g & 1;
    const int ra = kmt * 32 + (w & 3) * 8, rb = (kmt ^ 1) * 32 + (w & 3) * 8;
    u32x4 x0[5], x1[5], x2[5], x3[5];
    if constexpr (PRO) {
        asm volatile("" :: "s"(p.x), "s"(p.ws), "s"(p.wq), "s"(p.wk));
        const char* xb = p.x + (size_t)ti * 64 * 1280;
        const unsigned xvo = ((threadIdx.x >> 4) & 3) * 1280 + (threadIdx.x & 15) * 16;
        xload8(x0, x1, xb + (size_t)ra * 1280, xvo, xvo + 5120, w >> 2);
        xload8(x2, x3, xb + (size_t)rb * 1280, xvo, xvo + 5120, w >> 2);
    }
    const char* pc; asm volatile("s_getpc_b64 %0" : "=s"(pc));
    unsigned tag = 1u;
    char* ws = p.ws;
    STAMP(0);
    if (threadIdx.x < C_N) *lcnt(threadIdx.x) = (threadIdx.x == C_WP || threadIdx.x == C_WP + 1 || threadIdx.x == C_PP || threadIdx.x == C_PP + 1) ? 100u : 0u;
    const bool haskv = (g >> 2) == (ti & 1);
    const char* wqb = p.wq + g * 160;
#define LANE_CONSTS() int lane = __builtin_amdgcn_mbcnt_hi(~0u, __builtin_amdgcn_mbcnt_lo(~0u, 0u)); PIN(lane); \
    const int r = lane & 31, h = lane >> 5, tq = (lane & 15) >> 2, tp = lane & 3, tb = (lane >> 4) & 1; (void)r; (void)h; (void)tq; (void)tp; (void)tb

    if (w >= 4) {
        LANE_CONSTS();
        const int v = w - 4;
        unsigned epv = 0u;
        if constexpr (FUSED) {
            asm volatile("global_load_dword %0, %1, %2 sc1" : "=v"(epv) : "v"(0u), "s"(p.ws + WS_EP) : "memory");
            constexpr unsigned CODE_PF = 65536;
            const unsigned line = (unsigned)(j * 4 + v) * 64u + lane;
            if (line * 128u < CODE_PF) l2touch(pc + line * 128u);
        }
#define GET_TAG() do { if constexpr (FUSED) { asm volatile("s_nop 0\n\tv_readfirstlane_b32 %0, %1\n\ts_nop 3" : "=s"(tag) : "v"(epv) : "memory"); tag += 0x9E3779B1u; } } while (0)
        __builtin_amdgcn_s_setprio(3);
        if constexpr (PRO) {
            const int kvm = 2 * ti + (g & 1); const bool isV = (g >> 1) & 1;
            const char* wkvb = isV ? p.wv : p.wk;
            if (haskv && v < 2) {
#pragma unroll
                for (int n = 0; n < 13; ++n) {
                    const unsigned o = v * 12800 + (n < 12 ? n * 1024 : 11776);
                    glds16(o + lane * 16, wkvb, L_WKV + o);
                }
            }
            BLOCK_BARRIER();
            if (v >= 2) {
                const int v2 = v - 2;
                unsigned wo5[5];
#pragma unroll
                for (int i = 0; i < 5; ++i) { const unsigned c = (v2 + 2 * i) * 64 + lane, row = c / 10u, col = c - row * 10u; wo5[i] = row * 1280 + col * 16; }
#pragma unroll
                for (int n = 0; n < 20; ++n) glds16(wo5[n % 5], wqb + (n / 5) * 64 * 1280, L_WQ + (v2 + 2 * n) * 1024);
                u32x4 wst[3][10];
                auto wst_issue = [&](auto I) {
                    constexpr int rr = I.value / 10, i = I.value % 10;
                    GLOAD16N(wst[rr][i], wo5[i % 5], wqb + (4 + 2 * rr + i / 5) * 64 * 1280);
                };
                static_for<0, 10>(wst_issue);
                WAITV(10);
                GET_TAG(); lpost(C_TAG, tag);
                lpost(C_PP + v, 1);
                STAMP(2);
                static_for<10, 30>(wst_issue);
                static_for<0, 3>([&](auto R) {
                    constexpr int rr = R.value;
                    waitv5<20 - 10 * rr>(wst[rr][0], wst[rr][1], wst[rr][2], wst[rr][3], wst[rr][4]); waitv5<63>(wst[rr][5], wst[rr][6], wst[rr][7], wst[rr][8], wst[rr][9]);
                    lwait_all(C_AP, 4, 3 + rr, lane);
#pragma unroll
                    for (int i = 0; i < 10; ++i) *LP(u32x4, L_WQ + (rr & 1) * 20480 + (v2 + 2 * i) * 1024 + lane * 16) = wst[rr][i];
                    lpost(C_WP + v, rr + 1);
                });
            } else {
              if (haskv) {
                f32x16 acc = {};
                const unsigned wa = L_WKV + (v * 320 + 8 * h + tq) * 40 + tb * 32 + (tb ? 0 : tp * 8);
                unsigned xa[4];
#pragma unroll
                for (int s = 0; s < 4; ++s) xa[s] = L_PANEL + kmt * 40960 + v * 20480 + h * 512 + ((r ^ ((2 * s + h) & 7)) << 4);
                WAITV(0);
                GET_TAG();
                STAMP(2);
                bf16x8 wf[20], xf[20];
#pragma unroll
                for (int s = 0; s < 20; ++s) wf[s] = ldstr2(wa + s * 640, wa + s * 640 + 160);
                lwait_all(C_AP, 4, 1, lane);
#pragma unroll
                for (int s = 0; s < 20; ++s) xf[s] = ldsf(xa[s & 3] + s * 1024);
                SBAR();
                if (isV) {
#pragma unroll
                    for (int s = 0; s < 20; ++s) acc = MFMA(xf[s], wf[s], acc);
                } else {
#pragma unroll
                    for (int s = 0; s < 20; ++s) acc = MFMA(wf[s], xf[s], acc);
                }
                if (v) {
#pragma unroll
                    for (int e = 0; e < 4; ++e) *LP(f32x4, L_WKV + 12800 + e * 1024 + lane * 16) = f32x4{acc[4 * e], acc[4 * e + 1], acc[4 * e + 2], acc[4 * e + 3]};
                    lpost(C_KVP, 1);
                } else {
                    lwait(C_KVP, 1);
#pragma unroll
                    for (int e = 0; e < 4; ++e) {
                        const f32x4 t = *LP(const f32x4, L_WKV + 12800 + e * 1024 + lane * 16);
                        acc[4 * e] += t[0]; acc[4 * e + 1] += t[1]; acc[4 * e + 2] += t[2]; acc[4 * e + 3] += t[3];
                    }
                    const __amdgpu_buffer_rsrc_t rk = mkrsrc(ws + WS_KV, 32 * KVD);
                    const unsigned base = ti * KVD, ke = g & 1;
                    const u32x4 f0 = __builtin_bit_cast(u32x4, pack8(acc, 0, 1.f)), f1 = __builtin_bit_cast(u32x4, pack8(acc, 8, 1.f));
                    float mx;
                    if (!isV) {
                        __builtin_amdgcn_raw_buffer_store_b128(f0, rk, base + ke * 1024 + lane * 16, 0, 16);
                        const u32x2 k1 = {f1[0], f1[1]};
                        if (!h) __builtin_amdgcn_raw_buffer_store_b64(k1, rk, base + KV_K1 + r * 16 + ke * 8, 0, 16);
                        lpost(C_KVFIN, 1);
                        float n2 = 0.f;
#pragma unroll
                        for (int e = 0; e < 4; ++e) n2 += bflo(f0[e]) * bflo(f0[e]) + bfhi(f0[e]) * bfhi(f0[e]);
                        if (!h) n2 += bflo(f1[0]) * bflo(f1[0]) + bfhi(f1[0]) * bfhi(f1[0]) + bflo(f1[1]) * bflo(f1[1]) + bfhi(f1[1]) * bfhi(f1[1]);
                        n2 += __shfl_xor(n2, 32);
                        mx = (n2 == n2) ? n2 : INFINITY;
                    } else {
                        const unsigned cst = r == 20 ? 0x3f803f80u : 0u;
                        const u32x4 c4 = {cst, cst, cst, cst};
                        if (r <= 21) {
                            __builtin_amdgcn_raw_buffer_store_b128(r >= 20 ? c4 : f0, rk, base + KV_V + ke * KV_VT + h * 352 + r * 16, 0, 16);
                            __builtin_amdgcn_raw_buffer_store_b128(r >= 20 ? c4 : f1, rk, base + KV_V + ke * KV_VT + 704 + h * 352 + r * 16, 0, 16);
                        }
                        lpost(C_KVFIN, 1);
                        float vm = 0.f;
#pragma unroll
                        for (int e = 0; e < 16; ++e) { const float a = fabsf(acc[e]); vm = (a <= vm) ? vm : ((a == a) ? a : INFINITY); }
                        mx = r < 20 ? vm : 0.f;
                    }
                    mx = wave_max(mx);
                    WAITV(0);
                    STAMP(15);
                    if (lane < 8) __hip_atomic_store((gu64*)(ws + WS_KF + lane * 1024 + (isV ? 512 : 0)) + kvm, ((unsigned long long)tag << 32) | __float_as_uint(mx), RLX_AGENT);
                }
            }
              else { WAITV(0); GET_TAG(); }
              STAMP(3);
            }
            if (haskv) lwait(C_KVFIN, 1);
#pragma unroll
            for (int n = 0; n < 2; ++n) {
                unsigned L = n * 64 + lane; L = (j * 4 + v) * 120 + (L < 120u ? L : 119u);
                const unsigned m3 = L >= 7680u, rem = L - (m3 ? 7680u : 0u), row = rem / 12u;
                l2touch((m3 ? p.w3 : p.w1) + row * 3072 + (xc & 1) * 1536 + (rem - row * 12u) * 128);
            }
            lwait_all(C_AP, 4, GU_DELAY, lane);
            STAMP(4);
            {
                const char* w1s = p.w1 + g * 384 + v * 96; const char* w3s = p.w3 + g * 384 + v * 96;
                auto goff = [](unsigned idx) { const unsigned row = idx / 6u; return row * 3072 + (idx - row * 6u) * 16; };
                const unsigned gva = goff(lane), gvb = goff((lane - 8) & 63), gvc = goff(64 + (h ? (r - 8) & 31 : r));
                const unsigned gdst = L_GU + v * 9216;
                auto gdma = [&](auto S) { constexpr int s = S.value; glds_gu(gva, gvb, gvc, w1s + (size_t)s * 16 * 3072, w3s + (size_t)s * 16 * 3072, gdst + (s % 3) * 3072); };
                static_for<0, 3>(gdma);
                f32x16 acc[2][3] = {};
                unsigned bn[3][2], pa[4], pb[4];
#pragma unroll
                for (int n = 0; n < 3; ++n)
#pragma unroll
                    for (int t = 0; t < 2; ++t) {
                        const unsigned idx = 6 * (8 * h + 4 * t + tq) + 2 * n + (tp >> 1);
                        const unsigned o = idx < 64u ? (tb ? 1024 + ((idx + 8) & 63) * 16 : idx * 16) : (tb ? 2560 + ((idx - 56) & 31) * 16 : 2048 + (idx - 64) * 16);
                        bn[n][t] = gdst + o + (tp & 1) * 8;
                    }
#pragma unroll
                for (int s = 0; s < 4; ++s) { pa[s] = L_PANEL + h * 512 + ((r ^ ((2 * s + h) & 7)) << 4); pb[s] = pa[s] + 40960; PIN(pb[s]); }
                bf16x8 wf[2][3], xa[2], xb2[2];
                auto rdw = [&](auto S) { constexpr int s = S.value;
#pragma unroll
                    for (int n = 0; n < 3; ++n) wf[s & 1][n] = ldstr2(bn[n][0] + (s % 3) * 3072, bn[n][1] + (s % 3) * 3072); };
                auto rdx = [&](auto S) { constexpr int s = S.value; xa[s & 1] = ldsf(pa[s & 3] + s * 1024); xb2[s & 1] = ldsf(pb[s & 3] + s * 1024); };
                WAITV(8);
                rdx(IC<0>{}); rdw(IC<0>{});
                asm volatile("s_waitcnt lgkmcnt(0)" ::: "memory");
                gdma(IC<3>{});
                static_for<0, 40>([&](auto S) {
                    constexpr int s = S.value; constexpr bool nx = s + 1 < 40;
                    MPRIO(GU_HI, 0); acc[0][0] = MFMA(wf[s & 1][0], xa[s & 1], acc[0][0]); SBAR();
                    if constexpr (nx) rdx(IC<s + 1>{});
                    SBAR();
                    MPRIO(GU_HI, 1); acc[1][0] = MFMA(wf[s & 1][0], xb2[s & 1], acc[1][0]); SBAR();
                    if constexpr (nx) { WAITVN(4 * ((s + 3 < 39 ? s + 3 : 39) - (s + 1))); rdw(IC<s + 1>{}); }
                    SBAR();
                    MPRIO(GU_HI, 2); acc[0][1] = MFMA(wf[s & 1][1], xa[s & 1], acc[0][1]); SBAR();
                    MPRIO(GU_HI, 3); acc[1][1] = MFMA(wf[s & 1][1], xb2[s & 1], acc[1][1]); SBAR();
                    if constexpr (s + 4 < 40) { asm volatile("s_waitcnt lgkmcnt(0)" ::: "memory"); gdma(IC<s + 4>{}); }
                    SBAR();
                    MPRIO(GU_HI, 4); acc[0][2] = MFMA(wf[s & 1][2], xa[s & 1], acc[0][2]); SBAR();
                    MPRIO(GU_HI, 5); acc[1][2] = MFMA(wf[s & 1][2], xb2[s & 1], acc[1][2]); SBAR();
                });
                lpost(C_GP + v, 42);
                __builtin_amdgcn_s_setprio(3);
                STAMP(6);
                const __amdgpu_buffer_rsrc_t rh = mkrsrc(ws + WS_HT, 64 * 96 * 1024);
#pragma unroll
                for (int m = 0; m < 2; ++m)
#pragma unroll
                    for (int n = 0; n < 3; ++n) {
                        f32x16 hv;
#pragma unroll
                        for (int e = 0; e < 8; ++e) {
                            const float gg = acc[m][n][e];
                            hv[e] = gg * __builtin_amdgcn_rcpf(1.f + __builtin_amdgcn_exp2f(-1.4426950408889634f * gg)) * acc[m][n][e + 8];
                        }
                        __builtin_amdgcn_raw_buffer_store_b128(__builtin_bit_cast(u32x4, pack8(hv, 0, 1.f)), rh, ((2 * ti + m) * 96 + g * 12 + 3 * v + n) * 1024 + lane * 16, 0, 16);
                    }
                if constexpr (DN) {
                    WAITV(0);
                    if (lane < 2) __hip_atomic_store((gu32*)(ws + WS_HF) + (2 * ti + lane) * 32 + g * 4 + v, tag, RLX_AGENT);
                }
                __builtin_amdgcn_s_setprio(0);
                if constexpr (DN) {
#pragma unroll
                    for (int n = 0; n < 2; ++n) {
                        unsigned L = (j * 4 + v) * 85 + n * 64 + lane; L = L < 10880u ? L : 10879u;
                        const unsigned row = L / 5u;
                        l2touch((row < 1536u ? p.w2 + row * 1280 : p.wo + (row - 1536u) * 1280) + (xc & 1) * 640 + (L - row * 5u) * 128);
                    }
                }
                STAMP(7);
            }
        } else {
            BLOCK_BARRIER();
        }
        if constexpr (DN || WO) {
            LANE_CONSTS();
            const unsigned bq = ((tb * 32 + (tp >> 1) * 16) ^ ((unsigned)(tq >> 1) << 6)) + (tp & 1) * 8;
            const unsigned drow = lane >> 3, dcol = ((lane & 7) ^ (((lane >> 4) & 1) << 2)) * 16;
            const int G = xc >> 1, r0 = G < 2 ? 6 * G : 12 + 5 * (G - 2), nr = G < 2 ? 6 : 5;
            if constexpr (FUSED) {
                if (b == 255 && v == 0) {
                    gu32* f = (gu32*)(ws + WS_HF) + (lane >> 3) * 64 + (lane & 7) * 4;
                    _Pragma("clang loop unroll(disable)") for (unsigned spins = 0; spins < (1u << 14); ++spins) {
                        bool ok = true;
#pragma unroll
                        for (int n = 0; n < 4; ++n) ok &= __hip_atomic_load(f + n * 512, RLX_AGENT) == tag;
                        if (__all(ok)) break;
                        TMO(spins, 1u << 14, 303, tag);
                        __builtin_amdgcn_s_sleep(32);
                    }
                    if (lane == 0) __hip_atomic_store((gu32*)(ws + WS_EP), tag, RLX_AGENT);
                }
            }
            if (j < 5 * nr) {
                const int dr = r0 + j / 5, dc = (xc & 1) * 5 + j % 5;
                const int mfirst = dr < 20 ? 3 * dr : 60 + 2 * (dr - 20), nm = dr < 20 ? 3 : 2;
                const unsigned dvo = drow * 1280 + dcol;
                const unsigned myring = L_PANEL + v * 20480;
                f32x16 acc0 = {}, acc1 = {};
                if constexpr (PRO) { lwait_all(C_GP, 4, 42, lane); lwait_all(C_AP, 4, 8, lane); }
                if constexpr (DN) {
                    f32x16 da[3][2] = {};
                    const int drot = (j % 5) * 4;
                    const char* bsrc = p.w2 + (size_t)v * 384 * 1280 + dc * 128;
                    const char* asrc = ws + WS_HT + ((size_t)mfirst * 96 + v * 24) * 1024;
                    const unsigned am1 = lane * 16 + 98304, am2 = lane * 16 + (nm == 3 ? 196608 : 98304);
                    constexpr int DQ = DN_DQ;
                    u32x4 aq[DQ][3];
                    auto bissue = [&](auto K) {
                        constexpr int k = K.value;
                        int kr = k + drot; kr = kr >= 24 ? kr - 24 : kr;
                        const char* sb = bsrc + (size_t)kr * 16 * 1280;
                        glds16(dvo, sb, myring + (k % 10) * 2048); glds16(dvo + 8 * 1280, sb, myring + (k % 10) * 2048 + 1024);
                    };
                    auto woissue = [&](auto U) {
                        constexpr int u = U.value;
                        const char* sb = p.wo + (size_t)(v * 160 + u * 16) * 1280 + dc * 128;
                        glds16(dvo, sb, myring + u * 2048); glds16(dvo + 8 * 1280, sb, myring + u * 2048 + 1024);
                    };
                    auto aissue = [&](auto K) {
                        constexpr int k = K.value;
                        int kr = k + drot; kr = kr >= 24 ? kr - 24 : kr;
                        const char* sb = asrc + (size_t)kr * 1024;
                        GLOAD16S(aq[k % DQ][0], lane * 16, sb); GLOAD16S(aq[k % DQ][1], am1, sb); GLOAD16S(aq[k % DQ][2], am2, sb);
                    };
                    static_for<0, DQ>(bissue);
                    if constexpr (PRO) {
                        gu32* f = (gu32*)(ws + WS_HF) + (mfirst + ((lane >> 3) < nm ? (lane >> 3) : 0)) * 32 + 8 * v + (lane & 7);
                        _Pragma("clang loop unroll(disable)") for (unsigned spins = 0; spins < (1u << 14); ++spins) {
                            if (__all(__hip_atomic_load(f, RLX_AGENT) == tag)) break;
                            TMO(spins, 1u << 14, 302, tag);
                            __builtin_amdgcn_s_sleep(HF_SLEEP);
                        }
                        CFENCE();
                    }
                    STAMP(8);
                    static_for<0, DQ>(aissue);
                    const unsigned ba0 = myring + (4 * h + tq) * 128 + bq, ba1 = ba0 ^ 64;
                    bf16x8 w0[2], w1[2];
                    WAITVN(3 * DQ);
                    w0[0] = ldstr2(ba0, ba0 + 1024); w1[0] = ldstr2(ba1, ba1 + 1024);
                    static_for<0, 24>([&](auto K) {
                        constexpr int k = K.value;
                        waitv3<dn_younger(k, DQ, WO)>(aq[k % DQ][0], aq[k % DQ][1], aq[k % DQ][2]);
                        constexpr unsigned o = ((k + 1) % 10) * 2048;
#define DXF(m) __builtin_bit_cast(bf16x8, aq[k % DQ][m])
                        SBAR();
                        MPRIO(DN_HI, 0); da[0][0] = MFMA(w0[k & 1], DXF(0), da[0][0]); SBAR();
                        if constexpr (k + 1 < 24) w0[(k + 1) & 1] = ldstr2(ba0 + o, ba0 + o + 1024);
                        SBAR();
                        MPRIO(DN_HI, 1); da[0][1] = MFMA(w1[k & 1], DXF(0), da[0][1]); SBAR();
                        if constexpr (k + 1 < 24) w1[(k + 1) & 1] = ldstr2(ba1 + o, ba1 + o + 1024);
                        SBAR();
                        MPRIO(DN_HI, 2); da[1][0] = MFMA(w0[k & 1], DXF(1), da[1][0]); SBAR();
                        if constexpr (k + DQ < 24) bissue(IC<k + DQ>{});
                        if constexpr (WO && k >= 16) woissue(IC<(k + 8) % 10>{});
                        SBAR();
                        MPRIO(DN_HI, 3); da[1][1] = MFMA(w1[k & 1], DXF(1), da[1][1]); SBAR();
                        MPRIO(DN_HI, 4); da[2][0] = MFMA(w0[k & 1], DXF(2), da[2][0]); SBAR();
                        MPRIO(DN_HI, 5); da[2][1] = MFMA(w1[k & 1], DXF(2), da[2][1]); SBAR();
                        if constexpr (k + DQ < 24) aissue(IC<k + DQ>{});
                    });
                    __builtin_amdgcn_s_setprio(3);
                    STAMP(9);
                    if constexpr (WO) { CFENCE(); woissue(IC<2>{}); woissue(IC<3>{}); }
#pragma unroll
                    for (int n = 0; n < 2; ++n) {
#pragma unroll
                        for (int m = 0; m < 3; ++m) {
                            if (m != v) {
                                const unsigned dst = L_GU + (m * 3 + (v > m ? v - 1 : v)) * 4096 + lane * 16;
#pragma unroll
                                for (int e = 0; e < 4; ++e) *LP(f32x4, dst + e * 1024) = f32x4{da[m][n][4 * e], da[m][n][4 * e + 1], da[m][n][4 * e + 2], da[m][n][4 * e + 3]};
                            }
                        }
                        lpost(C_DP + v, 2 * n + 1); lwait_all(C_DP, 4, 2 * n + 1, lane);
                        if (v < 3) {
                            f32x16 t = v == 0 ? da[0][n] : (v == 1 ? da[1][n] : da[2][n]);
#pragma unroll
                            for (int o = 0; o < 3; ++o)
#pragma unroll
                                for (int e = 0; e < 4; ++e) {
                                    const f32x4 u = *LP(const f32x4, L_GU + (v * 3 + o) * 4096 + e * 1024 + lane * 16);
                                    t[4 * e] += u[0]; t[4 * e + 1] += u[1]; t[4 * e + 2] += u[2]; t[4 * e + 3] += u[3];
                                }
                            if (n) acc1 = t; else acc0 = t;
                        }
                        if (n == 0) { lpost(C_DP + v, 2); lwait_all(C_DP, 4, 2, lane); }
                    }
                    if constexpr (!WO) {
                        if (v < 3) {
                            float* pt = (float*)(ws + WS_PT) + (size_t)(b * 3 + v) * 2048 + lane;
#pragma unroll
                            for (int e = 0; e < 16; ++e) { pt[e * 64] = acc0[e]; pt[1024 + e * 64] = acc1[e]; }
                        }
                    }
                } else {
#pragma unroll
                    for (int u = 0; u < 20; ++u) glds16(dvo + u * 8 * 1280, p.wo + (size_t)v * 160 * 1280 + dc * 128, myring + u * 1024);
                    if (v < 3) {
                        const float* pt = (const float*)(ws + WS_PT) + (size_t)(b * 3 + v) * 2048 + lane;
#pragma unroll
                        for (int e = 0; e < 16; ++e) { acc0[e] = pt[e * 64]; acc1[e] = pt[1024 + e * 64]; }
                    }
                }
                if constexpr (WO) { WAITV(0); lpost(C_DP + v, 4); }
                if constexpr (WO && ATT) {
                    if (v == 3) {
                        __builtin_amdgcn_s_setprio(0);
                        const int mi = (lane >> 4) < nm ? (lane >> 4) : 0;
                        gu64* f = (gu64*)(ws + WS_OF + (mfirst + mi) * 128) + (lane & 15);
                        _Pragma("clang loop unroll(disable)") for (unsigned spins = 0; spins < (1u << 14); ++spins) {
                            const unsigned long long x = __hip_atomic_load(f, RLX_AGENT);
                            const unsigned long long ok = __ballot((unsigned)x == tag && (unsigned)(x >> 32) == tag);
                            const unsigned mine = (unsigned)(ok >> ((lane & 3) * 16));
                            *((volatile AS3 unsigned*)lcnt(C_OS) + (lane & 3)) = ((mine & 0xffu) == 0xffu ? 1u : 0u) | ((mine & 0xff00u) == 0xff00u ? 2u : 0u);
                            if (ok == ~0ull) break;
                            TMO(spins, 1u << 14, 300, tag);
                            __builtin_amdgcn_s_sleep(OF_SLEEP);
                        }
                    }
                }
                if (v < nm) {
                    const int m = mfirst + v;
                    const unsigned ba0 = L_PANEL + (4 * h + tq) * 128 + bq, ba1 = ba0 ^ 64;
                    if constexpr (WO) {
                        const char* xrow = p.x + (size_t)(m * 32 + r) * 1280 + dc * 128 + h * 16;
                        u32x4 xres[4];
#pragma unroll
                        for (int e = 0; e < 4; ++e) xres[e] = *(const u32x4*)(xrow + e * 32);
                        lwait_all(C_DP, 4, 4, lane);
                        const __amdgpu_buffer_rsrc_t ro = mkrsrc(ws + WS_OT + (size_t)m * 40960, 40960);
                        int hf = 0;
                        f32x16 t0[2] = {}, t1[2] = {};
#pragma unroll
                        for (int it = 0; it < 2; ++it) {
                            if constexpr (ATT) {
                                _Pragma("clang loop unroll(disable)") for (unsigned spins = 0; spins < (1u << 17); ++spins) {
                                    const unsigned ok = __builtin_amdgcn_readfirstlane(*((volatile AS3 unsigned*)lcnt(C_OS) + v));
                                    if (it == 0) { if (ok) { hf = (ok & 1) ? 0 : 1; break; } }
                                    else if (ok == 3) break;
                                    __builtin_amdgcn_s_sleep(1);
                                }
                                CFENCE();
                            }
                            if (it == 0) STAMP(10);
                            u32x4 of[20];
#pragma unroll
                            for (int s = 0; s < 20; ++s) of[s] = __builtin_amdgcn_raw_buffer_load_b128(ro, lane * 16 + s * 1024, hf * 20480, 16);
                            const unsigned a0 = ba0 + hf * 40960, a1 = ba1 + hf * 40960, l0 = a0 + (36 * h + 16) * 128, l1 = a1 + (36 * h + 16) * 128;
                            bf16x8 w0[2], w1[2];
                            w0[0] = ldstr2(a0, a0 + 1024); w1[0] = ldstr2(a1, a1 + 1024);
                            static_for<0, 20>([&](auto S) {
                                constexpr int s = S.value, n = s + 1, gg = n / 5, jj = n % 5;
                                const bf16x8 xf = __builtin_bit_cast(bf16x8, of[s]);
                                if constexpr (n < 20) {
                                    if constexpr (jj < 4) {
                                        constexpr unsigned o = (80 * gg + 20 * jj) * 128;
                                        w0[n & 1] = ldstr2(a0 + o, a0 + o + 1024); w1[n & 1] = ldstr2(a1 + o, a1 + o + 1024);
                                    } else {
                                        constexpr unsigned o = 80 * gg * 128;
                                        w0[n & 1] = ldstr2(l0 + o, l0 + o + 2560); w1[n & 1] = ldstr2(l1 + o, l1 + o + 2560);
                                    }
                                }
                                SBAR();
                                t0[it] = MFMA(w0[s & 1], xf, t0[it]); t1[it] = MFMA(w1[s & 1], xf, t1[it]);
                                SBAR();
                            });
                            hf ^= 1;
                        }
                        acc0 += t0[0] + t0[1]; acc1 += t1[0] + t1[1];
                        char* orow = p.out + (size_t)(m * 32 + r) * 1280 + dc * 128 + h * 16;
#pragma unroll
                        for (int n = 0; n < 2; ++n) {
                            const f32x16& a = n ? acc1 : acc0;
#pragma unroll
                            for (int qq = 0; qq < 2; ++qq) {
                                float lo[4], hi[4];
#pragma unroll
                                for (int e = 0; e < 4; ++e) {
                                    auto sw = __builtin_amdgcn_permlane32_swap(__float_as_uint(a[8 * qq + e]), __float_as_uint(a[8 * qq + 4 + e]), false, false);
                                    lo[e] = __uint_as_float(sw[0]); hi[e] = __uint_as_float(sw[1]);
                                }
                                const u32x4 xv = xres[2 * n + qq];
                                const u32x4 ov = {pk2(lo[0] + bflo(xv[0]), lo[1] + bfhi(xv[0])), pk2(lo[2] + bflo(xv[1]), lo[3] + bfhi(xv[1])),
                                                  pk2(hi[0] + bflo(xv[2]), hi[1] + bfhi(xv[2])), pk2(hi[2] + bflo(xv[3]), hi[3] + bfhi(xv[3]))};
                                *(u32x4*)(orow + (2 * n + qq) * 32) = ov;
                            }
                        }
                        STAMP(11);
                    }
                }
            }
        }
    } else {
        LANE_CONSTS();
        bf16x8 Q0a, Q0b; u32x2 q1a, q1b;
        const int head = 4 * g + w;
        const AttSync as{lane, ws + WS_KV, (unsigned)((w < 3 ? w * 3072 : CHUNK - 3072) + lane * 16), (unsigned)(w < 3 ? w * 3072 : CHUNK - 3072), w};
        __builtin_amdgcn_s_setprio(3);
        unsigned long long fk = 0, fv = 0; bool early = false;
        if constexpr (PRO) {
            BLOCK_BARRIER();
            waitv5<15>(x0[0], x0[1], x0[2], x0[3], x0[4]);
            STAMP(1);
            ln_rows(x0, ra, lane);
            waitv5<10>(x1[0], x1[1], x1[2], x1[3], x1[4]);
            ln_rows(x1, ra + 4, lane);
            lpost(C_AP + w, 1);
            waitv5<5>(x2[0], x2[1], x2[2], x2[3], x2[4]);
            ln_rows(x2, rb, lane);
            waitv5<0>(x3[0], x3[1], x3[2], x3[3], x3[4]);
            ln_rows(x3, rb + 4, lane);
            lpost(C_AP + w, 2);
            unsigned pa[4];
#pragma unroll
            for (int s = 0; s < 4; ++s) pa[s] = L_PANEL + h * 512 + ((r ^ ((2 * s + h) & 7)) << 4);
            f32x16 qa = {}, qb = {};
            const unsigned wa = L_WQ + (8 * h + tq) * 160 + w * 40 + tb * 32 + (tb ? 0 : tp * 8);
            bf16x8 fw[3][4], fa[2][4], fb[2][4];
            auto rdw = [&](auto S, auto S4) { constexpr int st = S.value, s4 = S4.value; const unsigned o = wa + (st & 3) * 10240 + s4 * 2560; fw[st % 3][s4] = ldstr2(o, o + 640); };
            lwait_all(C_PP, 4, 1, lane);
            static_for<0, 4>([&](auto I) { rdw(IC<0>{}, I); rdw(IC<1>{}, I); });
            if constexpr (FUSED) tag = __builtin_amdgcn_readfirstlane(*(volatile AS3 unsigned*)lcnt(C_TAG));
            lpost(C_AP + w, 3);
            lwait_all(C_AP, 4, 2, lane);
            STAMP(2);
#pragma unroll
            for (int s4 = 0; s4 < 4; ++s4) { fa[0][s4] = ldsf(pa[s4] + s4 * 1024); fb[0][s4] = ldsf(pa[s4] + 40960 + s4 * 1024); }
            unsigned pk = 0;
            static_for<0, 10>([&](auto S) {
                constexpr int st = S.value, nx = st + 1;
                if constexpr (st == 3 || st == 5 || st == 7) lcheck(pk, C_WP, (st - 1) / 2, lane);
                if constexpr (st == KV_PEEK && ATT) gload8x2_sc1(fk, fv, lane * 8, ws + WS_KF + xc * 1024);
                if constexpr (st == 8 && ATT) {
                    waitv8x2(fk, fv);
                    early = __all((unsigned)(fk >> 32) == tag && (unsigned)(fv >> 32) == tag);
                    if (early) { lwait_all(C_AP, 4, 6, lane); att_dma(as, 0); att_dma(as, 1); }
                    STAMP(8);
                }
                static_for<0, 4>([&](auto S4) {
                    constexpr int s4 = S4.value;
                    qa = MFMA(fw[st % 3][s4], fa[st & 1][s4], qa); SBAR();
                    if constexpr (st == 0) rdw(IC<2>{}, S4);
                    if constexpr (st == 1) rdw(IC<3>{}, S4);
                    if constexpr (st == 3 || st == 5 || st == 7) { rdw(IC<st + 1>{}, S4); rdw(IC<st + 2>{}, S4); }
                    SBAR();
                    qb = MFMA(fw[st % 3][s4], fb[st & 1][s4], qb); SBAR();
                    if constexpr (nx < 10) { fa[nx & 1][s4] = ldsf(pa[s4] + (nx * 4 + s4) * 1024); fb[nx & 1][s4] = ldsf(pa[s4] + 40960 + (nx * 4 + s4) * 1024); SBAR(); }
                    if constexpr (s4 == 2 && (st == 2 || st == 4 || st == 6)) { pk = lpeek(C_WP, lane); SBAR(); }
                });
                if constexpr (st == 1 || st == 3 || st == 5) lpost(C_AP + w, 4 + st / 2);
            });
            lpost(C_AP + w, 8);
            Q0a = pack8(qa, 0, QSCALE); Q0b = pack8(qb, 0, QSCALE);
            q1a = u32x2{pk2(qa[8] * QSCALE, qa[9] * QSCALE), pk2(qa[10] * QSCALE, qa[11] * QSCALE)};
            q1b = u32x2{pk2(qb[8] * QSCALE, qb[9] * QSCALE), pk2(qb[10] * QSCALE, qb[11] * QSCALE)};
            if (h) { q1a = u32x2{0u, 0u}; q1b = u32x2{0u, 0u}; }
            if constexpr (!ATT) {
                char* qt = ws + WS_QT + (size_t)((2 * ti) * 32 + head) * 2048 + lane * 16;
                *(bf16x8*)qt = Q0a; *(u32x2*)(qt + 1024) = q1a; *(bf16x8*)(qt + 65536) = Q0b; *(u32x2*)(qt + 65536 + 1024) = q1b;
            }
            STAMP(3);
        } else {
            BLOCK_BARRIER();
            if constexpr (ATT) {
                const char* qt = ws + WS_QT + (size_t)((2 * ti) * 32 + head) * 2048 + lane * 16;
                Q0a = *(const bf16x8*)qt; q1a = *(const u32x2*)(qt + 1024); Q0b = *(const bf16x8*)(qt + 65536); q1b = *(const u32x2*)(qt + 65536 + 1024);
            }
        }
        if constexpr (ATT) {
            float na = 0.f, nb = 0.f;
            {
                const u32x4 a0 = __builtin_bit_cast(u32x4, Q0a), b0 = __builtin_bit_cast(u32x4, Q0b);
#pragma unroll
                for (int e = 0; e < 4; ++e) { na += bflo(a0[e]) * bflo(a0[e]) + bfhi(a0[e]) * bfhi(a0[e]); nb += bflo(b0[e]) * bflo(b0[e]) + bfhi(b0[e]) * bfhi(b0[e]); }
#pragma unroll
                for (int e = 0; e < 2; ++e) { na += bflo(q1a[e]) * bflo(q1a[e]) + bfhi(q1a[e]) * bfhi(q1a[e]); nb += bflo(q1b[e]) * bflo(q1b[e]) + bfhi(q1b[e]) * bfhi(q1b[e]); }
                const auto sa = __builtin_amdgcn_permlane32_swap(__float_as_uint(na), __float_as_uint(na), false, false), sb = __builtin_amdgcn_permlane32_swap(__float_as_uint(nb), __float_as_uint(nb), false, false);
                na = __uint_as_float(sa[0]) + __uint_as_float(sa[1]); nb = __uint_as_float(sb[0]) + __uint_as_float(sb[1]);
            }
            if (!early) {
                gu64* f = (gu64*)(ws + WS_KF + xc * 1024) + lane;
                _Pragma("clang loop unroll(disable)") for (unsigned spins = 0; spins < (1u << 14); ++spins) {
                    fk = __hip_atomic_load(f, RLX_AGENT); fv = __hip_atomic_load(f + 64, RLX_AGENT);
                    if (__all((unsigned)(fk >> 32) == tag && (unsigned)(fv >> 32) == tag)) break;
                    TMO(spins, 1u << 14, 301, tag);
                    __builtin_amdgcn_s_sleep(4);
                }
                if constexpr (PRO) lwait_all(C_AP, 4, 6, lane);
                att_dma(as, 0); att_dma(as, 1);
            }
            STAMP(4);
            const float kn2 = wave_max(__uint_as_float((unsigned)fk)), vmx = wave_max(__uint_as_float((unsigned)fv));
            const float Ma = __builtin_amdgcn_sqrtf(na * kn2) * 1.01f, Mb = __builtin_amdgcn_sqrtf(nb * kn2) * 1.01f;
            const bool fast = __all(Ma <= BOUND_MAX && Mb <= BOUND_MAX && vmx <= 1e30f);
            const u32x2 a1 = {(fast && h) ? pk2(-Ma, 0.f) : q1a[0], q1a[1]}, b1 = {(fast && h) ? pk2(-Mb, 0.f) : q1b[0], q1b[1]};
            f32x16 Oa = {}, Ob = {};
            WAITV(3); lpost(C_AP + w, AP0); lwait_all(C_AP, 4, AP0, lane);
            att_dma(as, 2);
            __builtin_amdgcn_s_setprio(ATT_PRIO);
            STAMP(5); STAMPC(12);
            if (fast) att_fast(as, Q0a, __builtin_bit_cast(s16x4, a1), Q0b, __builtin_bit_cast(s16x4, b1), Oa, Ob);
            else att_safe(as, Q0a, __builtin_bit_cast(s16x4, a1), Q0b, __builtin_bit_cast(s16x4, b1), Oa, Ob);
            STAMP(6); STAMPC(13);
            const __amdgpu_buffer_rsrc_t ro = mkrsrc(ws + WS_OT, 64 * 40 * 1024);
#pragma unroll
            for (int q = 0; q < 2; ++q) {
                const f32x16& O = q ? Ob : Oa;
                const auto lsw = __builtin_amdgcn_permlane32_swap(__float_as_uint(O[8]), __float_as_uint(O[8]), false, false);
                const float inv = __builtin_amdgcn_rcpf(__uint_as_float(lsw[1]));
                const unsigned base = ((2 * ti + q) * 40 + 5 * g) * 1024;
                __builtin_amdgcn_raw_buffer_store_b128(__builtin_bit_cast(u32x4, pack8(O, 0, inv)), ro, base + w * 1024 + lane * 16, 0, 16);
                if (!h) {
                    u32x2 t = {pk2(O[8] * inv, O[9] * inv), pk2(O[10] * inv, O[11] * inv)};
                    __builtin_amdgcn_raw_buffer_store_b64(t, ro, base + 4096 + ((w >> 1) * 32 + r) * 16 + (w & 1) * 8, 0, 16);
                }
            }
            if constexpr (WO) {
                WAITV(0);
                if (lane < 2) __hip_atomic_store((gu32*)(ws + WS_OF) + (2 * ti + lane) * 32 + head, tag, RLX_AGENT);
            }
            STAMP(7);
        }
    }
}

template <int PH>
static void launch(const Params& p, hipStream_t stream) {
    (void)hipFuncSetAttribute((const void*)mega<PH>, hipFuncAttributeMaxDynamicSharedMemorySize, L_TOTAL);
    mega<PH><<<256, 512, L_TOTAL, stream>>>(p);
}

extern "C" void kernel_launch(void* const* d_in, const int* in_sizes, int n_in, void* d_out, int out_size, void* d_ws, size_t ws_size, hipStream_t stream) {
    Params p;
    p.x = (const char*)d_in[0]; p.wq = (const char*)d_in[1]; p.wk = (const char*)d_in[2]; p.wv = (const char*)d_in[3]; p.wo = (const char*)d_in[4];
    p.w1 = (const char*)d_in[6]; p.w3 = (const char*)d_in[7]; p.w2 = (const char*)d_in[8];
    p.out = (char*)d_out; p.ws = (char*)d_ws;
    launch<27>(p, stream);
}
```

```cpp
#include <hip/hip_runtime.h>
#include <hip/hip_bf16.h>
#include <stdint.h>

#define AS1 __attribute__((address_space(1)))
#define AS3 __attribute__((address_space(3)))
typedef __bf16 bf16x2 __attribute__((ext_vector_type(2)));
typedef __bf16 bf16x8 __attribute__((ext_vector_type(8)));
typedef short s16x4 __attribute__((ext_vector_type(4)));
typedef short s16x8 __attribute__((ext_vector_type(8)));
typedef float f32x2 __attribute__((ext_vector_type(2)));
typedef float f32x4 __attribute__((ext_vector_type(4)));
typedef float f32x16 __attribute__((ext_vector_type(16)));
typedef unsigned u32x2 __attribute__((ext_vector_type(2)));
typedef unsigned u32x4 __attribute__((ext_vector_type(4)));
typedef AS1 unsigned gu32;
typedef AS1 unsigned long long gu64;

#define ATT_PRIO 2
#define GEM_PRIO 0
#define GU_HI 0
#define DN_HI 0
#define MPRIO(mask, i) __builtin_amdgcn_s_setprio(((mask) >> (i)) & 1 ? 3 : 0)
#define GU_DELAY 8
#define GU_ROT 4
#define GU_GQ 5
#define OF_SLEEP 8
#define HF_SLEEP 20
#define DN_DQ 8
#define KV_PEEK 6
#define KV_CHECK 8
#define SPLIT_MODE 0

constexpr int SEQ = 2048, DM = 640, FF = 1536;
constexpr unsigned WS_KF = 0;
constexpr unsigned WS_HF = 16384;
constexpr unsigned WS_OF = 32768;
constexpr unsigned WS_EP = 49152;
constexpr unsigned KVD = 5376;
constexpr unsigned KV_K1 = 2048, KV_V = 2560, KV_VT = 1408;
constexpr unsigned WS_KV = 65536;
constexpr unsigned WS_HT = WS_KV + 176128;
constexpr unsigned WS_OT = WS_HT + 64 * 96 * 1024;
constexpr unsigned WS_QT = WS_OT + 64 * 40 * 1024;
constexpr unsigned WS_PT = WS_QT + 64 * 32 * 2048;
constexpr unsigned WS_ST = WS_PT + 256 * 3 * 32 * 256;
constexpr unsigned L_PANEL = 0;
constexpr unsigned L_KV = 81920;
constexpr unsigned L_WQ = 81920;
constexpr unsigned L_GU = 124928;
constexpr unsigned GSL = 12320;
constexpr unsigned L_WKV = 124928;
constexpr unsigned L_MISC = 161920;
constexpr unsigned L_TOTAL = 163840;
constexpr unsigned CHUNK = 2 * KVD;
constexpr int NCHUNK = 16;
enum { C_AP = 0  , C_GP = 4  , C_DP = 8  , C_PP = 12  , C_WP = 16  , C_KVP = 20, C_KVFIN, C_TAG, C_OS = 24  , C_N = 28 };

struct Params { const char* x; const char* wq; const char* wk; const char* wv; const char* wo; const char* w1; const char* w3; const char* w2; char* out; char* ws; };

#define SBAR() __builtin_amdgcn_sched_barrier(0)
#define PIN(x) asm volatile("" : "+v"(x))
#define CFENCE() asm volatile("" ::: "memory")
#define WAITV(n) asm volatile("s_waitcnt vmcnt(" #n ")" ::: "memory")
#define MFMA(a, b, c) __builtin_amdgcn_mfma_f32_32x32x16_bf16((a), (b), (c), 0, 0, 0)
#define MFMA8(a, b, c) __builtin_amdgcn_mfma_f32_32x32x8bf16_1k((a), (b), (c), 0, 0, 0)
#define RLX_AGENT __ATOMIC_RELAXED, __HIP_MEMORY_SCOPE_AGENT
#define RLX_WG __ATOMIC_RELAXED, __HIP_MEMORY_SCOPE_WORKGROUP

#define TMO(spins, lim, code, tgt) do {} while (0)
#define STAMP(id) do {} while (0)
#define STAMPC(id) do {} while (0)

#define LP(T, off) ((AS3 T*)(uintptr_t)(unsigned)(off))
__device__ __forceinline__ bf16x8 ldsf(unsigned off) { return *LP(const bf16x8, off); }
__device__ __forceinline__ s16x4 ldstr(unsigned off) { return __builtin_amdgcn_ds_read_tr16_b64_v4i16(LP(s16x4, off)); }
__device__ __forceinline__ bf16x8 ldstr2(unsigned off0, unsigned off1) {
    s16x4 a = ldstr(off0), b = ldstr(off1);
    return __builtin_bit_cast(bf16x8, __builtin_shufflevector(a, b, 0, 1, 2, 3, 4, 5, 6, 7));
}
template <int E> __device__ __forceinline__ s16x4 half4(bf16x8 v) { const s16x8 t = __builtin_bit_cast(s16x8, v); return __builtin_shufflevector(t, t, 4 * E, 4 * E + 1, 4 * E + 2, 4 * E + 3); }
__device__ __forceinline__ unsigned pk2(float lo, float hi) { bf16x2 v = {(__bf16)lo, (__bf16)hi}; return __builtin_bit_cast(unsigned, v); }
__device__ __forceinline__ float bflo(unsigned u) { return __uint_as_float(u << 16); }
__device__ __forceinline__ float bfhi(unsigned u) { return __uint_as_float(u & 0xffff0000u); }
__device__ __forceinline__ float rbf(float f) { return (float)(__bf16)f; }

__device__ __forceinline__ void glds16(unsigned voff, const char* sbase, unsigned ldsdst) {
    unsigned keep;
    asm volatile("s_mov_b32 %0, m0\n\ts_mov_b32 m0, %3\n\ts_nop 2\n\tglobal_load_lds_dwordx4 %1, %2\n\ts_mov_b32 m0, %0"
                 : "=&s"(keep) : "v"(voff), "s"(sbase), "s"(ldsdst) : "memory");
}
__device__ __forceinline__ void glds16_sc1(unsigned voff, const char* sbase, unsigned ldsdst) {
    unsigned keep;
    asm volatile("s_mov_b32 %0, m0\n\ts_mov_b32 m0, %3\n\ts_nop 2\n\tglobal_load_lds_dwordx4 %1, %2 sc1\n\ts_mov_b32 m0, %0"
                 : "=&s"(keep) : "v"(voff), "s"(sbase), "s"(ldsdst) : "memory");
}
__device__ __forceinline__ void glds16x3_sc1(unsigned voff, const char* sbase, unsigned ldsdst) {
    unsigned keep;
    asm volatile("s_mov_b32 %0, m0\n\ts_mov_b32 m0, %3\n\ts_nop 2\n\tglobal_load_lds_dwordx4 %1, %2 sc1\n\tglobal_load_lds_dwordx4 %1, %2 offset:1024 sc1\n\tglobal_load_lds_dwordx4 %1, %2 offset:2048 sc1\n\ts_mov_b32 m0, %0"
                 : "=&s"(keep) : "v"(voff), "s"(sbase), "s"(ldsdst) : "memory");
}
__device__ __forceinline__ void glds16x3v(unsigned v0, unsigned v1, unsigned v2, const char* sbase, unsigned ldsdst) {
    unsigned keep;
    asm volatile("s_mov_b32 %0, m0\n\ts_mov_b32 m0, %5\n\ts_nop 0\n\tglobal_load_lds_dwordx4 %1, %4\n\tglobal_load_lds_dwordx4 %2, %4 offset:1024\n\tglobal_load_lds_dwordx4 %3, %4 offset:2048\n\ts_mov_b32 m0, %0"
                 : "=&s"(keep) : "v"(v0), "v"(v1), "v"(v2), "s"(sbase), "s"(ldsdst) : "memory");
}
__device__ __forceinline__ void glds_gu(unsigned va, unsigned vb, unsigned vc, const char* s1, const char* s3, unsigned ldsdst) {
    unsigned keep;
    asm volatile("s_mov_b32 %0, m0\n\ts_mov_b32 m0, %8\n\ts_nop 0\n\tglobal_load_lds_dwordx4 %1, %4\n\tglobal_load_lds_dwordx4 %2, %5 offset:1024\n\t"
                 "s_mov_b32 exec_hi, 0\n\tglobal_load_lds_dwordx4 %3, %6 offset:2048\n\ts_not_b64 exec, exec\n\tglobal_load_lds_dwordx4 %3, %7 offset:2048\n\t"
                 "s_mov_b64 exec, -1\n\ts_mov_b32 m0, %0"
                 : "=&s"(keep) : "v"(va), "v"(vb), "v"(vc), "s"(s1), "s"(s3 - 1024), "s"(s1 - 2048), "s"(s3 - 2048), "s"(ldsdst) : "memory", "scc");
}
__device__ __forceinline__ void l2touch(const char* ga) {
    unsigned keep;
    asm volatile("s_mov_b32 %0, m0\n\ts_mov_b32 m0, %2\n\ts_nop 0\n\tglobal_load_lds_dword %1, off\n\ts_mov_b32 m0, %0" : "=&s"(keep) : "v"(ga), "s"(L_MISC + 1024) : "memory");
}
#define GLOAD16(dst, voff, sbase, imm) asm volatile("s_nop 4\n\tglobal_load_dwordx4 %0, %1, %2 offset:%3" : "=v"(dst) : "v"(voff), "s"(sbase), "i"(imm) : "memory")

__device__ __forceinline__ AS3 unsigned* lcnt(int c) { return LP(unsigned, L_MISC) + c; }
__device__ __forceinline__ void larrive(int c, int lane) { CFENCE(); if (lane == 0) __hip_atomic_fetch_add(lcnt(c), 1u, RLX_WG); CFENCE(); }
__device__ __forceinline__ void lset(int c, unsigned v, int lane) { CFENCE(); if (lane == 0) __hip_atomic_store(lcnt(c), v, RLX_WG); CFENCE(); }
__device__ __forceinline__ void lwait(int c, unsigned target) {
    CFENCE();
    _Pragma("clang loop unroll(disable)") for (unsigned spins = 0; spins < (1u << 22); ++spins) {
        if ((unsigned)__builtin_amdgcn_readfirstlane(__hip_atomic_load(lcnt(c), RLX_WG)) >= target) break;
        TMO(spins, 1u << 16, 100 + c, target);
        __builtin_amdgcn_s_sleep(1);
    }
    CFENCE();
}
__device__ __forceinline__ void lwait_all(int c, int n, unsigned target, int lane) {
    CFENCE();
    AS3 unsigned* q = lcnt(c) + (lane < n ? lane : 0);
    _Pragma("clang loop unroll(disable)") for (unsigned spins = 0; spins < (1u << 16); ++spins) {
        if (__all(__hip_atomic_load(q, RLX_WG) >= target)) break;
        TMO(spins, 1u << 16, 200 + c, target);
        __builtin_amdgcn_s_sleep(1);
    }
    CFENCE();
}
__device__ __forceinline__ void lpost(int c, unsigned v) { CFENCE(); *(volatile AS3 unsigned*)lcnt(c) = v; CFENCE(); }
__device__ __forceinline__ void linc(unsigned vaddr, unsigned vone) {
    asm volatile("s_mov_b64 exec, 1\n\tds_add_u32 %0, %1\n\ts_mov_b64 exec, -1" :: "v"(vaddr), "v"(vone) : "memory");
}
__device__ __forceinline__ unsigned lpeek(int c, int lane) { return *((volatile AS3 unsigned*)lcnt(c) + (lane & 3)); }
__device__ __forceinline__ void lcheck(unsigned pk, int c, unsigned target, int lane) {
    if (__builtin_expect(__any(pk < target), 0)) lwait_all(c, 4, target, lane);
    CFENCE();
}
__device__ __forceinline__ void gwait(const char* ws, unsigned off, int n, unsigned tag, int lane) {
    gu32* f = (gu32*)(ws + off) + (lane < n ? lane : 0);
    _Pragma("clang loop unroll(disable)") for (unsigned spins = 0; spins < (1u << 14); ++spins) {
        unsigned v = __hip_atomic_load(f, RLX_AGENT);
        if (__all(v == tag)) break;
        TMO(spins, 1u << 14, 300, off);
        __builtin_amdgcn_s_sleep(6);
    }
    CFENCE();
}
__device__ __forceinline__ float gwait64(const char* ws, unsigned off, unsigned tag, int lane) {
    gu64* f = (gu64*)(ws + off) + lane;
    unsigned long long v = 0;
    _Pragma("clang loop unroll(disable)") for (unsigned spins = 0; spins < (1u << 14); ++spins) {
        v = __hip_atomic_load(f, RLX_AGENT);
        if (__all((unsigned)(v >> 32) == tag)) break;
        __builtin_amdgcn_s_sleep(6);
    }
    CFENCE();
    return __uint_as_float((unsigned)v);
}
__device__ __forceinline__ __amdgpu_buffer_rsrc_t mkrsrc(const char* base, unsigned bytes) {
    return __builtin_amdgcn_make_buffer_rsrc((void*)base, 0, bytes, 0x00020000);
}
__device__ __forceinline__ float dpp_sum16(float v) {
    v += __int_as_float(__builtin_amdgcn_update_dpp(0, __float_as_int(v), 0x128, 0xf, 0xf, false));
    v += __int_as_float(__builtin_amdgcn_update_dpp(0, __float_as_int(v), 0x124, 0xf, 0xf, false));
    v += __int_as_float(__builtin_amdgcn_update_dpp(0, __float_as_int(v), 0x122, 0xf, 0xf, false));
    v += __int_as_float(__builtin_amdgcn_update_dpp(0, __float_as_int(v), 0x121, 0xf, 0xf, false));
    return v;
}
__device__ __forceinline__ float wave_max(float v) {
    int x = __float_as_int(v);
    x = max(x, __builtin_amdgcn_update_dpp(0, x, 0x128, 0xf, 0xf, false)); x = max(x, __builtin_amdgcn_update_dpp(0, x, 0x124, 0xf, 0xf, false));
    x = max(x, __builtin_amdgcn_update_dpp(0, x, 0x122, 0xf, 0xf, false)); x = max(x, __builtin_amdgcn_update_dpp(0, x, 0x121, 0xf, 0xf, false));
    const int a = max(__builtin_amdgcn_readlane(x, 0), __builtin_amdgcn_readlane(x, 16)), b = max(__builtin_amdgcn_readlane(x, 32), __builtin_amdgcn_readlane(x, 48));
    return __int_as_float(max(a, b));
}
__device__ __forceinline__ bf16x8 pack8(const f32x16& a, int b, float sc) {
    u32x4 r = {pk2(a[b] * sc, a[b + 1] * sc), pk2(a[b + 2] * sc, a[b + 3] * sc), pk2(a[b + 4] * sc, a[b + 5] * sc), pk2(a[b + 6] * sc, a[b + 7] * sc)};
    return __builtin_bit_cast(bf16x8, r);
}

template <int I> struct IC { static constexpr int value = I; };
template <int B, int E, class F> __device__ __forceinline__ void static_for(F&& f) { if constexpr (B < E) { f(IC<B>{}); static_for<B + 1, E>(f); } }
#define WAITVN(n) asm volatile("s_waitcnt vmcnt(%0)" :: "i"(n) : "memory")
__device__ __forceinline__ void GLOAD16S(u32x4& dst, unsigned voff, const char* sbase) { asm volatile("global_load_dwordx4 %0, %1, %2 sc1" : "=v"(dst) : "v"(voff), "s"(sbase) : "memory"); }
__device__ __forceinline__ void GLOAD16N(u32x4& dst, unsigned voff, const char* sbase) { asm volatile("global_load_dwordx4 %0, %1, %2" : "=v"(dst) : "v"(voff), "s"(sbase) : "memory"); }
__device__ __forceinline__ void GLOAD16N_IF(u32x4& dst, unsigned voff, const char* sbase, int cond) {
    asm volatile("s_cmp_eq_u32 %3, 0\n\ts_cbranch_scc1 1f\n\tglobal_load_dwordx4 %0, %1, %2\n1:" : "=v"(dst) : "v"(voff), "s"(sbase), "s"(cond) : "memory", "scc"); }
template <int N> __device__ __forceinline__ void waitv5(u32x4& a, u32x4& b, u32x4& c, u32x4& d, u32x4& e) { asm volatile("s_waitcnt vmcnt(%5)" : "+v"(a), "+v"(b), "+v"(c), "+v"(d), "+v"(e) : "i"(N) : "memory"); }
__device__ __forceinline__ void gload8x2_sc1(unsigned long long& a, unsigned long long& b, unsigned voff, const char* sbase) {
    asm volatile("global_load_dwordx2 %0, %2, %3 sc1\n\tglobal_load_dwordx2 %1, %2, %3 offset:512 sc1" : "=&v"(a), "=&v"(b) : "v"(voff), "s"(sbase) : "memory"); }
__device__ __forceinline__ void waitv8x2(unsigned long long& a, unsigned long long& b) { asm volatile("s_waitcnt vmcnt(0)" : "+v"(a), "+v"(b) :: "memory"); }
__device__ __forceinline__ void PIN2(u32x4& a, u32x4& b) { asm volatile("" : "+v"(a), "+v"(b) :: "memory"); }
__device__ __forceinline__ u32x4 scale8(unsigned x0, unsigned x1, unsigned x2, unsigned x3, unsigned clo, unsigned chi, float c) {
    unsigned o0, o1, o2, o3; float t0, t1, t2, t3, t4, t5, t6, t7;
    asm("v_dot2_f32_bf16 %4, %12, %16, %18\n\tv_dot2_f32_bf16 %5, %12, %17, %18\n\tv_dot2_f32_bf16 %6, %13, %16, %18\n\tv_dot2_f32_bf16 %7, %13, %17, %18\n\t"
        "v_dot2_f32_bf16 %8, %14, %16, %18\n\tv_dot2_f32_bf16 %9, %14, %17, %18\n\tv_dot2_f32_bf16 %10, %15, %16, %18\n\tv_dot2_f32_bf16 %11, %15, %17, %18\n\t"
        "v_cvt_pk_bf16_f32 %0, %4, %5\n\tv_cvt_pk_bf16_f32 %1, %6, %7\n\tv_cvt_pk_bf16_f32 %2, %8, %9\n\tv_cvt_pk_bf16_f32 %3, %10, %11"
        : "=&v"(o0), "=&v"(o1), "=&v"(o2), "=&v"(o3), "=&v"(t0), "=&v"(t1), "=&v"(t2), "=&v"(t3), "=&v"(t4), "=&v"(t5), "=&v"(t6), "=&v"(t7)
        : "v"(x0), "v"(x1), "v"(x2), "v"(x3), "v"(clo), "v"(chi), "v"(c));
    return u32x4{o0, o1, o2, o3};
}
template <int N> __device__ __forceinline__ void waitv3(u32x4& a, u32x4& b, u32x4& c) { asm volatile("s_waitcnt vmcnt(%3)" : "+v"(a), "+v"(b), "+v"(c) : "i"(N) : "memory"); }

constexpr int dn_younger(int k, int D, bool wo) {
    int n = 0, la = 0, lb = 0;
    for (int i = 0; i < D; ++i) { n += 2; if (i == k + 1) lb = n; }
    for (int i = 0; i < D; ++i) { n += 3; if (i == k) la = n; }
    for (int s = 0; s < k; ++s) {
        if (s + D < 24) { n += 2; if (s + D == k + 1) lb = n; }
        if (wo && s >= 16) n += 2;
        if (s + D < 24) { n += 3; if (s + D == k) la = n; }
    }
    return n - (la > lb ? la : lb);
}
constexpr float QSCALE = 0.22360679774997896f * 1.4426950408889634f;
#define BOUND_LIM 50.f
constexpr float BOUND_MAX = BOUND_LIM;

struct AttSync { int lane; const char* kv; unsigned dvoff; unsigned wo; int w; };
__device__ __forceinline__ unsigned kvslot(int c) { return L_KV + ((c + 2) & 3) * CHUNK; }
__device__ __forceinline__ void att_dma(const AttSync& a, int c) { glds16x3_sc1(a.dvoff, a.kv + (size_t)c * CHUNK, kvslot(c) + a.wo); }
constexpr unsigned AP0 = 16;
#define RING_X(c) do { WAITV(3); lpost(C_AP + as.w, AP0 + (c) + 1); } while (0)
#define RP_A
#define RP_B
#define RING_Y(c, pk) do { RP_A lcheck(pk, C_AP, AP0 + (c) + 1, lane); const int cn_ = (c) + 3 < NCHUNK ? (c) + 3 : NCHUNK - 1;   \
                           glds16x3_sc1(as.dvoff, as.kv + (size_t)cn_ * CHUNK, kvslot((c) + 3) + as.wo); RP_B } while (0)

__device__ __forceinline__ void att_fast(const AttSync& as, bf16x8 Q0a, s16x4 Q1a, bf16x8 Q0b, s16x4 Q1b, f32x16& Oa, f32x16& Ob) {
    const int lane = as.lane, r = lane & 31, h = lane >> 5;
    const unsigned ak0 = lane * 16, ak1 = h ? KV_V + 20 * 16 : KV_K1 + r * 16;
    const unsigned av = KV_V + h * 352 + (r <= 20 ? r : 21) * 16;
    const f32x16 zero = {};
    f32x16 Sa, Sb = {};
    bf16x8 K0[2], K1[2], V0[2], V1[2];
    u32x4 Pa0, Pa1, Pb0 = {}, Pb1 = {};
    unsigned pk = 0;
    unsigned long long rp0 = 0, rp1 = 0;
    V0[1] = bf16x8{}; V1[1] = bf16x8{};
    unsigned vk0 = L_KV + ak0, vk1 = L_KV + ak1, vv = L_KV + av;
    PIN(vk0); PIN(vk1); PIN(vv);
    K0[0] = ldsf(vk0 + 2 * CHUNK); K1[0] = ldsf(vk1 + 2 * CHUNK); K0[1] = ldsf(vk0 + 2 * CHUNK + 1024); V1[0] = ldsf(vv + 2 * CHUNK + 704); V0[0] = ldsf(vv + 2 * CHUNK);
    Sa = MFMA(K0[0], Q0a, zero); Sa = MFMA8(half4<0>(K1[0]), Q1a, Sa);
#define EX4(S, i) do { S[i] = __builtin_amdgcn_exp2f(S[i]); S[i + 1] = __builtin_amdgcn_exp2f(S[i + 1]); S[i + 2] = __builtin_amdgcn_exp2f(S[i + 2]); S[i + 3] = __builtin_amdgcn_exp2f(S[i + 3]); } while (0)
#define GAP(MF, LD, T, k, PW, j, S, i) do { MF; SBAR(); LD; PW[j] = pk2(T[k], T[k + 1]); PW[j + 1] = pk2(T[k + 2], T[k + 3]); PIN(PW); SBAR(); EX4(S, i); PIN(S); SBAR(); } while (0)
#define BC(x) __builtin_bit_cast(bf16x8, x)
#define STEP(e, d, KN, L3, LK0, LK1, LV) do { \
        GAP(Ob = MFMA(V0[e ^ 1], BC(Pb0), Ob), (void)0,                  Sb, 12, Pb1, 2, Sa, 0); \
        GAP(Sb = MFMA(K0[e], Q0b, zero),       (void)0,                  Sa, 0, Pa0, 0, Sa, 4); \
        GAP(Sb = MFMA8(half4<e>(K1[d]), Q1b, Sb), L3,                       Sa, 4, Pa0, 2, Sa, 8); \
        GAP(Ob = MFMA(V1[e ^ 1], BC(Pb1), Ob), K0[e] = ldsf(LK0),        Sa, 8, Pa1, 0, Sa, 12); \
        GAP(Oa = MFMA(V0[e], BC(Pa0), Oa),     LK1,                      Sa, 12, Pa1, 2, Sb, 0); \
        GAP(Sa = MFMA(K0[e ^ 1], Q0a, zero),   V1[e ^ 1] = ldsf((LV) + 704), Sb, 0, Pb0, 0, Sb, 4); \
        GAP(Sa = MFMA8(half4<e ^ 1>(K1[KN]), Q1a, Sa), V0[e ^ 1] = ldsf(LV),     Sb, 4, Pb0, 2, Sb, 8); \
        GAP(Oa = MFMA(V1[e], BC(Pa1), Oa),     (void)0,                  Sb, 8, Pb1, 0, Sb, 12); \
    } while (0)
#define CHUNK4(c, B, N) do { \
        STEP(0, 0, 0, (void)0, vk0 + (B) + KVD, K1[1] = ldsf(vk1 + (B) + KVD), vv + (B) + KV_VT); \
        RING_X(c); \
        STEP(1, 0, 1, pk = lpeek(C_AP, lane), vk0 + (B) + KVD + 1024, (void)0, vv + (B) + KVD); \
        RING_Y(c, pk); \
        STEP(0, 1, 1, (void)0, vk0 + (N), K1[0] = ldsf(vk1 + (N)), vv + (B) + KVD + KV_VT); \
        STEP(1, 1, 0, (void)0, vk0 + (N) + 1024, (void)0, vv + (N)); \
    } while (0)
    for (int c = 0; c < NCHUNK; c += 4) {
        CHUNK4(c, 2 * CHUNK, 3 * CHUNK); CHUNK4(c + 1, 3 * CHUNK, 0); CHUNK4(c + 2, 0, CHUNK); CHUNK4(c + 3, CHUNK, 2 * CHUNK);
    }
#undef CHUNK4
    Pb1[2] = pk2(Sb[12], Sb[13]); Pb1[3] = pk2(Sb[14], Sb[15]);
    Ob = MFMA(V0[1], BC(Pb0), Ob); Ob = MFMA(V1[1], BC(Pb1), Ob);
#undef STEP
#undef GAP
#undef EX4
}

__device__ __forceinline__ void att_safe(const AttSync& as, bf16x8 Q0a, s16x4 Q1a, bf16x8 Q0b, s16x4 Q1b, f32x16& Oa, f32x16& Ob) {
    const int lane = as.lane, r = lane & 31, h = lane >> 5;
    const unsigned ak0 = lane * 16, ak1 = h ? KV_V + 20 * 16 : KV_K1 + r * 16;
    const unsigned av = KV_V + h * 352 + (r <= 20 ? r : 21) * 16;
    const f32x16 zero = {};
    float ma = -INFINITY, mb = -INFINITY;
    unsigned long long rp0 = 0, rp1 = 0;
    for (int c = 0; c < NCHUNK; ++c) {
        const unsigned b0 = kvslot(c);
        for (int t = 0; t < 4; ++t) {
            if (t == 1) RING_X(c);
            if (t == 2) RING_Y(c, 0u);
            const unsigned base = b0 + (t >> 1) * KVD; const int e = t & 1;
            bf16x8 K0 = ldsf(base + ak0 + e * 1024), K1 = ldsf(base + ak1), V0 = ldsf(base + av + e * KV_VT), V1 = ldsf(base + av + e * KV_VT + 704);
#pragma unroll
            for (int q = 0; q < 2; ++q) {
                f32x16 S = MFMA(K0, q ? Q0b : Q0a, zero); S = MFMA8(e ? half4<1>(K1) : half4<0>(K1), q ? Q1b : Q1a, S);
                float mx = S[0];
#pragma unroll
                for (int i = 1; i < 16; ++i) mx = fmaxf(mx, S[i]);
                mx = fmaxf(mx, __shfl_xor(mx, 32));
                float& m = q ? mb : ma; f32x16& O = q ? Ob : Oa;
                const float mn = fmaxf(m, mx);
                const float al = __builtin_amdgcn_exp2f(m - mn);
                m = mn;
#pragma unroll
                for (int i = 0; i < 16; ++i) { O[i] *= al; S[i] = __builtin_amdgcn_exp2f(S[i] - mn); }
                O = MFMA(V0, pack8(S, 0, 1.f), O); O = MFMA(V1, pack8(S, 8, 1.f), O);
            }
        }
    }
}

__device__ __forceinline__ void xload8(u32x4 (&x)[5], u32x4 (&y)[5], const char* xb, unsigned vo, unsigned vo2, int skip) {
    asm volatile("s_cmp_lg_u32 %13, 0\n\ts_cbranch_scc1 1f\n\t"
                 "global_load_dwordx4 %0, %10, %12\n\tglobal_load_dwordx4 %1, %10, %12 offset:256\n\tglobal_load_dwordx4 %2, %10, %12 offset:512\n\t"
                 "global_load_dwordx4 %3, %10, %12 offset:768\n\tglobal_load_dwordx4 %4, %10, %12 offset:1024\n\t"
                 "global_load_dwordx4 %5, %11, %12\n\tglobal_load_dwordx4 %6, %11, %12 offset:256\n\tglobal_load_dwordx4 %7, %11, %12 offset:512\n\t"
                 "global_load_dwordx4 %8, %11, %12 offset:768\n\tglobal_load_dwordx4 %9, %11, %12 offset:1024\n1:"
                 : "=&v"(x[0]), "=&v"(x[1]), "=&v"(x[2]), "=&v"(x[3]), "=&v"(x[4]), "=&v"(y[0]), "=&v"(y[1]), "=&v"(y[2]), "=&v"(y[3]), "=&v"(y[4])
                 : "v"(vo), "v"(vo2), "s"(xb), "s"(skip) : "memory", "scc");
}
__device__ __forceinline__ void xload4(u32x4 (&x)[5], const char* xb, unsigned vo) {
    asm volatile("global_load_dwordx4 %0, %5, %6\n\tglobal_load_dwordx4 %1, %5, %6 offset:256\n\tglobal_load_dwordx4 %2, %5, %6 offset:512\n\t"
                 "global_load_dwordx4 %3, %5, %6 offset:768\n\tglobal_load_dwordx4 %4, %5, %6 offset:1024"
                 : "=&v"(x[0]), "=&v"(x[1]), "=&v"(x[2]), "=&v"(x[3]), "=&v"(x[4]) : "v"(vo), "s"(xb) : "memory");
}
__device__ __forceinline__ void ln_rows(const u32x4 (&xr)[5], int row0, int lane) {
    f32x2 xv[20];
    f32x2 s0 = {0.f, 0.f}, s1 = {0.f, 0.f}, q0 = {0.f, 0.f}, q1 = {0.f, 0.f};
#pragma unroll
    for (int i = 0; i < 20; i += 2) {
        const unsigned a = xr[i >> 2][i & 3], b = xr[i >> 2][(i & 3) + 1];
        xv[i] = f32x2{bflo(a), bfhi(a)}; xv[i + 1] = f32x2{bflo(b), bfhi(b)};
        s0 += xv[i]; q0 = __builtin_elementwise_fma(xv[i], xv[i], q0);
        s1 += xv[i + 1]; q1 = __builtin_elementwise_fma(xv[i + 1], xv[i + 1], q1);
    }
    s0 += s1; q0 += q1;
    const float s = dpp_sum16(s0[0] + s0[1]), ss = dpp_sum16(q0[0] + q0[1]);
    const float mean = s * (1.f / 640.f);
    const float var = fmaxf(ss * (1.f / 640.f) - mean * mean, 0.f);
    const float rs = __builtin_amdgcn_rcpf(rbf(__builtin_amdgcn_sqrtf(rbf(rbf(var) + 1e-5f)))), mo = -rbf(mean) * rs;
    const f32x2 rs2 = {rs, rs}, mo2 = {mo, mo};
    const int row = row0 + (lane >> 4), c = lane & 15;
    const unsigned wa = L_PANEL + (row >> 5) * 40960 + (c >> 1) * 1024 + (c & 1) * 512 + (((row & 31) ^ (c & 7)) << 4);
#pragma unroll
    for (int u = 0; u < 5; ++u) {
        u32x4 o;
#pragma unroll
        for (int e = 0; e < 4; ++e) { const f32x2 y = __builtin_elementwise_fma(xv[4 * u + e], rs2, mo2); o[e] = pk2(y[0], y[1]); }
        *LP(u32x4, wa + u * 8192) = o;
    }
}
#define BLOCK_BARRIER() asm volatile("s_waitcnt lgkmcnt(0)\n\ts_barrier" ::: "memory")

template <int PH>
__global__ void __launch_bounds__(512, 2) mega(Params p) {
    constexpr bool PRO = PH & 1, ATT = PH & 2, DN = PH & 8, WO = PH & 16;
    const int w = __builtin_amdgcn_readfirstlane(threadIdx.x >> 6);
    const int b = blockIdx.x, xc = b & 7, j = b >> 3;
    const int ti = (xc >> 1) * 8 + (j >> 2), g = (xc & 1) * 4 + (j & 3);
    constexpr bool FUSED = PH == 27;
    const int kmt = g & 1;
    const int ra = kmt * 32 + (w & 3) * 8, rb = (kmt ^ 1) * 32 + (w & 3) * 8;
    u32x4 x0[5], x1[5], x2[5], x3[5];
    if constexpr (PRO) {
        asm volatile("" :: "s"(p.x), "s"(p.ws), "s"(p.wq), "s"(p.wk));
        const char* xb = p.x + (size_t)ti * 64 * 1280;
        const unsigned xvo = ((threadIdx.x >> 4) & 3) * 1280 + (threadIdx.x & 15) * 16;
        xload8(x0, x1, xb + (size_t)ra * 1280, xvo, xvo + 5120, w >> 2);
        xload8(x2, x3, xb + (size_t)rb * 1280, xvo, xvo + 5120, w >> 2);
    }
    const char* pc; asm volatile("s_getpc_b64 %0" : "=s"(pc));
    unsigned tag = 1u;
    char* ws = p.ws;
    STAMP(0);
    if (threadIdx.x < C_N) *lcnt(threadIdx.x) = (threadIdx.x == C_WP || threadIdx.x == C_WP + 1 || threadIdx.x == C_PP || threadIdx.x == C_PP + 1) ? 100u : 0u;
    const bool haskv = (g >> 2) == (ti & 1);
    const char* wqb = p.wq + g * 160;
#define LANE_CONSTS() int lane = __builtin_amdgcn_mbcnt_hi(~0u, __builtin_amdgcn_mbcnt_lo(~0u, 0u)); PIN(lane); \
    const int r = lane & 31, h = lane >> 5, tq = (lane & 15) >> 2, tp = lane & 3, tb = (lane >> 4) & 1; (void)r; (void)h; (void)tq; (void)tp; (void)tb

    if (w >= 4) {
        LANE_CONSTS();
        const int v = w - 4;
        unsigned epv = 0u;
        if constexpr (FUSED) {
            asm volatile("global_load_dword %0, %1, %2 sc1" : "=v"(epv) : "v"(0u), "s"(p.ws + WS_EP) : "memory");
            constexpr unsigned CODE_PF = 65536;
            const unsigned line = (unsigned)(j * 4 + v) * 64u + lane;
            if (line * 128u < CODE_PF) l2touch(pc + line * 128u);
        }
#define GET_TAG() do { if constexpr (FUSED) { asm volatile("s_nop 0\n\tv_readfirstlane_b32 %0, %1\n\ts_nop 3" : "=s"(tag) : "v"(epv) : "memory"); tag += 0x9E3779B1u; } } while (0)
        __builtin_amdgcn_s_setprio(3);
        if constexpr (PRO) {
            const int kvm = 2 * ti + (g & 1); const bool isV = (g >> 1) & 1;
            const char* wkvb = isV ? p.wv : p.wk;
            if (haskv && v < 2) {
#pragma unroll
                for (int n = 0; n < 13; ++n) {
                    const unsigned o = v * 12800 + (n < 12 ? n * 1024 : 11776);
                    glds16(o + lane * 16, wkvb, L_WKV + o);
                }
            }
            BLOCK_BARRIER();
            if (v >= 2) {
                const int v2 = v - 2;
                unsigned wo5[5];
#pragma unroll
                for (int i = 0; i < 5; ++i) { const unsigned c = (v2 + 2 * i) * 64 + lane, row = c / 10u, col = c - row * 10u; wo5[i] = row * 1280 + col * 16; }
#pragma unroll
                for (int n = 0; n < 20; ++n) glds16(wo5[n % 5], wqb + (n / 5) * 64 * 1280, L_WQ + (v2 + 2 * n) * 1024);
                u32x4 wst[3][10];
                auto wst_issue = [&](auto I) {
                    constexpr int rr = I.value / 10, i = I.value % 10;
                    GLOAD16N(wst[rr][i], wo5[i % 5], wqb + (4 + 2 * rr + i / 5) * 64 * 1280);
                };
                static_for<0, 10>(wst_issue);
                WAITV(10);
                GET_TAG(); lpost(C_TAG, tag);
                lpost(C_PP + v, 1);
                STAMP(2);
                if (haskv) lwait(C_KVFIN, 1);
                static_for<10, 30>(wst_issue);
                static_for<0, 3>([&](auto R) {
                    constexpr int rr = R.value;
                    waitv5<20 - 10 * rr>(wst[rr][0], wst[rr][1], wst[rr][2], wst[rr][3], wst[rr][4]); waitv5<63>(wst[rr][5], wst[rr][6], wst[rr][7], wst[rr][8], wst[rr][9]);
                    lwait_all(C_AP, 4, 3 + rr, lane);
#pragma unroll
                    for (int i = 0; i < 10; ++i) *LP(u32x4, L_WQ + (rr & 1) * 20480 + (v2 + 2 * i) * 1024 + lane * 16) = wst[rr][i];
                    lpost(C_WP + v, rr + 1);
                });
            } else {
              if (haskv) {
                f32x16 acc = {};
                const unsigned wa = L_WKV + (v * 320 + 8 * h + tq) * 40 + tb * 32 + (tb ? 0 : tp * 8);
                unsigned xa[4];
#pragma unroll
                for (int s = 0; s < 4; ++s) xa[s] = L_PANEL + kmt * 40960 + v * 20480 + h * 512 + ((r ^ ((2 * s + h) & 7)) << 4);
                WAITV(0);
                GET_TAG();
                STAMP(2);
                bf16x8 wf[20], xf[20];
#pragma unroll
                for (int s = 0; s < 20; ++s) wf[s] = ldstr2(wa + s * 640, wa + s * 640 + 160);
                lwait_all(C_AP, 4, 1, lane);
#pragma unroll
                for (int s = 0; s < 20; ++s) xf[s] = ldsf(xa[s & 3] + s * 1024);
                SBAR();
                if (isV) {
#pragma unroll
                    for (int s = 0; s < 20; ++s) acc = MFMA(xf[s], wf[s], acc);
                } else {
#pragma unroll
                    for (int s = 0; s < 20; ++s) acc = MFMA(wf[s], xf[s], acc);
                }
                if (v) {
#pragma unroll
                    for (int e = 0; e < 4; ++e) *LP(f32x4, L_WKV + 12800 + e * 1024 + lane * 16) = f32x4{acc[4 * e], acc[4 * e + 1], acc[4 * e + 2], acc[4 * e + 3]};
                    lpost(C_KVP, 1);
                } else {
                    lwait(C_KVP, 1);
#pragma unroll
                    for (int e = 0; e < 4; ++e) {
                        const f32x4 t = *LP(const f32x4, L_WKV + 12800 + e * 1024 + lane * 16);
                        acc[4 * e] += t[0]; acc[4 * e + 1] += t[1]; acc[4 * e + 2] += t[2]; acc[4 * e + 3] += t[3];
                    }
                    const __amdgpu_buffer_rsrc_t rk = mkrsrc(ws + WS_KV, 32 * KVD);
                    const unsigned base = ti * KVD, ke = g & 1;
                    const u32x4 f0 = __builtin_bit_cast(u32x4, pack8(acc, 0, 1.f)), f1 = __builtin_bit_cast(u32x4, pack8(acc, 8, 1.f));
                    float mx;
                    if (!isV) {
                        __builtin_amdgcn_raw_buffer_store_b128(f0, rk, base + ke * 1024 + lane * 16, 0, 16);
                        const u32x2 k1 = {f1[0], f1[1]};
                        if (!h) __builtin_amdgcn_raw_buffer_store_b64(k1, rk, base + KV_K1 + r * 16 + ke * 8, 0, 16);
                        lpost(C_KVFIN, 1);
                        float n2 = 0.f;
#pragma unroll
                        for (int e = 0; e < 4; ++e) n2 += bflo(f0[e]) * bflo(f0[e]) + bfhi(f0[e]) * bfhi(f0[e]);
                        if (!h) n2 += bflo(f1[0]) * bflo(f1[0]) + bfhi(f1[0]) * bfhi(f1[0]) + bflo(f1[1]) * bflo(f1[1]) + bfhi(f1[1]) * bfhi(f1[1]);
                        n2 += __shfl_xor(n2, 32);
                        mx = (n2 == n2) ? n2 : INFINITY;
                    } else {
                        const unsigned cst = r == 20 ? 0x3f803f80u : 0u;
                        const u32x4 c4 = {cst, cst, cst, cst};
                        if (r <= 21) {
                            __builtin_amdgcn_raw_buffer_store_b128(r >= 20 ? c4 : f0, rk, base + KV_V + ke * KV_VT + h * 352 + r * 16, 0, 16);
                            __builtin_amdgcn_raw_buffer_store_b128(r >= 20 ? c4 : f1, rk, base + KV_V + ke * KV_VT + 704 + h * 352 + r * 16, 0, 16);
                        }
                        lpost(C_KVFIN, 1);
                        float vm = 0.f;
#pragma unroll
                        for (int e = 0; e < 16; ++e) { const float a = fabsf(acc[e]); vm = (a <= vm) ? vm : ((a == a) ? a : INFINITY); }
                        mx = r < 20 ? vm : 0.f;
                    }
                    mx = wave_max(mx);
                    WAITV(0);
                    STAMP(15);
                    if (lane < 8) __hip_atomic_store((gu64*)(ws + WS_KF + lane * 1024 + (isV ? 512 : 0)) + kvm, ((unsigned long long)tag << 32) | __float_as_uint(mx), RLX_AGENT);
                }
            }
              else { WAITV(0); GET_TAG(); }
              STAMP(3);
            }
            if (haskv) lwait(C_KVFIN, 1);
#pragma unroll
            for (int n = 0; n < 2; ++n) {
                unsigned L = n * 64 + lane; L = (j * 4 + v) * 120 + (L < 120u ? L : 119u);
                const unsigned m3 = L >= 7680u, rem = L - (m3 ? 7680u : 0u), row = rem / 12u;
                l2touch((m3 ? p.w3 : p.w1) + row * 3072 + (xc & 1) * 1536 + (rem - row * 12u) * 128);
            }
            lwait_all(C_AP, 4, GU_DELAY, lane);
            STAMP(4);
            {
                const char* w1s = p.w1 + g * 384 + v * 96; const char* w3s = p.w3 + g * 384 + v * 96;
                auto goff = [](unsigned idx) { const unsigned row = idx / 6u; return row * 3072 + (idx - row * 6u) * 16; };
                const unsigned gva = goff(lane), gvb = goff((lane - 8) & 63), gvc = goff(64 + (h ? (r - 8) & 31 : r));
                const unsigned gdst = L_GU + v * 9216;
                auto gdma = [&](auto S) { constexpr int s = S.value; glds_gu(gva, gvb, gvc, w1s + (size_t)s * 16 * 3072, w3s + (size_t)s * 16 * 3072, gdst + (s % 3) * 3072); };
                static_for<0, 3>(gdma);
                f32x16 acc[2][3] = {};
                unsigned bn[3][2], pa[4], pb[4];
#pragma unroll
                for (int n = 0; n < 3; ++n)
#pragma unroll
                    for (int t = 0; t < 2; ++t) {
                        const unsigned idx = 6 * (8 * h + 4 * t + tq) + 2 * n + (tp >> 1);
                        const unsigned o = idx < 64u ? (tb ? 1024 + ((idx + 8) & 63) * 16 : idx * 16) : (tb ? 2560 + ((idx - 56) & 31) * 16 : 2048 + (idx - 64) * 16);
                        bn[n][t] = gdst + o + (tp & 1) * 8;
                    }
#pragma unroll
                for (int s = 0; s < 4; ++s) { pa[s] = L_PANEL + h * 512 + ((r ^ ((2 * s + h) & 7)) << 4); pb[s] = pa[s] + 40960; PIN(pb[s]); }
                bf16x8 wf[2][3], xa[2], xb2[2];
                auto rdw = [&](auto S) { constexpr int s = S.value;
#pragma unroll
                    for (int n = 0; n < 3; ++n) wf[s & 1][n] = ldstr2(bn[n][0] + (s % 3) * 3072, bn[n][1] + (s % 3) * 3072); };
                auto rdx = [&](auto S) { constexpr int s = S.value; xa[s & 1] = ldsf(pa[s & 3] + s * 1024); xb2[s & 1] = ldsf(pb[s & 3] + s * 1024); };
                WAITV(8);
                rdx(IC<0>{}); rdw(IC<0>{});
                asm volatile("s_waitcnt lgkmcnt(0)" ::: "memory");
                gdma(IC<3>{});
                static_for<0, 40>([&](auto S) {
                    constexpr int s = S.value; constexpr bool nx = s + 1 < 40;
                    MPRIO(GU_HI, 0); acc[0][0] = MFMA(wf[s & 1][0], xa[s & 1], acc[0][0]); SBAR();
                    if constexpr (nx) rdx(IC<s + 1>{});
                    SBAR();
                    MPRIO(GU_HI, 1); acc[1][0] = MFMA(wf[s & 1][0], xb2[s & 1], acc[1][0]); SBAR();
                    if constexpr (nx) { WAITVN(4 * ((s + 3 < 39 ? s + 3 : 39) - (s + 1))); rdw(IC<s + 1>{}); }
                    SBAR();
                    MPRIO(GU_HI, 2); acc[0][1] = MFMA(wf[s & 1][1], xa[s & 1], acc[0][1]); SBAR();
                    MPRIO(GU_HI, 3); acc[1][1] = MFMA(wf[s & 1][1], xb2[s & 1], acc[1][1]); SBAR();
                    if constexpr (s + 4 < 40) { asm volatile("s_waitcnt lgkmcnt(0)" ::: "memory"); gdma(IC<s + 4>{}); }
                    SBAR();
                    MPRIO(GU_HI, 4); acc[0][2] = MFMA(wf[s & 1][2], xa[s & 1], acc[0][2]); SBAR();
                    MPRIO(GU_HI, 5); acc[1][2] = MFMA(wf[s & 1][2], xb2[s & 1], acc[1][2]); SBAR();
                });
                lpost(C_GP + v, 42);
                __builtin_amdgcn_s_setprio(3);
                STAMP(6);
                const __amdgpu_buffer_rsrc_t rh = mkrsrc(ws + WS_HT, 64 * 96 * 1024);
#pragma unroll
                for (int m = 0; m < 2; ++m)
#pragma unroll
                    for (int n = 0; n < 3; ++n) {
                        f32x16 hv;
#pragma unroll
                        for (int e = 0; e < 8; ++e) {
                            const float gg = acc[m][n][e];
                            hv[e] = gg * __builtin_amdgcn_rcpf(1.f + __builtin_amdgcn_exp2f(-1.4426950408889634f * gg)) * acc[m][n][e + 8];
                        }
                        __builtin_amdgcn_raw_buffer_store_b128(__builtin_bit_cast(u32x4, pack8(hv, 0, 1.f)), rh, ((2 * ti + m) * 96 + g * 12 + 3 * v + n) * 1024 + lane * 16, 0, 16);
                    }
                if constexpr (DN) {
                    WAITV(0);
                    if (lane < 2) __hip_atomic_store((gu32*)(ws + WS_HF) + (2 * ti + lane) * 32 + g * 4 + v, tag, RLX_AGENT);
                }
                __builtin_amdgcn_s_setprio(0);
                if constexpr (DN) {
#pragma unroll
                    for (int n = 0; n < 2; ++n) {
                        unsigned L = (j * 4 + v) * 85 + n * 64 + lane; L = L < 10880u ? L : 10879u;
                        const unsigned row = L / 5u;
                        l2touch((row < 1536u ? p.w2 + row * 1280 : p.wo + (row - 1536u) * 1280) + (xc & 1) * 640 + (L - row * 5u) * 128);
                    }
                }
                STAMP(7);
            }
        } else {
            BLOCK_BARRIER();
        }
        if constexpr (DN || WO) {
            LANE_CONSTS();
            const unsigned bq = ((tb * 32 + (tp >> 1) * 16) ^ ((unsigned)(tq >> 1) << 6)) + (tp & 1) * 8;
            const unsigned drow = lane >> 3, dcol = ((lane & 7) ^ (((lane >> 4) & 1) << 2)) * 16;
            const int G = xc >> 1, r0 = G < 2 ? 6 * G : 12 + 5 * (G - 2), nr = G < 2 ? 6 : 5;
            if constexpr (FUSED) {
                if (b == 255 && v == 0) {
                    gu32* f = (gu32*)(ws + WS_HF) + (lane >> 3) * 64 + (lane & 7) * 4;
                    _Pragma("clang loop unroll(disable)") for (unsigned spins = 0; spins < (1u << 14); ++spins) {
                        bool ok = true;
#pragma unroll
                        for (int n = 0; n < 4; ++n) ok &= __hip_atomic_load(f + n * 512, RLX_AGENT) == tag;
                        if (__all(ok)) break;
                        TMO(spins, 1u << 14, 303, tag);
                        __builtin_amdgcn_s_sleep(32);
                    }
                    if (lane == 0) __hip_atomic_store((gu32*)(ws + WS_EP), tag, RLX_AGENT);
                }
            }
            if (j < 5 * nr) {
                const int dr = r0 + j / 5, dc = (xc & 1) * 5 + j % 5;
                const int mfirst = dr < 20 ? 3 * dr : 60 + 2 * (dr - 20), nm = dr < 20 ? 3 : 2;
                const unsigned dvo = drow * 1280 + dcol;
                const unsigned myring = L_PANEL + v * 20480;
                f32x16 acc0 = {}, acc1 = {};
                if constexpr (PRO) { lwait_all(C_GP, 4, 42, lane); lwait_all(C_AP, 4, 8, lane); }
                if constexpr (DN) {
                    f32x16 da[3][2] = {};
                    const int drot = (j % 5) * 4;
                    const char* bsrc = p.w2 + (size_t)v * 384 * 1280 + dc * 128;
                    const char* asrc = ws + WS_HT + ((size_t)mfirst * 96 + v * 24) * 1024;
                    const unsigned am1 = lane * 16 + 98304, am2 = lane * 16 + (nm == 3 ? 196608 : 98304);
                    constexpr int DQ = DN_DQ;
                    u32x4 aq[DQ][3];
                    auto bissue = [&](auto K) {
                        constexpr int k = K.value;
                        int kr = k + drot; kr = kr >= 24 ? kr - 24 : kr;
                        const char* sb = bsrc + (size_t)kr * 16 * 1280;
                        glds16(dvo, sb, myring + (k % 10) * 2048); glds16(dvo + 8 * 1280, sb, myring + (k % 10) * 2048 + 1024);
                    };
                    auto woissue = [&](auto U) {
                        constexpr int u = U.value;
                        const char* sb = p.wo + (size_t)(v * 160 + u * 16) * 1280 + dc * 128;
                        glds16(dvo, sb, myring + u * 2048); glds16(dvo + 8 * 1280, sb, myring + u * 2048 + 1024);
                    };
                    auto aissue = [&](auto K) {
                        constexpr int k = K.value;
                        int kr = k + drot; kr = kr >= 24 ? kr - 24 : kr;
                        const char* sb = asrc + (size_t)kr * 1024;
                        GLOAD16S(aq[k % DQ][0], lane * 16, sb); GLOAD16S(aq[k % DQ][1], am1, sb); GLOAD16S(aq[k % DQ][2], am2, sb);
                    };
                    static_for<0, DQ>(bissue);
                    if constexpr (PRO) {
                        gu32* f = (gu32*)(ws + WS_HF) + (mfirst + ((lane >> 3) < nm ? (lane >> 3) : 0)) * 32 + 8 * v + (lane & 7);
                        _Pragma("clang loop unroll(disable)") for (unsigned spins = 0; spins < (1u << 14); ++spins) {
                            if (__all(__hip_atomic_load(f, RLX_AGENT) == tag)) break;
                            TMO(spins, 1u << 14, 302, tag);
                            __builtin_amdgcn_s_sleep(HF_SLEEP);
                        }
                        CFENCE();
                    }
                    STAMP(8);
                    static_for<0, DQ>(aissue);
                    const unsigned ba0 = myring + (4 * h + tq) * 128 + bq, ba1 = ba0 ^ 64;
                    bf16x8 w0[2], w1[2];
                    WAITVN(3 * DQ);
                    w0[0] = ldstr2(ba0, ba0 + 1024); w1[0] = ldstr2(ba1, ba1 + 1024);
                    static_for<0, 24>([&](auto K) {
                        constexpr int k = K.value;
                        waitv3<dn_younger(k, DQ, WO)>(aq[k % DQ][0], aq[k % DQ][1], aq[k % DQ][2]);
                        constexpr unsigned o = ((k + 1) % 10) * 2048;
#define DXF(m) __builtin_bit_cast(bf16x8, aq[k % DQ][m])
                        SBAR();
                        MPRIO(DN_HI, 0); da[0][0] = MFMA(w0[k & 1], DXF(0), da[0][0]); SBAR();
                        if constexpr (k + 1 < 24) w0[(k + 1) & 1] = ldstr2(ba0 + o, ba0 + o + 1024);
                        SBAR();
                        MPRIO(DN_HI, 1); da[0][1] = MFMA(w1[k & 1], DXF(0), da[0][1]); SBAR();
                        if constexpr (k + 1 < 24) w1[(k + 1) & 1] = ldstr2(ba1 + o, ba1 + o + 1024);
                        SBAR();
                        MPRIO(DN_HI, 2); da[1][0] = MFMA(w0[k & 1], DXF(1), da[1][0]); SBAR();
                        if constexpr (k + DQ < 24) bissue(IC<k + DQ>{});
                        if constexpr (WO && k >= 16) woissue(IC<(k + 8) % 10>{});
                        SBAR();
                        MPRIO(DN_HI, 3); da[1][1] = MFMA(w1[k & 1], DXF(1), da[1][1]); SBAR();
                        MPRIO(DN_HI, 4); da[2][0] = MFMA(w0[k & 1], DXF(2), da[2][0]); SBAR();
                        MPRIO(DN_HI, 5); da[2][1] = MFMA(w1[k & 1], DXF(2), da[2][1]); SBAR();
                        if constexpr (k + DQ < 24) aissue(IC<k + DQ>{});
                    });
                    __builtin_amdgcn_s_setprio(3);
                    STAMP(9);
                    if constexpr (WO) { CFENCE(); woissue(IC<2>{}); woissue(IC<3>{}); }
#pragma unroll
                    for (int n = 0; n < 2; ++n) {
#pragma unroll
                        for (int m = 0; m < 3; ++m) {
                            if (m != v) {
                                const unsigned dst = L_GU + (m * 3 + (v > m ? v - 1 : v)) * 4096 + lane * 16;
#pragma unroll
                                for (int e = 0; e < 4; ++e) *LP(f32x4, dst + e * 1024) = f32x4{da[m][n][4 * e], da[m][n][4 * e + 1], da[m][n][4 * e + 2], da[m][n][4 * e + 3]};
                            }
                        }
                        lpost(C_DP + v, 2 * n + 1); lwait_all(C_DP, 4, 2 * n + 1, lane);
                        if (v < 3) {
                            f32x16 t = v == 0 ? da[0][n] : (v == 1 ? da[1][n] : da[2][n]);
#pragma unroll
                            for (int o = 0; o < 3; ++o)
#pragma unroll
                                for (int e = 0; e < 4; ++e) {
                                    const f32x4 u = *LP(const f32x4, L_GU + (v * 3 + o) * 4096 + e * 1024 + lane * 16);
                                    t[4 * e] += u[0]; t[4 * e + 1] += u[1]; t[4 * e + 2] += u[2]; t[4 * e + 3] += u[3];
                                }
                            if (n) acc1 = t; else acc0 = t;
                        }
                        if (n == 0) { lpost(C_DP + v, 2); lwait_all(C_DP, 4, 2, lane); }
                    }
                    if constexpr (!WO) {
                        if (v < 3) {
                            float* pt = (float*)(ws + WS_PT) + (size_t)(b * 3 + v) * 2048 + lane;
#pragma unroll
                            for (int e = 0; e < 16; ++e) { pt[e * 64] = acc0[e]; pt[1024 + e * 64] = acc1[e]; }
                        }
                    }
                } else {
#pragma unroll
                    for (int u = 0; u < 20; ++u) glds16(dvo + u * 8 * 1280, p.wo + (size_t)v * 160 * 1280 + dc * 128, myring + u * 1024);
                    if (v < 3) {
                        const float* pt = (const float*)(ws + WS_PT) + (size_t)(b * 3 + v) * 2048 + lane;
#pragma unroll
                        for (int e = 0; e < 16; ++e) { acc0[e] = pt[e * 64]; acc1[e] = pt[1024 + e * 64]; }
                    }
                }
                if constexpr (WO) { WAITV(0); lpost(C_DP + v, 4); }
                if constexpr (WO && ATT) {
                    if (v == 3) {
                        __builtin_amdgcn_s_setprio(0);
                        const int mi = (lane >> 4) < nm ? (lane >> 4) : 0;
                        gu64* f = (gu64*)(ws + WS_OF + (mfirst + mi) * 128) + (lane & 15);
                        _Pragma("clang loop unroll(disable)") for (unsigned spins = 0; spins < (1u << 14); ++spins) {
                            const unsigned long long x = __hip_atomic_load(f, RLX_AGENT);
                            const unsigned long long ok = __ballot((unsigned)x == tag && (unsigned)(x >> 32) == tag);
                            const unsigned mine = (unsigned)(ok >> ((lane & 3) * 16));
                            *((volatile AS3 unsigned*)lcnt(C_OS) + (lane & 3)) = ((mine & 0xffu) == 0xffu ? 1u : 0u) | ((mine & 0xff00u) == 0xff00u ? 2u : 0u);
                            if (ok == ~0ull) break;
                            TMO(spins, 1u << 14, 300, tag);
                            __builtin_amdgcn_s_sleep(OF_SLEEP);
                        }
                    }
                }
                if (v < nm) {
                    const int m = mfirst + v;
                    const unsigned ba0 = L_PANEL + (4 * h + tq) * 128 + bq, ba1 = ba0 ^ 64;
                    if constexpr (WO) {
                        const char* xrow = p.x + (size_t)(m * 32 + r) * 1280 + dc * 128 + h * 16;
                        u32x4 xres[4];
#pragma unroll
                        for (int e = 0; e < 4; ++e) xres[e] = *(const u32x4*)(xrow + e * 32);
                        lwait_all(C_DP, 4, 4, lane);
                        const __amdgpu_buffer_rsrc_t ro = mkrsrc(ws + WS_OT + (size_t)m * 40960, 40960);
                        int hf = 0;
                        f32x16 t0[2] = {}, t1[2] = {};
#pragma unroll
                        for (int it = 0; it < 2; ++it) {
                            if constexpr (ATT) {
                                _Pragma("clang loop unroll(disable)") for (unsigned spins = 0; spins < (1u << 17); ++spins) {
                                    const unsigned ok = __builtin_amdgcn_readfirstlane(*((volatile AS3 unsigned*)lcnt(C_OS) + v));
                                    if (it == 0) { if (ok) { hf = (ok & 1) ? 0 : 1; break; } }
                                    else if (ok == 3) break;
                                    __builtin_amdgcn_s_sleep(1);
                                }
                                CFENCE();
                            }
                            if (it == 0) STAMP(10);
                            u32x4 of[20];
#pragma unroll
                            for (int s = 0; s < 20; ++s) of[s] = __builtin_amdgcn_raw_buffer_load_b128(ro, lane * 16 + s * 1024, hf * 20480, 16);
                            const unsigned a0 = ba0 + hf * 40960, a1 = ba1 + hf * 40960, l0 = a0 + (36 * h + 16) * 128, l1 = a1 + (36 * h + 16) * 128;
                            bf16x8 w0[2], w1[2];
                            w0[0] = ldstr2(a0, a0 + 1024); w1[0] = ldstr2(a1, a1 + 1024);
                            static_for<0, 20>([&](auto S) {
                                constexpr int s = S.value, n = s + 1, gg = n / 5, jj = n % 5;
                                const bf16x8 xf = __builtin_bit_cast(bf16x8, of[s]);
                                if constexpr (n < 20) {
                                    if constexpr (jj < 4) {
                                        constexpr unsigned o = (80 * gg + 20 * jj) * 128;
                                        w0[n & 1] = ldstr2(a0 + o, a0 + o + 1024); w1[n & 1] = ldstr2(a1 + o, a1 + o + 1024);
                                    } else {
                                        constexpr unsigned o = 80 * gg * 128;
                                        w0[n & 1] = ldstr2(l0 + o, l0 + o + 2560); w1[n & 1] = ldstr2(l1 + o, l1 + o + 2560);
                                    }
                                }
                                SBAR();
                                t0[it] = MFMA(w0[s & 1], xf, t0[it]); t1[it] = MFMA(w1[s & 1], xf, t1[it]);
                                SBAR();
                            });
                            hf ^= 1;
                        }
                        acc0 += t0[0] + t0[1]; acc1 += t1[0] + t1[1];
                        char* orow = p.out + (size_t)(m * 32 + r) * 1280 + dc * 128 + h * 16;
#pragma unroll
                        for (int n = 0; n < 2; ++n) {
                            const f32x16& a = n ? acc1 : acc0;
#pragma unroll
                            for (int qq = 0; qq < 2; ++qq) {
                                float lo[4], hi[4];
#pragma unroll
                                for (int e = 0; e < 4; ++e) {
                                    auto sw = __builtin_amdgcn_permlane32_swap(__float_as_uint(a[8 * qq + e]), __float_as_uint(a[8 * qq + 4 + e]), false, false);
                                    lo[e] = __uint_as_float(sw[0]); hi[e] = __uint_as_float(sw[1]);
                                }
                                const u32x4 xv = xres[2 * n + qq];
                                const u32x4 ov = {pk2(lo[0] + bflo(xv[0]), lo[1] + bfhi(xv[0])), pk2(lo[2] + bflo(xv[1]), lo[3] + bfhi(xv[1])),
                                                  pk2(hi[0] + bflo(xv[2]), hi[1] + bfhi(xv[2])), pk2(hi[2] + bflo(xv[3]), hi[3] + bfhi(xv[3]))};
                                *(u32x4*)(orow + (2 * n + qq) * 32) = ov;
                            }
                        }
                        STAMP(11);
                    }
                }
            }
        }
    } else {
        LANE_CONSTS();
        bf16x8 Q0a, Q0b; u32x2 q1a, q1b;
        const int head = 4 * g + w;
        const AttSync as{lane, ws + WS_KV, (unsigned)((w < 3 ? w * 3072 : CHUNK - 3072) + lane * 16), (unsigned)(w < 3 ? w * 3072 : CHUNK - 3072), w};
        __builtin_amdgcn_s_setprio(3);
        unsigned long long fk = 0, fv = 0; bool early = false;
        if constexpr (PRO) {
            BLOCK_BARRIER();
            waitv5<15>(x0[0], x0[1], x0[2], x0[3], x0[4]);
            STAMP(1);
            ln_rows(x0, ra, lane);
            waitv5<10>(x1[0], x1[1], x1[2], x1[3], x1[4]);
            ln_rows(x1, ra + 4, lane);
            lpost(C_AP + w, 1);
            waitv5<5>(x2[0], x2[1], x2[2], x2[3], x2[4]);
            ln_rows(x2, rb, lane);
            waitv5<0>(x3[0], x3[1], x3[2], x3[3], x3[4]);
            ln_rows(x3, rb + 4, lane);
            lpost(C_AP + w, 2);
            unsigned pa[4];
#pragma unroll
            for (int s = 0; s < 4; ++s) pa[s] = L_PANEL + h * 512 + ((r ^ ((2 * s + h) & 7)) << 4);
            f32x16 qa = {}, qb = {};
            const unsigned wa = L_WQ + (8 * h + tq) * 160 + w * 40 + tb * 32 + (tb ? 0 : tp * 8);
            bf16x8 fw[3][4], fa[2][4], fb[2][4];
            auto rdw = [&](auto S, auto S4) { constexpr int st = S.value, s4 = S4.value; const unsigned o = wa + (st & 3) * 10240 + s4 * 2560; fw[st % 3][s4] = ldstr2(o, o + 640); };
            lwait_all(C_PP, 4, 1, lane);
            static_for<0, 4>([&](auto I) { rdw(IC<0>{}, I); rdw(IC<1>{}, I); });
            if constexpr (FUSED) tag = __builtin_amdgcn_readfirstlane(*(volatile AS3 unsigned*)lcnt(C_TAG));
            lpost(C_AP + w, 3);
            lwait_all(C_AP, 4, 2, lane);
            STAMP(2);
#pragma unroll
            for (int s4 = 0; s4 < 4; ++s4) { fa[0][s4] = ldsf(pa[s4] + s4 * 1024); fb[0][s4] = ldsf(pa[s4] + 40960 + s4 * 1024); }
            unsigned pk = 0;
            static_for<0, 10>([&](auto S) {
                constexpr int st = S.value, nx = st + 1;
                if constexpr (st == 3 || st == 5 || st == 7) lcheck(pk, C_WP, (st - 1) / 2, lane);
                if constexpr (st == KV_PEEK && ATT) gload8x2_sc1(fk, fv, lane * 8, ws + WS_KF + xc * 1024);
                if constexpr (st == KV_CHECK && ATT) {
                    waitv8x2(fk, fv);
                    early = __all((unsigned)(fk >> 32) == tag && (unsigned)(fv >> 32) == tag);
                    if (early) { lwait_all(C_AP, 4, 6, lane); att_dma(as, 0); att_dma(as, 1); }
                    STAMP(8);
                }
                static_for<0, 4>([&](auto S4) {
                    constexpr int s4 = S4.value;
                    qa = MFMA(fw[st % 3][s4], fa[st & 1][s4], qa); SBAR();
                    if constexpr (st == 0) rdw(IC<2>{}, S4);
                    if constexpr (st == 1) rdw(IC<3>{}, S4);
                    if constexpr (st == 3 || st == 5 || st == 7) { rdw(IC<st + 1>{}, S4); rdw(IC<st + 2>{}, S4); }
                    SBAR();
                    qb = MFMA(fw[st % 3][s4], fb[st & 1][s4], qb); SBAR();
                    if constexpr (nx < 10) { fa[nx & 1][s4] = ldsf(pa[s4] + (nx * 4 + s4) * 1024); fb[nx & 1][s4] = ldsf(pa[s4] + 40960 + (nx * 4 + s4) * 1024); SBAR(); }
                    if constexpr (s4 == 2 && (st == 2 || st == 4 || st == 6)) { pk = lpeek(C_WP, lane); SBAR(); }
                });
                if constexpr (st == 1 || st == 3 || st == 5) lpost(C_AP + w, 4 + st / 2);
            });
            lpost(C_AP + w, 8);
            Q0a = pack8(qa, 0, QSCALE); Q0b = pack8(qb, 0, QSCALE);
            q1a = u32x2{pk2(qa[8] * QSCALE, qa[9] * QSCALE), pk2(qa[10] * QSCALE, qa[11] * QSCALE)};
            q1b = u32x2{pk2(qb[8] * QSCALE, qb[9] * QSCALE), pk2(qb[10] * QSCALE, qb[11] * QSCALE)};
            if (h) { q1a = u32x2{0u, 0u}; q1b = u32x2{0u, 0u}; }
            if constexpr (!ATT) {
                char* qt = ws + WS_QT + (size_t)((2 * ti) * 32 + head) * 2048 + lane * 16;
                *(bf16x8*)qt = Q0a; *(u32x2*)(qt + 1024) = q1a; *(bf16x8*)(qt + 65536) = Q0b; *(u32x2*)(qt + 65536 + 1024) = q1b;
            }
            STAMP(3);
        } else {
            BLOCK_BARRIER();
            if constexpr (ATT) {
                const char* qt = ws + WS_QT + (size_t)((2 * ti) * 32 + head) * 2048 + lane * 16;
                Q0a = *(const bf16x8*)qt; q1a = *(const u32x2*)(qt + 1024); Q0b = *(const bf16x8*)(qt + 65536); q1b = *(const u32x2*)(qt + 65536 + 1024);
            }
        }
        if constexpr (ATT) {
            float na = 0.f, nb = 0.f;
            {
                const u32x4 a0 = __builtin_bit_cast(u32x4, Q0a), b0 = __builtin_bit_cast(u32x4, Q0b);
#pragma unroll
                for (int e = 0; e < 4; ++e) { na += bflo(a0[e]) * bflo(a0[e]) + bfhi(a0[e]) * bfhi(a0[e]); nb += bflo(b0[e]) * bflo(b0[e]) + bfhi(b0[e]) * bfhi(b0[e]); }
#pragma unroll
                for (int e = 0; e < 2; ++e) { na += bflo(q1a[e]) * bflo(q1a[e]) + bfhi(q1a[e]) * bfhi(q1a[e]); nb += bflo(q1b[e]) * bflo(q1b[e]) + bfhi(q1b[e]) * bfhi(q1b[e]); }
                const auto sa = __builtin_amdgcn_permlane32_swap(__float_as_uint(na), __float_as_uint(na), false, false), sb = __builtin_amdgcn_permlane32_swap(__float_as_uint(nb), __float_as_uint(nb), false, false);
                na = __uint_as_float(sa[0]) + __uint_as_float(sa[1]); nb = __uint_as_float(sb[0]) + __uint_as_float(sb[1]);
            }
            if (!early) {
                gu64* f = (gu64*)(ws + WS_KF + xc * 1024) + lane;
                _Pragma("clang loop unroll(disable)") for (unsigned spins = 0; spins < (1u << 14); ++spins) {
                    fk = __hip_atomic_load(f, RLX_AGENT); fv = __hip_atomic_load(f + 64, RLX_AGENT);
                    if (__all((unsigned)(fk >> 32) == tag && (unsigned)(fv >> 32) == tag)) break;
                    TMO(spins, 1u << 14, 301, tag);
                    __builtin_amdgcn_s_sleep(4);
                }
                if constexpr (PRO) lwait_all(C_AP, 4, 6, lane);
                att_dma(as, 0); att_dma(as, 1);
            }
            STAMP(4);
            const float kn2 = wave_max(__uint_as_float((unsigned)fk)), vmx = wave_max(__uint_as_float((unsigned)fv));
            const float Ma = __builtin_amdgcn_sqrtf(na * kn2) * 1.01f, Mb = __builtin_amdgcn_sqrtf(nb * kn2) * 1.01f;
            const bool fast = __all(Ma <= BOUND_MAX && Mb <= BOUND_MAX && vmx <= 1e30f);
            const u32x2 a1 = {(fast && h) ? pk2(-Ma, 0.f) : q1a[0], q1a[1]}, b1 = {(fast && h) ? pk2(-Mb, 0.f) : q1b[0], q1b[1]};
            f32x16 Oa = {}, Ob = {};
            WAITV(3); lpost(C_AP + w, AP0); lwait_all(C_AP, 4, AP0, lane);
            att_dma(as, 2);
            __builtin_amdgcn_s_setprio(ATT_PRIO);
            STAMP(5); STAMPC(12);
            if (fast) att_fast(as, Q0a, __builtin_bit_cast(s16x4, a1), Q0b, __builtin_bit_cast(s16x4, b1), Oa, Ob);
            else att_safe(as, Q0a, __builtin_bit_cast(s16x4, a1), Q0b, __builtin_bit_cast(s16x4, b1), Oa, Ob);
            STAMP(6); STAMPC(13);
            const __amdgpu_buffer_rsrc_t ro = mkrsrc(ws + WS_OT, 64 * 40 * 1024);
#pragma unroll
            for (int q = 0; q < 2; ++q) {
                const f32x16& O = q ? Ob : Oa;
                const auto lsw = __builtin_amdgcn_permlane32_swap(__float_as_uint(O[8]), __float_as_uint(O[8]), false, false);
                const float inv = __builtin_amdgcn_rcpf(__uint_as_float(lsw[1]));
                const unsigned base = ((2 * ti + q) * 40 + 5 * g) * 1024;
                __builtin_amdgcn_raw_buffer_store_b128(__builtin_bit_cast(u32x4, pack8(O, 0, inv)), ro, base + w * 1024 + lane * 16, 0, 16);
                if (!h) {
                    u32x2 t = {pk2(O[8] * inv, O[9] * inv), pk2(O[10] * inv, O[11] * inv)};
                    __builtin_amdgcn_raw_buffer_store_b64(t, ro, base + 4096 + ((w >> 1) * 32 + r) * 16 + (w & 1) * 8, 0, 16);
                }
            }
            if constexpr (WO) {
                WAITV(0);
                if (lane < 2) __hip_atomic_store((gu32*)(ws + WS_OF) + (2 * ti + lane) * 32 + head, tag, RLX_AGENT);
            }
            STAMP(7);
        }
    }
}

template <int PH>
static void launch(const Params& p, hipStream_t stream) {
    (void)hipFuncSetAttribute((const void*)mega<PH>, hipFuncAttributeMaxDynamicSharedMemorySize, L_TOTAL);
    mega<PH><<<256, 512, L_TOTAL, stream>>>(p);
}

extern "C" void kernel_launch(void* const* d_in, const int* in_sizes, int n_in, void* d_out, int out_size, void* d_ws, size_t ws_size, hipStream_t stream) {
    Params p;
    p.x = (const char*)d_in[0]; p.wq = (const char*)d_in[1]; p.wk = (const char*)d_in[2]; p.wv = (const char*)d_in[3]; p.wo = (const char*)d_in[4];
    p.w1 = (const char*)d_in[6]; p.w3 = (const char*)d_in[7]; p.w2 = (const char*)d_in[8];
    p.out = (char*)d_out; p.ws = (char*)d_ws;
    launch<27>(p, stream);
}
```

```cpp
#include <hip/hip_runtime.h>
#include <hip/hip_bf16.h>
#include <stdint.h>

#define AS1 __attribute__((address_space(1)))
#define AS3 __attribute__((address_space(3)))
typedef __bf16 bf16x2 __attribute__((ext_vector_type(2)));
typedef __bf16 bf16x8 __attribute__((ext_vector_type(8)));
typedef short s16x4 __attribute__((ext_vector_type(4)));
typedef short s16x8 __attribute__((ext_vector_type(8)));
typedef float f32x2 __attribute__((ext_vector_type(2)));
typedef float f32x4 __attribute__((ext_vector_type(4)));
typedef float f32x16 __attribute__((ext_vector_type(16)));
typedef unsigned u32x2 __attribute__((ext_vector_type(2)));
typedef unsigned u32x4 __attribute__((ext_vector_type(4)));
typedef AS1 unsigned gu32;
typedef AS1 unsigned long long gu64;

#define ATT_PRIO 2
#define GEM_PRIO 0
#define GU_HI 0
#define DN_HI 0
#define MPRIO(mask, i) __builtin_amdgcn_s_setprio(((mask) >> (i)) & 1 ? 3 : 0)
#define GU_DELAY 6
#define GU_ROT 4
#define GU_GQ 5
#define OF_SLEEP 8
#define HF_SLEEP 20
#define DN_DQ 8
#define KV_PEEK 6
#define KV_CHECK 8
#define SPLIT_MODE 0

constexpr int SEQ = 2048, DM = 640, FF = 1536;
constexpr unsigned WS_KF = 0;
constexpr unsigned WS_HF = 16384;
constexpr unsigned WS_OF = 32768;
constexpr unsigned WS_EP = 49152;
constexpr unsigned KVD = 5376;
constexpr unsigned KV_K1 = 2048, KV_V = 2560, KV_VT = 1408;
constexpr unsigned WS_KV = 65536;
constexpr unsigned WS_HT = WS_KV + 176128;
constexpr unsigned WS_OT = WS_HT + 64 * 96 * 1024;
constexpr unsigned WS_QT = WS_OT + 64 * 40 * 1024;
constexpr unsigned WS_PT = WS_QT + 64 * 32 * 2048;
constexpr unsigned WS_ST = WS_PT + 256 * 3 * 32 * 256;
constexpr unsigned L_PANEL = 0;
constexpr unsigned L_KV = 81920;
constexpr unsigned L_WQ = 81920;
constexpr unsigned L_GU = 124928;
constexpr unsigned GSL = 12320;
constexpr unsigned L_WKV = 124928;
constexpr unsigned L_MISC = 161920;
constexpr unsigned L_TOTAL = 163840;
constexpr unsigned CHUNK = 2 * KVD;
constexpr int NCHUNK = 16;
enum { C_AP = 0  , C_GP = 4  , C_DP = 8  , C_PP = 12  , C_WP = 16  , C_KVP = 20, C_KVFIN, C_TAG, C_KVRDY, C_OS = 24  , C_KN = 28, C_VM, C_N };

struct Params { const char* x; const char* wq; const char* wk; const char* wv; const char* wo; const char* w1; const char* w3; const char* w2; char* out; char* ws; };

#define SBAR() __builtin_amdgcn_sched_barrier(0)
#define PIN(x) asm volatile("" : "+v"(x))
#define CFENCE() asm volatile("" ::: "memory")
#define WAITV(n) asm volatile("s_waitcnt vmcnt(" #n ")" ::: "memory")
#define MFMA(a, b, c) __builtin_amdgcn_mfma_f32_32x32x16_bf16((a), (b), (c), 0, 0, 0)
#define MFMA8(a, b, c) __builtin_amdgcn_mfma_f32_32x32x8bf16_1k((a), (b), (c), 0, 0, 0)
#define RLX_AGENT __ATOMIC_RELAXED, __HIP_MEMORY_SCOPE_AGENT
#define RLX_WG __ATOMIC_RELAXED, __HIP_MEMORY_SCOPE_WORKGROUP

#define TMO(spins, lim, code, tgt) do {} while (0)
#define STAMP(id) do {} while (0)
#define STAMPC(id) do {} while (0)

#define LP(T, off) ((AS3 T*)(uintptr_t)(unsigned)(off))
__device__ __forceinline__ bf16x8 ldsf(unsigned off) { return *LP(const bf16x8, off); }
__device__ __forceinline__ s16x4 ldstr(unsigned off) { return __builtin_amdgcn_ds_read_tr16_b64_v4i16(LP(s16x4, off)); }
__device__ __forceinline__ bf16x8 ldstr2(unsigned off0, unsigned off1) {
    s16x4 a = ldstr(off0), b = ldstr(off1);
    return __builtin_bit_cast(bf16x8, __builtin_shufflevector(a, b, 0, 1, 2, 3, 4, 5, 6, 7));
}
template <int E> __device__ __forceinline__ s16x4 half4(bf16x8 v) { const s16x8 t = __builtin_bit_cast(s16x8, v); return __builtin_shufflevector(t, t, 4 * E, 4 * E + 1, 4 * E + 2, 4 * E + 3); }
__device__ __forceinline__ unsigned pk2(float lo, float hi) { bf16x2 v = {(__bf16)lo, (__bf16)hi}; return __builtin_bit_cast(unsigned, v); }
__device__ __forceinline__ float bflo(unsigned u) { return __uint_as_float(u << 16); }
__device__ __forceinline__ float bfhi(unsigned u) { return __uint_as_float(u & 0xffff0000u); }
__device__ __forceinline__ float rbf(float f) { return (float)(__bf16)f; }

__device__ __forceinline__ void glds16(unsigned voff, const char* sbase, unsigned ldsdst) {
    unsigned keep;
    asm volatile("s_mov_b32 %0, m0\n\ts_mov_b32 m0, %3\n\ts_nop 2\n\tglobal_load_lds_dwordx4 %1, %2\n\ts_mov_b32 m0, %0"
                 : "=&s"(keep) : "v"(voff), "s"(sbase), "s"(ldsdst) : "memory");
}
__device__ __forceinline__ void glds16_sc1(unsigned voff, const char* sbase, unsigned ldsdst) {
    unsigned keep;
    asm volatile("s_mov_b32 %0, m0\n\ts_mov_b32 m0, %3\n\ts_nop 2\n\tglobal_load_lds_dwordx4 %1, %2 sc1\n\ts_mov_b32 m0, %0"
                 : "=&s"(keep) : "v"(voff), "s"(sbase), "s"(ldsdst) : "memory");
}
__device__ __forceinline__ void glds16x3_sc1(unsigned voff, const char* sbase, unsigned ldsdst) {
    unsigned keep;
    asm volatile("s_mov_b32 %0, m0\n\ts_mov_b32 m0, %3\n\ts_nop 2\n\tglobal_load_lds_dwordx4 %1, %2 sc1\n\tglobal_load_lds_dwordx4 %1, %2 offset:1024 sc1\n\tglobal_load_lds_dwordx4 %1, %2 offset:2048 sc1\n\ts_mov_b32 m0, %0"
                 : "=&s"(keep) : "v"(voff), "s"(sbase), "s"(ldsdst) : "memory");
}
__device__ __forceinline__ void glds16x3v(unsigned v0, unsigned v1, unsigned v2, const char* sbase, unsigned ldsdst) {
    unsigned keep;
    asm volatile("s_mov_b32 %0, m0\n\ts_mov_b32 m0, %5\n\ts_nop 0\n\tglobal_load_lds_dwordx4 %1, %4\n\tglobal_load_lds_dwordx4 %2, %4 offset:1024\n\tglobal_load_lds_dwordx4 %3, %4 offset:2048\n\ts_mov_b32 m0, %0"
                 : "=&s"(keep) : "v"(v0), "v"(v1), "v"(v2), "s"(sbase), "s"(ldsdst) : "memory");
}
__device__ __forceinline__ void glds_gu(unsigned va, unsigned vb, unsigned vc, const char* s1, const char* s3, unsigned ldsdst) {
    unsigned keep;
    asm volatile("s_mov_b32 %0, m0\n\ts_mov_b32 m0, %8\n\ts_nop 0\n\tglobal_load_lds_dwordx4 %1, %4\n\tglobal_load_lds_dwordx4 %2, %5 offset:1024\n\t"
                 "s_mov_b32 exec_hi, 0\n\tglobal_load_lds_dwordx4 %3, %6 offset:2048\n\ts_not_b64 exec, exec\n\tglobal_load_lds_dwordx4 %3, %7 offset:2048\n\t"
                 "s_mov_b64 exec, -1\n\ts_mov_b32 m0, %0"
                 : "=&s"(keep) : "v"(va), "v"(vb), "v"(vc), "s"(s1), "s"(s3 - 1024), "s"(s1 - 2048), "s"(s3 - 2048), "s"(ldsdst) : "memory", "scc");
}
__device__ __forceinline__ void l2touch(const char* ga) {
    unsigned keep;
    asm volatile("s_mov_b32 %0, m0\n\ts_mov_b32 m0, %2\n\ts_nop 0\n\tglobal_load_lds_dword %1, off\n\ts_mov_b32 m0, %0" : "=&s"(keep) : "v"(ga), "s"(L_MISC + 1024) : "memory");
}
#define GLOAD16(dst, voff, sbase, imm) asm volatile("s_nop 4\n\tglobal_load_dwordx4 %0, %1, %2 offset:%3" : "=v"(dst) : "v"(voff), "s"(sbase), "i"(imm) : "memory")

__device__ __forceinline__ AS3 unsigned* lcnt(int c) { return LP(unsigned, L_MISC) + c; }
__device__ __forceinline__ void larrive(int c, int lane) { CFENCE(); if (lane == 0) __hip_atomic_fetch_add(lcnt(c), 1u, RLX_WG); CFENCE(); }
__device__ __forceinline__ void lset(int c, unsigned v, int lane) { CFENCE(); if (lane == 0) __hip_atomic_store(lcnt(c), v, RLX_WG); CFENCE(); }
__device__ __forceinline__ void lwait(int c, unsigned target) {
    CFENCE();
    _Pragma("clang loop unroll(disable)") for (unsigned spins = 0; spins < (1u << 22); ++spins) {
        if ((unsigned)__builtin_amdgcn_readfirstlane(__hip_atomic_load(lcnt(c), RLX_WG)) >= target) break;
        TMO(spins, 1u << 16, 100 + c, target);
        __builtin_amdgcn_s_sleep(1);
    }
    CFENCE();
}
__device__ __forceinline__ void lwait_all(int c, int n, unsigned target, int lane) {
    CFENCE();
    AS3 unsigned* q = lcnt(c) + (lane < n ? lane : 0);
    _Pragma("clang loop unroll(disable)") for (unsigned spins = 0; spins < (1u << 16); ++spins) {
        if (__all(__hip_atomic_load(q, RLX_WG) >= target)) break;
        TMO(spins, 1u << 16, 200 + c, target);
        __builtin_amdgcn_s_sleep(1);
    }
    CFENCE();
}
__device__ __forceinline__ void lpost(int c, unsigned v) { CFENCE(); *(volatile AS3 unsigned*)lcnt(c) = v; CFENCE(); }
__device__ __forceinline__ void linc(unsigned vaddr, unsigned vone) {
    asm volatile("s_mov_b64 exec, 1\n\tds_add_u32 %0, %1\n\ts_mov_b64 exec, -1" :: "v"(vaddr), "v"(vone) : "memory");
}
__device__ __forceinline__ unsigned lpeek(int c, int lane) { return *((volatile AS3 unsigned*)lcnt(c) + (lane & 3)); }
__device__ __forceinline__ void lcheck(unsigned pk, int c, unsigned target, int lane) {
    if (__builtin_expect(__any(pk < target), 0)) lwait_all(c, 4, target, lane);
    CFENCE();
}
__device__ __forceinline__ void gwait(const char* ws, unsigned off, int n, unsigned tag, int lane) {
    gu32* f = (gu32*)(ws + off) + (lane < n ? lane : 0);
    _Pragma("clang loop unroll(disable)") for (unsigned spins = 0; spins < (1u << 14); ++spins) {
        unsigned v = __hip_atomic_load(f, RLX_AGENT);
        if (__all(v == tag)) break;
        TMO(spins, 1u << 14, 300, off);
        __builtin_amdgcn_s_sleep(6);
    }
    CFENCE();
}
__device__ __forceinline__ float gwait64(const char* ws, unsigned off, unsigned tag, int lane) {
    gu64* f = (gu64*)(ws + off) + lane;
    unsigned long long v = 0;
    _Pragma("clang loop unroll(disable)") for (unsigned spins = 0; spins < (1u << 14); ++spins) {
        v = __hip_atomic_load(f, RLX_AGENT);
        if (__all((unsigned)(v >> 32) == tag)) break;
        __builtin_amdgcn_s_sleep(6);
    }
    CFENCE();
    return __uint_as_float((unsigned)v);
}
__device__ __forceinline__ __amdgpu_buffer_rsrc_t mkrsrc(const char* base, unsigned bytes) {
    return __builtin_amdgcn_make_buffer_rsrc((void*)base, 0, bytes, 0x00020000);
}
__device__ __forceinline__ float dpp_sum16(float v) {
    v += __int_as_float(__builtin_amdgcn_update_dpp(0, __float_as_int(v), 0x128, 0xf, 0xf, false));
    v += __int_as_float(__builtin_amdgcn_update_dpp(0, __float_as_int(v), 0x124, 0xf, 0xf, false));
    v += __int_as_float(__builtin_amdgcn_update_dpp(0, __float_as_int(v), 0x122, 0xf, 0xf, false));
    v += __int_as_float(__builtin_amdgcn_update_dpp(0, __float_as_int(v), 0x121, 0xf, 0xf, false));
    return v;
}
__device__ __forceinline__ float wave_max(float v) {
    int x = __float_as_int(v);
    x = max(x, __builtin_amdgcn_update_dpp(0, x, 0x128, 0xf, 0xf, false)); x = max(x, __builtin_amdgcn_update_dpp(0, x, 0x124, 0xf, 0xf, false));
    x = max(x, __builtin_amdgcn_update_dpp(0, x, 0x122, 0xf, 0xf, false)); x = max(x, __builtin_amdgcn_update_dpp(0, x, 0x121, 0xf, 0xf, false));
    const int a = max(__builtin_amdgcn_readlane(x, 0), __builtin_amdgcn_readlane(x, 16)), b = max(__builtin_amdgcn_readlane(x, 32), __builtin_amdgcn_readlane(x, 48));
    return __int_as_float(max(a, b));
}
__device__ __forceinline__ bf16x8 pack8(const f32x16& a, int b, float sc) {
    u32x4 r = {pk2(a[b] * sc, a[b + 1] * sc), pk2(a[b + 2] * sc, a[b + 3] * sc), pk2(a[b + 4] * sc, a[b + 5] * sc), pk2(a[b + 6] * sc, a[b + 7] * sc)};
    return __builtin_bit_cast(bf16x8, r);
}

template <int I> struct IC { static constexpr int value = I; };
template <int B, int E, class F> __device__ __forceinline__ void static_for(F&& f) { if constexpr (B < E) { f(IC<B>{}); static_for<B + 1, E>(f); } }
#define WAITVN(n) asm volatile("s_waitcnt vmcnt(%0)" :: "i"(n) : "memory")
__device__ __forceinline__ void GLOAD16S(u32x4& dst, unsigned voff, const char* sbase) { asm volatile("global_load_dwordx4 %0, %1, %2 sc1" : "=v"(dst) : "v"(voff), "s"(sbase) : "memory"); }
__device__ __forceinline__ void GLOAD16N(u32x4& dst, unsigned voff, const char* sbase) { asm volatile("global_load_dwordx4 %0, %1, %2" : "=v"(dst) : "v"(voff), "s"(sbase) : "memory"); }
__device__ __forceinline__ void GLOAD16N_IF(u32x4& dst, unsigned voff, const char* sbase, int cond) {
    asm volatile("s_cmp_eq_u32 %3, 0\n\ts_cbranch_scc1 1f\n\tglobal_load_dwordx4 %0, %1, %2\n1:" : "=v"(dst) : "v"(voff), "s"(sbase), "s"(cond) : "memory", "scc"); }
template <int N> __device__ __forceinline__ void waitv5(u32x4& a, u32x4& b, u32x4& c, u32x4& d, u32x4& e) { asm volatile("s_waitcnt vmcnt(%5)" : "+v"(a), "+v"(b), "+v"(c), "+v"(d), "+v"(e) : "i"(N) : "memory"); }
__device__ __forceinline__ void gload8x2_sc1(unsigned long long& a, unsigned long long& b, unsigned voff, const char* sbase) {
    asm volatile("global_load_dwordx2 %0, %2, %3 sc1\n\tglobal_load_dwordx2 %1, %2, %3 offset:512 sc1" : "=&v"(a), "=&v"(b) : "v"(voff), "s"(sbase) : "memory"); }
__device__ __forceinline__ void waitv8x2(unsigned long long& a, unsigned long long& b) { asm volatile("s_waitcnt vmcnt(0)" : "+v"(a), "+v"(b) :: "memory"); }
__device__ __forceinline__ void PIN2(u32x4& a, u32x4& b) { asm volatile("" : "+v"(a), "+v"(b) :: "memory"); }
__device__ __forceinline__ u32x4 scale8(unsigned x0, unsigned x1, unsigned x2, unsigned x3, unsigned clo, unsigned chi, float c) {
    unsigned o0, o1, o2, o3; float t0, t1, t2, t3, t4, t5, t6, t7;
    asm("v_dot2_f32_bf16 %4, %12, %16, %18\n\tv_dot2_f32_bf16 %5, %12, %17, %18\n\tv_dot2_f32_bf16 %6, %13, %16, %18\n\tv_dot2_f32_bf16 %7, %13, %17, %18\n\t"
        "v_dot2_f32_bf16 %8, %14, %16, %18\n\tv_dot2_f32_bf16 %9, %14, %17, %18\n\tv_dot2_f32_bf16 %10, %15, %16, %18\n\tv_dot2_f32_bf16 %11, %15, %17, %18\n\t"
        "v_cvt_pk_bf16_f32 %0, %4, %5\n\tv_cvt_pk_bf16_f32 %1, %6, %7\n\tv_cvt_pk_bf16_f32 %2, %8, %9\n\tv_cvt_pk_bf16_f32 %3, %10, %11"
        : "=&v"(o0), "=&v"(o1), "=&v"(o2), "=&v"(o3), "=&v"(t0), "=&v"(t1), "=&v"(t2), "=&v"(t3), "=&v"(t4), "=&v"(t5), "=&v"(t6), "=&v"(t7)
        : "v"(x0), "v"(x1), "v"(x2), "v"(x3), "v"(clo), "v"(chi), "v"(c));
    return u32x4{o0, o1, o2, o3};
}
template <int N> __device__ __forceinline__ void waitv3(u32x4& a, u32x4& b, u32x4& c) { asm volatile("s_waitcnt vmcnt(%3)" : "+v"(a), "+v"(b), "+v"(c) : "i"(N) : "memory"); }

constexpr int dn_younger(int k, int D, bool wo) {
    int n = 0, la = 0, lb = 0;
    for (int i = 0; i < D; ++i) { n += 2; if (i == k + 1) lb = n; }
    for (int i = 0; i < D; ++i) { n += 3; if (i == k) la = n; }
    for (int s = 0; s < k; ++s) {
        if (s + D < 24) { n += 2; if (s + D == k + 1) lb = n; }
        if (wo && s >= 16) n += 2;
        if (s + D < 24) { n += 3; if (s + D == k) la = n; }
    }
    return n - (la > lb ? la : lb);
}
constexpr float QSCALE = 0.22360679774997896f * 1.4426950408889634f;
#define BOUND_LIM 50.f
constexpr float BOUND_MAX = BOUND_LIM;

struct AttSync { int lane; const char* kv; unsigned dvoff; unsigned wo; int w; };
__device__ __forceinline__ unsigned kvslot(int c) { return L_KV + ((c + 2) & 3) * CHUNK; }
__device__ __forceinline__ void att_dma(const AttSync& a, int c) { glds16x3_sc1(a.dvoff, a.kv + (size_t)c * CHUNK, kvslot(c) + a.wo); }
constexpr unsigned AP0 = 16;
#define RING_X(c) do { WAITV(3); lpost(C_AP + as.w, AP0 + (c) + 1); } while (0)
#define RP_A
#define RP_B
#define RING_Y(c, pk) do { RP_A lcheck(pk, C_AP, AP0 + (c) + 1, lane); const int cn_ = (c) + 3 < NCHUNK ? (c) + 3 : NCHUNK - 1;   \
                           glds16x3_sc1(as.dvoff, as.kv + (size_t)cn_ * CHUNK, kvslot((c) + 3) + as.wo); RP_B } while (0)

__device__ __forceinline__ void att_fast(const AttSync& as, bf16x8 Q0a, s16x4 Q1a, bf16x8 Q0b, s16x4 Q1b, f32x16& Oa, f32x16& Ob) {
    const int lane = as.lane, r = lane & 31, h = lane >> 5;
    const unsigned ak0 = lane * 16, ak1 = h ? KV_V + 20 * 16 : KV_K1 + r * 16;
    const unsigned av = KV_V + h * 352 + (r <= 20 ? r : 21) * 16;
    const f32x16 zero = {};
    f32x16 Sa, Sb = {};
    bf16x8 K0[2], K1[2], V0[2], V1[2];
    u32x4 Pa0, Pa1, Pb0 = {}, Pb1 = {};
    unsigned pk = 0;
    unsigned long long rp0 = 0, rp1 = 0;
    V0[1] = bf16x8{}; V1[1] = bf16x8{};
    unsigned vk0 = L_KV + ak0, vk1 = L_KV + ak1, vv = L_KV + av;
    PIN(vk0); PIN(vk1); PIN(vv);
    K0[0] = ldsf(vk0 + 2 * CHUNK); K1[0] = ldsf(vk1 + 2 * CHUNK); K0[1] = ldsf(vk0 + 2 * CHUNK + 1024); V1[0] = ldsf(vv + 2 * CHUNK + 704); V0[0] = ldsf(vv + 2 * CHUNK);
    Sa = MFMA(K0[0], Q0a, zero); Sa = MFMA8(half4<0>(K1[0]), Q1a, Sa);
#define EX4(S, i) do { S[i] = __builtin_amdgcn_exp2f(S[i]); S[i + 1] = __builtin_amdgcn_exp2f(S[i + 1]); S[i + 2] = __builtin_amdgcn_exp2f(S[i + 2]); S[i + 3] = __builtin_amdgcn_exp2f(S[i + 3]); } while (0)
#define GAP(MF, LD, T, k, PW, j, S, i) do { MF; SBAR(); LD; PW[j] = pk2(T[k], T[k + 1]); PW[j + 1] = pk2(T[k + 2], T[k + 3]); PIN(PW); SBAR(); EX4(S, i); PIN(S); SBAR(); } while (0)
#define BC(x) __builtin_bit_cast(bf16x8, x)
#define STEP(e, d, KN, L3, LK0, LK1, LV) do { \
        GAP(Ob = MFMA(V0[e ^ 1], BC(Pb0), Ob), (void)0,                  Sb, 12, Pb1, 2, Sa, 0); \
        GAP(Sb = MFMA(K0[e], Q0b, zero),       (void)0,                  Sa, 0, Pa0, 0, Sa, 4); \
        GAP(Sb = MFMA8(half4<e>(K1[d]), Q1b, Sb), L3,                       Sa, 4, Pa0, 2, Sa, 8); \
        GAP(Ob = MFMA(V1[e ^ 1], BC(Pb1), Ob), K0[e] = ldsf(LK0),        Sa, 8, Pa1, 0, Sa, 12); \
        GAP(Oa = MFMA(V0[e], BC(Pa0), Oa),     LK1,                      Sa, 12, Pa1, 2, Sb, 0); \
        GAP(Sa = MFMA(K0[e ^ 1], Q0a, zero),   V1[e ^ 1] = ldsf((LV) + 704), Sb, 0, Pb0, 0, Sb, 4); \
        GAP(Sa = MFMA8(half4<e ^ 1>(K1[KN]), Q1a, Sa), V0[e ^ 1] = ldsf(LV),     Sb, 4, Pb0, 2, Sb, 8); \
        GAP(Oa = MFMA(V1[e], BC(Pa1), Oa),     (void)0,                  Sb, 8, Pb1, 0, Sb, 12); \
    } while (0)
#define CHUNK4(c, B, N) do { \
        STEP(0, 0, 0, (void)0, vk0 + (B) + KVD, K1[1] = ldsf(vk1 + (B) + KVD), vv + (B) + KV_VT); \
        RING_X(c); \
        STEP(1, 0, 1, pk = lpeek(C_AP, lane), vk0 + (B) + KVD + 1024, (void)0, vv + (B) + KVD); \
        RING_Y(c, pk); \
        STEP(0, 1, 1, (void)0, vk0 + (N), K1[0] = ldsf(vk1 + (N)), vv + (B) + KVD + KV_VT); \
        STEP(1, 1, 0, (void)0, vk0 + (N) + 1024, (void)0, vv + (N)); \
    } while (0)
    for (int c = 0; c < NCHUNK; c += 4) {
        CHUNK4(c, 2 * CHUNK, 3 * CHUNK); CHUNK4(c + 1, 3 * CHUNK, 0); CHUNK4(c + 2, 0, CHUNK); CHUNK4(c + 3, CHUNK, 2 * CHUNK);
    }
#undef CHUNK4
    Pb1[2] = pk2(Sb[12], Sb[13]); Pb1[3] = pk2(Sb[14], Sb[15]);
    Ob = MFMA(V0[1], BC(Pb0), Ob); Ob = MFMA(V1[1], BC(Pb1), Ob);
#undef STEP
#undef GAP
#undef EX4
}

__device__ __forceinline__ void att_safe(const AttSync& as, bf16x8 Q0a, s16x4 Q1a, bf16x8 Q0b, s16x4 Q1b, f32x16& Oa, f32x16& Ob) {
    const int lane = as.lane, r = lane & 31, h = lane >> 5;
    const unsigned ak0 = lane * 16, ak1 = h ? KV_V + 20 * 16 : KV_K1 + r * 16;
    const unsigned av = KV_V + h * 352 + (r <= 20 ? r : 21) * 16;
    const f32x16 zero = {};
    float ma = -INFINITY, mb = -INFINITY;
    unsigned long long rp0 = 0, rp1 = 0;
    for (int c = 0; c < NCHUNK; ++c) {
        const unsigned b0 = kvslot(c);
        for (int t = 0; t < 4; ++t) {
            if (t == 1) RING_X(c);
            if (t == 2) RING_Y(c, 0u);
            const unsigned base = b0 + (t >> 1) * KVD; const int e = t & 1;
            bf16x8 K0 = ldsf(base + ak0 + e * 1024), K1 = ldsf(base + ak1), V0 = ldsf(base + av + e * KV_VT), V1 = ldsf(base + av + e * KV_VT + 704);
#pragma unroll
            for (int q = 0; q < 2; ++q) {
                f32x16 S = MFMA(K0, q ? Q0b : Q0a, zero); S = MFMA8(e ? half4<1>(K1) : half4<0>(K1), q ? Q1b : Q1a, S);
                float mx = S[0];
#pragma unroll
                for (int i = 1; i < 16; ++i) mx = fmaxf(mx, S[i]);
                mx = fmaxf(mx, __shfl_xor(mx, 32));
                float& m = q ? mb : ma; f32x16& O = q ? Ob : Oa;
                const float mn = fmaxf(m, mx);
                const float al = __builtin_amdgcn_exp2f(m - mn);
                m = mn;
#pragma unroll
                for (int i = 0; i < 16; ++i) { O[i] *= al; S[i] = __builtin_amdgcn_exp2f(S[i] - mn); }
                O = MFMA(V0, pack8(S, 0, 1.f), O); O = MFMA(V1, pack8(S, 8, 1.f), O);
            }
        }
    }
}

__device__ __forceinline__ void xload8(u32x4 (&x)[5], u32x4 (&y)[5], const char* xb, unsigned vo, unsigned vo2, int skip) {
    asm volatile("s_cmp_lg_u32 %13, 0\n\ts_cbranch_scc1 1f\n\t"
                 "global_load_dwordx4 %0, %10, %12\n\tglobal_load_dwordx4 %1, %10, %12 offset:256\n\tglobal_load_dwordx4 %2, %10, %12 offset:512\n\t"
                 "global_load_dwordx4 %3, %10, %12 offset:768\n\tglobal_load_dwordx4 %4, %10, %12 offset:1024\n\t"
                 "global_load_dwordx4 %5, %11, %12\n\tglobal_load_dwordx4 %6, %11, %12 offset:256\n\tglobal_load_dwordx4 %7, %11, %12 offset:512\n\t"
                 "global_load_dwordx4 %8, %11, %12 offset:768\n\tglobal_load_dwordx4 %9, %11, %12 offset:1024\n1:"
                 : "=&v"(x[0]), "=&v"(x[1]), "=&v"(x[2]), "=&v"(x[3]), "=&v"(x[4]), "=&v"(y[0]), "=&v"(y[1]), "=&v"(y[2]), "=&v"(y[3]), "=&v"(y[4])
                 : "v"(vo), "v"(vo2), "s"(xb), "s"(skip) : "memory", "scc");
}
__device__ __forceinline__ void xload4(u32x4 (&x)[5], const char* xb, unsigned vo) {
    asm volatile("global_load_dwordx4 %0, %5, %6\n\tglobal_load_dwordx4 %1, %5, %6 offset:256\n\tglobal_load_dwordx4 %2, %5, %6 offset:512\n\t"
                 "global_load_dwordx4 %3, %5, %6 offset:768\n\tglobal_load_dwordx4 %4, %5, %6 offset:1024"
                 : "=&v"(x[0]), "=&v"(x[1]), "=&v"(x[2]), "=&v"(x[3]), "=&v"(x[4]) : "v"(vo), "s"(xb) : "memory");
}
__device__ __forceinline__ void ln_rows(const u32x4 (&xr)[5], int row0, int lane) {
    f32x2 xv[20];
    f32x2 s0 = {0.f, 0.f}, s1 = {0.f, 0.f}, q0 = {0.f, 0.f}, q1 = {0.f, 0.f};
#pragma unroll
    for (int i = 0; i < 20; i += 2) {
        const unsigned a = xr[i >> 2][i & 3], b = xr[i >> 2][(i & 3) + 1];
        xv[i] = f32x2{bflo(a), bfhi(a)}; xv[i + 1] = f32x2{bflo(b), bfhi(b)};
        s0 += xv[i]; q0 = __builtin_elementwise_fma(xv[i], xv[i], q0);
        s1 += xv[i + 1]; q1 = __builtin_elementwise_fma(xv[i + 1], xv[i + 1], q1);
    }
    s0 += s1; q0 += q1;
    const float s = dpp_sum16(s0[0] + s0[1]), ss = dpp_sum16(q0[0] + q0[1]);
    const float mean = s * (1.f / 640.f);
    const float var = fmaxf(ss * (1.f / 640.f) - mean * mean, 0.f);
    const float rs = __builtin_amdgcn_rcpf(rbf(__builtin_amdgcn_sqrtf(rbf(rbf(var) + 1e-5f)))), mo = -rbf(mean) * rs;
    const f32x2 rs2 = {rs, rs}, mo2 = {mo, mo};
    const int row = row0 + (lane >> 4), c = lane & 15;
    const unsigned wa = L_PANEL + (row >> 5) * 40960 + (c >> 1) * 1024 + (c & 1) * 512 + (((row & 31) ^ (c & 7)) << 4);
#pragma unroll
    for (int u = 0; u < 5; ++u) {
        u32x4 o;
#pragma unroll
        for (int e = 0; e < 4; ++e) { const f32x2 y = __builtin_elementwise_fma(xv[4 * u + e], rs2, mo2); o[e] = pk2(y[0], y[1]); }
        *LP(u32x4, wa + u * 8192) = o;
    }
}
#define BLOCK_BARRIER() asm volatile("s_waitcnt lgkmcnt(0)\n\ts_barrier" ::: "memory")

template <int PH>
__global__ void __launch_bounds__(512, 2) mega(Params p) {
    constexpr bool PRO = PH & 1, ATT = PH & 2, DN = PH & 8, WO = PH & 16;
    const int w = __builtin_amdgcn_readfirstlane(threadIdx.x >> 6);
    const int b = blockIdx.x, xc = b & 7, j = b >> 3;
    const int ti = (xc >> 1) * 8 + (j >> 2), g = (xc & 1) * 4 + (j & 3);
    constexpr bool FUSED = PH == 27;
    const int kmt = g & 1;
    const int ra = kmt * 32 + (w & 3) * 8, rb = (kmt ^ 1) * 32 + (w & 3) * 8;
    u32x4 x0[5], x1[5], x2[5], x3[5];
    if constexpr (PRO) {
        asm volatile("" :: "s"(p.x), "s"(p.ws), "s"(p.wq), "s"(p.wk));
        const char* xb = p.x + (size_t)ti * 64 * 1280;
        const unsigned xvo = ((threadIdx.x >> 4) & 3) * 1280 + (threadIdx.x & 15) * 16;
        xload8(x0, x1, xb + (size_t)ra * 1280, xvo, xvo + 5120, w >> 2);
        xload8(x2, x3, xb + (size_t)rb * 1280, xvo, xvo + 5120, w >> 2);
    }
    const char* pc; asm volatile("s_getpc_b64 %0" : "=s"(pc));
    unsigned tag = 1u;
    char* ws = p.ws;
    STAMP(0);
    if (threadIdx.x < C_N) *lcnt(threadIdx.x) = (threadIdx.x == C_WP || threadIdx.x == C_WP + 1 || threadIdx.x == C_PP || threadIdx.x == C_PP + 1) ? 100u : 0u;
    const bool haskv = (g >> 2) == (ti & 1);
    const char* wqb = p.wq + g * 160;
#define LANE_CONSTS() int lane = __builtin_amdgcn_mbcnt_hi(~0u, __builtin_amdgcn_mbcnt_lo(~0u, 0u)); PIN(lane); \
    const int r = lane & 31, h = lane >> 5, tq = (lane & 15) >> 2, tp = lane & 3, tb = (lane >> 4) & 1; (void)r; (void)h; (void)tq; (void)tp; (void)tb

    if (w >= 4) {
        LANE_CONSTS();
        const int v = w - 4;
        unsigned epv = 0u;
        if constexpr (FUSED) {
            asm volatile("global_load_dword %0, %1, %2 sc1" : "=v"(epv) : "v"(0u), "s"(p.ws + WS_EP) : "memory");
            constexpr unsigned CODE_PF = 65536;
            const unsigned line = (unsigned)(j * 4 + v) * 64u + lane;
            if (line * 128u < CODE_PF) l2touch(pc + line * 128u);
        }
#define GET_TAG() do { if constexpr (FUSED) { asm volatile("s_nop 0\n\tv_readfirstlane_b32 %0, %1\n\ts_nop 3" : "=s"(tag) : "v"(epv) : "memory"); tag += 0x9E3779B1u; } } while (0)
        __builtin_amdgcn_s_setprio(3);
        if constexpr (PRO) {
            const int kvm = 2 * ti + (g & 1); const bool isV = (g >> 1) & 1;
            const char* wkvb = isV ? p.wv : p.wk;
            if (haskv && v < 2) {
#pragma unroll
                for (int n = 0; n < 13; ++n) {
                    const unsigned o = v * 12800 + (n < 12 ? n * 1024 : 11776);
                    glds16(o + lane * 16, wkvb, L_WKV + o);
                }
            }
            BLOCK_BARRIER();
            if (v >= 2) {
                const int v2 = v - 2;
                unsigned wo5[5];
#pragma unroll
                for (int i = 0; i < 5; ++i) { const unsigned c = (v2 + 2 * i) * 64 + lane, row = c / 10u, col = c - row * 10u; wo5[i] = row * 1280 + col * 16; }
#pragma unroll
                for (int n = 0; n < 20; ++n) glds16(wo5[n % 5], wqb + (n / 5) * 64 * 1280, L_WQ + (v2 + 2 * n) * 1024);
                u32x4 wst[3][10];
                auto wst_issue = [&](auto I) {
                    constexpr int rr = I.value / 10, i = I.value % 10;
                    GLOAD16N(wst[rr][i], wo5[i % 5], wqb + (4 + 2 * rr + i / 5) * 64 * 1280);
                };
                static_for<0, 10>(wst_issue);
                WAITV(10);
                GET_TAG(); lpost(C_TAG, tag);
                lpost(C_PP + v, 1);
                STAMP(2);
                if (haskv) lwait(C_KVFIN, 1);
                static_for<10, 30>(wst_issue);
                static_for<0, 3>([&](auto R) {
                    constexpr int rr = R.value;
                    waitv5<20 - 10 * rr>(wst[rr][0], wst[rr][1], wst[rr][2], wst[rr][3], wst[rr][4]); waitv5<63>(wst[rr][5], wst[rr][6], wst[rr][7], wst[rr][8], wst[rr][9]);
                    lwait_all(C_AP, 4, 3 + rr, lane);
#pragma unroll
                    for (int i = 0; i < 10; ++i) *LP(u32x4, L_WQ + (rr & 1) * 20480 + (v2 + 2 * i) * 1024 + lane * 16) = wst[rr][i];
                    lpost(C_WP + v, rr + 1);
                });
            } else {
              if (haskv) {
                f32x16 acc = {};
                const unsigned wa = L_WKV + (v * 320 + 8 * h + tq) * 40 + tb * 32 + (tb ? 0 : tp * 8);
                unsigned xa[4];
#pragma unroll
                for (int s = 0; s < 4; ++s) xa[s] = L_PANEL + kmt * 40960 + v * 20480 + h * 512 + ((r ^ ((2 * s + h) & 7)) << 4);
                WAITV(0);
                GET_TAG();
                STAMP(2);
                bf16x8 wf[20], xf[20];
#pragma unroll
                for (int s = 0; s < 20; ++s) wf[s] = ldstr2(wa + s * 640, wa + s * 640 + 160);
                lwait_all(C_AP, 4, 1, lane);
#pragma unroll
                for (int s = 0; s < 20; ++s) xf[s] = ldsf(xa[s & 3] + s * 1024);
                SBAR();
                if (isV) {
#pragma unroll
                    for (int s = 0; s < 20; ++s) acc = MFMA(xf[s], wf[s], acc);
                } else {
#pragma unroll
                    for (int s = 0; s < 20; ++s) acc = MFMA(wf[s], xf[s], acc);
                }
                if (v) {
#pragma unroll
                    for (int e = 0; e < 4; ++e) *LP(f32x4, L_WKV + 12800 + e * 1024 + lane * 16) = f32x4{acc[4 * e], acc[4 * e + 1], acc[4 * e + 2], acc[4 * e + 3]};
                    lpost(C_KVP, 1);
                } else {
                    lwait(C_KVP, 1);
#pragma unroll
                    for (int e = 0; e < 4; ++e) {
                        const f32x4 t = *LP(const f32x4, L_WKV + 12800 + e * 1024 + lane * 16);
                        acc[4 * e] += t[0]; acc[4 * e + 1] += t[1]; acc[4 * e + 2] += t[2]; acc[4 * e + 3] += t[3];
                    }
                    const __amdgpu_buffer_rsrc_t rk = mkrsrc(ws + WS_KV, 32 * KVD);
                    const unsigned base = ti * KVD, ke = g & 1;
                    const u32x4 f0 = __builtin_bit_cast(u32x4, pack8(acc, 0, 1.f)), f1 = __builtin_bit_cast(u32x4, pack8(acc, 8, 1.f));
                    float mx;
                    if (!isV) {
                        __builtin_amdgcn_raw_buffer_store_b128(f0, rk, base + ke * 1024 + lane * 16, 0, 16);
                        const u32x2 k1 = {f1[0], f1[1]};
                        if (!h) __builtin_amdgcn_raw_buffer_store_b64(k1, rk, base + KV_K1 + r * 16 + ke * 8, 0, 16);
                        lpost(C_KVFIN, 1);
                        float n2 = 0.f;
#pragma unroll
                        for (int e = 0; e < 4; ++e) n2 += bflo(f0[e]) * bflo(f0[e]) + bfhi(f0[e]) * bfhi(f0[e]);
                        if (!h) n2 += bflo(f1[0]) * bflo(f1[0]) + bfhi(f1[0]) * bfhi(f1[0]) + bflo(f1[1]) * bflo(f1[1]) + bfhi(f1[1]) * bfhi(f1[1]);
                        n2 += __shfl_xor(n2, 32);
                        mx = (n2 == n2) ? n2 : INFINITY;
                    } else {
                        const unsigned cst = r == 20 ? 0x3f803f80u : 0u;
                        const u32x4 c4 = {cst, cst, cst, cst};
                        if (r <= 21) {
                            __builtin_amdgcn_raw_buffer_store_b128(r >= 20 ? c4 : f0, rk, base + KV_V + ke * KV_VT + h * 352 + r * 16, 0, 16);
                            __builtin_amdgcn_raw_buffer_store_b128(r >= 20 ? c4 : f1, rk, base + KV_V + ke * KV_VT + 704 + h * 352 + r * 16, 0, 16);
                        }
                        lpost(C_KVFIN, 1);
                        float vm = 0.f;
#pragma unroll
                        for (int e = 0; e < 16; ++e) { const float a = fabsf(acc[e]); vm = (a <= vm) ? vm : ((a == a) ? a : INFINITY); }
                        mx = r < 20 ? vm : 0.f;
                    }
                    mx = wave_max(mx);
                    WAITV(0);
                    STAMP(15);
                    if (lane < 8) __hip_atomic_store((gu64*)(ws + WS_KF + lane * 1024 + (isV ? 512 : 0)) + kvm, ((unsigned long long)tag << 32) | __float_as_uint(mx), RLX_AGENT);
                }
            }
              else { WAITV(0); GET_TAG(); }
              STAMP(3);
            }
            if (haskv) lwait(C_KVFIN, 1);
#pragma unroll
            for (int n = 0; n < 2; ++n) {
                unsigned L = n * 64 + lane; L = (j * 4 + v) * 120 + (L < 120u ? L : 119u);
                const unsigned m3 = L >= 7680u, rem = L - (m3 ? 7680u : 0u), row = rem / 12u;
                l2touch((m3 ? p.w3 : p.w1) + row * 3072 + (xc & 1) * 1536 + (rem - row * 12u) * 128);
            }
            lwait_all(C_AP, 4, ATT ? GU_DELAY : 8, lane);
            STAMP(4);
            {
                const char* w1s = p.w1 + g * 384 + v * 96; const char* w3s = p.w3 + g * 384 + v * 96;
                auto goff = [](unsigned idx) { const unsigned row = idx / 6u; return row * 3072 + (idx - row * 6u) * 16; };
                const unsigned gva = goff(lane), gvb = goff((lane - 8) & 63), gvc = goff(64 + (h ? (r - 8) & 31 : r));
                const unsigned gdst = L_GU + v * 9216;
                auto gdma = [&](auto S) { constexpr int s = S.value; glds_gu(gva, gvb, gvc, w1s + (size_t)s * 16 * 3072, w3s + (size_t)s * 16 * 3072, gdst + (s % 3) * 3072); };
                static_for<0, 3>(gdma);
                f32x16 acc[2][3] = {};
                unsigned bn[3][2], pa[4], pb[4];
#pragma unroll
                for (int n = 0; n < 3; ++n)
#pragma unroll
                    for (int t = 0; t < 2; ++t) {
                        const unsigned idx = 6 * (8 * h + 4 * t + tq) + 2 * n + (tp >> 1);
                        const unsigned o = idx < 64u ? (tb ? 1024 + ((idx + 8) & 63) * 16 : idx * 16) : (tb ? 2560 + ((idx - 56) & 31) * 16 : 2048 + (idx - 64) * 16);
                        bn[n][t] = gdst + o + (tp & 1) * 8;
                    }
#pragma unroll
                for (int s = 0; s < 4; ++s) { pa[s] = L_PANEL + h * 512 + ((r ^ ((2 * s + h) & 7)) << 4); pb[s] = pa[s] + 40960; PIN(pb[s]); }
                bf16x8 wf[2][3], xa[2], xb2[2];
                auto rdw = [&](auto S) { constexpr int s = S.value;
#pragma unroll
                    for (int n = 0; n < 3; ++n) wf[s & 1][n] = ldstr2(bn[n][0] + (s % 3) * 3072, bn[n][1] + (s % 3) * 3072); };
                auto rdx = [&](auto S) { constexpr int s = S.value; xa[s & 1] = ldsf(pa[s & 3] + s * 1024); xb2[s & 1] = ldsf(pb[s & 3] + s * 1024); };
                WAITV(8);
                rdx(IC<0>{}); rdw(IC<0>{});
                asm volatile("s_waitcnt lgkmcnt(0)" ::: "memory");
                gdma(IC<3>{});
                static_for<0, 40>([&](auto S) {
                    constexpr int s = S.value; constexpr bool nx = s + 1 < 40;
                    MPRIO(GU_HI, 0); acc[0][0] = MFMA(wf[s & 1][0], xa[s & 1], acc[0][0]); SBAR();
                    if constexpr (nx) rdx(IC<s + 1>{});
                    SBAR();
                    MPRIO(GU_HI, 1); acc[1][0] = MFMA(wf[s & 1][0], xb2[s & 1], acc[1][0]); SBAR();
                    if constexpr (nx) { WAITVN(4 * ((s + 3 < 39 ? s + 3 : 39) - (s + 1))); rdw(IC<s + 1>{}); }
                    SBAR();
                    MPRIO(GU_HI, 2); acc[0][1] = MFMA(wf[s & 1][1], xa[s & 1], acc[0][1]); SBAR();
                    MPRIO(GU_HI, 3); acc[1][1] = MFMA(wf[s & 1][1], xb2[s & 1], acc[1][1]); SBAR();
                    if constexpr (s + 4 < 40) { asm volatile("s_waitcnt lgkmcnt(0)" ::: "memory"); gdma(IC<s + 4>{}); }
                    SBAR();
                    MPRIO(GU_HI, 4); acc[0][2] = MFMA(wf[s & 1][2], xa[s & 1], acc[0][2]); SBAR();
                    MPRIO(GU_HI, 5); acc[1][2] = MFMA(wf[s & 1][2], xb2[s & 1], acc[1][2]); SBAR();
                });
                lpost(C_GP + v, 42);
                __builtin_amdgcn_s_setprio(3);
                STAMP(6);
                const __amdgpu_buffer_rsrc_t rh = mkrsrc(ws + WS_HT, 64 * 96 * 1024);
#pragma unroll
                for (int m = 0; m < 2; ++m)
#pragma unroll
                    for (int n = 0; n < 3; ++n) {
                        f32x16 hv;
#pragma unroll
                        for (int e = 0; e < 8; ++e) {
                            const float gg = acc[m][n][e];
                            hv[e] = gg * __builtin_amdgcn_rcpf(1.f + __builtin_amdgcn_exp2f(-1.4426950408889634f * gg)) * acc[m][n][e + 8];
                        }
                        __builtin_amdgcn_raw_buffer_store_b128(__builtin_bit_cast(u32x4, pack8(hv, 0, 1.f)), rh, ((2 * ti + m) * 96 + g * 12 + 3 * v + n) * 1024 + lane * 16, 0, 16);
                    }
                if constexpr (DN) {
                    WAITV(0);
                    if (lane < 2) __hip_atomic_store((gu32*)(ws + WS_HF) + (2 * ti + lane) * 32 + g * 4 + v, tag, RLX_AGENT);
                }
                __builtin_amdgcn_s_setprio(0);
                if constexpr (DN) {
#pragma unroll
                    for (int n = 0; n < 2; ++n) {
                        unsigned L = (j * 4 + v) * 85 + n * 64 + lane; L = L < 10880u ? L : 10879u;
                        const unsigned row = L / 5u;
                        l2touch((row < 1536u ? p.w2 + row * 1280 : p.wo + (row - 1536u) * 1280) + (xc & 1) * 640 + (L - row * 5u) * 128);
                    }
                }
                STAMP(7);
            }
        } else {
            BLOCK_BARRIER();
        }
        if constexpr (DN || WO) {
            LANE_CONSTS();
            const unsigned bq = ((tb * 32 + (tp >> 1) * 16) ^ ((unsigned)(tq >> 1) << 6)) + (tp & 1) * 8;
            const unsigned drow = lane >> 3, dcol = ((lane & 7) ^ (((lane >> 4) & 1) << 2)) * 16;
            const int G = xc >> 1, r0 = G < 2 ? 6 * G : 12 + 5 * (G - 2), nr = G < 2 ? 6 : 5;
            if constexpr (FUSED) {
                if (b == 255 && v == 0) {
                    gu32* f = (gu32*)(ws + WS_HF) + (lane >> 3) * 64 + (lane & 7) * 4;
                    _Pragma("clang loop unroll(disable)") for (unsigned spins = 0; spins < (1u << 14); ++spins) {
                        bool ok = true;
#pragma unroll
                        for (int n = 0; n < 4; ++n) ok &= __hip_atomic_load(f + n * 512, RLX_AGENT) == tag;
                        if (__all(ok)) break;
                        TMO(spins, 1u << 14, 303, tag);
                        __builtin_amdgcn_s_sleep(32);
                    }
                    if (lane == 0) __hip_atomic_store((gu32*)(ws + WS_EP), tag, RLX_AGENT);
                }
            }
            if (j < 5 * nr) {
                const int dr = r0 + j / 5, dc = (xc & 1) * 5 + j % 5;
                const int mfirst = dr < 20 ? 3 * dr : 60 + 2 * (dr - 20), nm = dr < 20 ? 3 : 2;
                const unsigned dvo = drow * 1280 + dcol;
                const unsigned myring = L_PANEL + v * 20480;
                f32x16 acc0 = {}, acc1 = {};
                if constexpr (PRO) { lwait_all(C_GP, 4, 42, lane); lwait_all(C_AP, 4, 8, lane); }
                if constexpr (DN) {
                    f32x16 da[3][2] = {};
                    const int drot = (j % 5) * 4;
                    const char* bsrc = p.w2 + (size_t)v * 384 * 1280 + dc * 128;
                    const char* asrc = ws + WS_HT + ((size_t)mfirst * 96 + v * 24) * 1024;
                    const unsigned am1 = lane * 16 + 98304, am2 = lane * 16 + (nm == 3 ? 196608 : 98304);
                    constexpr int DQ = DN_DQ;
                    u32x4 aq[DQ][3];
                    auto bissue = [&](auto K) {
                        constexpr int k = K.value;
                        int kr = k + drot; kr = kr >= 24 ? kr - 24 : kr;
                        const char* sb = bsrc + (size_t)kr * 16 * 1280;
                        glds16(dvo, sb, myring + (k % 10) * 2048); glds16(dvo + 8 * 1280, sb, myring + (k % 10) * 2048 + 1024);
                    };
                    auto woissue = [&](auto U) {
                        constexpr int u = U.value;
                        const char* sb = p.wo + (size_t)(v * 160 + u * 16) * 1280 + dc * 128;
                        glds16(dvo, sb, myring + u * 2048); glds16(dvo + 8 * 1280, sb, myring + u * 2048 + 1024);
                    };
                    auto aissue = [&](auto K) {
                        constexpr int k = K.value;
                        int kr = k + drot; kr = kr >= 24 ? kr - 24 : kr;
                        const char* sb = asrc + (size_t)kr * 1024;
                        GLOAD16S(aq[k % DQ][0], lane * 16, sb); GLOAD16S(aq[k % DQ][1], am1, sb); GLOAD16S(aq[k % DQ][2], am2, sb);
                    };
                    static_for<0, DQ>(bissue);
                    if constexpr (PRO) {
                        gu32* f = (gu32*)(ws + WS_HF) + (mfirst + ((lane >> 3) < nm ? (lane >> 3) : 0)) * 32 + 8 * v + (lane & 7);
                        _Pragma("clang loop unroll(disable)") for (unsigned spins = 0; spins < (1u << 14); ++spins) {
                            if (__all(__hip_atomic_load(f, RLX_AGENT) == tag)) break;
                            TMO(spins, 1u << 14, 302, tag);
                            __builtin_amdgcn_s_sleep(HF_SLEEP);
                        }
                        CFENCE();
                    }
                    STAMP(8);
                    static_for<0, DQ>(aissue);
                    const unsigned ba0 = myring + (4 * h + tq) * 128 + bq, ba1 = ba0 ^ 64;
                    bf16x8 w0[2], w1[2];
                    WAITVN(3 * DQ);
                    w0[0] = ldstr2(ba0, ba0 + 1024); w1[0] = ldstr2(ba1, ba1 + 1024);
                    static_for<0, 24>([&](auto K) {
                        constexpr int k = K.value;
                        waitv3<dn_younger(k, DQ, WO)>(aq[k % DQ][0], aq[k % DQ][1], aq[k % DQ][2]);
                        constexpr unsigned o = ((k + 1) % 10) * 2048;
#define DXF(m) __builtin_bit_cast(bf16x8, aq[k % DQ][m])
                        SBAR();
                        MPRIO(DN_HI, 0); da[0][0] = MFMA(w0[k & 1], DXF(0), da[0][0]); SBAR();
                        if constexpr (k + 1 < 24) w0[(k + 1) & 1] = ldstr2(ba0 + o, ba0 + o + 1024);
                        SBAR();
                        MPRIO(DN_HI, 1); da[0][1] = MFMA(w1[k & 1], DXF(0), da[0][1]); SBAR();
                        if constexpr (k + 1 < 24) w1[(k + 1) & 1] = ldstr2(ba1 + o, ba1 + o + 1024);
                        SBAR();
                        MPRIO(DN_HI, 2); da[1][0] = MFMA(w0[k & 1], DXF(1), da[1][0]); SBAR();
                        if constexpr (k + DQ < 24) bissue(IC<k + DQ>{});
                        if constexpr (WO && k >= 16) woissue(IC<(k + 8) % 10>{});
                        SBAR();
                        MPRIO(DN_HI, 3); da[1][1] = MFMA(w1[k & 1], DXF(1), da[1][1]); SBAR();
                        MPRIO(DN_HI, 4); da[2][0] = MFMA(w0[k & 1], DXF(2), da[2][0]); SBAR();
                        MPRIO(DN_HI, 5); da[2][1] = MFMA(w1[k & 1], DXF(2), da[2][1]); SBAR();
                        if constexpr (k + DQ < 24) aissue(IC<k + DQ>{});
                    });
                    __builtin_amdgcn_s_setprio(3);
                    STAMP(9);
                    if constexpr (WO) { CFENCE(); woissue(IC<2>{}); woissue(IC<3>{}); }
#pragma unroll
                    for (int n = 0; n < 2; ++n) {
#pragma unroll
                        for (int m = 0; m < 3; ++m) {
                            if (m != v) {
                                const unsigned dst = L_GU + (m * 3 + (v > m ? v - 1 : v)) * 4096 + lane * 16;
#pragma unroll
                                for (int e = 0; e < 4; ++e) *LP(f32x4, dst + e * 1024) = f32x4{da[m][n][4 * e], da[m][n][4 * e + 1], da[m][n][4 * e + 2], da[m][n][4 * e + 3]};
                            }
                        }
                        lpost(C_DP + v, 2 * n + 1); lwait_all(C_DP, 4, 2 * n + 1, lane);
                        if (v < 3) {
                            f32x16 t = v == 0 ? da[0][n] : (v == 1 ? da[1][n] : da[2][n]);
#pragma unroll
                            for (int o = 0; o < 3; ++o)
#pragma unroll
                                for (int e = 0; e < 4; ++e) {
                                    const f32x4 u = *LP(const f32x4, L_GU + (v * 3 + o) * 4096 + e * 1024 + lane * 16);
                                    t[4 * e] += u[0]; t[4 * e + 1] += u[1]; t[4 * e + 2] += u[2]; t[4 * e + 3] += u[3];
                                }
                            if (n) acc1 = t; else acc0 = t;
                        }
                        if (n == 0) { lpost(C_DP + v, 2); lwait_all(C_DP, 4, 2, lane); }
                    }
                    if constexpr (!WO) {
                        if (v < 3) {
                            float* pt = (float*)(ws + WS_PT) + (size_t)(b * 3 + v) * 2048 + lane;
#pragma unroll
                            for (int e = 0; e < 16; ++e) { pt[e * 64] = acc0[e]; pt[1024 + e * 64] = acc1[e]; }
                        }
                    }
                } else {
#pragma unroll
                    for (int u = 0; u < 20; ++u) glds16(dvo + u * 8 * 1280, p.wo + (size_t)v * 160 * 1280 + dc * 128, myring + u * 1024);
                    if (v < 3) {
                        const float* pt = (const float*)(ws + WS_PT) + (size_t)(b * 3 + v) * 2048 + lane;
#pragma unroll
                        for (int e = 0; e < 16; ++e) { acc0[e] = pt[e * 64]; acc1[e] = pt[1024 + e * 64]; }
                    }
                }
                if constexpr (WO) { WAITV(0); lpost(C_DP + v, 4); }
                if constexpr (WO && ATT) {
                    if (v == 3) {
                        __builtin_amdgcn_s_setprio(0);
                        const int mi = (lane >> 4) < nm ? (lane >> 4) : 0;
                        gu64* f = (gu64*)(ws + WS_OF + (mfirst + mi) * 128) + (lane & 15);
                        _Pragma("clang loop unroll(disable)") for (unsigned spins = 0; spins < (1u << 14); ++spins) {
                            const unsigned long long x = __hip_atomic_load(f, RLX_AGENT);
                            const unsigned long long ok = __ballot((unsigned)x == tag && (unsigned)(x >> 32) == tag);
                            const unsigned mine = (unsigned)(ok >> ((lane & 3) * 16));
                            *((volatile AS3 unsigned*)lcnt(C_OS) + (lane & 3)) = ((mine & 0xffu) == 0xffu ? 1u : 0u) | ((mine & 0xff00u) == 0xff00u ? 2u : 0u);
                            if (ok == ~0ull) break;
                            TMO(spins, 1u << 14, 300, tag);
                            __builtin_amdgcn_s_sleep(OF_SLEEP);
                        }
                    }
                }
                if (v < nm) {
                    const int m = mfirst + v;
                    const unsigned ba0 = L_PANEL + (4 * h + tq) * 128 + bq, ba1 = ba0 ^ 64;
                    if constexpr (WO) {
                        const char* xrow = p.x + (size_t)(m * 32 + r) * 1280 + dc * 128 + h * 16;
                        u32x4 xres[4];
#pragma unroll
                        for (int e = 0; e < 4; ++e) xres[e] = *(const u32x4*)(xrow + e * 32);
                        lwait_all(C_DP, 4, 4, lane);
                        const __amdgpu_buffer_rsrc_t ro = mkrsrc(ws + WS_OT + (size_t)m * 40960, 40960);
                        int hf = 0;
                        f32x16 t0[2] = {}, t1[2] = {};
#pragma unroll
                        for (int it = 0; it < 2; ++it) {
                            if constexpr (ATT) {
                                _Pragma("clang loop unroll(disable)") for (unsigned spins = 0; spins < (1u << 17); ++spins) {
                                    const unsigned ok = __builtin_amdgcn_readfirstlane(*((volatile AS3 unsigned*)lcnt(C_OS) + v));
                                    if (it == 0) { if (ok) { hf = (ok & 1) ? 0 : 1; break; } }
                                    else if (ok == 3) break;
                                    __builtin_amdgcn_s_sleep(1);
                                }
                                CFENCE();
                            }
                            if (it == 0) STAMP(10);
                            u32x4 of[20];
#pragma unroll
                            for (int s = 0; s < 20; ++s) of[s] = __builtin_amdgcn_raw_buffer_load_b128(ro, lane * 16 + s * 1024, hf * 20480, 16);
                            const unsigned a0 = ba0 + hf * 40960, a1 = ba1 + hf * 40960, l0 = a0 + (36 * h + 16) * 128, l1 = a1 + (36 * h + 16) * 128;
                            bf16x8 w0[2], w1[2];
                            w0[0] = ldstr2(a0, a0 + 1024); w1[0] = ldstr2(a1, a1 + 1024);
                            static_for<0, 20>([&](auto S) {
                                constexpr int s = S.value, n = s + 1, gg = n / 5, jj = n % 5;
                                const bf16x8 xf = __builtin_bit_cast(bf16x8, of[s]);
                                if constexpr (n < 20) {
                                    if constexpr (jj < 4) {
                                        constexpr unsigned o = (80 * gg + 20 * jj) * 128;
                                        w0[n & 1] = ldstr2(a0 + o, a0 + o + 1024); w1[n & 1] = ldstr2(a1 + o, a1 + o + 1024);
                                    } else {
                                        constexpr unsigned o = 80 * gg * 128;
                                        w0[n & 1] = ldstr2(l0 + o, l0 + o + 2560); w1[n & 1] = ldstr2(l1 + o, l1 + o + 2560);
                                    }
                                }
                                SBAR();
                                t0[it] = MFMA(w0[s & 1], xf, t0[it]); t1[it] = MFMA(w1[s & 1], xf, t1[it]);
                                SBAR();
                            });
                            hf ^= 1;
                        }
                        acc0 += t0[0] + t0[1]; acc1 += t1[0] + t1[1];
                        char* orow = p.out + (size_t)(m * 32 + r) * 1280 + dc * 128 + h * 16;
#pragma unroll
                        for (int n = 0; n < 2; ++n) {
                            const f32x16& a = n ? acc1 : acc0;
#pragma unroll
                            for (int qq = 0; qq < 2; ++qq) {
                                float lo[4], hi[4];
#pragma unroll
                                for (int e = 0; e < 4; ++e) {
                                    auto sw = __builtin_amdgcn_permlane32_swap(__float_as_uint(a[8 * qq + e]), __float_as_uint(a[8 * qq + 4 + e]), false, false);
                                    lo[e] = __uint_as_float(sw[0]); hi[e] = __uint_as_float(sw[1]);
                                }
                                const u32x4 xv = xres[2 * n + qq];
                                const u32x4 ov = {pk2(lo[0] + bflo(xv[0]), lo[1] + bfhi(xv[0])), pk2(lo[2] + bflo(xv[1]), lo[3] + bfhi(xv[1])),
                                                  pk2(hi[0] + bflo(xv[2]), hi[1] + bfhi(xv[2])), pk2(hi[2] + bflo(xv[3]), hi[3] + bfhi(xv[3]))};
                                *(u32x4*)(orow + (2 * n + qq) * 32) = ov;
                            }
                        }
                        STAMP(11);
                    }
                }
            }
        }
    } else {
        LANE_CONSTS();
        bf16x8 Q0a, Q0b; u32x2 q1a, q1b;
        const int head = 4 * g + w;
        const AttSync as{lane, ws + WS_KV, (unsigned)((w < 3 ? w * 3072 : CHUNK - 3072) + lane * 16), (unsigned)(w < 3 ? w * 3072 : CHUNK - 3072), w};
        __builtin_amdgcn_s_setprio(3);
        unsigned long long fk = 0, fv = 0; bool early = false;
        if constexpr (PRO) {
            BLOCK_BARRIER();
            waitv5<15>(x0[0], x0[1], x0[2], x0[3], x0[4]);
            STAMP(1);
            ln_rows(x0, ra, lane);
            waitv5<10>(x1[0], x1[1], x1[2], x1[3], x1[4]);
            ln_rows(x1, ra + 4, lane);
            lpost(C_AP + w, 1);
            waitv5<5>(x2[0], x2[1], x2[2], x2[3], x2[4]);
            ln_rows(x2, rb, lane);
            waitv5<0>(x3[0], x3[1], x3[2], x3[3], x3[4]);
            ln_rows(x3, rb + 4, lane);
            lpost(C_AP + w, 2);
            unsigned pa[4];
#pragma unroll
            for (int s = 0; s < 4; ++s) pa[s] = L_PANEL + h * 512 + ((r ^ ((2 * s + h) & 7)) << 4);
            f32x16 qa = {}, qb = {};
            const unsigned wa = L_WQ + (8 * h + tq) * 160 + w * 40 + tb * 32 + (tb ? 0 : tp * 8);
            bf16x8 fw[3][4], fa[2][4], fb[2][4];
            auto rdw = [&](auto S, auto S4) { constexpr int st = S.value, s4 = S4.value; const unsigned o = wa + (st & 3) * 10240 + s4 * 2560; fw[st % 3][s4] = ldstr2(o, o + 640); };
            lwait_all(C_PP, 4, 1, lane);
            static_for<0, 4>([&](auto I) { rdw(IC<0>{}, I); rdw(IC<1>{}, I); });
            if constexpr (FUSED) tag = __builtin_amdgcn_readfirstlane(*(volatile AS3 unsigned*)lcnt(C_TAG));
            lpost(C_AP + w, 3);
            lwait_all(C_AP, 4, 2, lane);
            STAMP(2);
#pragma unroll
            for (int s4 = 0; s4 < 4; ++s4) { fa[0][s4] = ldsf(pa[s4] + s4 * 1024); fb[0][s4] = ldsf(pa[s4] + 40960 + s4 * 1024); }
            unsigned pk = 0;
            static_for<0, 10>([&](auto S) {
                constexpr int st = S.value, nx = st + 1;
                if constexpr (st == 3 || st == 5 || st == 7) lcheck(pk, C_WP, (st - 1) / 2, lane);
                if constexpr (st == KV_PEEK && ATT) gload8x2_sc1(fk, fv, lane * 8, ws + WS_KF + xc * 1024);
                if constexpr (st == KV_CHECK && ATT) {
                    waitv8x2(fk, fv);
                    early = __all((unsigned)(fk >> 32) == tag && (unsigned)(fv >> 32) == tag);
                    if (early) { lwait_all(C_AP, 4, 6, lane); att_dma(as, 0); att_dma(as, 1); }
                    STAMP(8);
                }
                static_for<0, 4>([&](auto S4) {
                    constexpr int s4 = S4.value;
                    qa = MFMA(fw[st % 3][s4], fa[st & 1][s4], qa); SBAR();
                    if constexpr (st == 0) rdw(IC<2>{}, S4);
                    if constexpr (st == 1) rdw(IC<3>{}, S4);
                    if constexpr (st == 3 || st == 5 || st == 7) { rdw(IC<st + 1>{}, S4); rdw(IC<st + 2>{}, S4); }
                    SBAR();
                    qb = MFMA(fw[st % 3][s4], fb[st & 1][s4], qb); SBAR();
                    if constexpr (nx < 10) { fa[nx & 1][s4] = ldsf(pa[s4] + (nx * 4 + s4) * 1024); fb[nx & 1][s4] = ldsf(pa[s4] + 40960 + (nx * 4 + s4) * 1024); SBAR(); }
                    if constexpr (s4 == 2 && (st == 2 || st == 4 || st == 6)) { pk = lpeek(C_WP, lane); SBAR(); }
                });
                if constexpr (st == 1 || st == 3 || st == 5) lpost(C_AP + w, 4 + st / 2);
            });
            lpost(C_AP + w, 8);
            Q0a = pack8(qa, 0, QSCALE); Q0b = pack8(qb, 0, QSCALE);
            q1a = u32x2{pk2(qa[8] * QSCALE, qa[9] * QSCALE), pk2(qa[10] * QSCALE, qa[11] * QSCALE)};
            q1b = u32x2{pk2(qb[8] * QSCALE, qb[9] * QSCALE), pk2(qb[10] * QSCALE, qb[11] * QSCALE)};
            if (h) { q1a = u32x2{0u, 0u}; q1b = u32x2{0u, 0u}; }
            if constexpr (!ATT) {
                char* qt = ws + WS_QT + (size_t)((2 * ti) * 32 + head) * 2048 + lane * 16;
                *(bf16x8*)qt = Q0a; *(u32x2*)(qt + 1024) = q1a; *(bf16x8*)(qt + 65536) = Q0b; *(u32x2*)(qt + 65536 + 1024) = q1b;
            }
            STAMP(3);
        } else {
            BLOCK_BARRIER();
            if constexpr (ATT) {
                const char* qt = ws + WS_QT + (size_t)((2 * ti) * 32 + head) * 2048 + lane * 16;
                Q0a = *(const bf16x8*)qt; q1a = *(const u32x2*)(qt + 1024); Q0b = *(const bf16x8*)(qt + 65536); q1b = *(const u32x2*)(qt + 65536 + 1024);
            }
        }
        if constexpr (ATT) {
            float na = 0.f, nb = 0.f;
            {
                const u32x4 a0 = __builtin_bit_cast(u32x4, Q0a), b0 = __builtin_bit_cast(u32x4, Q0b);
#pragma unroll
                for (int e = 0; e < 4; ++e) { na += bflo(a0[e]) * bflo(a0[e]) + bfhi(a0[e]) * bfhi(a0[e]); nb += bflo(b0[e]) * bflo(b0[e]) + bfhi(b0[e]) * bfhi(b0[e]); }
#pragma unroll
                for (int e = 0; e < 2; ++e) { na += bflo(q1a[e]) * bflo(q1a[e]) + bfhi(q1a[e]) * bfhi(q1a[e]); nb += bflo(q1b[e]) * bflo(q1b[e]) + bfhi(q1b[e]) * bfhi(q1b[e]); }
                const auto sa = __builtin_amdgcn_permlane32_swap(__float_as_uint(na), __float_as_uint(na), false, false), sb = __builtin_amdgcn_permlane32_swap(__float_as_uint(nb), __float_as_uint(nb), false, false);
                na = __uint_as_float(sa[0]) + __uint_as_float(sa[1]); nb = __uint_as_float(sb[0]) + __uint_as_float(sb[1]);
            }
            if (!early && w == 0) {
                gu64* f = (gu64*)(ws + WS_KF + xc * 1024) + lane;
                _Pragma("clang loop unroll(disable)") for (unsigned spins = 0; spins < (1u << 14); ++spins) {
                    fk = __hip_atomic_load(f, RLX_AGENT); fv = __hip_atomic_load(f + 64, RLX_AGENT);
                    if (__all((unsigned)(fk >> 32) == tag && (unsigned)(fv >> 32) == tag)) break;
                    TMO(spins, 1u << 14, 301, tag);
                    __builtin_amdgcn_s_sleep(4);
                }
            }
            float kn2 = wave_max(__uint_as_float((unsigned)fk)), vmx = wave_max(__uint_as_float((unsigned)fv));
            if (w == 0) { lpost(C_KN, __float_as_uint(kn2)); lpost(C_VM, __float_as_uint(vmx)); lpost(C_KVRDY, 1); }
            if (!early) {
                lwait(C_KVRDY, 1);
                kn2 = __uint_as_float(*(volatile AS3 unsigned*)lcnt(C_KN)); vmx = __uint_as_float(*(volatile AS3 unsigned*)lcnt(C_VM));
                if constexpr (PRO) lwait_all(C_AP, 4, 6, lane);
                att_dma(as, 0); att_dma(as, 1);
            }
            STAMP(4);
            const float Ma = __builtin_amdgcn_sqrtf(na * kn2) * 1.01f, Mb = __builtin_amdgcn_sqrtf(nb * kn2) * 1.01f;
            const bool fast = __all(Ma <= BOUND_MAX && Mb <= BOUND_MAX && vmx <= 1e30f);
            const u32x2 a1 = {(fast && h) ? pk2(-Ma, 0.f) : q1a[0], q1a[1]}, b1 = {(fast && h) ? pk2(-Mb, 0.f) : q1b[0], q1b[1]};
            f32x16 Oa = {}, Ob = {};
            WAITV(3); lpost(C_AP + w, AP0); lwait_all(C_AP, 4, AP0, lane);
            att_dma(as, 2);
            __builtin_amdgcn_s_setprio(ATT_PRIO);
            STAMP(5); STAMPC(12);
            if (fast) att_fast(as, Q0a, __builtin_bit_cast(s16x4, a1), Q0b, __builtin_bit_cast(s16x4, b1), Oa, Ob);
            else att_safe(as, Q0a, __builtin_bit_cast(s16x4, a1), Q0b, __builtin_bit_cast(s16x4, b1), Oa, Ob);
            STAMP(6); STAMPC(13);
            const __amdgpu_buffer_rsrc_t ro = mkrsrc(ws + WS_OT, 64 * 40 * 1024);
#pragma unroll
            for (int q = 0; q < 2; ++q) {
                const f32x16& O = q ? Ob : Oa;
                const auto lsw = __builtin_amdgcn_permlane32_swap(__float_as_uint(O[8]), __float_as_uint(O[8]), false, false);
                const float inv = __builtin_amdgcn_rcpf(__uint_as_float(lsw[1]));
                const unsigned base = ((2 * ti + q) * 40 + 5 * g) * 1024;
                __builtin_amdgcn_raw_buffer_store_b128(__builtin_bit_cast(u32x4, pack8(O, 0, inv)), ro, base + w * 1024 + lane * 16, 0, 16);
                if (!h) {
                    u32x2 t = {pk2(O[8] * inv, O[9] * inv), pk2(O[10] * inv, O[11] * inv)};
                    __builtin_amdgcn_raw_buffer_store_b64(t, ro, base + 4096 + ((w >> 1) * 32 + r) * 16 + (w & 1) * 8, 0, 16);
                }
            }
            if constexpr (WO) {
                WAITV(0);
                if (lane < 2) __hip_atomic_store((gu32*)(ws + WS_OF) + (2 * ti + lane) * 32 + head, tag, RLX_AGENT);
            }
            STAMP(7);
        }
    }
}

template <int PH>
static void launch(const Params& p, hipStream_t stream) {
    (void)hipFuncSetAttribute((const void*)mega<PH>, hipFuncAttributeMaxDynamicSharedMemorySize, L_TOTAL);
    mega<PH><<<256, 512, L_TOTAL, stream>>>(p);
}

extern "C" void kernel_launch(void* const* d_in, const int* in_sizes, int n_in, void* d_out, int out_size, void* d_ws, size_t ws_size, hipStream_t stream) {
    Params p;
    p.x = (const char*)d_in[0]; p.wq = (const char*)d_in[1]; p.wk = (const char*)d_in[2]; p.wv = (const char*)d_in[3]; p.wo = (const char*)d_in[4];
    p.w1 = (const char*)d_in[6]; p.w3 = (const char*)d_in[7]; p.w2 = (const char*)d_in[8];
    p.out = (char*)d_out; p.ws = (char*)d_ws;
    launch<27>(p, stream);
}
```

```cpp
#include <hip/hip_runtime.h>
#include <hip/hip_bf16.h>
#include <stdint.h>

#define AS1 __attribute__((address_space(1)))
#define AS3 __attribute__((address_space(3)))
typedef __bf16 bf16x2 __attribute__((ext_vector_type(2)));
typedef __bf16 bf16x8 __attribute__((ext_vector_type(8)));
typedef short s16x4 __attribute__((ext_vector_type(4)));
typedef short s16x8 __attribute__((ext_vector_type(8)));
typedef float f32x2 __attribute__((ext_vector_type(2)));
typedef float f32x4 __attribute__((ext_vector_type(4)));
typedef float f32x16 __attribute__((ext_vector_type(16)));
typedef unsigned u32x2 __attribute__((ext_vector_type(2)));
typedef unsigned u32x4 __attribute__((ext_vector_type(4)));
typedef AS1 unsigned gu32;
typedef AS1 unsigned long long gu64;

constexpr unsigned WS_KF = 0;
constexpr unsigned WS_HF = 16384;
constexpr unsigned WS_OF = 32768;
constexpr unsigned WS_EP = 49152;
constexpr unsigned KVD = 5376;
constexpr unsigned KV_K1 = 2048, KV_V = 2560, KV_VT = 1408;
constexpr unsigned WS_KV = 65536;
constexpr unsigned WS_HT = WS_KV + 176128;
constexpr unsigned WS_OT = WS_HT + 64 * 96 * 1024;
constexpr unsigned L_PANEL = 0;
constexpr unsigned L_KV = 81920;
constexpr unsigned L_WQ = 81920;
constexpr unsigned L_GU = 124928;
constexpr unsigned L_WKV = 124928;
constexpr unsigned L_MISC = 161920;
constexpr unsigned L_TOTAL = 163840;
constexpr unsigned CHUNK = 2 * KVD;
constexpr int NCHUNK = 16;
enum { C_AP = 0  , C_GP = 4  , C_DP = 8  , C_PP = 12  , C_WP = 16  , C_KVP = 20, C_KVFIN, C_TAG, C_KVRDY, C_OS = 24  , C_KN = 28, C_VM, C_N };

struct Params { const char* x; const char* wq; const char* wk; const char* wv; const char* wo; const char* w1; const char* w3; const char* w2; char* out; char* ws; };


#define SBAR() __builtin_amdgcn_sched_barrier(0)
#define PIN(x) asm volatile("" : "+v"(x))
#define CFENCE() asm volatile("" ::: "memory")
#define WAITV(n) asm volatile("s_waitcnt vmcnt(" #n ")" ::: "memory")
#define MFMA(a, b, c) __builtin_amdgcn_mfma_f32_32x32x16_bf16((a), (b), (c), 0, 0, 0)
#define MFMA8(a, b, c) __builtin_amdgcn_mfma_f32_32x32x8bf16_1k((a), (b), (c), 0, 0, 0)
#define RLX_AGENT __ATOMIC_RELAXED, __HIP_MEMORY_SCOPE_AGENT
#define RLX_WG __ATOMIC_RELAXED, __HIP_MEMORY_SCOPE_WORKGROUP

#define LP(T, off) ((AS3 T*)(uintptr_t)(unsigned)(off))
__device__ __forceinline__ bf16x8 ldsf(unsigned off) { return *LP(const bf16x8, off); }
__device__ __forceinline__ s16x4 ldstr(unsigned off) { return __builtin_amdgcn_ds_read_tr16_b64_v4i16(LP(s16x4, off)); }
__device__ __forceinline__ bf16x8 ldstr2(unsigned off0, unsigned off1) {
    s16x4 a = ldstr(off0), b = ldstr(off1);
    return __builtin_bit_cast(bf16x8, __builtin_shufflevector(a, b, 0, 1, 2, 3, 4, 5, 6, 7));
}
template <int E> __device__ __forceinline__ s16x4 half4(bf16x8 v) { const s16x8 t = __builtin_bit_cast(s16x8, v); return __builtin_shufflevector(t, t, 4 * E, 4 * E + 1, 4 * E + 2, 4 * E + 3); }
__device__ __forceinline__ unsigned pk2(float lo, float hi) { bf16x2 v = {(__bf16)lo, (__bf16)hi}; return __builtin_bit_cast(unsigned, v); }
__device__ __forceinline__ float bflo(unsigned u) { return __uint_as_float(u << 16); }
__device__ __forceinline__ float bfhi(unsigned u) { return __uint_as_float(u & 0xffff0000u); }
__device__ __forceinline__ float rbf(float f) { return (float)(__bf16)f; }

__device__ __forceinline__ void glds16(unsigned voff, const char* sbase, unsigned ldsdst) {
    unsigned keep;
    asm volatile("s_mov_b32 %0, m0\n\ts_mov_b32 m0, %3\n\ts_nop 2\n\tglobal_load_lds_dwordx4 %1, %2\n\ts_mov_b32 m0, %0"
                 : "=&s"(keep) : "v"(voff), "s"(sbase), "s"(ldsdst) : "memory");
}
__device__ __forceinline__ void glds16x3_sc1(unsigned voff, const char* sbase, unsigned ldsdst) {
    unsigned keep;
    asm volatile("s_mov_b32 %0, m0\n\ts_mov_b32 m0, %3\n\ts_nop 2\n\tglobal_load_lds_dwordx4 %1, %2 sc1\n\tglobal_load_lds_dwordx4 %1, %2 offset:1024 sc1\n\tglobal_load_lds_dwordx4 %1, %2 offset:2048 sc1\n\ts_mov_b32 m0, %0"
                 : "=&s"(keep) : "v"(voff), "s"(sbase), "s"(ldsdst) : "memory");
}
__device__ __forceinline__ void glds_gu(unsigned va, unsigned vb, unsigned vc, const char* s1, const char* s3, unsigned ldsdst) {
    unsigned keep;
    asm volatile("s_mov_b32 %0, m0\n\ts_mov_b32 m0, %8\n\ts_nop 0\n\tglobal_load_lds_dwordx4 %1, %4\n\tglobal_load_lds_dwordx4 %2, %5 offset:1024\n\t"
                 "s_mov_b32 exec_hi, 0\n\tglobal_load_lds_dwordx4 %3, %6 offset:2048\n\ts_not_b64 exec, exec\n\tglobal_load_lds_dwordx4 %3, %7 offset:2048\n\t"
                 "s_mov_b64 exec, -1\n\ts_mov_b32 m0, %0"
                 : "=&s"(keep) : "v"(va), "v"(vb), "v"(vc), "s"(s1), "s"(s3 - 1024), "s"(s1 - 2048), "s"(s3 - 2048), "s"(ldsdst) : "memory", "scc");
}
__device__ __forceinline__ void l2touch(const char* ga) {
    unsigned keep;
    asm volatile("s_mov_b32 %0, m0\n\ts_mov_b32 m0, %2\n\ts_nop 0\n\tglobal_load_lds_dword %1, off\n\ts_mov_b32 m0, %0" : "=&s"(keep) : "v"(ga), "s"(L_MISC + 1024) : "memory");
}
__device__ __forceinline__ AS3 unsigned* lcnt(int c) { return LP(unsigned, L_MISC) + c; }
__device__ __forceinline__ void lwait(int c, unsigned target) {
    CFENCE();
    _Pragma("clang loop unroll(disable)") for (unsigned spins = 0; spins < (1u << 22); ++spins) {
        if ((unsigned)__builtin_amdgcn_readfirstlane(__hip_atomic_load(lcnt(c), RLX_WG)) >= target) break;
        __builtin_amdgcn_s_sleep(1);
    }
    CFENCE();
}
__device__ __forceinline__ void lwait_all(int c, int n, unsigned target, int lane) {
    CFENCE();
    AS3 unsigned* q = lcnt(c) + (lane < n ? lane : 0);
    _Pragma("clang loop unroll(disable)") for (unsigned spins = 0; spins < (1u << 16); ++spins) {
        if (__all(__hip_atomic_load(q, RLX_WG) >= target)) break;
        __builtin_amdgcn_s_sleep(1);
    }
    CFENCE();
}
__device__ __forceinline__ void lpost(int c, unsigned v) { CFENCE(); *(volatile AS3 unsigned*)lcnt(c) = v; CFENCE(); }
__device__ __forceinline__ unsigned lpeek(int c, int lane) { return *((volatile AS3 unsigned*)lcnt(c) + (lane & 3)); }
__device__ __forceinline__ void lcheck(unsigned pk, int c, unsigned target, int lane) {
    if (__builtin_expect(__any(pk < target), 0)) lwait_all(c, 4, target, lane);
    CFENCE();
}
__device__ __forceinline__ __amdgpu_buffer_rsrc_t mkrsrc(const char* base, unsigned bytes) {
    return __builtin_amdgcn_make_buffer_rsrc((void*)base, 0, bytes, 0x00020000);
}
__device__ __forceinline__ float dpp_sum16(float v) {
    v += __int_as_float(__builtin_amdgcn_update_dpp(0, __float_as_int(v), 0x128, 0xf, 0xf, false));
    v += __int_as_float(__builtin_amdgcn_update_dpp(0, __float_as_int(v), 0x124, 0xf, 0xf, false));
    v += __int_as_float(__builtin_amdgcn_update_dpp(0, __float_as_int(v), 0x122, 0xf, 0xf, false));
    v += __int_as_float(__builtin_amdgcn_update_dpp(0, __float_as_int(v), 0x121, 0xf, 0xf, false));
    return v;
}
__device__ __forceinline__ float wave_max(float v) {
    int x = __float_as_int(v);
    x = max(x, __builtin_amdgcn_update_dpp(0, x, 0x128, 0xf, 0xf, false)); x = max(x, __builtin_amdgcn_update_dpp(0, x, 0x124, 0xf, 0xf, false));
    x = max(x, __builtin_amdgcn_update_dpp(0, x, 0x122, 0xf, 0xf, false)); x = max(x, __builtin_amdgcn_update_dpp(0, x, 0x121, 0xf, 0xf, false));
    const int a = max(__builtin_amdgcn_readlane(x, 0), __builtin_amdgcn_readlane(x, 16)), b = max(__builtin_amdgcn_readlane(x, 32), __builtin_amdgcn_readlane(x, 48));
    return __int_as_float(max(a, b));
}
__device__ __forceinline__ bf16x8 pack8(const f32x16& a, int b, float sc) {
    u32x4 r = {pk2(a[b] * sc, a[b + 1] * sc), pk2(a[b + 2] * sc, a[b + 3] * sc), pk2(a[b + 4] * sc, a[b + 5] * sc), pk2(a[b + 6] * sc, a[b + 7] * sc)};
    return __builtin_bit_cast(bf16x8, r);
}

template <int I> struct IC { static constexpr int value = I; };
template <int B, int E, class F> __device__ __forceinline__ void static_for(F&& f) { if constexpr (B < E) { f(IC<B>{}); static_for<B + 1, E>(f); } }
#define WAITVN(n) asm volatile("s_waitcnt vmcnt(%0)" :: "i"(n) : "memory")
__device__ __forceinline__ void GLOAD16S(u32x4& dst, unsigned voff, const char* sbase) { asm volatile("global_load_dwordx4 %0, %1, %2 sc1" : "=v"(dst) : "v"(voff), "s"(sbase) : "memory"); }
__device__ __forceinline__ void GLOAD16N(u32x4& dst, unsigned voff, const char* sbase) { asm volatile("global_load_dwordx4 %0, %1, %2" : "=v"(dst) : "v"(voff), "s"(sbase) : "memory"); }
template <int N> __device__ __forceinline__ void waitv5(u32x4& a, u32x4& b, u32x4& c, u32x4& d, u32x4& e) { asm volatile("s_waitcnt vmcnt(%5)" : "+v"(a), "+v"(b), "+v"(c), "+v"(d), "+v"(e) : "i"(N) : "memory"); }
__device__ __forceinline__ void gload8x2_sc1(unsigned long long& a, unsigned long long& b, unsigned voff, const char* sbase) {
    asm volatile("global_load_dwordx2 %0, %2, %3 sc1\n\tglobal_load_dwordx2 %1, %2, %3 offset:512 sc1" : "=&v"(a), "=&v"(b) : "v"(voff), "s"(sbase) : "memory"); }
__device__ __forceinline__ void waitv8x2(unsigned long long& a, unsigned long long& b) { asm volatile("s_waitcnt vmcnt(0)" : "+v"(a), "+v"(b) :: "memory"); }
template <int N> __device__ __forceinline__ void waitv3(u32x4& a, u32x4& b, u32x4& c) { asm volatile("s_waitcnt vmcnt(%3)" : "+v"(a), "+v"(b), "+v"(c) : "i"(N) : "memory"); }

constexpr int dn_younger(int k, int D) {
    int n = 0, la = 0, lb = 0;
    for (int i = 0; i < D; ++i) { n += 2; if (i == k + 1) lb = n; }
    for (int i = 0; i < D; ++i) { n += 3; if (i == k) la = n; }
    for (int s = 0; s < k; ++s) {
        if (s + D < 24) { n += 2; if (s + D == k + 1) lb = n; }
        if (s >= 16) n += 2;
        if (s + D < 24) { n += 3; if (s + D == k) la = n; }
    }
    return n - (la > lb ? la : lb);
}
constexpr float QSCALE = 0.22360679774997896f * 1.4426950408889634f;
constexpr float BOUND_MAX = 50.f;

struct AttSync { int lane; const char* kv; unsigned dvoff; unsigned wo; int w; };
__device__ __forceinline__ unsigned kvslot(int c) { return L_KV + ((c + 2) & 3) * CHUNK; }
__device__ __forceinline__ void att_dma(const AttSync& a, int c) { glds16x3_sc1(a.dvoff, a.kv + (size_t)c * CHUNK, kvslot(c) + a.wo); }
constexpr unsigned AP0 = 16;
#define RING_X(c) do { WAITV(3); lpost(C_AP + as.w, AP0 + (c) + 1); } while (0)
#define RING_Y(c, pk) do { lcheck(pk, C_AP, AP0 + (c) + 1, lane); const int cn_ = (c) + 3 < NCHUNK ? (c) + 3 : NCHUNK - 1;   \
                           glds16x3_sc1(as.dvoff, as.kv + (size_t)cn_ * CHUNK, kvslot((c) + 3) + as.wo); } while (0)

__device__ __forceinline__ void att_fast(const AttSync& as, bf16x8 Q0a, s16x4 Q1a, bf16x8 Q0b, s16x4 Q1b, f32x16& Oa, f32x16& Ob) {
    const int lane = as.lane, r = lane & 31, h = lane >> 5;
    const unsigned ak0 = lane * 16, ak1 = h ? KV_V + 20 * 16 : KV_K1 + r * 16;
    const unsigned av = KV_V + h * 352 + (r <= 20 ? r : 21) * 16;
    const f32x16 zero = {};
    f32x16 Sa, Sb = {};
    bf16x8 K0[2], K1[2], V0[2], V1[2];
    u32x4 Pa0, Pa1, Pb0 = {}, Pb1 = {};
    unsigned pk = 0;
    V0[1] = bf16x8{}; V1[1] = bf16x8{};
    unsigned vk0 = L_KV + ak0, vk1 = L_KV + ak1, vv = L_KV + av;
    PIN(vk0); PIN(vk1); PIN(vv);
    K0[0] = ldsf(vk0 + 2 * CHUNK); K1[0] = ldsf(vk1 + 2 * CHUNK); K0[1] = ldsf(vk0 + 2 * CHUNK + 1024); V1[0] = ldsf(vv + 2 * CHUNK + 704); V0[0] = ldsf(vv + 2 * CHUNK);
    Sa = MFMA(K0[0], Q0a, zero); Sa = MFMA8(half4<0>(K1[0]), Q1a, Sa);
#define EX4(S, i) do { S[i] = __builtin_amdgcn_exp2f(S[i]); S[i + 1] = __builtin_amdgcn_exp2f(S[i + 1]); S[i + 2] = __builtin_amdgcn_exp2f(S[i + 2]); S[i + 3] = __builtin_amdgcn_exp2f(S[i + 3]); } while (0)
#define GAP(MF, LD, T, k, PW, j, S, i) do { MF; SBAR(); LD; PW[j] = pk2(T[k], T[k + 1]); PW[j + 1] = pk2(T[k + 2], T[k + 3]); PIN(PW); SBAR(); EX4(S, i); PIN(S); SBAR(); } while (0)
#define BC(x) __builtin_bit_cast(bf16x8, x)
#define STEP(e, d, KN, L3, LK0, LK1, LV) do { \
        GAP(Ob = MFMA(V0[e ^ 1], BC(Pb0), Ob), (void)0,                  Sb, 12, Pb1, 2, Sa, 0); \
        GAP(Sb = MFMA(K0[e], Q0b, zero),       (void)0,                  Sa, 0, Pa0, 0, Sa, 4); \
        GAP(Sb = MFMA8(half4<e>(K1[d]), Q1b, Sb), L3,                       Sa, 4, Pa0, 2, Sa, 8); \
        GAP(Ob = MFMA(V1[e ^ 1], BC(Pb1), Ob), K0[e] = ldsf(LK0),        Sa, 8, Pa1, 0, Sa, 12); \
        GAP(Oa = MFMA(V0[e], BC(Pa0), Oa),     LK1,                      Sa, 12, Pa1, 2, Sb, 0); \
        GAP(Sa = MFMA(K0[e ^ 1], Q0a, zero),   V1[e ^ 1] = ldsf((LV) + 704), Sb, 0, Pb0, 0, Sb, 4); \
        GAP(Sa = MFMA8(half4<e ^ 1>(K1[KN]), Q1a, Sa), V0[e ^ 1] = ldsf(LV),     Sb, 4, Pb0, 2, Sb, 8); \
        GAP(Oa = MFMA(V1[e], BC(Pa1), Oa),     (void)0,                  Sb, 8, Pb1, 0, Sb, 12); \
    } while (0)
#define CHUNK4(c, B, N) do { \
        STEP(0, 0, 0, (void)0, vk0 + (B) + KVD, K1[1] = ldsf(vk1 + (B) + KVD), vv + (B) + KV_VT); \
        RING_X(c); \
        STEP(1, 0, 1, pk = lpeek(C_AP, lane), vk0 + (B) + KVD + 1024, (void)0, vv + (B) + KVD); \
        RING_Y(c, pk); \
        STEP(0, 1, 1, (void)0, vk0 + (N), K1[0] = ldsf(vk1 + (N)), vv + (B) + KVD + KV_VT); \
        STEP(1, 1, 0, (void)0, vk0 + (N) + 1024, (void)0, vv + (N)); \
    } while (0)
    for (int c = 0; c < NCHUNK; c += 4) {
        CHUNK4(c, 2 * CHUNK, 3 * CHUNK); CHUNK4(c + 1, 3 * CHUNK, 0); CHUNK4(c + 2, 0, CHUNK); CHUNK4(c + 3, CHUNK, 2 * CHUNK);
    }
#undef CHUNK4
    Pb1[2] = pk2(Sb[12], Sb[13]); Pb1[3] = pk2(Sb[14], Sb[15]);
    Ob = MFMA(V0[1], BC(Pb0), Ob); Ob = MFMA(V1[1], BC(Pb1), Ob);
#undef STEP
#undef GAP
#undef EX4
}

__device__ __forceinline__ void att_safe(const AttSync& as, bf16x8 Q0a, s16x4 Q1a, bf16x8 Q0b, s16x4 Q1b, f32x16& Oa, f32x16& Ob) {
    const int lane = as.lane, r = lane & 31, h = lane >> 5;
    const unsigned ak0 = lane * 16, ak1 = h ? KV_V + 20 * 16 : KV_K1 + r * 16;
    const unsigned av = KV_V + h * 352 + (r <= 20 ? r : 21) * 16;
    const f32x16 zero = {};
    float ma = -INFINITY, mb = -INFINITY;
    for (int c = 0; c < NCHUNK; ++c) {
        const unsigned b0 = kvslot(c);
        for (int t = 0; t < 4; ++t) {
            if (t == 1) RING_X(c);
            if (t == 2) RING_Y(c, 0u);
            const unsigned base = b0 + (t >> 1) * KVD; const int e = t & 1;
            bf16x8 K0 = ldsf(base + ak0 + e * 1024), K1 = ldsf(base + ak1), V0 = ldsf(base + av + e * KV_VT), V1 = ldsf(base + av + e * KV_VT + 704);
#pragma unroll
            for (int q = 0; q < 2; ++q) {
                f32x16 S = MFMA(K0, q ? Q0b : Q0a, zero); S = MFMA8(e ? half4<1>(K1) : half4<0>(K1), q ? Q1b : Q1a, S);
                float mx = S[0];
#pragma unroll
                for (int i = 1; i < 16; ++i) mx = fmaxf(mx, S[i]);
                mx = fmaxf(mx, __shfl_xor(mx, 32));
                float& m = q ? mb : ma; f32x16& O = q ? Ob : Oa;
                const float mn = fmaxf(m, mx);
                const float al = __builtin_amdgcn_exp2f(m - mn);
                m = mn;
#pragma unroll
                for (int i = 0; i < 16; ++i) { O[i] *= al; S[i] = __builtin_amdgcn_exp2f(S[i] - mn); }
                O = MFMA(V0, pack8(S, 0, 1.f), O); O = MFMA(V1, pack8(S, 8, 1.f), O);
            }
        }
    }
}

__device__ __forceinline__ void xload8(u32x4 (&x)[5], u32x4 (&y)[5], const char* xb, unsigned vo, unsigned vo2, int skip) {
    asm volatile("s_cmp_lg_u32 %13, 0\n\ts_cbranch_scc1 1f\n\t"
                 "global_load_dwordx4 %0, %10, %12\n\tglobal_load_dwordx4 %1, %10, %12 offset:256\n\tglobal_load_dwordx4 %2, %10, %12 offset:512\n\t"
                 "global_load_dwordx4 %3, %10, %12 offset:768\n\tglobal_load_dwordx4 %4, %10, %12 offset:1024\n\t"
                 "global_load_dwordx4 %5, %11, %12\n\tglobal_load_dwordx4 %6, %11, %12 offset:256\n\tglobal_load_dwordx4 %7, %11, %12 offset:512\n\t"
                 "global_load_dwordx4 %8, %11, %12 offset:768\n\tglobal_load_dwordx4 %9, %11, %12 offset:1024\n1:"
                 : "=&v"(x[0]), "=&v"(x[1]), "=&v"(x[2]), "=&v"(x[3]), "=&v"(x[4]), "=&v"(y[0]), "=&v"(y[1]), "=&v"(y[2]), "=&v"(y[3]), "=&v"(y[4])
                 : "v"(vo), "v"(vo2), "s"(xb), "s"(skip) : "memory", "scc");
}
__device__ __forceinline__ void ln_rows(const u32x4 (&xr)[5], int row0, int lane) {
    f32x2 xv[20];
    f32x2 s0 = {0.f, 0.f}, s1 = {0.f, 0.f}, q0 = {0.f, 0.f}, q1 = {0.f, 0.f};
#pragma unroll
    for (int i = 0; i < 20; i += 2) {
        const unsigned a = xr[i >> 2][i & 3], b = xr[i >> 2][(i & 3) + 1];
        xv[i] = f32x2{bflo(a), bfhi(a)}; xv[i + 1] = f32x2{bflo(b), bfhi(b)};
        s0 += xv[i]; q0 = __builtin_elementwise_fma(xv[i], xv[i], q0);
        s1 += xv[i + 1]; q1 = __builtin_elementwise_fma(xv[i + 1], xv[i + 1], q1);
    }
    s0 += s1; q0 += q1;
    const float s = dpp_sum16(s0[0] + s0[1]), ss = dpp_sum16(q0[0] + q0[1]);
    const float mean = s * (1.f / 640.f);
    const float var = fmaxf(ss * (1.f / 640.f) - mean * mean, 0.f);
    const float rs = __builtin_amdgcn_rcpf(rbf(__builtin_amdgcn_sqrtf(rbf(rbf(var) + 1e-5f)))), mo = -rbf(mean) * rs;
    const f32x2 rs2 = {rs, rs}, mo2 = {mo, mo};
    const int row = row0 + (lane >> 4), c = lane & 15;
    const unsigned wa = L_PANEL + (row >> 5) * 40960 + (c >> 1) * 1024 + (c & 1) * 512 + (((row & 31) ^ (c & 7)) << 4);
#pragma unroll
    for (int u = 0; u < 5; ++u) {
        u32x4 o;
#pragma unroll
        for (int e = 0; e < 4; ++e) { const f32x2 y = __builtin_elementwise_fma(xv[4 * u + e], rs2, mo2); o[e] = pk2(y[0], y[1]); }
        *LP(u32x4, wa + u * 8192) = o;
    }
}
#define BLOCK_BARRIER() asm volatile("s_waitcnt lgkmcnt(0)\n\ts_barrier" ::: "memory")

__global__ void __launch_bounds__(512, 2) mega(Params p) {
    const int w = __builtin_amdgcn_readfirstlane(threadIdx.x >> 6);
    const int b = blockIdx.x, xc = b & 7, j = b >> 3;
    const int ti = (xc >> 1) * 8 + (j >> 2), g = (xc & 1) * 4 + (j & 3);
    const int kmt = g & 1;
    const int ra = kmt * 32 + (w & 3) * 8, rb = (kmt ^ 1) * 32 + (w & 3) * 8;
    u32x4 x0[5], x1[5], x2[5], x3[5];
    {
        asm volatile("" :: "s"(p.x), "s"(p.ws), "s"(p.wq), "s"(p.wk));
        const char* xb = p.x + (size_t)ti * 64 * 1280;
        const unsigned xvo = ((threadIdx.x >> 4) & 3) * 1280 + (threadIdx.x & 15) * 16;
        xload8(x0, x1, xb + (size_t)ra * 1280, xvo, xvo + 5120, w >> 2);
        xload8(x2, x3, xb + (size_t)rb * 1280, xvo, xvo + 5120, w >> 2);
    }
    const char* pc; asm volatile("s_getpc_b64 %0" : "=s"(pc));
    unsigned tag;
    char* ws = p.ws;
    if (threadIdx.x < C_N) *lcnt(threadIdx.x) = (threadIdx.x == C_WP || threadIdx.x == C_WP + 1 || threadIdx.x == C_PP || threadIdx.x == C_PP + 1) ? 100u : 0u;
    const bool haskv = (g >> 2) == (ti & 1);
    const char* wqb = p.wq + g * 160;
#define LANE_CONSTS() int lane = __builtin_amdgcn_mbcnt_hi(~0u, __builtin_amdgcn_mbcnt_lo(~0u, 0u)); PIN(lane); \
    const int r = lane & 31, h = lane >> 5, tq = (lane & 15) >> 2, tp = lane & 3, tb = (lane >> 4) & 1; (void)r; (void)h; (void)tq; (void)tp; (void)tb

    if (w >= 4) {
        LANE_CONSTS();
        const int v = w - 4;
        unsigned epv = 0u;
        {
            asm volatile("global_load_dword %0, %1, %2 sc1" : "=v"(epv) : "v"(0u), "s"(p.ws + WS_EP) : "memory");
            constexpr unsigned CODE_PF = 64512;
            const unsigned line = (unsigned)(j * 4 + v) * 64u + lane;
            if (line * 128u < CODE_PF) l2touch(pc + line * 128u);
        }
#define GET_TAG() do { { asm volatile("s_nop 0\n\tv_readfirstlane_b32 %0, %1\n\ts_nop 3" : "=s"(tag) : "v"(epv) : "memory"); tag += 0x9E3779B1u; } } while (0)
        __builtin_amdgcn_s_setprio(3);
        {
            const int kvm = 2 * ti + (g & 1); const bool isV = (g >> 1) & 1;
            const char* wkvb = isV ? p.wv : p.wk;
            if (haskv && v < 2) {
#pragma unroll
                for (int n = 0; n < 13; ++n) {
                    const unsigned o = v * 12800 + (n < 12 ? n * 1024 : 11776);
                    glds16(o + lane * 16, wkvb, L_WKV + o);
                }
            }
            BLOCK_BARRIER();
            if (v >= 2) {
                const int v2 = v - 2;
                unsigned wo5[5];
#pragma unroll
                for (int i = 0; i < 5; ++i) { const unsigned c = (v2 + 2 * i) * 64 + lane, row = c / 10u, col = c - row * 10u; wo5[i] = row * 1280 + col * 16; }
#pragma unroll
                for (int n = 0; n < 20; ++n) glds16(wo5[n % 5], wqb + (n / 5) * 64 * 1280, L_WQ + (v2 + 2 * n) * 1024);
                u32x4 wst[3][10];
                auto wst_issue = [&](auto I) {
                    constexpr int rr = I.value / 10, i = I.value % 10;
                    GLOAD16N(wst[rr][i], wo5[i % 5], wqb + (4 + 2 * rr + i / 5) * 64 * 1280);
                };
                static_for<0, 10>(wst_issue);
                WAITV(10);
                GET_TAG(); lpost(C_TAG, tag);
                lpost(C_PP + v, 1);
                if (haskv) lwait(C_KVFIN, 1);
                static_for<10, 30>(wst_issue);
                static_for<0, 3>([&](auto R) {
                    constexpr int rr = R.value;
                    waitv5<20 - 10 * rr>(wst[rr][0], wst[rr][1], wst[rr][2], wst[rr][3], wst[rr][4]); waitv5<63>(wst[rr][5], wst[rr][6], wst[rr][7], wst[rr][8], wst[rr][9]);
                    lwait_all(C_AP, 4, 3 + rr, lane);
#pragma unroll
                    for (int i = 0; i < 10; ++i) *LP(u32x4, L_WQ + (rr & 1) * 20480 + (v2 + 2 * i) * 1024 + lane * 16) = wst[rr][i];
                    lpost(C_WP + v, rr + 1);
                });
            } else {
              if (haskv) {
                f32x16 acc = {};
                const unsigned wa = L_WKV + (v * 320 + 8 * h + tq) * 40 + tb * 32 + (tb ? 0 : tp * 8);
                unsigned xa[4];
#pragma unroll
                for (int s = 0; s < 4; ++s) xa[s] = L_PANEL + kmt * 40960 + v * 20480 + h * 512 + ((r ^ ((2 * s + h) & 7)) << 4);
                WAITV(0);
                GET_TAG();
                bf16x8 wf[20], xf[20];
#pragma unroll
                for (int s = 0; s < 20; ++s) wf[s] = ldstr2(wa + s * 640, wa + s * 640 + 160);
                lwait_all(C_AP, 4, 1, lane);
#pragma unroll
                for (int s = 0; s < 20; ++s) xf[s] = ldsf(xa[s & 3] + s * 1024);
                SBAR();
                if (isV) {
#pragma unroll
                    for (int s = 0; s < 20; ++s) acc = MFMA(xf[s], wf[s], acc);
                } else {
#pragma unroll
                    for (int s = 0; s < 20; ++s) acc = MFMA(wf[s], xf[s], acc);
                }
                if (v) {
#pragma unroll
                    for (int e = 0; e < 4; ++e) *LP(f32x4, L_WKV + 12800 + e * 1024 + lane * 16) = f32x4{acc[4 * e], acc[4 * e + 1], acc[4 * e + 2], acc[4 * e + 3]};
                    lpost(C_KVP, 1);
                } else {
                    lwait(C_KVP, 1);
#pragma unroll
                    for (int e = 0; e < 4; ++e) {
                        const f32x4 t = *LP(const f32x4, L_WKV + 12800 + e * 1024 + lane * 16);
                        acc[4 * e] += t[0]; acc[4 * e + 1] += t[1]; acc[4 * e + 2] += t[2]; acc[4 * e + 3] += t[3];
                    }
                    const __amdgpu_buffer_rsrc_t rk = mkrsrc(ws + WS_KV, 32 * KVD);
                    const unsigned base = ti * KVD, ke = g & 1;
                    const u32x4 f0 = __builtin_bit_cast(u32x4, pack8(acc, 0, 1.f)), f1 = __builtin_bit_cast(u32x4, pack8(acc, 8, 1.f));
                    float mx;
                    if (!isV) {
                        __builtin_amdgcn_raw_buffer_store_b128(f0, rk, base + ke * 1024 + lane * 16, 0, 16);
                        const u32x2 k1 = {f1[0], f1[1]};
                        if (!h) __builtin_amdgcn_raw_buffer_store_b64(k1, rk, base + KV_K1 + r * 16 + ke * 8, 0, 16);
                        lpost(C_KVFIN, 1);
                        float n2 = 0.f;
#pragma unroll
                        for (int e = 0; e < 4; ++e) n2 += bflo(f0[e]) * bflo(f0[e]) + bfhi(f0[e]) * bfhi(f0[e]);
                        if (!h) n2 += bflo(f1[0]) * bflo(f1[0]) + bfhi(f1[0]) * bfhi(f1[0]) + bflo(f1[1]) * bflo(f1[1]) + bfhi(f1[1]) * bfhi(f1[1]);
                        n2 += __shfl_xor(n2, 32);
                        mx = (n2 == n2) ? n2 : INFINITY;
                    } else {
                        const unsigned cst = r == 20 ? 0x3f803f80u : 0u;
                        const u32x4 c4 = {cst, cst, cst, cst};
                        if (r <= 21) {
                            __builtin_amdgcn_raw_buffer_store_b128(r >= 20 ? c4 : f0, rk, base + KV_V + ke * KV_VT + h * 352 + r * 16, 0, 16);
                            __builtin_amdgcn_raw_buffer_store_b128(r >= 20 ? c4 : f1, rk, base + KV_V + ke * KV_VT + 704 + h * 352 + r * 16, 0, 16);
                        }
                        lpost(C_KVFIN, 1);
                        float vm = 0.f;
#pragma unroll
                        for (int e = 0; e < 16; ++e) { const float a = fabsf(acc[e]); vm = (a <= vm) ? vm : ((a == a) ? a : INFINITY); }
                        mx = r < 20 ? vm : 0.f;
                    }
                    mx = wave_max(mx);
                    WAITV(0);
                    if (lane < 8) __hip_atomic_store((gu64*)(ws + WS_KF + lane * 1024 + (isV ? 512 : 0)) + kvm, ((unsigned long long)tag << 32) | __float_as_uint(mx), RLX_AGENT);
                }
            }
              else { WAITV(0); GET_TAG(); }
            }
            if (haskv) lwait(C_KVFIN, 1);
#pragma unroll
            for (int n = 0; n < 2; ++n) {
                unsigned L = n * 64 + lane; L = (j * 4 + v) * 120 + (L < 120u ? L : 119u);
                const unsigned m3 = L >= 7680u, rem = L - (m3 ? 7680u : 0u), row = rem / 12u;
                l2touch((m3 ? p.w3 : p.w1) + row * 3072 + (xc & 1) * 1536 + (rem - row * 12u) * 128);
            }
            lwait_all(C_AP, 4, 6, lane);
            {
                const char* w1s = p.w1 + g * 384 + v * 96; const char* w3s = p.w3 + g * 384 + v * 96;
                auto goff = [](unsigned idx) { const unsigned row = idx / 6u; return row * 3072 + (idx - row * 6u) * 16; };
                const unsigned gva = goff(lane), gvb = goff((lane - 8) & 63), gvc = goff(64 + (h ? (r - 8) & 31 : r));
                const unsigned gdst = L_GU + v * 9216;
                auto gdma = [&](auto S) { constexpr int s = S.value; glds_gu(gva, gvb, gvc, w1s + (size_t)s * 16 * 3072, w3s + (size_t)s * 16 * 3072, gdst + (s % 3) * 3072); };
                static_for<0, 3>(gdma);
                f32x16 acc[2][3] = {};
                unsigned bn[3][2], pa[4], pb[4];
#pragma unroll
                for (int n = 0; n < 3; ++n)
#pragma unroll
                    for (int t = 0; t < 2; ++t) {
                        const unsigned idx = 6 * (8 * h + 4 * t + tq) + 2 * n + (tp >> 1);
                        const unsigned o = idx < 64u ? (tb ? 1024 + ((idx + 8) & 63) * 16 : idx * 16) : (tb ? 2560 + ((idx - 56) & 31) * 16 : 2048 + (idx - 64) * 16);
                        bn[n][t] = gdst + o + (tp & 1) * 8;
                    }
#pragma unroll
                for (int s = 0; s < 4; ++s) { pa[s] = L_PANEL + h * 512 + ((r ^ ((2 * s + h) & 7)) << 4); pb[s] = pa[s] + 40960; PIN(pb[s]); }
                bf16x8 wf[2][3], xa[2], xb2[2];
                auto rdw = [&](auto S) { constexpr int s = S.value;
#pragma unroll
                    for (int n = 0; n < 3; ++n) wf[s & 1][n] = ldstr2(bn[n][0] + (s % 3) * 3072, bn[n][1] + (s % 3) * 3072); };
                auto rdx = [&](auto S) { constexpr int s = S.value; xa[s & 1] = ldsf(pa[s & 3] + s * 1024); xb2[s & 1] = ldsf(pb[s & 3] + s * 1024); };
                WAITV(8);
                rdx(IC<0>{}); rdw(IC<0>{});
                asm volatile("s_waitcnt lgkmcnt(0)" ::: "memory");
                gdma(IC<3>{});
                __builtin_amdgcn_s_setprio(0);
                static_for<0, 40>([&](auto S) {
                    constexpr int s = S.value; constexpr bool nx = s + 1 < 40;
                    acc[0][0] = MFMA(wf[s & 1][0], xa[s & 1], acc[0][0]); SBAR();
                    if constexpr (nx) rdx(IC<s + 1>{});
                    SBAR();
                    acc[1][0] = MFMA(wf[s & 1][0], xb2[s & 1], acc[1][0]); SBAR();
                    if constexpr (nx) { WAITVN(4 * ((s + 3 < 39 ? s + 3 : 39) - (s + 1))); rdw(IC<s + 1>{}); }
                    SBAR();
                    acc[0][1] = MFMA(wf[s & 1][1], xa[s & 1], acc[0][1]); SBAR();
                    acc[1][1] = MFMA(wf[s & 1][1], xb2[s & 1], acc[1][1]); SBAR();
                    if constexpr (s + 4 < 40) { asm volatile("s_waitcnt lgkmcnt(0)" ::: "memory"); gdma(IC<s + 4>{}); }
                    SBAR();
                    acc[0][2] = MFMA(wf[s & 1][2], xa[s & 1], acc[0][2]); SBAR();
                    acc[1][2] = MFMA(wf[s & 1][2], xb2[s & 1], acc[1][2]); SBAR();
                });
                lpost(C_GP + v, 42);
                __builtin_amdgcn_s_setprio(3);
                const __amdgpu_buffer_rsrc_t rh = mkrsrc(ws + WS_HT, 64 * 96 * 1024);
#pragma unroll
                for (int m = 0; m < 2; ++m)
#pragma unroll
                    for (int n = 0; n < 3; ++n) {
                        f32x16 hv;
#pragma unroll
                        for (int e = 0; e < 8; ++e) {
                            const float gg = acc[m][n][e];
                            hv[e] = gg * __builtin_amdgcn_rcpf(1.f + __builtin_amdgcn_exp2f(-1.4426950408889634f * gg)) * acc[m][n][e + 8];
                        }
                        __builtin_amdgcn_raw_buffer_store_b128(__builtin_bit_cast(u32x4, pack8(hv, 0, 1.f)), rh, ((2 * ti + m) * 96 + g * 12 + 3 * v + n) * 1024 + lane * 16, 0, 16);
                    }
                {
                    WAITV(0);
                    if (lane < 2) __hip_atomic_store((gu32*)(ws + WS_HF) + (2 * ti + lane) * 32 + g * 4 + v, tag, RLX_AGENT);
                }
                __builtin_amdgcn_s_setprio(0);
                {
#pragma unroll
                    for (int n = 0; n < 2; ++n) {
                        unsigned L = (j * 4 + v) * 85 + n * 64 + lane; L = L < 10880u ? L : 10879u;
                        const unsigned row = L / 5u;
                        l2touch((row < 1536u ? p.w2 + row * 1280 : p.wo + (row - 1536u) * 1280) + (xc & 1) * 640 + (L - row * 5u) * 128);
                    }
                }
            }
        }
        {
            LANE_CONSTS();
            const unsigned bq = ((tb * 32 + (tp >> 1) * 16) ^ ((unsigned)(tq >> 1) << 6)) + (tp & 1) * 8;
            const unsigned drow = lane >> 3, dcol = ((lane & 7) ^ (((lane >> 4) & 1) << 2)) * 16;
            const int G = xc >> 1, r0 = G < 2 ? 6 * G : 12 + 5 * (G - 2), nr = G < 2 ? 6 : 5;
            {
                if (b == 255 && v == 0) {
                    gu32* f = (gu32*)(ws + WS_HF) + h * 64 + r;
                    _Pragma("clang loop unroll(disable)") for (unsigned spins = 0; spins < (1u << 14); ++spins) {
                        bool ok = true;
#pragma unroll
                        for (int n = 0; n < 16; ++n) ok &= __hip_atomic_load(f + n * 128, RLX_AGENT) == tag;
                        if (__all(ok)) break;
                        __builtin_amdgcn_s_sleep(32);
                    }
                    if (lane == 0) __hip_atomic_store((gu32*)(ws + WS_EP), tag, RLX_AGENT);
                }
            }
            if (j < 5 * nr) {
                const int dr = r0 + j / 5, dc = (xc & 1) * 5 + j % 5;
                const int mfirst = dr < 20 ? 3 * dr : 60 + 2 * (dr - 20), nm = dr < 20 ? 3 : 2;
                const unsigned dvo = drow * 1280 + dcol;
                const unsigned myring = L_PANEL + v * 20480;
                f32x16 acc0 = {}, acc1 = {};
                { lwait_all(C_GP, 4, 42, lane); lwait_all(C_AP, 4, 8, lane); }
                {
                    f32x16 da[3][2] = {};
                    const int drot = (j % 5) * 4;
                    const char* bsrc = p.w2 + (size_t)v * 384 * 1280 + dc * 128;
                    const char* asrc = ws + WS_HT + ((size_t)mfirst * 96 + v * 24) * 1024;
                    const unsigned am1 = lane * 16 + 98304, am2 = lane * 16 + (nm == 3 ? 196608 : 98304);
                    constexpr int DQ = 8;
                    u32x4 aq[DQ][3];
                    auto bissue = [&](auto K) {
                        constexpr int k = K.value;
                        int kr = k + drot; kr = kr >= 24 ? kr - 24 : kr;
                        const char* sb = bsrc + (size_t)kr * 16 * 1280;
                        glds16(dvo, sb, myring + (k % 10) * 2048); glds16(dvo + 8 * 1280, sb, myring + (k % 10) * 2048 + 1024);
                    };
                    auto woissue = [&](auto U) {
                        constexpr int u = U.value;
                        const char* sb = p.wo + (size_t)(v * 160 + u * 16) * 1280 + dc * 128;
                        glds16(dvo, sb, myring + u * 2048); glds16(dvo + 8 * 1280, sb, myring + u * 2048 + 1024);
                    };
                    auto aissue = [&](auto K) {
                        constexpr int k = K.value;
                        int kr = k + drot; kr = kr >= 24 ? kr - 24 : kr;
                        const char* sb = asrc + (size_t)kr * 1024;
                        GLOAD16S(aq[k % DQ][0], lane * 16, sb); GLOAD16S(aq[k % DQ][1], am1, sb); GLOAD16S(aq[k % DQ][2], am2, sb);
                    };
                    static_for<0, DQ>(bissue);
                    {
                        gu32* f = (gu32*)(ws + WS_HF) + (mfirst + ((lane >> 3) < nm ? (lane >> 3) : 0)) * 32 + 8 * v + (lane & 7);
                        _Pragma("clang loop unroll(disable)") for (unsigned spins = 0; spins < (1u << 14); ++spins) {
                            if (__all(__hip_atomic_load(f, RLX_AGENT) == tag)) break;
                            __builtin_amdgcn_s_sleep(20);
                        }
                        CFENCE();
                    }
                    static_for<0, DQ>(aissue);
                    const unsigned ba0 = myring + (4 * h + tq) * 128 + bq, ba1 = ba0 ^ 64;
                    bf16x8 w0[2], w1[2];
                    WAITVN(3 * DQ);
                    w0[0] = ldstr2(ba0, ba0 + 1024); w1[0] = ldstr2(ba1, ba1 + 1024);
                    static_for<0, 24>([&](auto K) {
                        constexpr int k = K.value;
                        waitv3<dn_younger(k, DQ)>(aq[k % DQ][0], aq[k % DQ][1], aq[k % DQ][2]);
                        constexpr unsigned o = ((k + 1) % 10) * 2048;
#define DXF(m) __builtin_bit_cast(bf16x8, aq[k % DQ][m])
                        SBAR();
                        da[0][0] = MFMA(w0[k & 1], DXF(0), da[0][0]); SBAR();
                        if constexpr (k + 1 < 24) w0[(k + 1) & 1] = ldstr2(ba0 + o, ba0 + o + 1024);
                        SBAR();
                        da[0][1] = MFMA(w1[k & 1], DXF(0), da[0][1]); SBAR();
                        if constexpr (k + 1 < 24) w1[(k + 1) & 1] = ldstr2(ba1 + o, ba1 + o + 1024);
                        SBAR();
                        da[1][0] = MFMA(w0[k & 1], DXF(1), da[1][0]); SBAR();
                        if constexpr (k + DQ < 24) bissue(IC<k + DQ>{});
                        if constexpr (k >= 16) woissue(IC<(k + 8) % 10>{});
                        SBAR();
                        da[1][1] = MFMA(w1[k & 1], DXF(1), da[1][1]); SBAR();
                        da[2][0] = MFMA(w0[k & 1], DXF(2), da[2][0]); SBAR();
                        da[2][1] = MFMA(w1[k & 1], DXF(2), da[2][1]); SBAR();
                        if constexpr (k + DQ < 24) aissue(IC<k + DQ>{});
                    });
                    __builtin_amdgcn_s_setprio(3);
                    { CFENCE(); woissue(IC<2>{}); woissue(IC<3>{}); }
#pragma unroll
                    for (int n = 0; n < 2; ++n) {
#pragma unroll
                        for (int m = 0; m < 3; ++m) {
                            if (m != v) {
                                const unsigned dst = L_GU + (m * 3 + (v > m ? v - 1 : v)) * 4096 + lane * 16;
#pragma unroll
                                for (int e = 0; e < 4; ++e) *LP(f32x4, dst + e * 1024) = f32x4{da[m][n][4 * e], da[m][n][4 * e + 1], da[m][n][4 * e + 2], da[m][n][4 * e + 3]};
                            }
                        }
                        lpost(C_DP + v, 2 * n + 1); lwait_all(C_DP, 4, 2 * n + 1, lane);
                        if (v < 3) {
                            f32x16 t = v == 0 ? da[0][n] : (v == 1 ? da[1][n] : da[2][n]);
#pragma unroll
                            for (int o = 0; o < 3; ++o)
#pragma unroll
                                for (int e = 0; e < 4; ++e) {
                                    const f32x4 u = *LP(const f32x4, L_GU + (v * 3 + o) * 4096 + e * 1024 + lane * 16);
                                    t[4 * e] += u[0]; t[4 * e + 1] += u[1]; t[4 * e + 2] += u[2]; t[4 * e + 3] += u[3];
                                }
                            if (n) acc1 = t; else acc0 = t;
                        }
                        if (n == 0) { lpost(C_DP + v, 2); lwait_all(C_DP, 4, 2, lane); }
                    }
                }
                { WAITV(0); lpost(C_DP + v, 4); }
                {
                    if (v == 3) {
                        __builtin_amdgcn_s_setprio(0);
                        const int mi = (lane >> 4) < nm ? (lane >> 4) : 0;
                        gu64* f = (gu64*)(ws + WS_OF + (mfirst + mi) * 128) + (lane & 15);
                        _Pragma("clang loop unroll(disable)") for (unsigned spins = 0; spins < (1u << 14); ++spins) {
                            const unsigned long long x = __hip_atomic_load(f, RLX_AGENT);
                            const unsigned long long ok = __ballot((unsigned)x == tag && (unsigned)(x >> 32) == tag);
                            const unsigned mine = (unsigned)(ok >> ((lane & 3) * 16));
                            *((volatile AS3 unsigned*)lcnt(C_OS) + (lane & 3)) = ((mine & 0xffu) == 0xffu ? 1u : 0u) | ((mine & 0xff00u) == 0xff00u ? 2u : 0u);
                            if (ok == ~0ull) break;
                            __builtin_amdgcn_s_sleep(8);
                        }
                    }
                }
                if (v < nm) {
                    const int m = mfirst + v;
                    const unsigned ba0 = L_PANEL + (4 * h + tq) * 128 + bq, ba1 = ba0 ^ 64;
                    {
                        const char* xrow = p.x + (size_t)(m * 32 + r) * 1280 + dc * 128 + h * 16;
                        u32x4 xres[4];
#pragma unroll
                        for (int e = 0; e < 4; ++e) xres[e] = *(const u32x4*)(xrow + e * 32);
                        lwait_all(C_DP, 4, 4, lane);
                        const __amdgpu_buffer_rsrc_t ro = mkrsrc(ws + WS_OT + (size_t)m * 40960, 40960);
                        int hf = 0;
                        f32x16 t0[2] = {}, t1[2] = {};
#pragma unroll
                        for (int it = 0; it < 2; ++it) {
                            {
                                _Pragma("clang loop unroll(disable)") for (unsigned spins = 0; spins < (1u << 17); ++spins) {
                                    const unsigned ok = __builtin_amdgcn_readfirstlane(*((volatile AS3 unsigned*)lcnt(C_OS) + v));
                                    if (it == 0) { if (ok) { hf = (ok & 1) ? 0 : 1; break; } }
                                    else if (ok == 3) break;
                                    __builtin_amdgcn_s_sleep(1);
                                }
                                CFENCE();
                            }
                            u32x4 of[20];
#pragma unroll
                            for (int s = 0; s < 20; ++s) of[s] = __builtin_amdgcn_raw_buffer_load_b128(ro, lane * 16 + s * 1024, hf * 20480, 16);
                            const unsigned a0 = ba0 + hf * 40960, a1 = ba1 + hf * 40960, l0 = a0 + (36 * h + 16) * 128, l1 = a1 + (36 * h + 16) * 128;
                            bf16x8 w0[2], w1[2];
                            w0[0] = ldstr2(a0, a0 + 1024); w1[0] = ldstr2(a1, a1 + 1024);
                            static_for<0, 20>([&](auto S) {
                                constexpr int s = S.value, n = s + 1, gg = n / 5, jj = n % 5;
                                const bf16x8 xf = __builtin_bit_cast(bf16x8, of[s]);
                                if constexpr (n < 20) {
                                    if constexpr (jj < 4) {
                                        constexpr unsigned o = (80 * gg + 20 * jj) * 128;
                                        w0[n & 1] = ldstr2(a0 + o, a0 + o + 1024); w1[n & 1] = ldstr2(a1 + o, a1 + o + 1024);
                                    } else {
                                        constexpr unsigned o = 80 * gg * 128;
                                        w0[n & 1] = ldstr2(l0 + o, l0 + o + 2560); w1[n & 1] = ldstr2(l1 + o, l1 + o + 2560);
                                    }
                                }
                                SBAR();
                                t0[it] = MFMA(w0[s & 1], xf, t0[it]); t1[it] = MFMA(w1[s & 1], xf, t1[it]);
                                SBAR();
                            });
                            hf ^= 1;
                        }
                        acc0 += t0[0] + t0[1]; acc1 += t1[0] + t1[1];
                        char* orow = p.out + (size_t)(m * 32 + r) * 1280 + dc * 128 + h * 16;
#pragma unroll
                        for (int n = 0; n < 2; ++n) {
                            const f32x16& a = n ? acc1 : acc0;
#pragma unroll
                            for (int qq = 0; qq < 2; ++qq) {
                                float lo[4], hi[4];
#pragma unroll
                                for (int e = 0; e < 4; ++e) {
                                    auto sw = __builtin_amdgcn_permlane32_swap(__float_as_uint(a[8 * qq + e]), __float_as_uint(a[8 * qq + 4 + e]), false, false);
                                    lo[e] = __uint_as_float(sw[0]); hi[e] = __uint_as_float(sw[1]);
                                }
                                const u32x4 xv = xres[2 * n + qq];
                                const u32x4 ov = {pk2(lo[0] + bflo(xv[0]), lo[1] + bfhi(xv[0])), pk2(lo[2] + bflo(xv[1]), lo[3] + bfhi(xv[1])),
                                                  pk2(hi[0] + bflo(xv[2]), hi[1] + bfhi(xv[2])), pk2(hi[2] + bflo(xv[3]), hi[3] + bfhi(xv[3]))};
                                *(u32x4*)(orow + (2 * n + qq) * 32) = ov;
                            }
                        }
                    }
                }
            }
        }
    } else {
        LANE_CONSTS();
        bf16x8 Q0a, Q0b; u32x2 q1a, q1b;
        const int head = 4 * g + w;
        const AttSync as{lane, ws + WS_KV, (unsigned)((w < 3 ? w * 3072 : CHUNK - 3072) + lane * 16), (unsigned)(w < 3 ? w * 3072 : CHUNK - 3072), w};
        __builtin_amdgcn_s_setprio(3);
        unsigned long long fk = 0, fv = 0; bool early = false;
        {
            BLOCK_BARRIER();
            waitv5<15>(x0[0], x0[1], x0[2], x0[3], x0[4]);
            ln_rows(x0, ra, lane);
            waitv5<10>(x1[0], x1[1], x1[2], x1[3], x1[4]);
            ln_rows(x1, ra + 4, lane);
            lpost(C_AP + w, 1);
            waitv5<5>(x2[0], x2[1], x2[2], x2[3], x2[4]);
            ln_rows(x2, rb, lane);
            waitv5<0>(x3[0], x3[1], x3[2], x3[3], x3[4]);
            ln_rows(x3, rb + 4, lane);
            lpost(C_AP + w, 2);
            unsigned pa[4];
#pragma unroll
            for (int s = 0; s < 4; ++s) pa[s] = L_PANEL + h * 512 + ((r ^ ((2 * s + h) & 7)) << 4);
            f32x16 qa = {}, qb = {};
            const unsigned wa = L_WQ + (8 * h + tq) * 160 + w * 40 + tb * 32 + (tb ? 0 : tp * 8);
            bf16x8 fw[3][4], fa[2][4], fb[2][4];
            auto rdw = [&](auto S, auto S4) { constexpr int st = S.value, s4 = S4.value; const unsigned o = wa + (st & 3) * 10240 + s4 * 2560; fw[st % 3][s4] = ldstr2(o, o + 640); };
            lwait_all(C_PP, 4, 1, lane);
            static_for<0, 4>([&](auto I) { rdw(IC<0>{}, I); rdw(IC<1>{}, I); });
            tag = __builtin_amdgcn_readfirstlane(*(volatile AS3 unsigned*)lcnt(C_TAG));
            lpost(C_AP + w, 3);
            lwait_all(C_AP, 4, 2, lane);
#pragma unroll
            for (int s4 = 0; s4 < 4; ++s4) { fa[0][s4] = ldsf(pa[s4] + s4 * 1024); fb[0][s4] = ldsf(pa[s4] + 40960 + s4 * 1024); }
            unsigned pk = 0;
            static_for<0, 10>([&](auto S) {
                constexpr int st = S.value, nx = st + 1;
                if constexpr (st == 3 || st == 5 || st == 7) lcheck(pk, C_WP, (st - 1) / 2, lane);
                if constexpr (st == 6) gload8x2_sc1(fk, fv, lane * 8, ws + WS_KF + xc * 1024);
                if constexpr (st == 8) {
                    waitv8x2(fk, fv);
                    early = __all((unsigned)(fk >> 32) == tag && (unsigned)(fv >> 32) == tag);
                    if (early) { lwait_all(C_AP, 4, 6, lane); att_dma(as, 0); att_dma(as, 1); }
                }
                static_for<0, 4>([&](auto S4) {
                    constexpr int s4 = S4.value;
                    qa = MFMA(fw[st % 3][s4], fa[st & 1][s4], qa); SBAR();
                    if constexpr (st == 0) rdw(IC<2>{}, S4);
                    if constexpr (st == 1) rdw(IC<3>{}, S4);
                    if constexpr (st == 3 || st == 5 || st == 7) { rdw(IC<st + 1>{}, S4); rdw(IC<st + 2>{}, S4); }
                    SBAR();
                    qb = MFMA(fw[st % 3][s4], fb[st & 1][s4], qb); SBAR();
                    if constexpr (nx < 10) { fa[nx & 1][s4] = ldsf(pa[s4] + (nx * 4 + s4) * 1024); fb[nx & 1][s4] = ldsf(pa[s4] + 40960 + (nx * 4 + s4) * 1024); SBAR(); }
                    if constexpr (s4 == 2 && (st == 2 || st == 4 || st == 6)) { pk = lpeek(C_WP, lane); SBAR(); }
                });
                if constexpr (st == 1 || st == 3 || st == 5) lpost(C_AP + w, 4 + st / 2);
            });
            lpost(C_AP + w, 8);
            Q0a = pack8(qa, 0, QSCALE); Q0b = pack8(qb, 0, QSCALE);
            q1a = u32x2{pk2(qa[8] * QSCALE, qa[9] * QSCALE), pk2(qa[10] * QSCALE, qa[11] * QSCALE)};
            q1b = u32x2{pk2(qb[8] * QSCALE, qb[9] * QSCALE), pk2(qb[10] * QSCALE, qb[11] * QSCALE)};
            if (h) { q1a = u32x2{0u, 0u}; q1b = u32x2{0u, 0u}; }
        }
        {
            float na = 0.f, nb = 0.f;
            {
                const u32x4 a0 = __builtin_bit_cast(u32x4, Q0a), b0 = __builtin_bit_cast(u32x4, Q0b);
#pragma unroll
                for (int e = 0; e < 4; ++e) { na += bflo(a0[e]) * bflo(a0[e]) + bfhi(a0[e]) * bfhi(a0[e]); nb += bflo(b0[e]) * bflo(b0[e]) + bfhi(b0[e]) * bfhi(b0[e]); }
#pragma unroll
                for (int e = 0; e < 2; ++e) { na += bflo(q1a[e]) * bflo(q1a[e]) + bfhi(q1a[e]) * bfhi(q1a[e]); nb += bflo(q1b[e]) * bflo(q1b[e]) + bfhi(q1b[e]) * bfhi(q1b[e]); }
                const auto sa = __builtin_amdgcn_permlane32_swap(__float_as_uint(na), __float_as_uint(na), false, false), sb = __builtin_amdgcn_permlane32_swap(__float_as_uint(nb), __float_as_uint(nb), false, false);
                na = __uint_as_float(sa[0]) + __uint_as_float(sa[1]); nb = __uint_as_float(sb[0]) + __uint_as_float(sb[1]);
            }
            if (!early && w == 0) {
                gu64* f = (gu64*)(ws + WS_KF + xc * 1024) + lane;
                _Pragma("clang loop unroll(disable)") for (unsigned spins = 0; spins < (1u << 14); ++spins) {
                    fk = __hip_atomic_load(f, RLX_AGENT); fv = __hip_atomic_load(f + 64, RLX_AGENT);
                    if (__all((unsigned)(fk >> 32) == tag && (unsigned)(fv >> 32) == tag)) break;
                    __builtin_amdgcn_s_sleep(4);
                }
            }
            float kn2 = wave_max(__uint_as_float((unsigned)fk)), vmx = wave_max(__uint_as_float((unsigned)fv));
            if (w == 0) { lpost(C_KN, __float_as_uint(kn2)); lpost(C_VM, __float_as_uint(vmx)); lpost(C_KVRDY, 1); }
            if (!early) {
                lwait(C_KVRDY, 1);
                kn2 = __uint_as_float(*(volatile AS3 unsigned*)lcnt(C_KN)); vmx = __uint_as_float(*(volatile AS3 unsigned*)lcnt(C_VM));
                lwait_all(C_AP, 4, 6, lane);
                att_dma(as, 0); att_dma(as, 1);
            }
            const float Ma = __builtin_amdgcn_sqrtf(na * kn2) * 1.01f, Mb = __builtin_amdgcn_sqrtf(nb * kn2) * 1.01f;
            const bool fast = __all(Ma <= BOUND_MAX && Mb <= BOUND_MAX && vmx <= 1e30f);
            const u32x2 a1 = {(fast && h) ? pk2(-Ma, 0.f) : q1a[0], q1a[1]}, b1 = {(fast && h) ? pk2(-Mb, 0.f) : q1b[0], q1b[1]};
            f32x16 Oa = {}, Ob = {};
            WAITV(3); lpost(C_AP + w, AP0); lwait_all(C_AP, 4, AP0, lane);
            att_dma(as, 2);
            __builtin_amdgcn_s_setprio(2);
            if (fast) att_fast(as, Q0a, __builtin_bit_cast(s16x4, a1), Q0b, __builtin_bit_cast(s16x4, b1), Oa, Ob);
            else att_safe(as, Q0a, __builtin_bit_cast(s16x4, a1), Q0b, __builtin_bit_cast(s16x4, b1), Oa, Ob);
            const __amdgpu_buffer_rsrc_t ro = mkrsrc(ws + WS_OT, 64 * 40 * 1024);
#pragma unroll
            for (int q = 0; q < 2; ++q) {
                const f32x16& O = q ? Ob : Oa;
                const auto lsw = __builtin_amdgcn_permlane32_swap(__float_as_uint(O[8]), __float_as_uint(O[8]), false, false);
                const float inv = __builtin_amdgcn_rcpf(__uint_as_float(lsw[1]));
                const unsigned base = ((2 * ti + q) * 40 + 5 * g) * 1024;
                __builtin_amdgcn_raw_buffer_store_b128(__builtin_bit_cast(u32x4, pack8(O, 0, inv)), ro, base + w * 1024 + lane * 16, 0, 16);
                if (!h) {
                    u32x2 t = {pk2(O[8] * inv, O[9] * inv), pk2(O[10] * inv, O[11] * inv)};
                    __builtin_amdgcn_raw_buffer_store_b64(t, ro, base + 4096 + ((w >> 1) * 32 + r) * 16 + (w & 1) * 8, 0, 16);
                }
            }
            {
                WAITV(0);
                if (lane < 2) __hip_atomic_store((gu32*)(ws + WS_OF) + (2 * ti + lane) * 32 + head, tag, RLX_AGENT);
            }
        }
    }
}

extern "C" void kernel_launch(void* const* d_in, const int* in_sizes, int n_in, void* d_out, int out_size, void* d_ws, size_t ws_size, hipStream_t stream) {
    Params p;
    p.x = (const char*)d_in[0]; p.wq = (const char*)d_in[1]; p.wk = (const char*)d_in[2]; p.wv = (const char*)d_in[3]; p.wo = (const char*)d_in[4];
    p.w1 = (const char*)d_in[6]; p.w3 = (const char*)d_in[7]; p.w2 = (const char*)d_in[8];
    p.out = (char*)d_out; p.ws = (char*)d_ws;
    (void)hipFuncSetAttribute((const void*)mega, hipFuncAttributeMaxDynamicSharedMemorySize, L_TOTAL);
    mega<<<256, 512, L_TOTAL, stream>>>(p);
}
```

```cpp
#include <hip/hip_runtime.h>
#include <hip/hip_bf16.h>
#include <stdint.h>

#define AS1 __attribute__((address_space(1)))
#define AS3 __attribute__((address_space(3)))
typedef __bf16 bf16x2 __attribute__((ext_vector_type(2)));
typedef __bf16 bf16x8 __attribute__((ext_vector_type(8)));
typedef short s16x4 __attribute__((ext_vector_type(4)));
typedef short s16x8 __attribute__((ext_vector_type(8)));
typedef float f32x2 __attribute__((ext_vector_type(2)));
typedef float f32x4 __attribute__((ext_vector_type(4)));
typedef float f32x16 __attribute__((ext_vector_type(16)));
typedef unsigned u32x2 __attribute__((ext_vector_type(2)));
typedef unsigned u32x4 __attribute__((ext_vector_type(4)));
typedef AS1 unsigned gu32;
typedef AS1 unsigned long long gu64;

constexpr unsigned WS_KF = 0;
constexpr unsigned WS_HF = 16384;
constexpr unsigned WS_OF = 32768;
constexpr unsigned WS_EP = 49152;
constexpr unsigned KVD = 5376;
constexpr unsigned KV_K1 = 2048, KV_V = 2560, KV_VT = 1408;
constexpr unsigned WS_KV = 65536;
constexpr unsigned WS_HT = WS_KV + 176128;
constexpr unsigned WS_OT = WS_HT + 64 * 96 * 1024;
constexpr unsigned L_PANEL = 0;
constexpr unsigned L_KV = 81920;
constexpr unsigned L_WQ = 81920;
constexpr unsigned L_GU = 124928;
constexpr unsigned L_WKV = 124928;
constexpr unsigned L_MISC = 161920;
constexpr unsigned L_TOTAL = 163840;
constexpr unsigned CHUNK = 2 * KVD;
constexpr int NCHUNK = 16;
enum { C_AP = 0  , C_GP = 4  , C_DP = 8  , C_PP = 12  , C_WP = 16  , C_KVP = 20, C_KVFIN, C_TAG, C_KVRDY, C_OS = 24  , C_KN = 28, C_VM, C_N };

struct Params { const char* x; const char* wq; const char* wk; const char* wv; const char* wo; const char* w1; const char* w3; const char* w2; char* out; char* ws; };


#define SBAR() __builtin_amdgcn_sched_barrier(0)
#define PIN(x) asm volatile("" : "+v"(x))
#define CFENCE() asm volatile("" ::: "memory")
#define WAITV(n) asm volatile("s_waitcnt vmcnt(" #n ")" ::: "memory")
#define MFMA(a, b, c) __builtin_amdgcn_mfma_f32_32x32x16_bf16((a), (b), (c), 0, 0, 0)
#define MFMA8(a, b, c) __builtin_amdgcn_mfma_f32_32x32x8bf16_1k((a), (b), (c), 0, 0, 0)
#define RLX_AGENT __ATOMIC_RELAXED, __HIP_MEMORY_SCOPE_AGENT
#define RLX_WG __ATOMIC_RELAXED, __HIP_MEMORY_SCOPE_WORKGROUP

#define LP(T, off) ((AS3 T*)(uintptr_t)(unsigned)(off))
__device__ __forceinline__ bf16x8 ldsf(unsigned off) { return *LP(const bf16x8, off); }
__device__ __forceinline__ s16x4 ldstr(unsigned off) { return __builtin_amdgcn_ds_read_tr16_b64_v4i16(LP(s16x4, off)); }
__device__ __forceinline__ bf16x8 ldstr2(unsigned off0, unsigned off1) {
    s16x4 a = ldstr(off0), b = ldstr(off1);
    return __builtin_bit_cast(bf16x8, __builtin_shufflevector(a, b, 0, 1, 2, 3, 4, 5, 6, 7));
}
template <int E> __device__ __forceinline__ s16x4 half4(bf16x8 v) { const s16x8 t = __builtin_bit_cast(s16x8, v); return __builtin_shufflevector(t, t, 4 * E, 4 * E + 1, 4 * E + 2, 4 * E + 3); }
__device__ __forceinline__ unsigned pk2(float lo, float hi) { bf16x2 v = {(__bf16)lo, (__bf16)hi}; return __builtin_bit_cast(unsigned, v); }
__device__ __forceinline__ float bflo(unsigned u) { return __uint_as_float(u << 16); }
__device__ __forceinline__ float bfhi(unsigned u) { return __uint_as_float(u & 0xffff0000u); }
__device__ __forceinline__ float rbf(float f) { return (float)(__bf16)f; }

__device__ __forceinline__ void glds16(unsigned voff, const char* sbase, unsigned ldsdst) {
    unsigned keep;
    asm volatile("s_mov_b32 %0, m0\n\ts_mov_b32 m0, %3\n\ts_nop 2\n\tglobal_load_lds_dwordx4 %1, %2\n\ts_mov_b32 m0, %0"
                 : "=&s"(keep) : "v"(voff), "s"(sbase), "s"(ldsdst) : "memory");
}
__device__ __forceinline__ void glds16x3_sc1(unsigned voff, const char* sbase, unsigned ldsdst) {
    unsigned keep;
    asm volatile("s_mov_b32 %0, m0\n\ts_mov_b32 m0, %3\n\ts_nop 2\n\tglobal_load_lds_dwordx4 %1, %2 sc1\n\tglobal_load_lds_dwordx4 %1, %2 offset:1024 sc1\n\tglobal_load_lds_dwordx4 %1, %2 offset:2048 sc1\n\ts_mov_b32 m0, %0"
                 : "=&s"(keep) : "v"(voff), "s"(sbase), "s"(ldsdst) : "memory");
}
__device__ __forceinline__ void glds_gu(unsigned va, unsigned vb, unsigned vc, const char* s1, const char* s3, unsigned ldsdst) {
    unsigned keep;
    asm volatile("s_mov_b32 %0, m0\n\ts_mov_b32 m0, %8\n\ts_nop 0\n\tglobal_load_lds_dwordx4 %1, %4\n\tglobal_load_lds_dwordx4 %2, %5 offset:1024\n\t"
                 "s_mov_b32 exec_hi, 0\n\tglobal_load_lds_dwordx4 %3, %6 offset:2048\n\ts_not_b64 exec, exec\n\tglobal_load_lds_dwordx4 %3, %7 offset:2048\n\t"
                 "s_mov_b64 exec, -1\n\ts_mov_b32 m0, %0"
                 : "=&s"(keep) : "v"(va), "v"(vb), "v"(vc), "s"(s1), "s"(s3 - 1024), "s"(s1 - 2048), "s"(s3 - 2048), "s"(ldsdst) : "memory", "scc");
}
__device__ __forceinline__ void l2touch(const char* ga) {
    unsigned keep;
    asm volatile("s_mov_b32 %0, m0\n\ts_mov_b32 m0, %2\n\ts_nop 0\n\tglobal_load_lds_dword %1, off\n\ts_mov_b32 m0, %0" : "=&s"(keep) : "v"(ga), "s"(L_MISC + 1024) : "memory");
}
__device__ __forceinline__ AS3 unsigned* lcnt(int c) { return LP(unsigned, L_MISC) + c; }
__device__ __forceinline__ void lwait(int c, unsigned target) {
    CFENCE();
    _Pragma("clang loop unroll(disable)") for (unsigned spins = 0; spins < (1u << 16); ++spins) {
        if ((unsigned)__builtin_amdgcn_readfirstlane(__hip_atomic_load(lcnt(c), RLX_WG)) >= target) break;
        __builtin_amdgcn_s_sleep(1);
    }
    CFENCE();
}
__device__ __forceinline__ void lwait_all(int c, int n, unsigned target, int lane) {
    CFENCE();
    AS3 unsigned* q = lcnt(c) + (lane < n ? lane : 0);
    _Pragma("clang loop unroll(disable)") for (unsigned spins = 0; spins < (1u << 16); ++spins) {
        if (__all(__hip_atomic_load(q, RLX_WG) >= target)) break;
        __builtin_amdgcn_s_sleep(1);
    }
    CFENCE();
}
__device__ __forceinline__ void lpost(int c, unsigned v) { CFENCE(); *(volatile AS3 unsigned*)lcnt(c) = v; CFENCE(); }
__device__ __forceinline__ unsigned lpeek(int c, int lane) { return *((volatile AS3 unsigned*)lcnt(c) + (lane & 3)); }
__device__ __forceinline__ void lcheck(unsigned pk, int c, unsigned target, int lane) {
    if (__builtin_expect(__any(pk < target), 0)) lwait_all(c, 4, target, lane);
    CFENCE();
}
__device__ __forceinline__ __amdgpu_buffer_rsrc_t mkrsrc(const char* base, unsigned bytes) {
    return __builtin_amdgcn_make_buffer_rsrc((void*)base, 0, bytes, 0x00020000);
}
__device__ __forceinline__ float dpp_sum16(float v) {
    v += __int_as_float(__builtin_amdgcn_update_dpp(0, __float_as_int(v), 0x128, 0xf, 0xf, false));
    v += __int_as_float(__builtin_amdgcn_update_dpp(0, __float_as_int(v), 0x124, 0xf, 0xf, false));
    v += __int_as_float(__builtin_amdgcn_update_dpp(0, __float_as_int(v), 0x122, 0xf, 0xf, false));
    v += __int_as_float(__builtin_amdgcn_update_dpp(0, __float_as_int(v), 0x121, 0xf, 0xf, false));
    return v;
}
__device__ __forceinline__ float wave_max(float v) {
    int x = __float_as_int(v);
    x = max(x, __builtin_amdgcn_update_dpp(0, x, 0x128, 0xf, 0xf, false)); x = max(x, __builtin_amdgcn_update_dpp(0, x, 0x124, 0xf, 0xf, false));
    x = max(x, __builtin_amdgcn_update_dpp(0, x, 0x122, 0xf, 0xf, false)); x = max(x, __builtin_amdgcn_update_dpp(0, x, 0x121, 0xf, 0xf, false));
    const int a = max(__builtin_amdgcn_readlane(x, 0), __builtin_amdgcn_readlane(x, 16)), b = max(__builtin_amdgcn_readlane(x, 32), __builtin_amdgcn_readlane(x, 48));
    return __int_as_float(max(a, b));
}
__device__ __forceinline__ bf16x8 pack8(const f32x16& a, int b, float sc) {
    u32x4 r = {pk2(a[b] * sc, a[b + 1] * sc), pk2(a[b + 2] * sc, a[b + 3] * sc), pk2(a[b + 4] * sc, a[b + 5] * sc), pk2(a[b + 6] * sc, a[b + 7] * sc)};
    return __builtin_bit_cast(bf16x8, r);
}

template <int I> struct IC { static constexpr int value = I; };
template <int B, int E, class F> __device__ __forceinline__ void static_for(F&& f) { if constexpr (B < E) { f(IC<B>{}); static_for<B + 1, E>(f); } }
#define WAITVN(n) asm volatile("s_waitcnt vmcnt(%0)" :: "i"(n) : "memory")
__device__ __forceinline__ void GLOAD16S(u32x4& dst, unsigned voff, const char* sbase) { asm volatile("global_load_dwordx4 %0, %1, %2 sc1" : "=v"(dst) : "v"(voff), "s"(sbase) : "memory"); }
__device__ __forceinline__ void GLOAD16N(u32x4& dst, unsigned voff, const char* sbase) { asm volatile("global_load_dwordx4 %0, %1, %2" : "=v"(dst) : "v"(voff), "s"(sbase) : "memory"); }
template <int N> __device__ __forceinline__ void waitv5(u32x4& a, u32x4& b, u32x4& c, u32x4& d, u32x4& e) { asm volatile("s_waitcnt vmcnt(%5)" : "+v"(a), "+v"(b), "+v"(c), "+v"(d), "+v"(e) : "i"(N) : "memory"); }
__device__ __forceinline__ void gload8x2_sc1(unsigned long long& a, unsigned long long& b, unsigned voff, const char* sbase) {
    asm volatile("global_load_dwordx2 %0, %2, %3 sc1\n\tglobal_load_dwordx2 %1, %2, %3 offset:512 sc1" : "=&v"(a), "=&v"(b) : "v"(voff), "s"(sbase) : "memory"); }
__device__ __forceinline__ void gload8x2_sc1_if(unsigned long long& a, unsigned long long& b, unsigned voff, const char* sbase, unsigned long long skip) {
    asm volatile("s_cmp_eq_u64 %4, -1\n\ts_cbranch_scc1 1f\n\tglobal_load_dwordx2 %0, %2, %3 sc1\n\tglobal_load_dwordx2 %1, %2, %3 offset:512 sc1\n1:" : "=&v"(a), "=&v"(b) : "v"(voff), "s"(sbase), "s"(skip) : "memory", "scc"); }
__device__ __forceinline__ void take8x2(unsigned long long& a, unsigned long long& b, unsigned long long sa, unsigned long long sb) {
    asm volatile("s_waitcnt vmcnt(0)\n\tv_mov_b64 %0, %2\n\tv_mov_b64 %1, %3" : "=&v"(a), "=&v"(b) : "v"(sa), "v"(sb) : "memory"); }
__device__ __forceinline__ void waitv8x2(unsigned long long& a, unsigned long long& b) { asm volatile("s_waitcnt vmcnt(0)" : "+v"(a), "+v"(b) :: "memory"); }
template <int N> __device__ __forceinline__ void waitv3(u32x4& a, u32x4& b, u32x4& c) { asm volatile("s_waitcnt vmcnt(%3)" : "+v"(a), "+v"(b), "+v"(c) : "i"(N) : "memory"); }

constexpr int dn_younger(int k, int D) {
    int n = 0, la = 0, lb = 0;
    for (int i = 0; i < D; ++i) { n += 2; if (i == k + 1) lb = n; }
    for (int i = 0; i < D; ++i) { n += 3; if (i == k) la = n; }
    for (int s = 0; s < k; ++s) {
        if (s + D < 24) { n += 2; if (s + D == k + 1) lb = n; }
        if (s >= 16) n += 2;
        if (s + D < 24) { n += 3; if (s + D == k) la = n; }
    }
    return n - (la > lb ? la : lb);
}
constexpr float QSCALE = 0.22360679774997896f * 1.4426950408889634f;
constexpr float BOUND_MAX = 50.f;

struct AttSync { int lane; const char* kv; unsigned dvoff; unsigned wo; int w; };
__device__ __forceinline__ unsigned kvslot(int c) { return L_KV + ((c + 2) & 3) * CHUNK; }
__device__ __forceinline__ void att_dma(const AttSync& a, int c) { glds16x3_sc1(a.dvoff, a.kv + (size_t)c * CHUNK, kvslot(c) + a.wo); }
constexpr unsigned AP0 = 16;
#define RING_X(c) do { WAITV(3); lpost(C_AP + as.w, AP0 + (c) + 1); } while (0)
#define RING_Y(c, pk) do { lcheck(pk, C_AP, AP0 + (c) + 1, lane); const int cn_ = (c) + 3 < NCHUNK ? (c) + 3 : NCHUNK - 1;   \
                           glds16x3_sc1(as.dvoff, as.kv + (size_t)cn_ * CHUNK, kvslot((c) + 3) + as.wo); } while (0)

__device__ __forceinline__ void att_fast(const AttSync& as, bf16x8 Q0a, s16x4 Q1a, bf16x8 Q0b, s16x4 Q1b, f32x16& Oa, f32x16& Ob) {
    const int lane = as.lane, r = lane & 31, h = lane >> 5;
    const unsigned ak0 = lane * 16, ak1 = h ? KV_V + 20 * 16 : KV_K1 + r * 16;
    const unsigned av = KV_V + h * 352 + (r <= 20 ? r : 21) * 16;
    const f32x16 zero = {};
    f32x16 Sa, Sb = {};
    bf16x8 K0[2], K1[2], V0[2], V1[2];
    u32x4 Pa0, Pa1, Pb0 = {}, Pb1 = {};
    unsigned pk = 0;
    V0[1] = bf16x8{}; V1[1] = bf16x8{};
    unsigned vk0 = L_KV + ak0, vk1 = L_KV + ak1, vv = L_KV + av;
    PIN(vk0); PIN(vk1); PIN(vv);
    K0[0] = ldsf(vk0 + 2 * CHUNK); K1[0] = ldsf(vk1 + 2 * CHUNK); K0[1] = ldsf(vk0 + 2 * CHUNK + 1024); V1[0] = ldsf(vv + 2 * CHUNK + 704); V0[0] = ldsf(vv + 2 * CHUNK);
    Sa = MFMA(K0[0], Q0a, zero); Sa = MFMA8(half4<0>(K1[0]), Q1a, Sa);
#define EX4(S, i) do { S[i] = __builtin_amdgcn_exp2f(S[i]); S[i + 1] = __builtin_amdgcn_exp2f(S[i + 1]); S[i + 2] = __builtin_amdgcn_exp2f(S[i + 2]); S[i + 3] = __builtin_amdgcn_exp2f(S[i + 3]); } while (0)
#define GAP(MF, LD, T, k, PW, j, S, i) do { MF; SBAR(); LD; PW[j] = pk2(T[k], T[k + 1]); PW[j + 1] = pk2(T[k + 2], T[k + 3]); PIN(PW); SBAR(); EX4(S, i); PIN(S); SBAR(); } while (0)
#define BC(x) __builtin_bit_cast(bf16x8, x)
#define STEP(e, d, KN, L3, LK0, LK1, LV) do { \
        GAP(Ob = MFMA(V0[e ^ 1], BC(Pb0), Ob), (void)0,                  Sb, 12, Pb1, 2, Sa, 0); \
        GAP(Sb = MFMA(K0[e], Q0b, zero),       (void)0,                  Sa, 0, Pa0, 0, Sa, 4); \
        GAP(Sb = MFMA8(half4<e>(K1[d]), Q1b, Sb), L3,                       Sa, 4, Pa0, 2, Sa, 8); \
        GAP(Ob = MFMA(V1[e ^ 1], BC(Pb1), Ob), K0[e] = ldsf(LK0),        Sa, 8, Pa1, 0, Sa, 12); \
        GAP(Oa = MFMA(V0[e], BC(Pa0), Oa),     LK1,                      Sa, 12, Pa1, 2, Sb, 0); \
        GAP(Sa = MFMA(K0[e ^ 1], Q0a, zero),   V1[e ^ 1] = ldsf((LV) + 704), Sb, 0, Pb0, 0, Sb, 4); \
        GAP(Sa = MFMA8(half4<e ^ 1>(K1[KN]), Q1a, Sa), V0[e ^ 1] = ldsf(LV),     Sb, 4, Pb0, 2, Sb, 8); \
        GAP(Oa = MFMA(V1[e], BC(Pa1), Oa),     (void)0,                  Sb, 8, Pb1, 0, Sb, 12); \
    } while (0)
#define CHUNK4(c, B, N) do { \
        STEP(0, 0, 0, (void)0, vk0 + (B) + KVD, K1[1] = ldsf(vk1 + (B) + KVD), vv + (B) + KV_VT); \
        RING_X(c); \
        STEP(1, 0, 1, pk = lpeek(C_AP, lane), vk0 + (B) + KVD + 1024, (void)0, vv + (B) + KVD); \
        RING_Y(c, pk); \
        STEP(0, 1, 1, (void)0, vk0 + (N), K1[0] = ldsf(vk1 + (N)), vv + (B) + KVD + KV_VT); \
        STEP(1, 1, 0, (void)0, vk0 + (N) + 1024, (void)0, vv + (N)); \
    } while (0)
    for (int c = 0; c < NCHUNK; c += 4) {
        CHUNK4(c, 2 * CHUNK, 3 * CHUNK); CHUNK4(c + 1, 3 * CHUNK, 0); CHUNK4(c + 2, 0, CHUNK); CHUNK4(c + 3, CHUNK, 2 * CHUNK);
    }
#undef CHUNK4
    Pb1[2] = pk2(Sb[12], Sb[13]); Pb1[3] = pk2(Sb[14], Sb[15]);
    Ob = MFMA(V0[1], BC(Pb0), Ob); Ob = MFMA(V1[1], BC(Pb1), Ob);
#undef STEP
#undef GAP
#undef EX4
}

__device__ __forceinline__ void att_safe(const AttSync& as, bf16x8 Q0a, s16x4 Q1a, bf16x8 Q0b, s16x4 Q1b, f32x16& Oa, f32x16& Ob) {
    const int lane = as.lane, r = lane & 31, h = lane >> 5;
    const unsigned ak0 = lane * 16, ak1 = h ? KV_V + 20 * 16 : KV_K1 + r * 16;
    const unsigned av = KV_V + h * 352 + (r <= 20 ? r : 21) * 16;
    const f32x16 zero = {};
    float ma = -INFINITY, mb = -INFINITY;
    for (int c = 0; c < NCHUNK; ++c) {
        const unsigned b0 = kvslot(c);
        for (int t = 0; t < 4; ++t) {
            if (t == 1) RING_X(c);
            if (t == 2) RING_Y(c, 0u);
            const unsigned base = b0 + (t >> 1) * KVD; const int e = t & 1;
            bf16x8 K0 = ldsf(base + ak0 + e * 1024), K1 = ldsf(base + ak1), V0 = ldsf(base + av + e * KV_VT), V1 = ldsf(base + av + e * KV_VT + 704);
#pragma unroll
            for (int q = 0; q < 2; ++q) {
                f32x16 S = MFMA(K0, q ? Q0b : Q0a, zero); S = MFMA8(e ? half4<1>(K1) : half4<0>(K1), q ? Q1b : Q1a, S);
                float mx = S[0];
#pragma unroll
                for (int i = 1; i < 16; ++i) mx = fmaxf(mx, S[i]);
                mx = fmaxf(mx, __shfl_xor(mx, 32));
                float& m = q ? mb : ma; f32x16& O = q ? Ob : Oa;
                const float mn = fmaxf(m, mx);
                const float al = __builtin_amdgcn_exp2f(m - mn);
                m = mn;
#pragma unroll
                for (int i = 0; i < 16; ++i) { O[i] *= al; S[i] = __builtin_amdgcn_exp2f(S[i] - mn); }
                O = MFMA(V0, pack8(S, 0, 1.f), O); O = MFMA(V1, pack8(S, 8, 1.f), O);
            }
        }
    }
}

__device__ __forceinline__ void xload8(u32x4 (&x)[5], u32x4 (&y)[5], const char* xb, unsigned vo, unsigned vo2, int skip) {
    asm volatile("s_cmp_lg_u32 %13, 0\n\ts_cbranch_scc1 1f\n\t"
                 "global_load_dwordx4 %0, %10, %12\n\tglobal_load_dwordx4 %1, %10, %12 offset:256\n\tglobal_load_dwordx4 %2, %10, %12 offset:512\n\t"
                 "global_load_dwordx4 %3, %10, %12 offset:768\n\tglobal_load_dwordx4 %4, %10, %12 offset:1024\n\t"
                 "global_load_dwordx4 %5, %11, %12\n\tglobal_load_dwordx4 %6, %11, %12 offset:256\n\tglobal_load_dwordx4 %7, %11, %12 offset:512\n\t"
                 "global_load_dwordx4 %8, %11, %12 offset:768\n\tglobal_load_dwordx4 %9, %11, %12 offset:1024\n1:"
                 : "=&v"(x[0]), "=&v"(x[1]), "=&v"(x[2]), "=&v"(x[3]), "=&v"(x[4]), "=&v"(y[0]), "=&v"(y[1]), "=&v"(y[2]), "=&v"(y[3]), "=&v"(y[4])
                 : "v"(vo), "v"(vo2), "s"(xb), "s"(skip) : "memory", "scc");
}
__device__ __forceinline__ void ln_rows(const u32x4 (&xr)[5], int row0, int lane) {
    f32x2 xv[20];
    f32x2 s0 = {0.f, 0.f}, s1 = {0.f, 0.f}, q0 = {0.f, 0.f}, q1 = {0.f, 0.f};
#pragma unroll
    for (int i = 0; i < 20; i += 2) {
        const unsigned a = xr[i >> 2][i & 3], b = xr[i >> 2][(i & 3) + 1];
        xv[i] = f32x2{bflo(a), bfhi(a)}; xv[i + 1] = f32x2{bflo(b), bfhi(b)};
        s0 += xv[i]; q0 = __builtin_elementwise_fma(xv[i], xv[i], q0);
        s1 += xv[i + 1]; q1 = __builtin_elementwise_fma(xv[i + 1], xv[i + 1], q1);
    }
    s0 += s1; q0 += q1;
    const float s = dpp_sum16(s0[0] + s0[1]), ss = dpp_sum16(q0[0] + q0[1]);
    const float mean = s * (1.f / 640.f);
    const float var = fmaxf(ss * (1.f / 640.f) - mean * mean, 0.f);
    const float rs = __builtin_amdgcn_rcpf(rbf(__builtin_amdgcn_sqrtf(rbf(rbf(var) + 1e-5f)))), mo = -rbf(mean) * rs;
    const f32x2 rs2 = {rs, rs}, mo2 = {mo, mo};
    const int row = row0 + (lane >> 4), c = lane & 15;
    const unsigned wa = L_PANEL + (row >> 5) * 40960 + (c >> 1) * 1024 + (c & 1) * 512 + (((row & 31) ^ (c & 7)) << 4);
#pragma unroll
    for (int u = 0; u < 5; ++u) {
        u32x4 o;
#pragma unroll
        for (int e = 0; e < 4; ++e) { const f32x2 y = __builtin_elementwise_fma(xv[4 * u + e], rs2, mo2); o[e] = pk2(y[0], y[1]); }
        *LP(u32x4, wa + u * 8192) = o;
    }
}
#define BLOCK_BARRIER() asm volatile("s_waitcnt lgkmcnt(0)\n\ts_barrier" ::: "memory")

__global__ void __launch_bounds__(512, 2) mega(Params p) {
    const int w = __builtin_amdgcn_readfirstlane(threadIdx.x >> 6);
    const int b = blockIdx.x, xc = b & 7, j = b >> 3;
    const int ti = (xc >> 1) * 8 + (j >> 2), g = (xc & 1) * 4 + (j & 3);
    const int kmt = g & 1;
    const int ra = kmt * 32 + (w & 3) * 8, rb = (kmt ^ 1) * 32 + (w & 3) * 8;
    u32x4 x0[5], x1[5], x2[5], x3[5];
    {
        asm volatile("" :: "s"(p.x), "s"(p.ws), "s"(p.wq), "s"(p.wk));
        const char* xb = p.x + (size_t)ti * 64 * 1280;
        const unsigned xvo = ((threadIdx.x >> 4) & 3) * 1280 + (threadIdx.x & 15) * 16;
        xload8(x0, x1, xb + (size_t)ra * 1280, xvo, xvo + 5120, w >> 2);
        xload8(x2, x3, xb + (size_t)rb * 1280, xvo, xvo + 5120, w >> 2);
    }
    const char* pc; asm volatile("s_getpc_b64 %0" : "=s"(pc));
    unsigned tag;
    char* ws = p.ws;
    if (threadIdx.x < C_N) *lcnt(threadIdx.x) = (threadIdx.x == C_WP || threadIdx.x == C_WP + 1 || threadIdx.x == C_PP || threadIdx.x == C_PP + 1) ? 100u : 0u;
    const bool haskv = (g >> 2) == (ti & 1);
    const char* wqb = p.wq + g * 160;
#define LANE_CONSTS() int lane = __builtin_amdgcn_mbcnt_hi(~0u, __builtin_amdgcn_mbcnt_lo(~0u, 0u)); PIN(lane); \
    const int r = lane & 31, h = lane >> 5, tq = (lane & 15) >> 2, tp = lane & 3, tb = (lane >> 4) & 1; (void)r; (void)h; (void)tq; (void)tp; (void)tb

    if (w >= 4) {
        LANE_CONSTS();
        const int v = w - 4;
        unsigned epv = 0u;
        {
            asm volatile("global_load_dword %0, %1, %2 sc1" : "=v"(epv) : "v"(0u), "s"(p.ws + WS_EP) : "memory");
            constexpr unsigned CODE_PF = 49152;
            const unsigned line = (unsigned)(j * 4 + v) * 64u + lane;
            if (line * 128u < CODE_PF) l2touch(pc + line * 128u);
        }
#define GET_TAG() do { { asm volatile("s_nop 0\n\tv_readfirstlane_b32 %0, %1\n\ts_nop 3" : "=s"(tag) : "v"(epv) : "memory"); tag += 0x9E3779B1u; } } while (0)
        __builtin_amdgcn_s_setprio(3);
        {
            const int kvm = 2 * ti + (g & 1); const bool isV = (g >> 1) & 1;
            const char* wkvb = isV ? p.wv : p.wk;
            if (haskv && v < 2) {
#pragma unroll
                for (int n = 0; n < 13; ++n) {
                    const unsigned o = v * 12800 + (n < 12 ? n * 1024 : 11776);
                    glds16(o + lane * 16, wkvb, L_WKV + o);
                }
            }
            BLOCK_BARRIER();
            if (v >= 2) {
                const int v2 = v - 2;
                unsigned wo5[5];
#pragma unroll
                for (int i = 0; i < 5; ++i) { const unsigned c = (v2 + 2 * i) * 64 + lane, row = c / 10u, col = c - row * 10u; wo5[i] = row * 1280 + col * 16; }
#pragma unroll
                for (int n = 0; n < 20; ++n) glds16(wo5[n % 5], wqb + (n / 5) * 64 * 1280, L_WQ + (v2 + 2 * n) * 1024);
                u32x4 wst[3][10];
                auto wst_issue = [&](auto I) {
                    constexpr int rr = I.value / 10, i = I.value % 10;
                    GLOAD16N(wst[rr][i], wo5[i % 5], wqb + (4 + 2 * rr + i / 5) * 64 * 1280);
                };
                static_for<0, 10>(wst_issue);
                WAITV(10);
                GET_TAG(); lpost(C_TAG, tag);
                lpost(C_PP + v, 1);
                if (haskv) lwait(C_KVFIN, 1);
                static_for<10, 30>(wst_issue);
                static_for<0, 3>([&](auto R) {
                    constexpr int rr = R.value;
                    waitv5<20 - 10 * rr>(wst[rr][0], wst[rr][1], wst[rr][2], wst[rr][3], wst[rr][4]); waitv5<63>(wst[rr][5], wst[rr][6], wst[rr][7], wst[rr][8], wst[rr][9]);
                    lwait_all(C_AP, 4, 3 + rr, lane);
#pragma unroll
                    for (int i = 0; i < 10; ++i) *LP(u32x4, L_WQ + (rr & 1) * 20480 + (v2 + 2 * i) * 1024 + lane * 16) = wst[rr][i];
                    lpost(C_WP + v, rr + 1);
                });
            } else {
              if (haskv) {
                f32x16 acc = {};
                const unsigned wa = L_WKV + (v * 320 + 8 * h + tq) * 40 + tb * 32 + (tb ? 0 : tp * 8);
                unsigned xa[4];
#pragma unroll
                for (int s = 0; s < 4; ++s) xa[s] = L_PANEL + kmt * 40960 + v * 20480 + h * 512 + ((r ^ ((2 * s + h) & 7)) << 4);
                WAITV(0);
                GET_TAG();
                bf16x8 wf[20], xf[20];
#pragma unroll
                for (int s = 0; s < 20; ++s) wf[s] = ldstr2(wa + s * 640, wa + s * 640 + 160);
                lwait_all(C_AP, 4, 1, lane);
#pragma unroll
                for (int s = 0; s < 20; ++s) xf[s] = ldsf(xa[s & 3] + s * 1024);
                SBAR();
                if (isV) {
#pragma unroll
                    for (int s = 0; s < 20; ++s) acc = MFMA(xf[s], wf[s], acc);
                } else {
#pragma unroll
                    for (int s = 0; s < 20; ++s) acc = MFMA(wf[s], xf[s], acc);
                }
                if (v) {
#pragma unroll
                    for (int e = 0; e < 4; ++e) *LP(f32x4, L_WKV + 12800 + e * 1024 + lane * 16) = f32x4{acc[4 * e], acc[4 * e + 1], acc[4 * e + 2], acc[4 * e + 3]};
                    lpost(C_KVP, 1);
                } else {
                    lwait(C_KVP, 1);
#pragma unroll
                    for (int e = 0; e < 4; ++e) {
                        const f32x4 t = *LP(const f32x4, L_WKV + 12800 + e * 1024 + lane * 16);
                        acc[4 * e] += t[0]; acc[4 * e + 1] += t[1]; acc[4 * e + 2] += t[2]; acc[4 * e + 3] += t[3];
                    }
                    const __amdgpu_buffer_rsrc_t rk = mkrsrc(ws + WS_KV, 32 * KVD);
                    const unsigned base = ti * KVD, ke = g & 1;
                    const u32x4 f0 = __builtin_bit_cast(u32x4, pack8(acc, 0, 1.f)), f1 = __builtin_bit_cast(u32x4, pack8(acc, 8, 1.f));
                    float mx;
                    if (!isV) {
                        __builtin_amdgcn_raw_buffer_store_b128(f0, rk, base + ke * 1024 + lane * 16, 0, 16);
                        const u32x2 k1 = {f1[0], f1[1]};
                        if (!h) __builtin_amdgcn_raw_buffer_store_b64(k1, rk, base + KV_K1 + r * 16 + ke * 8, 0, 16);
                        lpost(C_KVFIN, 1);
                        float n2 = 0.f;
#pragma unroll
                        for (int e = 0; e < 4; ++e) n2 += bflo(f0[e]) * bflo(f0[e]) + bfhi(f0[e]) * bfhi(f0[e]);
                        if (!h) n2 += bflo(f1[0]) * bflo(f1[0]) + bfhi(f1[0]) * bfhi(f1[0]) + bflo(f1[1]) * bflo(f1[1]) + bfhi(f1[1]) * bfhi(f1[1]);
                        n2 += __shfl_xor(n2, 32);
                        mx = (n2 == n2) ? n2 : INFINITY;
                    } else {
                        const unsigned cst = r == 20 ? 0x3f803f80u : 0u;
                        const u32x4 c4 = {cst, cst, cst, cst};
                        if (r <= 21) {
                            __builtin_amdgcn_raw_buffer_store_b128(r >= 20 ? c4 : f0, rk, base + KV_V + ke * KV_VT + h * 352 + r * 16, 0, 16);
                            __builtin_amdgcn_raw_buffer_store_b128(r >= 20 ? c4 : f1, rk, base + KV_V + ke * KV_VT + 704 + h * 352 + r * 16, 0, 16);
                        }
                        lpost(C_KVFIN, 1);
                        float vm = 0.f;
#pragma unroll
                        for (int e = 0; e < 16; ++e) { const float a = fabsf(acc[e]); vm = (a <= vm) ? vm : ((a == a) ? a : INFINITY); }
                        mx = r < 20 ? vm : 0.f;
                    }
                    mx = wave_max(mx);
                    WAITV(0);
                    if (lane < 8) __hip_atomic_store((gu64*)(ws + WS_KF + lane * 1024 + (isV ? 512 : 0)) + kvm, ((unsigned long long)tag << 32) | __float_as_uint(mx), RLX_AGENT);
                }
            }
              else { WAITV(0); GET_TAG(); }
            }
            if (haskv) lwait(C_KVFIN, 1);
#pragma unroll
            for (int n = 0; n < 2; ++n) {
                unsigned L = n * 64 + lane; L = (j * 4 + v) * 120 + (L < 120u ? L : 119u);
                const unsigned m3 = L >= 7680u, rem = L - (m3 ? 7680u : 0u), row = rem / 12u;
                l2touch((m3 ? p.w3 : p.w1) + row * 3072 + (xc & 1) * 1536 + (rem - row * 12u) * 128);
            }
            lwait_all(C_AP, 4, 6, lane);
            {
                const char* w1s = p.w1 + g * 384 + v * 96; const char* w3s = p.w3 + g * 384 + v * 96;
                auto goff = [](unsigned idx) { const unsigned row = idx / 6u; return row * 3072 + (idx - row * 6u) * 16; };
                const unsigned gva = goff(lane), gvb = goff((lane - 8) & 63), gvc = goff(64 + (h ? (r - 8) & 31 : r));
                const unsigned gdst = L_GU + v * 9216;
                auto gdma = [&](auto S) { constexpr int s = S.value; glds_gu(gva, gvb, gvc, w1s + (size_t)s * 16 * 3072, w3s + (size_t)s * 16 * 3072, gdst + (s % 3) * 3072); };
                static_for<0, 3>(gdma);
                f32x16 acc[2][3] = {};
                unsigned bn[3][2], pa[4], pb[4];
#pragma unroll
                for (int n = 0; n < 3; ++n)
#pragma unroll
                    for (int t = 0; t < 2; ++t) {
                        const unsigned idx = 6 * (8 * h + 4 * t + tq) + 2 * n + (tp >> 1);
                        const unsigned o = idx < 64u ? (tb ? 1024 + ((idx + 8) & 63) * 16 : idx * 16) : (tb ? 2560 + ((idx - 56) & 31) * 16 : 2048 + (idx - 64) * 16);
                        bn[n][t] = gdst + o + (tp & 1) * 8;
                    }
#pragma unroll
                for (int s = 0; s < 4; ++s) { pa[s] = L_PANEL + h * 512 + ((r ^ ((2 * s + h) & 7)) << 4); pb[s] = pa[s] + 40960; PIN(pb[s]); }
                bf16x8 wf[2][3], xa[2], xb2[2];
                auto rdw = [&](auto S) { constexpr int s = S.value;
#pragma unroll
                    for (int n = 0; n < 3; ++n) wf[s & 1][n] = ldstr2(bn[n][0] + (s % 3) * 3072, bn[n][1] + (s % 3) * 3072); };
                auto rdx = [&](auto S) { constexpr int s = S.value; xa[s & 1] = ldsf(pa[s & 3] + s * 1024); xb2[s & 1] = ldsf(pb[s & 3] + s * 1024); };
                WAITV(8);
                rdx(IC<0>{}); rdw(IC<0>{});
                asm volatile("s_waitcnt lgkmcnt(0)" ::: "memory");
                gdma(IC<3>{});
                __builtin_amdgcn_s_setprio(0);
                static_for<0, 40>([&](auto S) {
                    constexpr int s = S.value; constexpr bool nx = s + 1 < 40;
                    acc[0][0] = MFMA(wf[s & 1][0], xa[s & 1], acc[0][0]); SBAR();
                    if constexpr (nx) rdx(IC<s + 1>{});
                    SBAR();
                    acc[1][0] = MFMA(wf[s & 1][0], xb2[s & 1], acc[1][0]); SBAR();
                    if constexpr (nx) { WAITVN(4 * ((s + 3 < 39 ? s + 3 : 39) - (s + 1))); rdw(IC<s + 1>{}); }
                    SBAR();
                    acc[0][1] = MFMA(wf[s & 1][1], xa[s & 1], acc[0][1]); SBAR();
                    acc[1][1] = MFMA(wf[s & 1][1], xb2[s & 1], acc[1][1]); SBAR();
                    if constexpr (s + 4 < 40) { asm volatile("s_waitcnt lgkmcnt(0)" ::: "memory"); gdma(IC<s + 4>{}); }
                    SBAR();
                    acc[0][2] = MFMA(wf[s & 1][2], xa[s & 1], acc[0][2]); SBAR();
                    acc[1][2] = MFMA(wf[s & 1][2], xb2[s & 1], acc[1][2]); SBAR();
                });
                lpost(C_GP + v, 42);
                __builtin_amdgcn_s_setprio(3);
                const __amdgpu_buffer_rsrc_t rh = mkrsrc(ws + WS_HT, 64 * 96 * 1024);
#pragma unroll
                for (int m = 0; m < 2; ++m)
#pragma unroll
                    for (int n = 0; n < 3; ++n) {
                        f32x16 hv;
#pragma unroll
                        for (int e = 0; e < 8; ++e) {
                            const float gg = acc[m][n][e];
                            hv[e] = gg * __builtin_amdgcn_rcpf(1.f + __builtin_amdgcn_exp2f(-1.4426950408889634f * gg)) * acc[m][n][e + 8];
                        }
                        __builtin_amdgcn_raw_buffer_store_b128(__builtin_bit_cast(u32x4, pack8(hv, 0, 1.f)), rh, ((2 * ti + m) * 96 + g * 12 + 3 * v + n) * 1024 + lane * 16, 0, 16);
                    }
                {
                    WAITV(0);
                    if (lane < 2) __hip_atomic_store((gu32*)(ws + WS_HF) + (2 * ti + lane) * 32 + g * 4 + v, tag, RLX_AGENT);
                }
                __builtin_amdgcn_s_setprio(0);
                {
#pragma unroll
                    for (int n = 0; n < 2; ++n) {
                        unsigned L = (j * 4 + v) * 85 + n * 64 + lane; L = L < 10880u ? L : 10879u;
                        const unsigned row = L / 5u;
                        l2touch((row < 1536u ? p.w2 + row * 1280 : p.wo + (row - 1536u) * 1280) + (xc & 1) * 640 + (L - row * 5u) * 128);
                    }
                }
            }
        }
        {
            LANE_CONSTS();
            const unsigned bq = ((tb * 32 + (tp >> 1) * 16) ^ ((unsigned)(tq >> 1) << 6)) + (tp & 1) * 8;
            const unsigned drow = lane >> 3, dcol = ((lane & 7) ^ (((lane >> 4) & 1) << 2)) * 16;
            const int G = xc >> 1, r0 = G < 2 ? 6 * G : 12 + 5 * (G - 2), nr = G < 2 ? 6 : 5;
            {
                if (b == 255 && v == 0) {
                    gu32* f = (gu32*)(ws + WS_HF) + h * 64 + r;
                    _Pragma("clang loop unroll(disable)") for (unsigned spins = 0; spins < (1u << 14); ++spins) {
                        bool ok = true;
#pragma unroll
                        for (int n = 0; n < 16; ++n) ok &= __hip_atomic_load(f + n * 128, RLX_AGENT) == tag;
                        if (__all(ok)) break;
                        __builtin_amdgcn_s_sleep(32);
                    }
                    if (lane == 0) __hip_atomic_store((gu32*)(ws + WS_EP), tag, RLX_AGENT);
                }
            }
            if (j < 5 * nr) {
                const int dr = r0 + j / 5, dc = (xc & 1) * 5 + j % 5;
                const int mfirst = dr < 20 ? 3 * dr : 60 + 2 * (dr - 20), nm = dr < 20 ? 3 : 2;
                const unsigned dvo = drow * 1280 + dcol;
                const unsigned myring = L_PANEL + v * 20480;
                f32x16 acc0 = {}, acc1 = {};
                { lwait_all(C_GP, 4, 42, lane); lwait_all(C_AP, 4, 8, lane); }
                {
                    f32x16 da[3][2] = {};
                    const int drot = (j % 5) * 4;
                    const char* bsrc = p.w2 + (size_t)v * 384 * 1280 + dc * 128;
                    const char* asrc = ws + WS_HT + ((size_t)mfirst * 96 + v * 24) * 1024;
                    int mr[3]; unsigned am[3];
#pragma unroll
                    for (int mm = 0; mm < 3; ++mm) { mr[mm] = (mm + v) % 3; am[mm] = lane * 16 + (mr[mm] < nm ? mr[mm] : 0) * 98304; }
                    constexpr int DQ = 8;
                    u32x4 aq[DQ][3];
                    auto bissue = [&](auto K) {
                        constexpr int k = K.value;
                        int kr = k + drot; kr = kr >= 24 ? kr - 24 : kr;
                        const char* sb = bsrc + (size_t)kr * 16 * 1280;
                        glds16(dvo, sb, myring + (k % 10) * 2048); glds16(dvo + 8 * 1280, sb, myring + (k % 10) * 2048 + 1024);
                    };
                    auto woissue = [&](auto U) {
                        constexpr int u = U.value;
                        const char* sb = p.wo + (size_t)(v * 160 + u * 16) * 1280 + dc * 128;
                        glds16(dvo, sb, myring + u * 2048); glds16(dvo + 8 * 1280, sb, myring + u * 2048 + 1024);
                    };
                    auto aissue = [&](auto K) {
                        constexpr int k = K.value;
                        int kr = k + drot; kr = kr >= 24 ? kr - 24 : kr;
                        const char* sb = asrc + (size_t)kr * 1024;
                        GLOAD16S(aq[k % DQ][0], am[0], sb); GLOAD16S(aq[k % DQ][1], am[1], sb); GLOAD16S(aq[k % DQ][2], am[2], sb);
                    };
                    static_for<0, DQ>(bissue);
                    {
                        gu32* f = (gu32*)(ws + WS_HF) + (mfirst + ((lane >> 3) < nm ? (lane >> 3) : 0)) * 32 + 8 * v + (lane & 7);
                        _Pragma("clang loop unroll(disable)") for (unsigned spins = 0; spins < (1u << 14); ++spins) {
                            if (__all(__hip_atomic_load(f, RLX_AGENT) == tag)) break;
                            __builtin_amdgcn_s_sleep(20);
                        }
                        CFENCE();
                    }
                    static_for<0, DQ>(aissue);
                    const unsigned ba0 = myring + (4 * h + tq) * 128 + bq, ba1 = ba0 ^ 64;
                    bf16x8 w0[2], w1[2];
                    WAITVN(3 * DQ);
                    w0[0] = ldstr2(ba0, ba0 + 1024); w1[0] = ldstr2(ba1, ba1 + 1024);
                    static_for<0, 24>([&](auto K) {
                        constexpr int k = K.value;
                        waitv3<dn_younger(k, DQ)>(aq[k % DQ][0], aq[k % DQ][1], aq[k % DQ][2]);
                        constexpr unsigned o = ((k + 1) % 10) * 2048;
#define DXF(m) __builtin_bit_cast(bf16x8, aq[k % DQ][m])
                        SBAR();
                        da[0][0] = MFMA(w0[k & 1], DXF(0), da[0][0]); SBAR();
                        if constexpr (k + 1 < 24) w0[(k + 1) & 1] = ldstr2(ba0 + o, ba0 + o + 1024);
                        SBAR();
                        da[0][1] = MFMA(w1[k & 1], DXF(0), da[0][1]); SBAR();
                        if constexpr (k + 1 < 24) w1[(k + 1) & 1] = ldstr2(ba1 + o, ba1 + o + 1024);
                        SBAR();
                        da[1][0] = MFMA(w0[k & 1], DXF(1), da[1][0]); SBAR();
                        if constexpr (k + DQ < 24) bissue(IC<k + DQ>{});
                        if constexpr (k >= 16) woissue(IC<(k + 8) % 10>{});
                        SBAR();
                        da[1][1] = MFMA(w1[k & 1], DXF(1), da[1][1]); SBAR();
                        da[2][0] = MFMA(w0[k & 1], DXF(2), da[2][0]); SBAR();
                        da[2][1] = MFMA(w1[k & 1], DXF(2), da[2][1]); SBAR();
                        if constexpr (k + DQ < 24) aissue(IC<k + DQ>{});
                    });
                    __builtin_amdgcn_s_setprio(3);
                    { CFENCE(); woissue(IC<2>{}); woissue(IC<3>{}); }
#pragma unroll
                    for (int n = 0; n < 2; ++n) {
#pragma unroll
                        for (int m = 0; m < 3; ++m) {
                            if (m != 0 || v == 3) {
                                const unsigned dst = L_GU + (mr[m] * 3 + (v > mr[m] ? v - 1 : v)) * 4096 + lane * 16;
#pragma unroll
                                for (int e = 0; e < 4; ++e) *LP(f32x4, dst + e * 1024) = f32x4{da[m][n][4 * e], da[m][n][4 * e + 1], da[m][n][4 * e + 2], da[m][n][4 * e + 3]};
                            }
                        }
                        lpost(C_DP + v, 2 * n + 1); lwait_all(C_DP, 4, 2 * n + 1, lane);
                        if (v < 3) {
                            f32x16 t = da[0][n];
#pragma unroll
                            for (int o = 0; o < 3; ++o)
#pragma unroll
                                for (int e = 0; e < 4; ++e) {
                                    const f32x4 u = *LP(const f32x4, L_GU + (v * 3 + o) * 4096 + e * 1024 + lane * 16);
                                    t[4 * e] += u[0]; t[4 * e + 1] += u[1]; t[4 * e + 2] += u[2]; t[4 * e + 3] += u[3];
                                }
                            if (n) acc1 = t; else acc0 = t;
                        }
                        if (n == 0) { lpost(C_DP + v, 2); lwait_all(C_DP, 4, 2, lane); }
                    }
                }
                { WAITV(0); lpost(C_DP + v, 4); }
                {
                    if (v == 3) {
                        __builtin_amdgcn_s_setprio(0);
                        const int mi = (lane >> 4) < nm ? (lane >> 4) : 0;
                        gu64* f = (gu64*)(ws + WS_OF + (mfirst + mi) * 128) + (lane & 15);
                        _Pragma("clang loop unroll(disable)") for (unsigned spins = 0; spins < (1u << 14); ++spins) {
                            const unsigned long long x = __hip_atomic_load(f, RLX_AGENT);
                            const unsigned long long ok = __ballot((unsigned)x == tag && (unsigned)(x >> 32) == tag);
                            const unsigned mine = (unsigned)(ok >> ((lane & 3) * 16));
                            *((volatile AS3 unsigned*)lcnt(C_OS) + (lane & 3)) = ((mine & 0xffu) == 0xffu ? 1u : 0u) | ((mine & 0xff00u) == 0xff00u ? 2u : 0u);
                            if (ok == ~0ull) break;
                            __builtin_amdgcn_s_sleep(8);
                        }
                    }
                }
                if (v < nm) {
                    const int m = mfirst + v;
                    const unsigned ba0 = L_PANEL + (4 * h + tq) * 128 + bq, ba1 = ba0 ^ 64;
                    {
                        const char* xrow = p.x + (size_t)(m * 32 + r) * 1280 + dc * 128 + h * 16;
                        u32x4 xres[4];
#pragma unroll
                        for (int e = 0; e < 4; ++e) xres[e] = *(const u32x4*)(xrow + e * 32);
                        lwait_all(C_DP, 4, 4, lane);
                        const __amdgpu_buffer_rsrc_t ro = mkrsrc(ws + WS_OT + (size_t)m * 40960, 40960);
                        int hf = 0;
                        f32x16 t0[2] = {}, t1[2] = {};
#pragma unroll
                        for (int it = 0; it < 2; ++it) {
                            {
                                _Pragma("clang loop unroll(disable)") for (unsigned spins = 0; spins < (1u << 17); ++spins) {
                                    const unsigned ok = __builtin_amdgcn_readfirstlane(*((volatile AS3 unsigned*)lcnt(C_OS) + v));
                                    if (it == 0) { if (ok) { hf = (ok & 1) ? 0 : 1; break; } }
                                    else if (ok == 3) break;
                                    __builtin_amdgcn_s_sleep(1);
                                }
                                CFENCE();
                            }
                            u32x4 of[20];
#pragma unroll
                            for (int s = 0; s < 20; ++s) of[s] = __builtin_amdgcn_raw_buffer_load_b128(ro, lane * 16 + s * 1024, hf * 20480, 16);
                            const unsigned a0 = ba0 + hf * 40960, a1 = ba1 + hf * 40960, l0 = a0 + (36 * h + 16) * 128, l1 = a1 + (36 * h + 16) * 128;
                            bf16x8 w0[2], w1[2];
                            w0[0] = ldstr2(a0, a0 + 1024); w1[0] = ldstr2(a1, a1 + 1024);
                            static_for<0, 20>([&](auto S) {
                                constexpr int s = S.value, n = s + 1, gg = n / 5, jj = n % 5;
                                const bf16x8 xf = __builtin_bit_cast(bf16x8, of[s]);
                                if constexpr (n < 20) {
                                    if constexpr (jj < 4) {
                                        constexpr unsigned o = (80 * gg + 20 * jj) * 128;
                                        w0[n & 1] = ldstr2(a0 + o, a0 + o + 1024); w1[n & 1] = ldstr2(a1 + o, a1 + o + 1024);
                                    } else {
                                        constexpr unsigned o = 80 * gg * 128;
                                        w0[n & 1] = ldstr2(l0 + o, l0 + o + 2560); w1[n & 1] = ldstr2(l1 + o, l1 + o + 2560);
                                    }
                                }
                                SBAR();
                                t0[it] = MFMA(w0[s & 1], xf, t0[it]); t1[it] = MFMA(w1[s & 1], xf, t1[it]);
                                SBAR();
                            });
                            hf ^= 1;
                        }
                        acc0 += t0[0] + t0[1]; acc1 += t1[0] + t1[1];
                        char* orow = p.out + (size_t)(m * 32 + r) * 1280 + dc * 128 + h * 16;
#pragma unroll
                        for (int n = 0; n < 2; ++n) {
                            const f32x16& a = n ? acc1 : acc0;
#pragma unroll
                            for (int qq = 0; qq < 2; ++qq) {
                                float lo[4], hi[4];
#pragma unroll
                                for (int e = 0; e < 4; ++e) {
                                    auto sw = __builtin_amdgcn_permlane32_swap(__float_as_uint(a[8 * qq + e]), __float_as_uint(a[8 * qq + 4 + e]), false, false);
                                    lo[e] = __uint_as_float(sw[0]); hi[e] = __uint_as_float(sw[1]);
                                }
                                const u32x4 xv = xres[2 * n + qq];
                                const u32x4 ov = {pk2(lo[0] + bflo(xv[0]), lo[1] + bfhi(xv[0])), pk2(lo[2] + bflo(xv[1]), lo[3] + bfhi(xv[1])),
                                                  pk2(hi[0] + bflo(xv[2]), hi[1] + bfhi(xv[2])), pk2(hi[2] + bflo(xv[3]), hi[3] + bfhi(xv[3]))};
                                *(u32x4*)(orow + (2 * n + qq) * 32) = ov;
                            }
                        }
                    }
                }
            }
        }
    } else {
        LANE_CONSTS();
        bf16x8 Q0a, Q0b; u32x2 q1a, q1b;
        const int head = 4 * g + w;
        const AttSync as{lane, ws + WS_KV, (unsigned)((w < 3 ? w * 3072 : CHUNK - 3072) + lane * 16), (unsigned)(w < 3 ? w * 3072 : CHUNK - 3072), w};
        __builtin_amdgcn_s_setprio(3);
        unsigned long long fk = 0, fv = 0, fk2, fv2; bool early = false;
        {
            BLOCK_BARRIER();
            waitv5<15>(x0[0], x0[1], x0[2], x0[3], x0[4]);
            ln_rows(x0, ra, lane);
            waitv5<10>(x1[0], x1[1], x1[2], x1[3], x1[4]);
            ln_rows(x1, ra + 4, lane);
            lpost(C_AP + w, 1);
            waitv5<5>(x2[0], x2[1], x2[2], x2[3], x2[4]);
            ln_rows(x2, rb, lane);
            waitv5<0>(x3[0], x3[1], x3[2], x3[3], x3[4]);
            ln_rows(x3, rb + 4, lane);
            lpost(C_AP + w, 2);
            unsigned pa[4];
#pragma unroll
            for (int s = 0; s < 4; ++s) pa[s] = L_PANEL + h * 512 + ((r ^ ((2 * s + h) & 7)) << 4);
            f32x16 qa = {}, qb = {};
            const unsigned wa = L_WQ + (8 * h + tq) * 160 + w * 40 + tb * 32 + (tb ? 0 : tp * 8);
            bf16x8 fw[3][4], fa[2][4], fb[2][4];
            auto rdw = [&](auto S, auto S4) { constexpr int st = S.value, s4 = S4.value; const unsigned o = wa + (st & 3) * 10240 + s4 * 2560; fw[st % 3][s4] = ldstr2(o, o + 640); };
            lwait_all(C_PP, 4, 1, lane);
            static_for<0, 4>([&](auto I) { rdw(IC<0>{}, I); rdw(IC<1>{}, I); });
            tag = __builtin_amdgcn_readfirstlane(*(volatile AS3 unsigned*)lcnt(C_TAG));
            lpost(C_AP + w, 3);
            lwait_all(C_AP, 4, 2, lane);
#pragma unroll
            for (int s4 = 0; s4 < 4; ++s4) { fa[0][s4] = ldsf(pa[s4] + s4 * 1024); fb[0][s4] = ldsf(pa[s4] + 40960 + s4 * 1024); }
            unsigned pk = 0;
            static_for<0, 10>([&](auto S) {
                constexpr int st = S.value, nx = st + 1;
                if constexpr (st == 3 || st == 5 || st == 7) lcheck(pk, C_WP, (st - 1) / 2, lane);
                if constexpr (st == 6) gload8x2_sc1(fk, fv, lane * 8, ws + WS_KF + xc * 1024);
                if constexpr (st == 8) {
                    waitv8x2(fk, fv);
                    const unsigned long long up = __ballot((unsigned)(fk >> 32) == tag && (unsigned)(fv >> 32) == tag);
                    early = up == ~0ull;
                    gload8x2_sc1_if(fk2, fv2, lane * 8, ws + WS_KF + xc * 1024, up);
                    if (early) { lwait_all(C_AP, 4, 6, lane); att_dma(as, 0); att_dma(as, 1); }
                }
                static_for<0, 4>([&](auto S4) {
                    constexpr int s4 = S4.value;
                    qa = MFMA(fw[st % 3][s4], fa[st & 1][s4], qa); SBAR();
                    if constexpr (st == 0) rdw(IC<2>{}, S4);
                    if constexpr (st == 1) rdw(IC<3>{}, S4);
                    if constexpr (st == 3 || st == 5 || st == 7) { rdw(IC<st + 1>{}, S4); rdw(IC<st + 2>{}, S4); }
                    SBAR();
                    qb = MFMA(fw[st % 3][s4], fb[st & 1][s4], qb); SBAR();
                    if constexpr (nx < 10) { fa[nx & 1][s4] = ldsf(pa[s4] + (nx * 4 + s4) * 1024); fb[nx & 1][s4] = ldsf(pa[s4] + 40960 + (nx * 4 + s4) * 1024); SBAR(); }
                    if constexpr (s4 == 2 && (st == 2 || st == 4 || st == 6)) { pk = lpeek(C_WP, lane); SBAR(); }
                });
                if constexpr (st == 1 || st == 3 || st == 5) lpost(C_AP + w, 4 + st / 2);
            });
            lpost(C_AP + w, 8);
            Q0a = pack8(qa, 0, QSCALE); Q0b = pack8(qb, 0, QSCALE);
            q1a = u32x2{pk2(qa[8] * QSCALE, qa[9] * QSCALE), pk2(qa[10] * QSCALE, qa[11] * QSCALE)};
            q1b = u32x2{pk2(qb[8] * QSCALE, qb[9] * QSCALE), pk2(qb[10] * QSCALE, qb[11] * QSCALE)};
            if (h) { q1a = u32x2{0u, 0u}; q1b = u32x2{0u, 0u}; }
        }
        {
            float na = 0.f, nb = 0.f;
            {
                const u32x4 a0 = __builtin_bit_cast(u32x4, Q0a), b0 = __builtin_bit_cast(u32x4, Q0b);
#pragma unroll
                for (int e = 0; e < 4; ++e) { na += bflo(a0[e]) * bflo(a0[e]) + bfhi(a0[e]) * bfhi(a0[e]); nb += bflo(b0[e]) * bflo(b0[e]) + bfhi(b0[e]) * bfhi(b0[e]); }
#pragma unroll
                for (int e = 0; e < 2; ++e) { na += bflo(q1a[e]) * bflo(q1a[e]) + bfhi(q1a[e]) * bfhi(q1a[e]); nb += bflo(q1b[e]) * bflo(q1b[e]) + bfhi(q1b[e]) * bfhi(q1b[e]); }
                const auto sa = __builtin_amdgcn_permlane32_swap(__float_as_uint(na), __float_as_uint(na), false, false), sb = __builtin_amdgcn_permlane32_swap(__float_as_uint(nb), __float_as_uint(nb), false, false);
                na = __uint_as_float(sa[0]) + __uint_as_float(sa[1]); nb = __uint_as_float(sb[0]) + __uint_as_float(sb[1]);
            }
            if (!early) {
                take8x2(fk, fv, fk2, fv2);
                early = __all((unsigned)(fk >> 32) == tag && (unsigned)(fv >> 32) == tag);
                if (early) { lwait_all(C_AP, 4, 6, lane); att_dma(as, 0); att_dma(as, 1); }
            }
            if (!early && w == 0) {
                gu64* f = (gu64*)(ws + WS_KF + xc * 1024) + lane;
                _Pragma("clang loop unroll(disable)") for (unsigned spins = 0; spins < (1u << 14); ++spins) {
                    fk = __hip_atomic_load(f, RLX_AGENT); fv = __hip_atomic_load(f + 64, RLX_AGENT);
                    if (__all((unsigned)(fk >> 32) == tag && (unsigned)(fv >> 32) == tag)) break;
                    __builtin_amdgcn_s_sleep(4);
                }
            }
            float kn2 = wave_max(__uint_as_float((unsigned)fk)), vmx = wave_max(__uint_as_float((unsigned)fv));
            if (w == 0) { lpost(C_KN, __float_as_uint(kn2)); lpost(C_VM, __float_as_uint(vmx)); lpost(C_KVRDY, 1); }
            if (!early) {
                lwait(C_KVRDY, 1);
                kn2 = __uint_as_float(*(volatile AS3 unsigned*)lcnt(C_KN)); vmx = __uint_as_float(*(volatile AS3 unsigned*)lcnt(C_VM));
                lwait_all(C_AP, 4, 6, lane);
                att_dma(as, 0); att_dma(as, 1);
            }
            const float Ma = __builtin_amdgcn_sqrtf(na * kn2) * 1.01f, Mb = __builtin_amdgcn_sqrtf(nb * kn2) * 1.01f;
            const bool fast = __all(Ma <= BOUND_MAX && Mb <= BOUND_MAX && vmx <= 1e30f);
            const u32x2 a1 = {(fast && h) ? pk2(-Ma, 0.f) : q1a[0], q1a[1]}, b1 = {(fast && h) ? pk2(-Mb, 0.f) : q1b[0], q1b[1]};
            f32x16 Oa = {}, Ob = {};
            WAITV(3); lpost(C_AP + w, AP0); lwait_all(C_AP, 4, AP0, lane);
            att_dma(as, 2);
            __builtin_amdgcn_s_setprio(2);
            if (fast) att_fast(as, Q0a, __builtin_bit_cast(s16x4, a1), Q0b, __builtin_bit_cast(s16x4, b1), Oa, Ob);
            else att_safe(as, Q0a, __builtin_bit_cast(s16x4, a1), Q0b, __builtin_bit_cast(s16x4, b1), Oa, Ob);
            const __amdgpu_buffer_rsrc_t ro = mkrsrc(ws + WS_OT, 64 * 40 * 1024);
#pragma unroll
            for (int q = 0; q < 2; ++q) {
                const f32x16& O = q ? Ob : Oa;
                const auto lsw = __builtin_amdgcn_permlane32_swap(__float_as_uint(O[8]), __float_as_uint(O[8]), false, false);
                const float inv = __builtin_amdgcn_rcpf(__uint_as_float(lsw[1]));
                const unsigned base = ((2 * ti + q) * 40 + 5 * g) * 1024;
                __builtin_amdgcn_raw_buffer_store_b128(__builtin_bit_cast(u32x4, pack8(O, 0, inv)), ro, base + w * 1024 + lane * 16, 0, 16);
                if (!h) {
                    u32x2 t = {pk2(O[8] * inv, O[9] * inv), pk2(O[10] * inv, O[11] * inv)};
                    __builtin_amdgcn_raw_buffer_store_b64(t, ro, base + 4096 + ((w >> 1) * 32 + r) * 16 + (w & 1) * 8, 0, 16);
                }
            }
            {
                WAITV(0);
                if (lane < 2) __hip_atomic_store((gu32*)(ws + WS_OF) + (2 * ti + lane) * 32 + head, tag, RLX_AGENT);
            }
        }
    }
}

extern "C" void kernel_launch(void* const* d_in, const int* in_sizes, int n_in, void* d_out, int out_size, void* d_ws, size_t ws_size, hipStream_t stream) {
    Params p;
    p.x = (const char*)d_in[0]; p.wq = (const char*)d_in[1]; p.wk = (const char*)d_in[2]; p.wv = (const char*)d_in[3]; p.wo = (const char*)d_in[4];
    p.w1 = (const char*)d_in[6]; p.w3 = (const char*)d_in[7]; p.w2 = (const char*)d_in[8];
    p.out = (char*)d_out; p.ws = (char*)d_ws;
    (void)hipFuncSetAttribute((const void*)mega, hipFuncAttributeMaxDynamicSharedMemorySize, L_TOTAL);
    mega<<<256, 512, L_TOTAL, stream>>>(p);
}
```
